```python
import torch

def _softmax(x, axis=-1):
    return torch.softmax(x, dim=axis)
def _gelu(x):
    return torch.nn.functional.gelu(x, approximate="tanh")
def _silu(x):
    return torch.nn.functional.silu(x)
def _sigmoid(x):
    return torch.sigmoid(x)
def _layernorm(x, axis=-1, eps=1e-5):
    m = x.mean(dim=axis, keepdim=True)
    v = x.var(dim=axis, keepdim=True, unbiased=False)
    return (x - m) / torch.sqrt(v + eps)
def _scatter_add(tgt, idx, src, axis=0):
    return tgt.clone().index_add_(axis, idx, src)
def _segment_sum(src, seg, n):
    out = torch.zeros((n,) + src.shape[1:], dtype=src.dtype)
    return out.index_add_(0, seg, src)
def _pad(x, pw):
    flat = tuple(v for pair in reversed(pw) for v in pair)
    return torch.nn.functional.pad(x, flat)

kernel_name = 'recurrent_scan__mha_block_cached__mixer_s_mlp__prompt'

def setup_inputs():

    _w = (lambda a: (lambda t: t.to(torch.bfloat16) if t.is_floating_point() else t)(torch.as_tensor(a)))
    def _A_setup():
        B, Sq, D = 1, 2048, 576
        H, Sk, Dh = 32, 2048, 128
        return {
            "x": _w((lambda *s: torch.randn(*s, dtype=torch.bfloat16) * 0.05)(B, Sq, D)),
            "k_cache": _w((lambda *s: torch.randn(*s, dtype=torch.bfloat16) * 0.8)(B, H, Sk, Dh)),
            "v_cache": _w((lambda *s: torch.randn(*s, dtype=torch.bfloat16) * 0.8)(B, H, Sk, Dh)),
            "wq": _w((lambda *s: torch.randn(*s, dtype=torch.bfloat16) * 0.8)(D, H * Dh) * (D ** -0.5)),
            "wo": _w((lambda *s: torch.randn(*s, dtype=torch.bfloat16) * 0.8)(H * Dh, D) * ((H * Dh) ** -0.5)),
        }
    _A_in = _A_setup()

    def _B_setup():
        B, S, D, F = 1, 2048, 576, 4608
        return {
            "x":  _w((lambda *s: torch.randn(*s, dtype=torch.bfloat16) * 0.05)(B, S, D)),
            "w1": _w((lambda *s: torch.randn(*s, dtype=torch.bfloat16) * 3.2)(D, D) * (D ** -0.5)),
            "w2": _w((lambda *s: torch.randn(*s, dtype=torch.bfloat16) * 3.2)(D, D) * (D ** -0.5)),
            "w3": _w((lambda *s: torch.randn(*s, dtype=torch.bfloat16) * 3.2)(D, F) * (D ** -0.5)),
            "w4": _w((lambda *s: torch.randn(*s, dtype=torch.bfloat16) * 3.2)(F, D) * (F ** -0.5)),
        }
    _B_in = _B_setup()

    return {
        "A_x": _A_in["x"],
        "A_k_cache": _A_in["k_cache"],
        "A_v_cache": _A_in["v_cache"],
        "A_wq": _A_in["wq"],
        "A_wo": _A_in["wo"],
        "B_x": _B_in["x"],
        "B_w1": _B_in["w1"],
        "B_w2": _B_in["w2"],
        "B_w3": _B_in["w3"],
        "B_w4": _B_in["w4"],
    }

def reference(A_x, A_k_cache, A_v_cache, A_wq, A_wo, B_x, B_w1, B_w2, B_w3, B_w4):
    def _A_ref(x, k_cache, v_cache, wq, wo):
        B, Sq, D = x.shape
        H, Dh = 32, 128
        q = (lambda x, *d: x.permute(*d))((x @ wq).reshape(B, Sq, H, Dh), 0, 2, 1, 3)
        scores = torch.einsum("bhqd,bhkd->bhqk", q, k_cache) * (Dh ** -0.5)
        p = _softmax(scores, axis=-1)
        o = torch.einsum("bhqk,bhkd->bhqd", p, v_cache)
        return (lambda x, *d: x.permute(*d))(o, 0, 2, 1, 3).reshape(B, Sq, H * Dh) @ wo

    def _B_ref(x, w1, w2, w3, w4):
        B, S, D = x.shape
        h = _gelu(_layernorm(x, axis=-1) @ w1)
        c = D // 4
        g0 = _pad(h[:, :, 0*c:1*c], ((0, 0), (1, 0), (0, 0)))[:, :S, :]
        g1 = _pad(h[:, :, 1*c:2*c], ((0, 0), (0, 1), (0, 0)))[:, 1:, :]
        g2 = _pad(h[:, :, 2*c:3*c], ((0, 0), (2, 0), (0, 0)))[:, :S, :]
        g3 = _pad(h[:, :, 3*c:   ], ((0, 0), (0, 2), (0, 0)))[:, 2:, :]
        hs = torch.concatenate([g0, g1, g2, g3], axis=-1) @ w2
        x1 = x + hs
        h2 = _gelu(_layernorm(x1, axis=-1) @ w3) @ w4
        return x1 + h2

    x = A_x
    xn = _layernorm(x, axis=-1)
    s = _A_ref(xn, A_k_cache, A_v_cache, A_wq, A_wo)
    g = _silu(_B_ref(xn, B_w1, B_w2, B_w3, B_w4))
    return x + s * g
```

```cpp
#include <hip/hip_runtime.h>
#include <hip/hip_bf16.h>
#include <stdint.h>

typedef unsigned short u16;
using bf16x8 = __attribute__((ext_vector_type(8))) short;
using f32x16 = __attribute__((ext_vector_type(16))) float;
using f32x4 = __attribute__((ext_vector_type(4))) float;
#define DI __device__ __forceinline__

constexpr int S = 2048, DM = 576, H = 32, DH = 128, FF = 4608;

DI float bf2f(u16 v) { return __uint_as_float(((unsigned)v) << 16); }
DI u16 f2bf(float f) { __hip_bfloat16 b = __float2bfloat16(f); return *reinterpret_cast<u16*>(&b); }
DI float rbf(float f) { return bf2f(f2bf(f)); }
DI float gelu_t(float x) {
  float u = 0.7978845608028654f * (x + 0.044715f * x * x * x);
  return x / (1.f + __expf(-2.f * u));
}

#define MIMIC 0

DI void ln_row(const float* v, float* o) {
  float s = 0;
  for (int i = 0; i < 9; ++i) s += v[i];
  for (int d = 32; d; d >>= 1) s += __shfl_xor(s, d);
  float m = s * (1.f / DM);
  float q = 0;
  for (int i = 0; i < 9; ++i) { float d = v[i] - m; q += d * d; }
  for (int d = 32; d; d >>= 1) q += __shfl_xor(q, d);
  float var = q * (1.f / DM);
  float r = rsqrtf(var + 1e-5f);
  for (int i = 0; i < 9; ++i) o[i] = rbf((v[i] - m) * r);
}

__global__ void k_ln2(const u16* x, u16* xn, u16* xn2) {
  int row = blockIdx.x * 4 + (threadIdx.x >> 6), lane = threadIdx.x & 63;
  float v[9], o[9], o2[9];
  for (int i = 0; i < 9; ++i) v[i] = bf2f(x[row * DM + i * 64 + lane]);
  ln_row(v, o);
  ln_row(o, o2);
  for (int i = 0; i < 9; ++i) { xn[row * DM + i * 64 + lane] = f2bf(o[i]); xn2[row * DM + i * 64 + lane] = f2bf(o2[i]); }
}
__global__ void k_ln1(const u16* x, u16* xn) {
  int row = blockIdx.x * 4 + (threadIdx.x >> 6), lane = threadIdx.x & 63;
  float v[9], o[9];
  for (int i = 0; i < 9; ++i) v[i] = bf2f(x[row * DM + i * 64 + lane]);
  ln_row(v, o);
  for (int i = 0; i < 9; ++i) xn[row * DM + i * 64 + lane] = f2bf(o[i]);
}

template <int EPI>
__global__ __launch_bounds__(256) void k_gemm(const u16* A, const u16* W, u16* C, int M, int N, int K,
                                              const u16* r1, const u16* r2, const u16* r3) {
  __shared__ u16 As[64][40];
  __shared__ u16 Bs[64][40];
  int tid = threadIdx.x, lane = tid & 63, w = tid >> 6, wr = w >> 1, wc = w & 1;
  int m0 = blockIdx.y * 64, n0 = blockIdx.x * 64;
  f32x16 acc = {};
  for (int k0 = 0; k0 < K; k0 += 32) {
    {
      int r = tid >> 2, c = (tid & 3) * 8;
      int gr = m0 + r;
      bf16x8 v = {};
      if (EPI == 2) {
        int g = (k0 + c) / 144;
        int sr = gr + (g == 0 ? -1 : g == 1 ? 1 : g == 2 ? -2 : 2);
        if (sr >= 0 && sr < M) v = *(const bf16x8*)(A + (long)sr * K + k0 + c);
      } else v = *(const bf16x8*)(A + (long)gr * K + k0 + c);
      *(bf16x8*)&As[r][c] = v;
      int kk = tid >> 3, nn = (tid & 7) * 8;
      bf16x8 b = *(const bf16x8*)(W + (long)(k0 + kk) * N + n0 + nn);
      for (int j = 0; j < 8; ++j) Bs[nn + j][kk] = b[j];
    }
    __syncthreads();
    for (int ks = 0; ks < 2; ++ks) {
      bf16x8 a = *(const bf16x8*)&As[wr * 32 + (lane & 31)][ks * 16 + (lane >> 5) * 8];
      bf16x8 b = *(const bf16x8*)&Bs[wc * 32 + (lane & 31)][ks * 16 + (lane >> 5) * 8];
      acc = __builtin_amdgcn_mfma_f32_32x32x16_bf16(a, b, acc, 0, 0, 0);
    }
    __syncthreads();
  }
  for (int r = 0; r < 16; ++r) {
    int row = m0 + wr * 32 + (r & 3) + 8 * (r >> 2) + 4 * (lane >> 5), col = n0 + wc * 32 + (lane & 31);
    long idx = (long)row * N + col;
    float v = acc[r];
    if (EPI == 1) v = gelu_t(rbf(v));
    if (EPI == 2) v = bf2f(r1[idx]) + rbf(v);
    if (EPI == 3) {
      float b = rbf(bf2f(r1[idx]) + rbf(v));
      float g = rbf(b / (1.f + __expf(-b)));
      v = bf2f(r3[idx]) + rbf(bf2f(r2[idx]) * g);
    }
    C[idx] = f2bf(v);
  }
}

__global__ __launch_bounds__(256) void k_attn(const u16* Q, const u16* Kc, const u16* Vc, u16* O) {
  __shared__ u16 Ks[32][136];
  __shared__ u16 Vt[128][40];
  int tid = threadIdx.x, lane = tid & 63, w = tid >> 6, r32 = lane & 31, hi = lane >> 5;
  int h = blockIdx.y, q0 = blockIdx.x * 128 + w * 32;
  bf16x8 qf[8];
  for (int d = 0; d < 8; ++d) qf[d] = *(const bf16x8*)(Q + (long)(q0 + r32) * (H * DH) + h * DH + d * 16 + hi * 8);
  f32x16 o[4] = {};
  float m = -1e30f, l = 0;
  const float c = 0.088388347648318440f * 1.4426950408889634f;
  for (int k0 = 0; k0 < S; k0 += 32) {
    __syncthreads();
    for (int i = 0; i < 2; ++i) {
      int e = tid + i * 256, r = e >> 4, cc = (e & 15) * 8;
      *(bf16x8*)&Ks[r][cc] = *(const bf16x8*)(Kc + ((long)h * S + k0 + r) * DH + cc);
      bf16x8 v = *(const bf16x8*)(Vc + ((long)h * S + k0 + r) * DH + cc);
      for (int j = 0; j < 8; ++j) Vt[cc + j][r] = v[j];
    }
    __syncthreads();
    f32x16 s = {};
    for (int d = 0; d < 8; ++d) {
      bf16x8 kf = *(const bf16x8*)&Ks[r32][d * 16 + hi * 8];
      s = __builtin_amdgcn_mfma_f32_32x32x16_bf16(kf, qf[d], s, 0, 0, 0);
    }
    float mx = s[0];
    for (int r = 1; r < 16; ++r) mx = fmaxf(mx, s[r]);
    mx = fmaxf(mx, __shfl_xor(mx, 32));
    float mn = fmaxf(m, mx), al = exp2f((m - mn) * c);
    m = mn;
    float ps = 0;
    for (int r = 0; r < 16; ++r) { s[r] = exp2f((s[r] - mn) * c); ps += s[r]; }
    ps += __shfl_xor(ps, 32);
    l = l * al + ps;
    for (int d = 0; d < 4; ++d) for (int r = 0; r < 16; ++r) o[d][r] *= al;
    for (int ks = 0; ks < 2; ++ks) {
      bf16x8 p;
      for (int j = 0; j < 8; ++j) p[j] = (short)f2bf(s[8 * ks + j]);
      for (int d = 0; d < 4; ++d) {
        bf16x8 vf;
        for (int j = 0; j < 8; ++j) vf[j] = Vt[d * 32 + r32][16 * ks + 8 * (j >> 2) + 4 * hi + (j & 3)];
        o[d] = __builtin_amdgcn_mfma_f32_32x32x16_bf16(vf, p, o[d], 0, 0, 0);
      }
    }
  }
  float il = 1.f / l;
  for (int d = 0; d < 4; ++d) for (int r = 0; r < 16; ++r) {
    int dd = d * 32 + (r & 3) + 8 * (r >> 2) + 4 * hi;
    O[(long)(q0 + r32) * (H * DH) + h * DH + dd] = f2bf(o[d][r] * il);
  }
}

extern "C" void kernel_launch(void* const* d_in, const int* in_sizes, int n_in, void* d_out, int out_size,
                              void* d_ws, size_t ws_size, hipStream_t stream) {
  const u16* x = (const u16*)d_in[0];
  const u16* kc = (const u16*)d_in[1];
  const u16* vc = (const u16*)d_in[2];
  const u16* wq = (const u16*)d_in[3];
  const u16* wo = (const u16*)d_in[4];
  const u16* w1 = (const u16*)d_in[6];
  const u16* w2 = (const u16*)d_in[7];
  const u16* w3 = (const u16*)d_in[8];
  const u16* w4 = (const u16*)d_in[9];
  u16* out = (u16*)d_out;
  char* ws = (char*)d_ws;
  size_t sz = (size_t)S * DM * 2;
  u16* xn = (u16*)(ws); u16* xn2 = (u16*)(ws + sz); u16* hbuf = (u16*)(ws + 2 * sz); u16* x1 = (u16*)(ws + 3 * sz);
  u16* x1n = (u16*)(ws + 4 * sz); u16* sb = (u16*)(ws + 5 * sz);
  u16* q = (u16*)(ws + 8 * sz); u16* o = q + (size_t)S * H * DH; u16* g = o + (size_t)S * H * DH;
  k_ln2<<<S / 4, 256, 0, stream>>>(x, xn, xn2);
  k_gemm<1><<<dim3(DM / 64, S / 64), 256, 0, stream>>>(xn2, w1, hbuf, S, DM, DM, nullptr, nullptr, nullptr);
  k_gemm<2><<<dim3(DM / 64, S / 64), 256, 0, stream>>>(hbuf, w2, x1, S, DM, DM, xn, nullptr, nullptr);
  k_ln1<<<S / 4, 256, 0, stream>>>(x1, x1n);
  k_gemm<1><<<dim3(FF / 64, S / 64), 256, 0, stream>>>(x1n, w3, g, S, FF, DM, nullptr, nullptr, nullptr);
  k_gemm<0><<<dim3(H * DH / 64, S / 64), 256, 0, stream>>>(xn, wq, q, S, H * DH, DM, nullptr, nullptr, nullptr);
  k_attn<<<dim3(S / 128, H), 256, 0, stream>>>(q, kc, vc, o);
  k_gemm<0><<<dim3(DM / 64, S / 64), 256, 0, stream>>>(o, wo, sb, S, DM, H * DH, nullptr, nullptr, nullptr);
  k_gemm<3><<<dim3(DM / 64, S / 64), 256, 0, stream>>>(g, w4, out, S, DM, FF, x1, sb, x);
}
```

```cpp
#include <hip/hip_runtime.h>
#include <hip/hip_bf16.h>
#include <stdint.h>

typedef unsigned short u16;
constexpr int LNK = 10, NM2 = 2, W2E = 4;
using f32x2 = __attribute__((ext_vector_type(2))) float;
using u32x4 = __attribute__((ext_vector_type(4))) unsigned;
using u32x2 = __attribute__((ext_vector_type(2))) unsigned;
using bf16x2v = __attribute__((ext_vector_type(2))) __bf16;
#define DI __device__ __forceinline__
#define RSRC(p, n) __builtin_amdgcn_make_buffer_rsrc((void*)(p), (short)0, (int)(n), 0x00020000)

DI unsigned pack2(float lo, float hi) { f32x2 v = {lo, hi}; return __builtin_bit_cast(unsigned, __builtin_convertvector(v, bf16x2v)); }
DI float blo(unsigned w) { return __uint_as_float(w << 16); }
DI float bhi(unsigned w) { return __uint_as_float(w & 0xffff0000u); }
DI float rbf(float f) { return blo(pack2(f, f)); }
DI unsigned rfl(unsigned x) { return __builtin_amdgcn_readfirstlane(x); }

typedef unsigned long long u64;
constexpr unsigned FL_XN = 0, FL_H = 256, FL_X1 = 512, FL_Y = 768, FL_O = 1024, FL_G = 1280, FL_S = 1536, FL_EPOCH = 255, FL_BYTES = 2048 * 64;
DI void st16(__amdgpu_buffer_rsrc_t srd, unsigned voff, u32x4 v) {
  asm volatile("buffer_store_dwordx4 %0, %1, %2, 0 offen sc1\n\ts_nop 1" :: "v"(v), "v"(voff), "s"(srd) : "memory");
}

constexpr size_t SZ_A = 2048u * 1152u;
constexpr size_t WS_XN = 0, WS_H = SZ_A, WS_X1 = 2 * SZ_A, WS_Y = 3 * SZ_A, WS_O = 4 * SZ_A, WS_G = WS_O + 2048u * 8192u, WS_P = WS_G + 2048u * 9216u,
                 WS_GAP = WS_P + 8u * 4718592u, WS_FL = WS_GAP + 1048576u, WS_N2 = WS_FL + 1048576u  , WS_END = WS_N2 + SZ_A;


DI void dma3(__amdgpu_buffer_rsrc_t srd, const unsigned (&wd)[3], unsigned m0, unsigned soff) {
  asm volatile("s_mov_b32 m0, %0\n\ts_nop 0\n\t"
               "buffer_load_dwordx4 %2, %5, %1 offen lds\n\t"
               "buffer_load_dwordx4 %3, %5, %1 offen offset:1024 lds\n\t"
               "buffer_load_dwordx4 %4, %5, %1 offen offset:2048 lds"
               :: "s"(m0), "s"(soff), "v"(wd[0]), "v"(wd[1]), "v"(wd[2]), "s"(srd) : "memory");
}
DI void sg_chunk_dma(__amdgpu_buffer_rsrc_t srd, const unsigned (&wd)[3], unsigned slot, unsigned w, int c) {
  const unsigned m = slot + w * 6144u, so = (64u * c + 16u * w) * 1152u;
  dma3(srd, wd, m, so);
  dma3(srd, wd, m + 3072u, so + 8u * 1152u);
}
DI void wq_chunk_dma(__amdgpu_buffer_rsrc_t srd, const unsigned (&wd)[4], unsigned ldsb, unsigned w, int c) {
  asm volatile("s_mov_b32 m0, %0\n\ts_nop 0\n\t"
               "buffer_load_dwordx4 %2, %6, %1 offen lds\n\t"
               "buffer_load_dwordx4 %3, %6, %1 offen offset:1024 lds\n\t"
               "buffer_load_dwordx4 %4, %6, %1 offen offset:2048 lds\n\t"
               "buffer_load_dwordx4 %5, %6, %1 offen offset:3072 lds"
               :: "s"(ldsb + 49152u + c * 16384u + w * 4096u), "s"((64u * c + 16u * w) * 8192u), "v"(wd[0]), "v"(wd[1]), "v"(wd[2]), "v"(wd[3]), "s"(srd) : "memory");
}
DI void w_lane_offsets(unsigned l, unsigned pitch, unsigned (&wd)[3]) {
  const unsigned g = l >> 4, p = l & 3u, q4 = (l >> 2) & 3u;
#pragma unroll
  for (int ty = 0; ty < 3; ++ty) {
    const unsigned bi = 4u * ty + g, rg = bi >= 6u ? 1u : 0u, nb = bi - 6u * rg;
    wd[ty] = (4u * rg + q4) * pitch + nb * 64u + ((p ^ (2u * rg)) * 16u) + 2048u - 1024u * ty;
  }
}
DI unsigned frag_off(unsigned mb, unsigned kk, unsigned KK) { return (((mb >> 2) * KK + kk) * 4u + (mb & 3u)) * 1024u; }

template <int CTRL> DI float dpp_add(float v) {
  return v + __builtin_bit_cast(float, __builtin_amdgcn_update_dpp(0, __builtin_bit_cast(int, v), CTRL, 0xf, 0xf, true));
}
DI float sum16(float v) { return dpp_add<0x140>(dpp_add<0x141>(dpp_add<0x4E>(dpp_add<0xB1>(v)))); }
DI void ln36(const f32x2 (&x)[18], float& m, float& r) {
  f32x2 s = x[0], s2 = x[1];
#pragma unroll
  for (int i = 2; i < 18; i += 2) { s += x[i]; s2 += x[i + 1]; }
  s += s2;
  m = sum16(s[0] + s[1]) * (1.f / 576.f);
  f32x2 q = {0.f, 0.f}, q2 = {0.f, 0.f};
#pragma unroll
  for (int i = 0; i < 18; i += 2) {
    const f32x2 d = x[i] - m, d2 = x[i + 1] - m;
    q = __builtin_elementwise_fma(d, d, q); q2 = __builtin_elementwise_fma(d2, d2, q2);
  }
  q += q2;
  r = __builtin_amdgcn_rcpf(rbf(__builtin_amdgcn_sqrtf(rbf(rbf(sum16(q[0] + q[1]) * (1.f / 576.f)) + 1e-5f))));
  m = rbf(m);
}
DI unsigned lo32(const void* p) { return (unsigned)(size_t)p; }
DI unsigned hi16(const void* p) { return (unsigned)((size_t)p >> 32) & 0xffffu; }

extern "C" __global__ __launch_bounds__(256, 1)
void mega(const u16* __restrict__ x, const u16* __restrict__ Kc, const u16* __restrict__ Vc, const u16* __restrict__ wq, const u16* __restrict__ wo,
          const u16* __restrict__ w1, const u16* __restrict__ w2, const u16* __restrict__ w3, const u16* __restrict__ w4,
          u16* __restrict__ out, char* __restrict__ ws) {
  extern __shared__ __attribute__((aligned(16))) char lds[];
  const unsigned b = blockIdx.x, w = rfl(threadIdx.x >> 6), l = threadIdx.x & 63u, ldsb = (unsigned)(size_t)lds;
  const unsigned j = b >> 3;
  const bool spare = j >= 24u;
  const unsigned kind = spare ? 0u : 1u + (b & 1u), bs = spare ? 0u : b >> 1;
  const bool lnrole = spare || (kind == 2u && j >= 8u);
  const unsigned lnmb = spare ? b - 128u : (b >> 1) - 32u;
  u32x2 xr[9];
  unsigned csink = 0;
  {
    const __amdgpu_buffer_rsrc_t srdX0 = RSRC((const char*)x + (size_t)(lnrole ? lnmb : 0u) * 18432u, lnrole ? 18432u : 0u);
    const unsigned xo = (threadIdx.x >> 4) * 1152u + (threadIdx.x & 15u) * 8u;
#pragma unroll
    for (int jj = 0; jj < 9; ++jj) asm volatile("buffer_load_dwordx2 %0, %1, %2, 0 offen offset:%3" : "=v"(xr[jj]) : "v"(xo), "s"(srdX0), "i"(128 * jj) : "memory");
    u64 pc; unsigned clen;
    asm volatile("s_getpc_b64 %0\nLM_PC0:\n\ts_mov_b32 %1, LM_FIN-LM_PC0" : "=s"(pc), "=s"(clen));
    const __amdgpu_buffer_rsrc_t srdC = RSRC((const char*)(size_t)pc, clen);
    asm volatile("buffer_load_dword %0, %1, %2, %3 offen" : "+{v226}"(csink) : "v"(threadIdx.x * 64u), "s"(srdC), "s"((j & 7u) * 16384u) : "memory");
    __builtin_amdgcn_sched_barrier(0);
  }
  const __amdgpu_buffer_rsrc_t srdF = RSRC(ws + WS_FL, FL_BYTES);
  const __amdgpu_buffer_rsrc_t srdA = RSRC(ws, WS_END);
  const unsigned sidx = b - 192u;
  const unsigned r = spare ? 192u + sidx : (kind - 1u) * 96u + bs;
  const unsigned rb = bs / 3u, cb3 = bs - 3u * rb;
  const unsigned hc = (b & 7u) * 4u + ((b >> 3) & 3u), qblk = b >> 5, mb64 = 4u * qblk + w;
  const unsigned c16 = l & 15u, g = l >> 4, p4 = l & 3u, q4 = (l >> 2) & 3u;
  unsigned wd1[3], wqd[4];
  w_lane_offsets(l, 1152u, wd1);
#pragma unroll
  for (int i = 0; i < 4; ++i) wqd[i] = q4 * 8192u + g * 64u + p4 * 16u + i * (32768u - 1024u);
  const char* wqp = (const char*)wq + hc * 256u;
  const __amdgpu_buffer_rsrc_t srdWq = RSRC(wqp, 576u * 8192u - hc * 256u);
  const char* wsp = (const char*)(kind == 1u ? w1 : w2) + cb3 * 384u - 2048;
  const unsigned wsn = spare ? 0u : 576u * 1152u - cb3 * 384u + 2048u;
  const __amdgpu_buffer_rsrc_t srdWs = RSRC(wsp, wsn);
#define TOUCHES() do { \
  asm volatile("buffer_load_dword %0, %1, %2, %3 offen" : "+{v226}"(csink) \
               : "v"((l >> 3) * 1152u + min(l & 7u, 5u) * 64u), "s"(srdWs), "s"(((rb * 4u + w) % 72u) * 8u * 1152u + 2048u) : "memory"); \
  asm volatile("buffer_load_dword %0, %1, %2, %3 offen" : "+{v226}"(csink) \
               : "v"((l >> 1) * 8192u + (l & 1u) * 128u), "s"(srdWq), "s"((qblk * 4u + w) * 18u * 8192u) : "memory"); } while (0)
  const unsigned sp = b & 7u, cb4 = (b >> 3) & 3u;
  unsigned pv, pv2, kb[4], kd[4], vb[2], vd[2], wd9[3], ho[4], ho2[4], wb, t1, pg, l16, xfl;
  auto tables = [&](auto&& hook) __attribute__((always_inline)) {
    pv = 0; pv2 = 0;
#define PSET(i, val) asm volatile("v_writelane_b32 %0, %1, " #i : "+v"(pv) : "s"((unsigned)(val)))
#define PSET2(i, val) asm volatile("v_writelane_b32 %0, %1, " #i : "+v"(pv2) : "s"((unsigned)(val)))
#define PSETV(i, val) asm volatile("s_nop 1\n\tv_writelane_b32 %0, %1, " #i : "+v"(pv) : "s"(rfl((unsigned)(val))))
    const unsigned col3 = 288u * hc, ca3 = col3 & ~127u, col5 = 288u * cb4, ca5 = col5 & ~127u;
    const char* kp = (const char*)Kc + (size_t)hc * 524288u;
    const char* vp = (const char*)Vc + (size_t)hc * 524288u;
    const char* op = ws + WS_O + ((size_t)mb64 * 128u + hc * 4u) * 4096u;
    const char* w3p = (const char*)w3 + ca3 - 2048;
    const char* gp = ws + WS_G + ((size_t)mb64 * 144u + ((9u * hc) >> 1)) * 4096u;
    const unsigned srole = b & 1u, sq = sp >> 1, krows = srole ? 1024u : 1152u;
    const char* x5p = srole ? ws + WS_O + ((size_t)mb64 * 128u + sq * 32u) * 4096u : ws + WS_G + ((size_t)mb64 * 144u + sq * 36u) * 4096u;
    const char* w5p = (srole ? (const char*)wo : (const char*)w4) + (size_t)sq * krows * 1152u + ca5 - 2048;
    const unsigned k0 = (0x0E090500u >> (8u * cb4)) & 255u;
    const unsigned k2 = srole ? 0u : cb4 == 0u ? 5u : 0u, k4 = srole ? 4u : cb4 <= 1u ? 9u : 5u;
    const unsigned k6 = srole ? 8u : cb4 <= 2u ? 14u : 9u, k8 = srole ? 12u : 4u, k9 = srole ? 14u : 18u;
    const unsigned wrow = w * 18432u;
    const char* pp = ws + WS_P + ((size_t)((sp * 8u + qblk) * 4u + cb4) * 4u + w) * 18432u;
    const unsigned wid = b * 4u + w;
    const char* wmp = wid < 324u ? (const char*)w3 + wid * 16384u : wid < 612u ? (const char*)wo + (wid - 324u) * 16384u : (const char*)w4 + (wid - 612u) * 16384u;
    const unsigned xw = (b >> 3) * 4u + w;
    const char* t3p = (const char*)w3 + sp * 1152u + (size_t)(xw * 9u / 2u) * 9216u;
    const unsigned fmb = 16u * qblk + 2u * sp, fn = 9u * cb4 + w;
    const unsigned hmask = kind != 2u ? 0u : (rb == 0u ? 0x1f8u : rb == 31u ? 0x03fu : 0x1ffu);
    hook(0);
    PSET2(0, lo32(x));
    PSET2(1, hi16(x));
    PSET2(2, lo32(out));
    PSET2(3, hi16(out));
    PSET2(4, (unsigned)WS_P + (qblk * 4u + cb4) * 73728u + sp * 9216u + w * 1024u);
    PSET2(5, (unsigned)WS_X1 + frag_off(fmb, fn >> 1, 18u) + (fn & 1u) * 8u);
    PSET2(6, 16u * fmb * 1152u + fn * 32u);
    hook(1);
    PSET2(7, 0xffu & ~(1u << sp));
    PSET2(8, wid);
    PSET2(9, lo32(wmp));
    PSET2(10, hi16(wmp));
    PSET2(11, wid < 936u ? 16384u : 0u);
    PSET2(12, (unsigned)WS_Y + (xw + 1u) * 18432u);
    PSET2(13, lo32(t3p));
    hook(2);
    PSET2(14, hi16(t3p));
    PSET2(15, min(5u, 576u - xw * 9u / 2u) * 9216u - 8064u);
    PSET2(16, (srole ? k2 : k0) * 36864u + wrow);
    PSET2(17, (srole ? k4 : k2) * 36864u + wrow);
    PSET2(18, k4 * 36864u + wrow);
    PSET2(19, k6 * 36864u + wrow);
    PSET2(20, (w < 2u ? k8 : 12u) * 36864u + wrow);
    hook(3);
    PSET2(21, k9 * 36864u + wrow);
    PSET2(22, k2 * 4096u);
    PSET2(23, k4 * 4096u);
    PSET2(24, k6 * 4096u);
    PSET2(25, k8 * 4096u);
    PSET2(26, 13u * 4096u);
    PSET2(27, k9 * 4096u);
    hook(4);
    PSET(0, lo32(ws));
    PSET(1, hi16(ws));
    PSET(2, lo32(ws + WS_FL));
    PSET(3, hi16(ws + WS_FL));
    PSET(4, lo32(wqp));
    PSET(5, hi16(wqp));
    PSET(6, 576u * 8192u - hc * 256u);
    hook(5);
    PSET(7, lo32(kp));
    PSET(8, hi16(kp));
    PSET(9, lo32(vp));
    PSET(10, hi16(vp));
    PSET(11, lo32(op));
    PSET(12, hi16(op));
    PSETV(13, lo32(wsp));
    hook(6);
    PSETV(14, hi16(wsp));
    PSET(15, wsn);
    PSET(16, lo32(w3p));
    PSET(17, hi16(w3p));
    PSET(18, 576u * 9216u - ca3 + 2048u);
    PSET(19, lo32(gp));
    PSET(20, hi16(gp));
    hook(7);
    PSET(21, lo32(x5p));
    PSET(22, hi16(x5p));
    PSET(23, srole * 16u * 8192u);
    PSET(24, lo32(w5p));
    PSET(25, hi16(w5p));
    PSET(26, krows * 1152u - ca5 + 2048u);
    PSET(27, srole);
    hook(8);
    PSET(28, srole * 0xffu);
    PSET(29, (1u - srole) * 0xffu);
    PSET(30, lo32(pp));
    PSET(31, hi16(pp));
    PSET(32, kind == 2u ? 2u * SZ_A : 0u);
    PSET(33, w);
    PSET(34, spare ? 5u : 0u);
    hook(9);
    PSET(35, kind);
    PSET(36, (unsigned)WS_N2 + (4u * rb + 2u * (w & 1u)) * 128u);
    PSET(37, ldsb);
    PSET(38, (unsigned)WS_XN + mb64 * 73728u);
    PSET(39, (unsigned)WS_Y + mb64 * 73728u);
    PSET(40, (NM2 == 2 || kind == 1u) ? frag_off(4u * rb + 2u * (w & 1u), 6u * cb3 + 3u * (w >> 1), 18u) : frag_off(4u * rb + w, 6u * cb3, 18u));
    PSET(41, frag_off(2u * sidx + (w & 1u), 9u * (w >> 1), 18u));
    hook(10);
    PSET(42, (col3 & 127u) >> 5);
    PSET(43, (col5 & 127u) >> 5);
    PSET(44, hc & 1u);
    PSET(45, kind == 1u ? 0x3fu : 0xfu);
    PSET(46, FL_H + r);
    PSET(47, FL_H + 3u * rb - 3u);
    PSET(48, hmask);
    hook(11);
    PSET(49, FL_X1 + r);
    PSET(50, FL_X1 + 96u + 3u * (sidx >> 1));
    PSET(51, FL_Y + sidx);
    PSET(52, FL_Y + 2u * mb64);
    PSET(53, FL_O + qblk * 32u + hc);
    PSET(54, FL_G + qblk * 32u + hc);
    PSET(55, FL_O + qblk * 32u + sq * 8u);
    PSET(56, FL_S + b);
    PSET(57, (((b >> 3) * 4u + w) & 31u) * 4096u);
#undef PSET
#undef PSETV
#undef PSET2
#pragma unroll
    for (int i = 0; i < 4; ++i) {
      kb[i] = ldsb + 65536u + c16 * 256u + 16u * ((4u * i + g) ^ c16);
      kd[i] = g * 256u + 16u * (c16 ^ (4u * i + g));
      const int rs = (int)(64u * rb + (NM2 == 2 ? 32u * (w & 1u) : 16u * w) + c16) + (i == 0 ? -1 : i == 1 ? 1 : i == 2 ? -2 : 2), r2 = rs + 16;
      ho[i] = (rs < 0 || rs >= 2048) ? 0x80000000u : frag_off((unsigned)rs >> 4, 0, 18u) + (((unsigned)rs & 15u) + 16u * g) * 16u;
      ho2[i] = r2 >= 2048 ? 0x80000000u : frag_off((unsigned)r2 >> 4, 0, 18u) + (((unsigned)r2 & 15u) + 16u * g) * 16u;
    }
#pragma unroll
    for (int e = 0; e < 2; ++e) {
      vb[e] = ldsb + g * 1024u + q4 * 64u + ((e ^ (g & 1u)) * 32u) + p4 * 8u;
      vd[e] = q4 * 256u + g * 64u + (p4 ^ (2u * e)) * 16u;
    }
    w_lane_offsets(l, 9216u, wd9);
    wb = ldsb + 49152u + g * 1024u + q4 * 64u + p4 * 16u;
    t1 = ldsb + g * 1536u + q4 * 64u + p4 * 8u; pg = (g & 1u) * 32u; l16 = l * 16u;
    xfl = (FL_XN + (l < 4u ? 4u * mb64 + l : 4u * rb + 2u * (w & 1u) + l - 4u)) * 64u;
    asm volatile("" : "+v"(kb[0]), "+v"(kb[1]), "+v"(kb[2]), "+v"(kb[3]), "+v"(kd[0]), "+v"(kd[1]), "+v"(kd[2]), "+v"(kd[3]), "+v"(vb[0]), "+v"(vb[1]),
                      "+v"(vd[0]), "+v"(vd[1]), "+v"(wd9[0]), "+v"(wd9[1]), "+v"(wd9[2]), "+v"(ho[0]), "+v"(ho[1]), "+v"(ho[2]), "+v"(ho[3]),
                      "+v"(ho2[0]), "+v"(ho2[1]), "+v"(ho2[2]), "+v"(ho2[3]), "+v"(wb), "+v"(t1), "+v"(pg), "+v"(xfl));
  };
  u64 ep;
  asm volatile("s_load_dwordx2 %0, %1, 0x0" : "=s"(ep) : "s"(ws + WS_FL + FL_EPOCH * 64u) : "memory");
  if (lnrole) {
    const unsigned mb = lnmb, row = 4u * w + g;
    TOUCHES();
    tables([&](int k) __attribute__((always_inline)) {
      if (k != LNK) return;
      asm volatile("s_waitcnt vmcnt(3)" : "+v"(xr[0]), "+v"(xr[1]), "+v"(xr[2]), "+v"(xr[3]), "+v"(xr[4]), "+v"(xr[5]), "+v"(xr[6]), "+v"(xr[7]), "+v"(xr[8]) :: "memory");
      char* stg = lds + 114688u + (c16 >> 3) * 1088u + p4 * 272u + row * 16u + (q4 & 1u) * 8u;
      f32x2 v[18];
#pragma unroll
      for (int jj = 0; jj < 9; ++jj) { v[2 * jj] = f32x2{blo(xr[jj][0]), bhi(xr[jj][0])}; v[2 * jj + 1] = f32x2{blo(xr[jj][1]), bhi(xr[jj][1])}; }
      float m1, r1, m2, r2;
      ln36(v, m1, r1);
#pragma unroll
      for (int jj = 0; jj < 9; ++jj) {
        const f32x2 y0 = __builtin_elementwise_fma(v[2 * jj], f32x2{r1, r1}, f32x2{-m1 * r1, -m1 * r1}), y1 = __builtin_elementwise_fma(v[2 * jj + 1], f32x2{r1, r1}, f32x2{-m1 * r1, -m1 * r1});
        const unsigned p0 = pack2(y0[0], y0[1]), p1 = pack2(y1[0], y1[1]);
        v[2 * jj] = f32x2{blo(p0), bhi(p0)}; v[2 * jj + 1] = f32x2{blo(p1), bhi(p1)};
        *(u32x2*)(stg + jj * 2176u) = u32x2{p0, p1};
      }
      ln36(v, m2, r2);
      if (c16 == 0u) *(f32x2*)(lds + 114688u + 19584u + row * 8u) = f32x2{r1 * r2, -(m1 * r1 + m2) * r2};
      asm volatile("s_waitcnt lgkmcnt(0)\n\ts_barrier" ::: "memory");
      const unsigned f0 = w * 9u / 2u;
      const unsigned dst = (unsigned)WS_XN + frag_off(mb, f0, 18u) + l * 16u;
      u32x4 fr[5];
#pragma unroll
      for (int i = 0; i < 5; ++i) fr[i] = *(const u32x4*)(lds + 114688u + (f0 + i) * 1088u + g * 272u + c16 * 16u);
      const u32x4 stt = *(const u32x4*)(lds + 114688u + 19584u + (l & 7u) * 16u);
#pragma unroll
      for (int i = 0; i < 5; ++i) st16(srdA, dst + i * 4096u, fr[i]);
      st16(srdA, (w == 3u && l < 8u) ? (unsigned)WS_N2 + mb * 128u + l * 16u : 0x80000000u, stt);
    });
    asm volatile("s_waitcnt vmcnt(0) lgkmcnt(0)\n\ts_barrier" ::: "memory");
    asm volatile("" : "+s"(ep));
    const u64 tag = ep + 0x9E3779B97F4A7C15ull;
    if (threadIdx.x == 0) asm volatile("buffer_store_dwordx2 %0, %1, %2, 0 offen sc1" :: "v"(tag), "v"((FL_XN + mb) * 64u), "s"(srdF) : "memory");
#pragma unroll
    for (int c = 0; c < 4; ++c) wq_chunk_dma(srdWq, wqd, ldsb, w, c);
  } else {
    const __amdgpu_buffer_rsrc_t srdXt = RSRC((const char*)x + (size_t)rb * 73728u, kind == 1u ? 73728u : 0u);
    asm volatile("s_waitcnt vmcnt(1)" : "+v"(xr[0]), "+v"(xr[1]), "+v"(xr[2]), "+v"(xr[3]), "+v"(xr[4]), "+v"(xr[5]), "+v"(xr[6]), "+v"(xr[7]), "+v"(xr[8]) :: "memory");
    TOUCHES();
    const unsigned xa = (l >> 3) * 1152u + 16u * ((l & 7u) ^ g), xb = (l >> 3) * 1152u + 16u * ((l & 7u) ^ (g + 4u)) + 8192u;
    tables([&](int k) __attribute__((always_inline)) {
      if (kind == 1u) {
        if (k < 9)
          asm volatile("s_mov_b32 m0, %0\n\ts_nop 0\n\tbuffer_load_dwordx4 %2, %4, %1 offen lds\n\tbuffer_load_dwordx4 %3, %4, %1 offen offset:1024 lds"
                       :: "s"(ldsb + 49152u + k * 8192u + w * 2048u), "s"(w * 18432u + k * 128u), "v"(xa), "v"(xb), "s"(srdXt) : "memory");
        else if (k == 9) sg_chunk_dma(srdWs, wd1, ldsb, w, 0);
      } else if (k % 3 == 0) wq_chunk_dma(srdWq, wqd, ldsb, w, k / 3);
    });
  }
  if (kind == 2u) {
#pragma unroll
    for (int c = 0; c < W2E; ++c) sg_chunk_dma(srdWs, wd1, ldsb + (c >> 1) * 114688u + (c & 1) * 24576u, w, c);
  }
  asm volatile("s_waitcnt lgkmcnt(0)" : "+s"(ep) :: "memory");
  const u64 tag = ep + 0x9E3779B97F4A7C15ull;
  {
    asm volatile(
"s_nop 1\n"
"v_readlane_b32 s64,v210,0\n"
"v_readlane_b32 s65,v210,1\n"
"s_mov_b32 s66,0x5340000\n"
"s_mov_b32 s67,0x20000\n"
"v_readlane_b32 s68,v210,2\n"
"v_readlane_b32 s69,v210,3\n"
"s_mov_b32 s70,0x20000\n"
"s_mov_b32 s71,0x20000\n"
"v_readlane_b32 s72,v210,4\n"
"v_readlane_b32 s73,v210,5\n"
"v_readlane_b32 s74,v210,6\n"
"s_mov_b32 s75,0x20000\n"
"v_readlane_b32 s92,v210,13\n"
"v_readlane_b32 s93,v210,14\n"
"v_readlane_b32 s94,v210,15\n"
"s_mov_b32 s95,0x20000\n"
"v_readlane_b32 s76,v210,7\n"
"v_readlane_b32 s77,v210,8\n"
"s_mov_b32 s78,0x80000\n"
"s_mov_b32 s79,0x20000\n"
"v_readlane_b32 s80,v210,9\n"
"v_readlane_b32 s81,v210,10\n"
"s_mov_b32 s82,0x80000\n"
"s_mov_b32 s83,0x20000\n"
"v_readlane_b32 s88,v210,0\n"
"v_readlane_b32 s89,v210,1\n"
"v_readlane_b32 s90,v210,32\n"
"s_mov_b32 s91,0x20000\n"
"v_readlane_b32 s35,v210,35\n"
"v_readlane_b32 s53,v210,38\n"
"v_readlane_b32 s59,v210,36\n"
"v_readlane_b32 s48,v210,33\n"
"v_readlane_b32 s49,v210,34\n"
"v_readlane_b32 s41,v210,37\n"
"v_readlane_b32 s58,v210,40\n"
"s_lshl_b32 s61,s48,12\n"
"s_add_u32 s63,s41,s61\n"
"s_add_u32 s62,s63,65536\n"
"s_add_u32 s50,s63,49152\n"
"s_mul_i32 s51,s48,6144\n"
"s_add_u32 s51,s51,s41\n"
"s_lshl_b32 s52,s48,17\n"
"s_mul_i32 s60,s48,18432\n"
"s_mov_b32 s54,0\n"
"s_lshr_b32 s43,s48,1\n"
"s_mul_i32 s43,s43,768\n"
"v_xor_b32 v236,0,v212\n"
"v_add3_u32 v236,v236,s43,v211\n"
"v_add_u32 v238,49152,v236\n"
"v_add_u32 v240,114688,v236\n"
"v_xor_b32 v237,32,v212\n"
"v_add3_u32 v237,v237,s43,v211\n"
"v_add_u32 v239,49152,v237\n"
"v_add_u32 v241,114688,v237\n"
"v_readlane_b32 s46,v210,45\n"
"s_mov_b32 s47,0\n"
"s_cmp_eq_u32 s35,1\n"
"s_cbranch_scc1 LM_G1\n"
"s_mov_b32 s55,0x20000\n"
"buffer_load_dwordx2 v[226:227],v223,s[68:71],0 offen sc1\n"
"s_sleep 8\n"
"LM_XP1:\n"
"buffer_load_dwordx2 v[242:243],v223,s[68:71],0 offen sc1\n"
"s_waitcnt vmcnt(1)\n"
"v_cmp_ne_u64 vcc,v[226:227],v[224:225]\n"
"s_nop 1\n"
"s_and_b64 s[44:45],vcc,s[46:47]\n"
"s_cbranch_scc0 LM_XD2\n"
"buffer_load_dwordx2 v[226:227],v223,s[68:71],0 offen sc1\n"
"s_waitcnt vmcnt(1)\n"
"v_cmp_ne_u64 vcc,v[242:243],v[224:225]\n"
"s_nop 1\n"
"s_and_b64 s[44:45],vcc,s[46:47]\n"
"s_cbranch_scc0 LM_XD2\n"
"s_sub_u32 s55,s55,1\n"
"s_cbranch_scc0 LM_XP1\n"
"LM_XD2:\n"
"s_add_u32 s40,s53,0\n"
"buffer_load_dwordx4 a[0:3],v209,s[64:67],s40 offen sc1\n"
"buffer_load_dwordx4 a[4:7],v209,s[64:67],s40 offen offset:1024 sc1\n"
"buffer_load_dwordx4 a[8:11],v209,s[64:67],s40 offen offset:2048 sc1\n"
"buffer_load_dwordx4 a[12:15],v209,s[64:67],s40 offen offset:3072 sc1\n"
"s_add_u32 s40,s53,4096\n"
"buffer_load_dwordx4 a[16:19],v209,s[64:67],s40 offen sc1\n"
"buffer_load_dwordx4 a[20:23],v209,s[64:67],s40 offen offset:1024 sc1\n"
"buffer_load_dwordx4 a[24:27],v209,s[64:67],s40 offen offset:2048 sc1\n"
"buffer_load_dwordx4 a[28:31],v209,s[64:67],s40 offen offset:3072 sc1\n"
"s_add_u32 s40,s53,8192\n"
"buffer_load_dwordx4 a[32:35],v209,s[64:67],s40 offen sc1\n"
"buffer_load_dwordx4 a[36:39],v209,s[64:67],s40 offen offset:1024 sc1\n"
"buffer_load_dwordx4 a[40:43],v209,s[64:67],s40 offen offset:2048 sc1\n"
"buffer_load_dwordx4 a[44:47],v209,s[64:67],s40 offen offset:3072 sc1\n"
"s_add_u32 s40,s53,12288\n"
"buffer_load_dwordx4 a[48:51],v209,s[64:67],s40 offen sc1\n"
"buffer_load_dwordx4 a[52:55],v209,s[64:67],s40 offen offset:1024 sc1\n"
"buffer_load_dwordx4 a[56:59],v209,s[64:67],s40 offen offset:2048 sc1\n"
"buffer_load_dwordx4 a[60:63],v209,s[64:67],s40 offen offset:3072 sc1\n"
"s_barrier\n"
"s_branch LM_QP\n"
"LM_G1:\n"
"v_mov_b32 v232,0x3f803f80\n"
"v_mov_b32 v233,0x3f803f80\n"
"v_mov_b32 v234,0x3f803f80\n"
"v_mov_b32 v235,0x3f803f80\n"
"v_lshrrev_b32 v0,8,v209\n"
"v_mul_u32_u24 v1,1536,v0\n"
"v_add3_u32 v184,v1,s43,v211\n"
"v_add_u32 v185,49152,v184\n"
"v_add_u32 v186,114688,v184\n"
"v_bfe_u32 v1,v209,5,3\n"
"v_xor_b32 v0,v0,v1\n"
"v_lshlrev_b32 v0,4,v0\n"
"v_and_b32 v1,0xf0,v209\n"
"v_lshl_add_u32 v0,v1,3,v0\n"
"s_and_b32 s42,s48,1\n"
"s_lshl_b32 s42,s42,12\n"
"s_add_u32 s42,s42,s41\n"
"s_add_u32 s42,s42,49152\n"
"v_add_u32 v0,s42,v0\n"
"v_xor_b32 v1,64,v0\n"
"v_add_u32 v2,32768,v0\n"
"v_add_u32 v3,32768,v1\n"
"s_waitcnt vmcnt(6)\n"
"s_barrier\n"
"ds_read_b128 a[128:131],v0\n"
"ds_read_b128 a[132:135],v0 offset:2048\n"
"s_add_u32 m0,s51,24576\n"
"s_add_u32 s42,s60,73728\n"
"buffer_load_dwordx4 v213,s[92:95],s42 offen lds\n"
"ds_read_b128 a[136:139],v1\n"
"ds_read_b128 a[140:143],v1 offset:2048\n"
"buffer_load_dwordx4 v214,s[92:95],s42 offen offset:1024 lds\n"
"ds_read_b128 a[144:147],v0 offset:8192\n"
"ds_read_b128 a[148:151],v0 offset:10240\n"
"ds_read_b128 a[152:155],v1 offset:8192\n"
"ds_read_b128 a[156:159],v1 offset:10240\n"
"buffer_load_dwordx4 v215,s[92:95],s42 offen offset:2048 lds\n"
"ds_read_b128 a[160:163],v0 offset:16384\n"
"ds_read_b128 a[164:167],v0 offset:18432\n"
"s_add_u32 m0,s51,27648\n"
"s_add_u32 s42,s60,82944\n"
"buffer_load_dwordx4 v213,s[92:95],s42 offen lds\n"
"ds_read_b128 a[168:171],v1 offset:16384\n"
"ds_read_b128 a[172:175],v1 offset:18432\n"
"ds_read_b128 a[176:179],v0 offset:24576\n"
"ds_read_b128 a[180:183],v0 offset:26624\n"
"buffer_load_dwordx4 v214,s[92:95],s42 offen offset:1024 lds\n"
"ds_read_b128 a[184:187],v1 offset:24576\n"
"ds_read_b128 a[188:191],v1 offset:26624\n"
"buffer_load_dwordx4 v215,s[92:95],s42 offen offset:2048 lds\n"
"ds_read_b128 a[192:195],v2\n"
"ds_read_b128 a[196:199],v2 offset:2048\n"
"ds_read_b128 v[48:51],v3\n"
"ds_read_b128 v[52:55],v3 offset:2048\n"
"s_add_u32 m0,s51,139264\n"
"s_add_u32 s42,s60,147456\n"
"buffer_load_dwordx4 v213,s[92:95],s42 offen lds\n"
"ds_read_b128 v[56:59],v2 offset:8192\n"
"ds_read_b128 v[60:63],v2 offset:10240\n"
"buffer_load_dwordx4 v214,s[92:95],s42 offen offset:1024 lds\n"
"ds_read_b128 v[64:67],v3 offset:8192\n"
"ds_read_b128 v[68:71],v3 offset:10240\n"
"ds_read_b128 v[72:75],v2 offset:16384\n"
"ds_read_b128 v[76:79],v2 offset:18432\n"
"buffer_load_dwordx4 v215,s[92:95],s42 offen offset:2048 lds\n"
"ds_read_b128 v[80:83],v3 offset:16384\n"
"ds_read_b128 v[84:87],v3 offset:18432\n"
"s_add_u32 m0,s51,142336\n"
"s_add_u32 s42,s60,156672\n"
"buffer_load_dwordx4 v213,s[92:95],s42 offen lds\n"
"ds_read_b128 v[88:91],v2 offset:24576\n"
"ds_read_b128 v[92:95],v2 offset:26624\n"
"ds_read_b128 v[96:99],v3 offset:24576\n"
"ds_read_b128 v[100:103],v3 offset:26624\n"
"buffer_load_dwordx4 v214,s[92:95],s42 offen offset:1024 lds\n"
"ds_read_b128 v[104:107],v2 offset:32768\n"
"ds_read_b128 v[108:111],v2 offset:34816\n"
"buffer_load_dwordx4 v215,s[92:95],s42 offen offset:2048 lds\n"
"ds_read_b128 v[112:115],v3 offset:32768\n"
"ds_read_b128 v[116:119],v3 offset:34816\n"
"s_waitcnt vmcnt(12) lgkmcnt(0)\n"
"s_barrier\n"
"ds_read_b64_tr_b16 a[208:209],v184\n"
"ds_read_b64_tr_b16 a[210:211],v184 offset:1568\n"
"ds_read_b64_tr_b16 a[212:213],v184 offset:12288\n"
"ds_read_b64_tr_b16 a[214:215],v184 offset:13856\n"
"ds_read_b64_tr_b16 a[216:217],v184 offset:32\n"
"ds_read_b64_tr_b16 a[218:219],v184 offset:1536\n"
"ds_read_b64_tr_b16 a[220:221],v184 offset:12320\n"
"ds_read_b64_tr_b16 a[222:223],v184 offset:13824\n"
"ds_read_b64_tr_b16 a[224:225],v184 offset:256\n"
"ds_read_b64_tr_b16 a[226:227],v184 offset:1824\n"
"ds_read_b64_tr_b16 a[228:229],v184 offset:12544\n"
"ds_read_b64_tr_b16 a[230:231],v184 offset:14112\n"
"ds_read_b64_tr_b16 a[232:233],v184 offset:288\n"
"ds_read_b64_tr_b16 a[234:235],v184 offset:1792\n"
"ds_read_b64_tr_b16 a[236:237],v184 offset:12576\n"
"ds_read_b64_tr_b16 a[238:239],v184 offset:14080\n"
"s_waitcnt lgkmcnt(12)\n"
"v_mfma_f32_16x16x32_bf16 v[0:3],a[208:211],a[128:131],0\n"
"ds_read_b64_tr_b16 a[240:241],v184 offset:512\n"
"v_mfma_f32_16x16x32_bf16 v[0:3],a[212:215],a[136:139],v[0:3]\n"
"ds_read_b64_tr_b16 a[242:243],v184 offset:2080\n"
"s_add_u32 m0,s51,49152\n"
"s_add_u32 s42,s60,221184\n"
"buffer_load_dwordx4 v213,s[92:95],s42 offen lds\n"
"v_mfma_f32_16x16x32_bf16 v[4:7],a[208:211],a[132:135],0\n"
"ds_read_b64_tr_b16 a[244:245],v184 offset:12800\n"
"v_mfma_f32_16x16x32_bf16 v[4:7],a[212:215],a[140:143],v[4:7]\n"
"ds_read_b64_tr_b16 a[246:247],v184 offset:14368\n"
"buffer_load_dwordx4 v214,s[92:95],s42 offen offset:1024 lds\n"
"v_mfma_f32_16x16x32_bf16 v[120:123],a[208:211],v[232:235],0\n"
"v_mfma_f32_16x16x32_bf16 v[120:123],a[212:215],v[232:235],v[120:123]\n"
"s_waitcnt lgkmcnt(12)\n"
"v_mfma_f32_16x16x32_bf16 v[8:11],a[216:219],a[128:131],0\n"
"ds_read_b64_tr_b16 a[248:249],v184 offset:544\n"
"v_mfma_f32_16x16x32_bf16 v[8:11],a[220:223],a[136:139],v[8:11]\n"
"ds_read_b64_tr_b16 a[250:251],v184 offset:2048\n"
"buffer_load_dwordx4 v215,s[92:95],s42 offen offset:2048 lds\n"
"v_mfma_f32_16x16x32_bf16 v[12:15],a[216:219],a[132:135],0\n"
"ds_read_b64_tr_b16 a[252:253],v184 offset:12832\n"
"v_mfma_f32_16x16x32_bf16 v[12:15],a[220:223],a[140:143],v[12:15]\n"
"ds_read_b64_tr_b16 a[254:255],v184 offset:14336\n"
"s_add_u32 m0,s51,52224\n"
"s_add_u32 s42,s60,230400\n"
"buffer_load_dwordx4 v213,s[92:95],s42 offen lds\n"
"v_mfma_f32_16x16x32_bf16 v[124:127],a[216:219],v[232:235],0\n"
"v_mfma_f32_16x16x32_bf16 v[124:127],a[220:223],v[232:235],v[124:127]\n"
"s_waitcnt vmcnt(10) lgkmcnt(12)\n"
"s_barrier\n"
"v_mfma_f32_16x16x32_bf16 v[16:19],a[224:227],a[128:131],0\n"
"ds_read_b64_tr_b16 a[208:209],v184 offset:24576\n"
"v_mfma_f32_16x16x32_bf16 v[16:19],a[228:231],a[136:139],v[16:19]\n"
"ds_read_b64_tr_b16 a[210:211],v184 offset:26144\n"
"buffer_load_dwordx4 v214,s[92:95],s42 offen offset:1024 lds\n"
"v_mfma_f32_16x16x32_bf16 v[20:23],a[224:227],a[132:135],0\n"
"ds_read_b64_tr_b16 a[212:213],v184 offset:36864\n"
"v_mfma_f32_16x16x32_bf16 v[20:23],a[228:231],a[140:143],v[20:23]\n"
"ds_read_b64_tr_b16 a[214:215],v184 offset:38432\n"
"buffer_load_dwordx4 v215,s[92:95],s42 offen offset:2048 lds\n"
"v_mfma_f32_16x16x32_bf16 v[128:131],a[224:227],v[232:235],0\n"
"v_mfma_f32_16x16x32_bf16 v[128:131],a[228:231],v[232:235],v[128:131]\n"
"s_waitcnt lgkmcnt(12)\n"
"v_mfma_f32_16x16x32_bf16 v[24:27],a[232:235],a[128:131],0\n"
"ds_read_b64_tr_b16 a[216:217],v184 offset:24608\n"
"v_mfma_f32_16x16x32_bf16 v[24:27],a[236:239],a[136:139],v[24:27]\n"
"ds_read_b64_tr_b16 a[218:219],v184 offset:26112\n"
"s_add_u32 m0,s51,73728\n"
"s_add_u32 s42,s60,294912\n"
"buffer_load_dwordx4 v213,s[92:95],s42 offen lds\n"
"v_mfma_f32_16x16x32_bf16 v[28:31],a[232:235],a[132:135],0\n"
"ds_read_b64_tr_b16 a[220:221],v184 offset:36896\n"
"v_mfma_f32_16x16x32_bf16 v[28:31],a[236:239],a[140:143],v[28:31]\n"
"ds_read_b64_tr_b16 a[222:223],v184 offset:38400\n"
"buffer_load_dwordx4 v214,s[92:95],s42 offen offset:1024 lds\n"
"v_mfma_f32_16x16x32_bf16 v[132:135],a[232:235],v[232:235],0\n"
"v_mfma_f32_16x16x32_bf16 v[132:135],a[236:239],v[232:235],v[132:135]\n"
"s_waitcnt lgkmcnt(12)\n"
"v_mfma_f32_16x16x32_bf16 v[32:35],a[240:243],a[128:131],0\n"
"ds_read_b64_tr_b16 a[224:225],v184 offset:24832\n"
"v_mfma_f32_16x16x32_bf16 v[32:35],a[244:247],a[136:139],v[32:35]\n"
"ds_read_b64_tr_b16 a[226:227],v184 offset:26400\n"
"buffer_load_dwordx4 v215,s[92:95],s42 offen offset:2048 lds\n"
"v_mfma_f32_16x16x32_bf16 v[36:39],a[240:243],a[132:135],0\n"
"ds_read_b64_tr_b16 a[228:229],v184 offset:37120\n"
"v_mfma_f32_16x16x32_bf16 v[36:39],a[244:247],a[140:143],v[36:39]\n"
"ds_read_b64_tr_b16 a[230:231],v184 offset:38688\n"
"s_add_u32 m0,s51,76800\n"
"s_add_u32 s42,s60,304128\n"
"buffer_load_dwordx4 v213,s[92:95],s42 offen lds\n"
"v_mfma_f32_16x16x32_bf16 v[136:139],a[240:243],v[232:235],0\n"
"v_mfma_f32_16x16x32_bf16 v[136:139],a[244:247],v[232:235],v[136:139]\n"
"s_waitcnt lgkmcnt(12)\n"
"v_mfma_f32_16x16x32_bf16 v[40:43],a[248:251],a[128:131],0\n"
"ds_read_b64_tr_b16 a[232:233],v184 offset:24864\n"
"v_mfma_f32_16x16x32_bf16 v[40:43],a[252:255],a[136:139],v[40:43]\n"
"ds_read_b64_tr_b16 a[234:235],v184 offset:26368\n"
"buffer_load_dwordx4 v214,s[92:95],s42 offen offset:1024 lds\n"
"v_mfma_f32_16x16x32_bf16 v[44:47],a[248:251],a[132:135],0\n"
"ds_read_b64_tr_b16 a[236:237],v184 offset:37152\n"
"v_mfma_f32_16x16x32_bf16 v[44:47],a[252:255],a[140:143],v[44:47]\n"
"ds_read_b64_tr_b16 a[238:239],v184 offset:38656\n"
"buffer_load_dwordx4 v215,s[92:95],s42 offen offset:2048 lds\n"
"v_mfma_f32_16x16x32_bf16 v[140:143],a[248:251],v[232:235],0\n"
"v_mfma_f32_16x16x32_bf16 v[140:143],a[252:255],v[232:235],v[140:143]\n"
"s_waitcnt lgkmcnt(12)\n"
"v_mfma_f32_16x16x32_bf16 v[0:3],a[208:211],a[144:147],v[0:3]\n"
"ds_read_b64_tr_b16 a[240:241],v184 offset:25088\n"
"v_mfma_f32_16x16x32_bf16 v[0:3],a[212:215],a[152:155],v[0:3]\n"
"ds_read_b64_tr_b16 a[242:243],v184 offset:26656\n"
"s_add_u32 m0,s51,114688\n"
"s_add_u32 s42,s60,368640\n"
"buffer_load_dwordx4 v213,s[92:95],s42 offen lds\n"
"v_mfma_f32_16x16x32_bf16 v[4:7],a[208:211],a[148:151],v[4:7]\n"
"ds_read_b64_tr_b16 a[244:245],v184 offset:37376\n"
"v_mfma_f32_16x16x32_bf16 v[4:7],a[212:215],a[156:159],v[4:7]\n"
"ds_read_b64_tr_b16 a[246:247],v184 offset:38944\n"
"buffer_load_dwordx4 v214,s[92:95],s42 offen offset:1024 lds\n"
"v_mfma_f32_16x16x32_bf16 v[120:123],a[208:211],v[232:235],v[120:123]\n"
"v_mfma_f32_16x16x32_bf16 v[120:123],a[212:215],v[232:235],v[120:123]\n"
"s_waitcnt lgkmcnt(12)\n"
"v_mfma_f32_16x16x32_bf16 v[8:11],a[216:219],a[144:147],v[8:11]\n"
"ds_read_b64_tr_b16 a[248:249],v184 offset:25120\n"
"v_mfma_f32_16x16x32_bf16 v[8:11],a[220:223],a[152:155],v[8:11]\n"
"ds_read_b64_tr_b16 a[250:251],v184 offset:26624\n"
"buffer_load_dwordx4 v215,s[92:95],s42 offen offset:2048 lds\n"
"v_mfma_f32_16x16x32_bf16 v[12:15],a[216:219],a[148:151],v[12:15]\n"
"ds_read_b64_tr_b16 a[252:253],v184 offset:37408\n"
"v_mfma_f32_16x16x32_bf16 v[12:15],a[220:223],a[156:159],v[12:15]\n"
"ds_read_b64_tr_b16 a[254:255],v184 offset:38912\n"
"s_add_u32 m0,s51,117760\n"
"s_add_u32 s42,s60,377856\n"
"buffer_load_dwordx4 v213,s[92:95],s42 offen lds\n"
"v_mfma_f32_16x16x32_bf16 v[124:127],a[216:219],v[232:235],v[124:127]\n"
"v_mfma_f32_16x16x32_bf16 v[124:127],a[220:223],v[232:235],v[124:127]\n"
"s_waitcnt vmcnt(16) lgkmcnt(12)\n"
"s_barrier\n"
"v_mfma_f32_16x16x32_bf16 v[16:19],a[224:227],a[144:147],v[16:19]\n"
"ds_read_b64_tr_b16 a[208:209],v186 offset:24576\n"
"v_mfma_f32_16x16x32_bf16 v[16:19],a[228:231],a[152:155],v[16:19]\n"
"ds_read_b64_tr_b16 a[210:211],v186 offset:26144\n"
"buffer_load_dwordx4 v214,s[92:95],s42 offen offset:1024 lds\n"
"v_mfma_f32_16x16x32_bf16 v[20:23],a[224:227],a[148:151],v[20:23]\n"
"ds_read_b64_tr_b16 a[212:213],v186 offset:36864\n"
"v_mfma_f32_16x16x32_bf16 v[20:23],a[228:231],a[156:159],v[20:23]\n"
"ds_read_b64_tr_b16 a[214:215],v186 offset:38432\n"
"buffer_load_dwordx4 v215,s[92:95],s42 offen offset:2048 lds\n"
"v_mfma_f32_16x16x32_bf16 v[128:131],a[224:227],v[232:235],v[128:131]\n"
"v_mfma_f32_16x16x32_bf16 v[128:131],a[228:231],v[232:235],v[128:131]\n"
"s_waitcnt lgkmcnt(12)\n"
"v_mfma_f32_16x16x32_bf16 v[24:27],a[232:235],a[144:147],v[24:27]\n"
"ds_read_b64_tr_b16 a[216:217],v186 offset:24608\n"
"v_mfma_f32_16x16x32_bf16 v[24:27],a[236:239],a[152:155],v[24:27]\n"
"ds_read_b64_tr_b16 a[218:219],v186 offset:26112\n"
"s_add_u32 m0,s51,0\n"
"s_add_u32 s42,s60,442368\n"
"buffer_load_dwordx4 v213,s[92:95],s42 offen lds\n"
"v_mfma_f32_16x16x32_bf16 v[28:31],a[232:235],a[148:151],v[28:31]\n"
"ds_read_b64_tr_b16 a[220:221],v186 offset:36896\n"
"v_mfma_f32_16x16x32_bf16 v[28:31],a[236:239],a[156:159],v[28:31]\n"
"ds_read_b64_tr_b16 a[222:223],v186 offset:38400\n"
"buffer_load_dwordx4 v214,s[92:95],s42 offen offset:1024 lds\n"
"v_mfma_f32_16x16x32_bf16 v[132:135],a[232:235],v[232:235],v[132:135]\n"
"v_mfma_f32_16x16x32_bf16 v[132:135],a[236:239],v[232:235],v[132:135]\n"
"s_waitcnt lgkmcnt(12)\n"
"v_mfma_f32_16x16x32_bf16 v[32:35],a[240:243],a[144:147],v[32:35]\n"
"ds_read_b64_tr_b16 a[224:225],v186 offset:24832\n"
"v_mfma_f32_16x16x32_bf16 v[32:35],a[244:247],a[152:155],v[32:35]\n"
"ds_read_b64_tr_b16 a[226:227],v186 offset:26400\n"
"buffer_load_dwordx4 v215,s[92:95],s42 offen offset:2048 lds\n"
"v_mfma_f32_16x16x32_bf16 v[36:39],a[240:243],a[148:151],v[36:39]\n"
"ds_read_b64_tr_b16 a[228:229],v186 offset:37120\n"
"v_mfma_f32_16x16x32_bf16 v[36:39],a[244:247],a[156:159],v[36:39]\n"
"ds_read_b64_tr_b16 a[230:231],v186 offset:38688\n"
"s_add_u32 m0,s51,3072\n"
"s_add_u32 s42,s60,451584\n"
"buffer_load_dwordx4 v213,s[92:95],s42 offen lds\n"
"v_mfma_f32_16x16x32_bf16 v[136:139],a[240:243],v[232:235],v[136:139]\n"
"v_mfma_f32_16x16x32_bf16 v[136:139],a[244:247],v[232:235],v[136:139]\n"
"s_waitcnt lgkmcnt(12)\n"
"v_mfma_f32_16x16x32_bf16 v[40:43],a[248:251],a[144:147],v[40:43]\n"
"ds_read_b64_tr_b16 a[232:233],v186 offset:24864\n"
"v_mfma_f32_16x16x32_bf16 v[40:43],a[252:255],a[152:155],v[40:43]\n"
"ds_read_b64_tr_b16 a[234:235],v186 offset:26368\n"
"buffer_load_dwordx4 v214,s[92:95],s42 offen offset:1024 lds\n"
"v_mfma_f32_16x16x32_bf16 v[44:47],a[248:251],a[148:151],v[44:47]\n"
"ds_read_b64_tr_b16 a[236:237],v186 offset:37152\n"
"v_mfma_f32_16x16x32_bf16 v[44:47],a[252:255],a[156:159],v[44:47]\n"
"ds_read_b64_tr_b16 a[238:239],v186 offset:38656\n"
"buffer_load_dwordx4 v215,s[92:95],s42 offen offset:2048 lds\n"
"v_mfma_f32_16x16x32_bf16 v[140:143],a[248:251],v[232:235],v[140:143]\n"
"v_mfma_f32_16x16x32_bf16 v[140:143],a[252:255],v[232:235],v[140:143]\n"
"s_waitcnt lgkmcnt(12)\n"
"v_mfma_f32_16x16x32_bf16 v[0:3],a[208:211],a[160:163],v[0:3]\n"
"ds_read_b64_tr_b16 a[240:241],v186 offset:25088\n"
"v_mfma_f32_16x16x32_bf16 v[0:3],a[212:215],a[168:171],v[0:3]\n"
"ds_read_b64_tr_b16 a[242:243],v186 offset:26656\n"
"v_mfma_f32_16x16x32_bf16 v[4:7],a[208:211],a[164:167],v[4:7]\n"
"ds_read_b64_tr_b16 a[244:245],v186 offset:37376\n"
"v_mfma_f32_16x16x32_bf16 v[4:7],a[212:215],a[172:175],v[4:7]\n"
"ds_read_b64_tr_b16 a[246:247],v186 offset:38944\n"
"v_mfma_f32_16x16x32_bf16 v[120:123],a[208:211],v[232:235],v[120:123]\n"
"v_mfma_f32_16x16x32_bf16 v[120:123],a[212:215],v[232:235],v[120:123]\n"
"s_waitcnt lgkmcnt(12)\n"
"v_mfma_f32_16x16x32_bf16 v[8:11],a[216:219],a[160:163],v[8:11]\n"
"ds_read_b64_tr_b16 a[248:249],v186 offset:25120\n"
"v_mfma_f32_16x16x32_bf16 v[8:11],a[220:223],a[168:171],v[8:11]\n"
"ds_read_b64_tr_b16 a[250:251],v186 offset:26624\n"
"v_mfma_f32_16x16x32_bf16 v[12:15],a[216:219],a[164:167],v[12:15]\n"
"ds_read_b64_tr_b16 a[252:253],v186 offset:37408\n"
"v_mfma_f32_16x16x32_bf16 v[12:15],a[220:223],a[172:175],v[12:15]\n"
"ds_read_b64_tr_b16 a[254:255],v186 offset:38912\n"
"v_mfma_f32_16x16x32_bf16 v[124:127],a[216:219],v[232:235],v[124:127]\n"
"v_mfma_f32_16x16x32_bf16 v[124:127],a[220:223],v[232:235],v[124:127]\n"
"s_waitcnt vmcnt(18) lgkmcnt(12)\n"
"s_barrier\n"
"v_mfma_f32_16x16x32_bf16 v[16:19],a[224:227],a[160:163],v[16:19]\n"
"ds_read_b64_tr_b16 a[208:209],v185\n"
"v_mfma_f32_16x16x32_bf16 v[16:19],a[228:231],a[168:171],v[16:19]\n"
"ds_read_b64_tr_b16 a[210:211],v185 offset:1568\n"
"s_add_u32 m0,s51,24576\n"
"s_add_u32 s42,s60,516096\n"
"buffer_load_dwordx4 v213,s[92:95],s42 offen lds\n"
"v_mfma_f32_16x16x32_bf16 v[20:23],a[224:227],a[164:167],v[20:23]\n"
"ds_read_b64_tr_b16 a[212:213],v185 offset:12288\n"
"v_mfma_f32_16x16x32_bf16 v[20:23],a[228:231],a[172:175],v[20:23]\n"
"ds_read_b64_tr_b16 a[214:215],v185 offset:13856\n"
"buffer_load_dwordx4 v214,s[92:95],s42 offen offset:1024 lds\n"
"v_mfma_f32_16x16x32_bf16 v[128:131],a[224:227],v[232:235],v[128:131]\n"
"v_mfma_f32_16x16x32_bf16 v[128:131],a[228:231],v[232:235],v[128:131]\n"
"s_waitcnt lgkmcnt(12)\n"
"v_mfma_f32_16x16x32_bf16 v[24:27],a[232:235],a[160:163],v[24:27]\n"
"ds_read_b64_tr_b16 a[216:217],v185 offset:32\n"
"v_mfma_f32_16x16x32_bf16 v[24:27],a[236:239],a[168:171],v[24:27]\n"
"ds_read_b64_tr_b16 a[218:219],v185 offset:1536\n"
"buffer_load_dwordx4 v215,s[92:95],s42 offen offset:2048 lds\n"
"v_mfma_f32_16x16x32_bf16 v[28:31],a[232:235],a[164:167],v[28:31]\n"
"ds_read_b64_tr_b16 a[220:221],v185 offset:12320\n"
"v_mfma_f32_16x16x32_bf16 v[28:31],a[236:239],a[172:175],v[28:31]\n"
"ds_read_b64_tr_b16 a[222:223],v185 offset:13824\n"
"s_add_u32 m0,s51,27648\n"
"s_add_u32 s42,s60,525312\n"
"buffer_load_dwordx4 v213,s[92:95],s42 offen lds\n"
"v_mfma_f32_16x16x32_bf16 v[132:135],a[232:235],v[232:235],v[132:135]\n"
"v_mfma_f32_16x16x32_bf16 v[132:135],a[236:239],v[232:235],v[132:135]\n"
"s_waitcnt lgkmcnt(12)\n"
"v_mfma_f32_16x16x32_bf16 v[32:35],a[240:243],a[160:163],v[32:35]\n"
"ds_read_b64_tr_b16 a[224:225],v185 offset:256\n"
"v_mfma_f32_16x16x32_bf16 v[32:35],a[244:247],a[168:171],v[32:35]\n"
"ds_read_b64_tr_b16 a[226:227],v185 offset:1824\n"
"buffer_load_dwordx4 v214,s[92:95],s42 offen offset:1024 lds\n"
"v_mfma_f32_16x16x32_bf16 v[36:39],a[240:243],a[164:167],v[36:39]\n"
"ds_read_b64_tr_b16 a[228:229],v185 offset:12544\n"
"v_mfma_f32_16x16x32_bf16 v[36:39],a[244:247],a[172:175],v[36:39]\n"
"ds_read_b64_tr_b16 a[230:231],v185 offset:14112\n"
"buffer_load_dwordx4 v215,s[92:95],s42 offen offset:2048 lds\n"
"v_mfma_f32_16x16x32_bf16 v[136:139],a[240:243],v[232:235],v[136:139]\n"
"v_mfma_f32_16x16x32_bf16 v[136:139],a[244:247],v[232:235],v[136:139]\n"
"s_waitcnt lgkmcnt(12)\n"
"v_mfma_f32_16x16x32_bf16 v[40:43],a[248:251],a[160:163],v[40:43]\n"
"ds_read_b64_tr_b16 a[232:233],v185 offset:288\n"
"v_mfma_f32_16x16x32_bf16 v[40:43],a[252:255],a[168:171],v[40:43]\n"
"ds_read_b64_tr_b16 a[234:235],v185 offset:1792\n"
"v_mfma_f32_16x16x32_bf16 v[44:47],a[248:251],a[164:167],v[44:47]\n"
"ds_read_b64_tr_b16 a[236:237],v185 offset:12576\n"
"v_mfma_f32_16x16x32_bf16 v[44:47],a[252:255],a[172:175],v[44:47]\n"
"ds_read_b64_tr_b16 a[238:239],v185 offset:14080\n"
"v_mfma_f32_16x16x32_bf16 v[140:143],a[248:251],v[232:235],v[140:143]\n"
"v_mfma_f32_16x16x32_bf16 v[140:143],a[252:255],v[232:235],v[140:143]\n"
"s_waitcnt lgkmcnt(12)\n"
"v_mfma_f32_16x16x32_bf16 v[0:3],a[208:211],a[176:179],v[0:3]\n"
"ds_read_b64_tr_b16 a[240:241],v185 offset:512\n"
"v_mfma_f32_16x16x32_bf16 v[0:3],a[212:215],a[184:187],v[0:3]\n"
"ds_read_b64_tr_b16 a[242:243],v185 offset:2080\n"
"v_mfma_f32_16x16x32_bf16 v[4:7],a[208:211],a[180:183],v[4:7]\n"
"ds_read_b64_tr_b16 a[244:245],v185 offset:12800\n"
"v_mfma_f32_16x16x32_bf16 v[4:7],a[212:215],a[188:191],v[4:7]\n"
"ds_read_b64_tr_b16 a[246:247],v185 offset:14368\n"
"v_mfma_f32_16x16x32_bf16 v[120:123],a[208:211],v[232:235],v[120:123]\n"
"v_mfma_f32_16x16x32_bf16 v[120:123],a[212:215],v[232:235],v[120:123]\n"
"s_waitcnt lgkmcnt(12)\n"
"v_mfma_f32_16x16x32_bf16 v[8:11],a[216:219],a[176:179],v[8:11]\n"
"ds_read_b64_tr_b16 a[248:249],v185 offset:544\n"
"v_mfma_f32_16x16x32_bf16 v[8:11],a[220:223],a[184:187],v[8:11]\n"
"ds_read_b64_tr_b16 a[250:251],v185 offset:2048\n"
"v_mfma_f32_16x16x32_bf16 v[12:15],a[216:219],a[180:183],v[12:15]\n"
"ds_read_b64_tr_b16 a[252:253],v185 offset:12832\n"
"v_mfma_f32_16x16x32_bf16 v[12:15],a[220:223],a[188:191],v[12:15]\n"
"ds_read_b64_tr_b16 a[254:255],v185 offset:14336\n"
"v_mfma_f32_16x16x32_bf16 v[124:127],a[216:219],v[232:235],v[124:127]\n"
"v_mfma_f32_16x16x32_bf16 v[124:127],a[220:223],v[232:235],v[124:127]\n"
"s_waitcnt vmcnt(18) lgkmcnt(12)\n"
"s_barrier\n"
"v_mfma_f32_16x16x32_bf16 v[16:19],a[224:227],a[176:179],v[16:19]\n"
"ds_read_b64_tr_b16 a[208:209],v185 offset:24576\n"
"v_mfma_f32_16x16x32_bf16 v[16:19],a[228:231],a[184:187],v[16:19]\n"
"ds_read_b64_tr_b16 a[210:211],v185 offset:26144\n"
"s_add_u32 m0,s51,139264\n"
"s_add_u32 s42,s60,589824\n"
"buffer_load_dwordx4 v213,s[92:95],s42 offen lds\n"
"v_mfma_f32_16x16x32_bf16 v[20:23],a[224:227],a[180:183],v[20:23]\n"
"ds_read_b64_tr_b16 a[212:213],v185 offset:36864\n"
"v_mfma_f32_16x16x32_bf16 v[20:23],a[228:231],a[188:191],v[20:23]\n"
"ds_read_b64_tr_b16 a[214:215],v185 offset:38432\n"
"buffer_load_dwordx4 v214,s[92:95],s42 offen offset:1024 lds\n"
"v_mfma_f32_16x16x32_bf16 v[128:131],a[224:227],v[232:235],v[128:131]\n"
"v_mfma_f32_16x16x32_bf16 v[128:131],a[228:231],v[232:235],v[128:131]\n"
"s_waitcnt lgkmcnt(12)\n"
"v_mfma_f32_16x16x32_bf16 v[24:27],a[232:235],a[176:179],v[24:27]\n"
"ds_read_b64_tr_b16 a[216:217],v185 offset:24608\n"
"v_mfma_f32_16x16x32_bf16 v[24:27],a[236:239],a[184:187],v[24:27]\n"
"ds_read_b64_tr_b16 a[218:219],v185 offset:26112\n"
"buffer_load_dwordx4 v215,s[92:95],s42 offen offset:2048 lds\n"
"v_mfma_f32_16x16x32_bf16 v[28:31],a[232:235],a[180:183],v[28:31]\n"
"ds_read_b64_tr_b16 a[220:221],v185 offset:36896\n"
"v_mfma_f32_16x16x32_bf16 v[28:31],a[236:239],a[188:191],v[28:31]\n"
"ds_read_b64_tr_b16 a[222:223],v185 offset:38400\n"
"s_add_u32 m0,s51,142336\n"
"s_add_u32 s42,s60,599040\n"
"buffer_load_dwordx4 v213,s[92:95],s42 offen lds\n"
"v_mfma_f32_16x16x32_bf16 v[132:135],a[232:235],v[232:235],v[132:135]\n"
"v_mfma_f32_16x16x32_bf16 v[132:135],a[236:239],v[232:235],v[132:135]\n"
"s_waitcnt lgkmcnt(12)\n"
"v_mfma_f32_16x16x32_bf16 v[32:35],a[240:243],a[176:179],v[32:35]\n"
"ds_read_b64_tr_b16 a[224:225],v185 offset:24832\n"
"v_mfma_f32_16x16x32_bf16 v[32:35],a[244:247],a[184:187],v[32:35]\n"
"ds_read_b64_tr_b16 a[226:227],v185 offset:26400\n"
"buffer_load_dwordx4 v214,s[92:95],s42 offen offset:1024 lds\n"
"v_mfma_f32_16x16x32_bf16 v[36:39],a[240:243],a[180:183],v[36:39]\n"
"ds_read_b64_tr_b16 a[228:229],v185 offset:37120\n"
"v_mfma_f32_16x16x32_bf16 v[36:39],a[244:247],a[188:191],v[36:39]\n"
"ds_read_b64_tr_b16 a[230:231],v185 offset:38688\n"
"buffer_load_dwordx4 v215,s[92:95],s42 offen offset:2048 lds\n"
"v_mfma_f32_16x16x32_bf16 v[136:139],a[240:243],v[232:235],v[136:139]\n"
"v_mfma_f32_16x16x32_bf16 v[136:139],a[244:247],v[232:235],v[136:139]\n"
"s_waitcnt lgkmcnt(12)\n"
"v_mfma_f32_16x16x32_bf16 v[40:43],a[248:251],a[176:179],v[40:43]\n"
"ds_read_b64_tr_b16 a[232:233],v185 offset:24864\n"
"v_mfma_f32_16x16x32_bf16 v[40:43],a[252:255],a[184:187],v[40:43]\n"
"ds_read_b64_tr_b16 a[234:235],v185 offset:26368\n"
"v_mfma_f32_16x16x32_bf16 v[44:47],a[248:251],a[180:183],v[44:47]\n"
"ds_read_b64_tr_b16 a[236:237],v185 offset:37152\n"
"v_mfma_f32_16x16x32_bf16 v[44:47],a[252:255],a[188:191],v[44:47]\n"
"ds_read_b64_tr_b16 a[238:239],v185 offset:38656\n"
"v_mfma_f32_16x16x32_bf16 v[140:143],a[248:251],v[232:235],v[140:143]\n"
"v_mfma_f32_16x16x32_bf16 v[140:143],a[252:255],v[232:235],v[140:143]\n"
"s_waitcnt lgkmcnt(12)\n"
"v_mfma_f32_16x16x32_bf16 v[0:3],a[208:211],a[192:195],v[0:3]\n"
"ds_read_b64_tr_b16 a[240:241],v185 offset:25088\n"
"v_mfma_f32_16x16x32_bf16 v[0:3],a[212:215],v[48:51],v[0:3]\n"
"ds_read_b64_tr_b16 a[242:243],v185 offset:26656\n"
"v_mfma_f32_16x16x32_bf16 v[4:7],a[208:211],a[196:199],v[4:7]\n"
"ds_read_b64_tr_b16 a[244:245],v185 offset:37376\n"
"v_mfma_f32_16x16x32_bf16 v[4:7],a[212:215],v[52:55],v[4:7]\n"
"ds_read_b64_tr_b16 a[246:247],v185 offset:38944\n"
"v_mfma_f32_16x16x32_bf16 v[120:123],a[208:211],v[232:235],v[120:123]\n"
"v_mfma_f32_16x16x32_bf16 v[120:123],a[212:215],v[232:235],v[120:123]\n"
"s_waitcnt lgkmcnt(12)\n"
"v_mfma_f32_16x16x32_bf16 v[8:11],a[216:219],a[192:195],v[8:11]\n"
"ds_read_b64_tr_b16 a[248:249],v185 offset:25120\n"
"v_mfma_f32_16x16x32_bf16 v[8:11],a[220:223],v[48:51],v[8:11]\n"
"ds_read_b64_tr_b16 a[250:251],v185 offset:26624\n"
"v_mfma_f32_16x16x32_bf16 v[12:15],a[216:219],a[196:199],v[12:15]\n"
"ds_read_b64_tr_b16 a[252:253],v185 offset:37408\n"
"v_mfma_f32_16x16x32_bf16 v[12:15],a[220:223],v[52:55],v[12:15]\n"
"ds_read_b64_tr_b16 a[254:255],v185 offset:38912\n"
"v_mfma_f32_16x16x32_bf16 v[124:127],a[216:219],v[232:235],v[124:127]\n"
"v_mfma_f32_16x16x32_bf16 v[124:127],a[220:223],v[232:235],v[124:127]\n"
"s_waitcnt vmcnt(18) lgkmcnt(12)\n"
"s_barrier\n"
"v_mfma_f32_16x16x32_bf16 v[16:19],a[224:227],a[192:195],v[16:19]\n"
"ds_read_b64_tr_b16 a[208:209],v186\n"
"v_mfma_f32_16x16x32_bf16 v[16:19],a[228:231],v[48:51],v[16:19]\n"
"ds_read_b64_tr_b16 a[210:211],v186 offset:1568\n"
"v_mfma_f32_16x16x32_bf16 v[20:23],a[224:227],a[196:199],v[20:23]\n"
"ds_read_b64_tr_b16 a[212:213],v186 offset:12288\n"
"v_mfma_f32_16x16x32_bf16 v[20:23],a[228:231],v[52:55],v[20:23]\n"
"ds_read_b64_tr_b16 a[214:215],v186 offset:13856\n"
"v_mfma_f32_16x16x32_bf16 v[128:131],a[224:227],v[232:235],v[128:131]\n"
"v_mfma_f32_16x16x32_bf16 v[128:131],a[228:231],v[232:235],v[128:131]\n"
"s_waitcnt lgkmcnt(12)\n"
"v_mfma_f32_16x16x32_bf16 v[24:27],a[232:235],a[192:195],v[24:27]\n"
"ds_read_b64_tr_b16 a[216:217],v186 offset:32\n"
"v_mfma_f32_16x16x32_bf16 v[24:27],a[236:239],v[48:51],v[24:27]\n"
"ds_read_b64_tr_b16 a[218:219],v186 offset:1536\n"
"v_mfma_f32_16x16x32_bf16 v[28:31],a[232:235],a[196:199],v[28:31]\n"
"ds_read_b64_tr_b16 a[220:221],v186 offset:12320\n"
"v_mfma_f32_16x16x32_bf16 v[28:31],a[236:239],v[52:55],v[28:31]\n"
"ds_read_b64_tr_b16 a[222:223],v186 offset:13824\n"
"v_mfma_f32_16x16x32_bf16 v[132:135],a[232:235],v[232:235],v[132:135]\n"
"v_mfma_f32_16x16x32_bf16 v[132:135],a[236:239],v[232:235],v[132:135]\n"
"s_waitcnt lgkmcnt(12)\n"
"v_mfma_f32_16x16x32_bf16 v[32:35],a[240:243],a[192:195],v[32:35]\n"
"ds_read_b64_tr_b16 a[224:225],v186 offset:256\n"
"v_mfma_f32_16x16x32_bf16 v[32:35],a[244:247],v[48:51],v[32:35]\n"
"ds_read_b64_tr_b16 a[226:227],v186 offset:1824\n"
"v_mfma_f32_16x16x32_bf16 v[36:39],a[240:243],a[196:199],v[36:39]\n"
"ds_read_b64_tr_b16 a[228:229],v186 offset:12544\n"
"v_mfma_f32_16x16x32_bf16 v[36:39],a[244:247],v[52:55],v[36:39]\n"
"ds_read_b64_tr_b16 a[230:231],v186 offset:14112\n"
"v_mfma_f32_16x16x32_bf16 v[136:139],a[240:243],v[232:235],v[136:139]\n"
"v_mfma_f32_16x16x32_bf16 v[136:139],a[244:247],v[232:235],v[136:139]\n"
"s_waitcnt lgkmcnt(12)\n"
"v_mfma_f32_16x16x32_bf16 v[40:43],a[248:251],a[192:195],v[40:43]\n"
"ds_read_b64_tr_b16 a[232:233],v186 offset:288\n"
"v_mfma_f32_16x16x32_bf16 v[40:43],a[252:255],v[48:51],v[40:43]\n"
"ds_read_b64_tr_b16 a[234:235],v186 offset:1792\n"
"v_mfma_f32_16x16x32_bf16 v[44:47],a[248:251],a[196:199],v[44:47]\n"
"ds_read_b64_tr_b16 a[236:237],v186 offset:12576\n"
"v_mfma_f32_16x16x32_bf16 v[44:47],a[252:255],v[52:55],v[44:47]\n"
"ds_read_b64_tr_b16 a[238:239],v186 offset:14080\n"
"v_mfma_f32_16x16x32_bf16 v[140:143],a[248:251],v[232:235],v[140:143]\n"
"v_mfma_f32_16x16x32_bf16 v[140:143],a[252:255],v[232:235],v[140:143]\n"
"s_waitcnt lgkmcnt(12)\n"
"v_mfma_f32_16x16x32_bf16 v[0:3],a[208:211],v[56:59],v[0:3]\n"
"ds_read_b64_tr_b16 a[240:241],v186 offset:512\n"
"v_mfma_f32_16x16x32_bf16 v[0:3],a[212:215],v[64:67],v[0:3]\n"
"ds_read_b64_tr_b16 a[242:243],v186 offset:2080\n"
"v_mfma_f32_16x16x32_bf16 v[4:7],a[208:211],v[60:63],v[4:7]\n"
"ds_read_b64_tr_b16 a[244:245],v186 offset:12800\n"
"v_mfma_f32_16x16x32_bf16 v[4:7],a[212:215],v[68:71],v[4:7]\n"
"ds_read_b64_tr_b16 a[246:247],v186 offset:14368\n"
"v_mfma_f32_16x16x32_bf16 v[120:123],a[208:211],v[232:235],v[120:123]\n"
"v_mfma_f32_16x16x32_bf16 v[120:123],a[212:215],v[232:235],v[120:123]\n"
"s_waitcnt lgkmcnt(12)\n"
"v_mfma_f32_16x16x32_bf16 v[8:11],a[216:219],v[56:59],v[8:11]\n"
"ds_read_b64_tr_b16 a[248:249],v186 offset:544\n"
"v_mfma_f32_16x16x32_bf16 v[8:11],a[220:223],v[64:67],v[8:11]\n"
"ds_read_b64_tr_b16 a[250:251],v186 offset:2048\n"
"v_mfma_f32_16x16x32_bf16 v[12:15],a[216:219],v[60:63],v[12:15]\n"
"ds_read_b64_tr_b16 a[252:253],v186 offset:12832\n"
"v_mfma_f32_16x16x32_bf16 v[12:15],a[220:223],v[68:71],v[12:15]\n"
"ds_read_b64_tr_b16 a[254:255],v186 offset:14336\n"
"v_mfma_f32_16x16x32_bf16 v[124:127],a[216:219],v[232:235],v[124:127]\n"
"v_mfma_f32_16x16x32_bf16 v[124:127],a[220:223],v[232:235],v[124:127]\n"
"s_waitcnt vmcnt(12) lgkmcnt(12)\n"
"s_barrier\n"
"v_mfma_f32_16x16x32_bf16 v[16:19],a[224:227],v[56:59],v[16:19]\n"
"ds_read_b64_tr_b16 a[208:209],v184\n"
"v_mfma_f32_16x16x32_bf16 v[16:19],a[228:231],v[64:67],v[16:19]\n"
"ds_read_b64_tr_b16 a[210:211],v184 offset:1568\n"
"s_add_u32 m0,s50,0\n"
"s_add_u32 s39,s52,0\n"
"buffer_load_dwordx4 v205,s[72:75],s39 offen lds\n"
"v_mfma_f32_16x16x32_bf16 v[20:23],a[224:227],v[60:63],v[20:23]\n"
"ds_read_b64_tr_b16 a[212:213],v184 offset:12288\n"
"v_mfma_f32_16x16x32_bf16 v[20:23],a[228:231],v[68:71],v[20:23]\n"
"ds_read_b64_tr_b16 a[214:215],v184 offset:13856\n"
"buffer_load_dwordx4 v206,s[72:75],s39 offen offset:1024 lds\n"
"v_mfma_f32_16x16x32_bf16 v[128:131],a[224:227],v[232:235],v[128:131]\n"
"v_mfma_f32_16x16x32_bf16 v[128:131],a[228:231],v[232:235],v[128:131]\n"
"s_waitcnt lgkmcnt(12)\n"
"v_mfma_f32_16x16x32_bf16 v[24:27],a[232:235],v[56:59],v[24:27]\n"
"ds_read_b64_tr_b16 a[216:217],v184 offset:32\n"
"v_mfma_f32_16x16x32_bf16 v[24:27],a[236:239],v[64:67],v[24:27]\n"
"ds_read_b64_tr_b16 a[218:219],v184 offset:1536\n"
"buffer_load_dwordx4 v207,s[72:75],s39 offen offset:2048 lds\n"
"v_mfma_f32_16x16x32_bf16 v[28:31],a[232:235],v[60:63],v[28:31]\n"
"ds_read_b64_tr_b16 a[220:221],v184 offset:12320\n"
"v_mfma_f32_16x16x32_bf16 v[28:31],a[236:239],v[68:71],v[28:31]\n"
"ds_read_b64_tr_b16 a[222:223],v184 offset:13824\n"
"buffer_load_dwordx4 v208,s[72:75],s39 offen offset:3072 lds\n"
"v_mfma_f32_16x16x32_bf16 v[132:135],a[232:235],v[232:235],v[132:135]\n"
"v_mfma_f32_16x16x32_bf16 v[132:135],a[236:239],v[232:235],v[132:135]\n"
"s_waitcnt lgkmcnt(12)\n"
"v_mfma_f32_16x16x32_bf16 v[32:35],a[240:243],v[56:59],v[32:35]\n"
"ds_read_b64_tr_b16 a[224:225],v184 offset:256\n"
"v_mfma_f32_16x16x32_bf16 v[32:35],a[244:247],v[64:67],v[32:35]\n"
"ds_read_b64_tr_b16 a[226:227],v184 offset:1824\n"
"s_add_u32 m0,s50,16384\n"
"s_add_u32 s39,s52,524288\n"
"buffer_load_dwordx4 v205,s[72:75],s39 offen lds\n"
"v_mfma_f32_16x16x32_bf16 v[36:39],a[240:243],v[60:63],v[36:39]\n"
"ds_read_b64_tr_b16 a[228:229],v184 offset:12544\n"
"v_mfma_f32_16x16x32_bf16 v[36:39],a[244:247],v[68:71],v[36:39]\n"
"ds_read_b64_tr_b16 a[230:231],v184 offset:14112\n"
"buffer_load_dwordx4 v206,s[72:75],s39 offen offset:1024 lds\n"
"v_mfma_f32_16x16x32_bf16 v[136:139],a[240:243],v[232:235],v[136:139]\n"
"v_mfma_f32_16x16x32_bf16 v[136:139],a[244:247],v[232:235],v[136:139]\n"
"s_waitcnt lgkmcnt(12)\n"
"v_mfma_f32_16x16x32_bf16 v[40:43],a[248:251],v[56:59],v[40:43]\n"
"ds_read_b64_tr_b16 a[232:233],v184 offset:288\n"
"v_mfma_f32_16x16x32_bf16 v[40:43],a[252:255],v[64:67],v[40:43]\n"
"ds_read_b64_tr_b16 a[234:235],v184 offset:1792\n"
"buffer_load_dwordx4 v207,s[72:75],s39 offen offset:2048 lds\n"
"v_mfma_f32_16x16x32_bf16 v[44:47],a[248:251],v[60:63],v[44:47]\n"
"ds_read_b64_tr_b16 a[236:237],v184 offset:12576\n"
"v_mfma_f32_16x16x32_bf16 v[44:47],a[252:255],v[68:71],v[44:47]\n"
"ds_read_b64_tr_b16 a[238:239],v184 offset:14080\n"
"buffer_load_dwordx4 v208,s[72:75],s39 offen offset:3072 lds\n"
"v_mfma_f32_16x16x32_bf16 v[140:143],a[248:251],v[232:235],v[140:143]\n"
"v_mfma_f32_16x16x32_bf16 v[140:143],a[252:255],v[232:235],v[140:143]\n"
"s_waitcnt lgkmcnt(12)\n"
"v_mfma_f32_16x16x32_bf16 v[0:3],a[208:211],v[72:75],v[0:3]\n"
"ds_read_b64_tr_b16 a[240:241],v184 offset:512\n"
"v_mfma_f32_16x16x32_bf16 v[0:3],a[212:215],v[80:83],v[0:3]\n"
"ds_read_b64_tr_b16 a[242:243],v184 offset:2080\n"
"buffer_load_dwordx2 v[226:227],v223,s[68:71],0 offen sc1\n"
"v_mfma_f32_16x16x32_bf16 v[4:7],a[208:211],v[76:79],v[4:7]\n"
"ds_read_b64_tr_b16 a[244:245],v184 offset:12800\n"
"v_mfma_f32_16x16x32_bf16 v[4:7],a[212:215],v[84:87],v[4:7]\n"
"ds_read_b64_tr_b16 a[246:247],v184 offset:14368\n"
"v_mfma_f32_16x16x32_bf16 v[120:123],a[208:211],v[232:235],v[120:123]\n"
"v_mfma_f32_16x16x32_bf16 v[120:123],a[212:215],v[232:235],v[120:123]\n"
"s_waitcnt lgkmcnt(12)\n"
"v_mfma_f32_16x16x32_bf16 v[8:11],a[216:219],v[72:75],v[8:11]\n"
"ds_read_b64_tr_b16 a[248:249],v184 offset:544\n"
"v_mfma_f32_16x16x32_bf16 v[8:11],a[220:223],v[80:83],v[8:11]\n"
"ds_read_b64_tr_b16 a[250:251],v184 offset:2048\n"
"v_mfma_f32_16x16x32_bf16 v[12:15],a[216:219],v[76:79],v[12:15]\n"
"ds_read_b64_tr_b16 a[252:253],v184 offset:12832\n"
"v_mfma_f32_16x16x32_bf16 v[12:15],a[220:223],v[84:87],v[12:15]\n"
"ds_read_b64_tr_b16 a[254:255],v184 offset:14336\n"
"v_mfma_f32_16x16x32_bf16 v[124:127],a[216:219],v[232:235],v[124:127]\n"
"v_mfma_f32_16x16x32_bf16 v[124:127],a[220:223],v[232:235],v[124:127]\n"
"s_waitcnt vmcnt(15) lgkmcnt(12)\n"
"s_barrier\n"
"v_mfma_f32_16x16x32_bf16 v[16:19],a[224:227],v[72:75],v[16:19]\n"
"ds_read_b64_tr_b16 a[208:209],v184 offset:24576\n"
"v_mfma_f32_16x16x32_bf16 v[16:19],a[228:231],v[80:83],v[16:19]\n"
"ds_read_b64_tr_b16 a[210:211],v184 offset:26144\n"
"s_add_u32 m0,s50,32768\n"
"s_add_u32 s39,s52,1048576\n"
"buffer_load_dwordx4 v205,s[72:75],s39 offen lds\n"
"v_mfma_f32_16x16x32_bf16 v[20:23],a[224:227],v[76:79],v[20:23]\n"
"ds_read_b64_tr_b16 a[212:213],v184 offset:36864\n"
"v_mfma_f32_16x16x32_bf16 v[20:23],a[228:231],v[84:87],v[20:23]\n"
"ds_read_b64_tr_b16 a[214:215],v184 offset:38432\n"
"buffer_load_dwordx4 v206,s[72:75],s39 offen offset:1024 lds\n"
"v_mfma_f32_16x16x32_bf16 v[128:131],a[224:227],v[232:235],v[128:131]\n"
"v_mfma_f32_16x16x32_bf16 v[128:131],a[228:231],v[232:235],v[128:131]\n"
"s_waitcnt lgkmcnt(12)\n"
"v_mfma_f32_16x16x32_bf16 v[24:27],a[232:235],v[72:75],v[24:27]\n"
"ds_read_b64_tr_b16 a[216:217],v184 offset:24608\n"
"v_mfma_f32_16x16x32_bf16 v[24:27],a[236:239],v[80:83],v[24:27]\n"
"ds_read_b64_tr_b16 a[218:219],v184 offset:26112\n"
"buffer_load_dwordx4 v207,s[72:75],s39 offen offset:2048 lds\n"
"v_mfma_f32_16x16x32_bf16 v[28:31],a[232:235],v[76:79],v[28:31]\n"
"ds_read_b64_tr_b16 a[220:221],v184 offset:36896\n"
"v_mfma_f32_16x16x32_bf16 v[28:31],a[236:239],v[84:87],v[28:31]\n"
"ds_read_b64_tr_b16 a[222:223],v184 offset:38400\n"
"buffer_load_dwordx4 v208,s[72:75],s39 offen offset:3072 lds\n"
"v_mfma_f32_16x16x32_bf16 v[132:135],a[232:235],v[232:235],v[132:135]\n"
"v_mfma_f32_16x16x32_bf16 v[132:135],a[236:239],v[232:235],v[132:135]\n"
"s_waitcnt lgkmcnt(12)\n"
"v_mfma_f32_16x16x32_bf16 v[32:35],a[240:243],v[72:75],v[32:35]\n"
"ds_read_b64_tr_b16 a[224:225],v184 offset:24832\n"
"v_mfma_f32_16x16x32_bf16 v[32:35],a[244:247],v[80:83],v[32:35]\n"
"ds_read_b64_tr_b16 a[226:227],v184 offset:26400\n"
"s_add_u32 m0,s50,49152\n"
"s_add_u32 s39,s52,1572864\n"
"buffer_load_dwordx4 v205,s[72:75],s39 offen lds\n"
"v_mfma_f32_16x16x32_bf16 v[36:39],a[240:243],v[76:79],v[36:39]\n"
"ds_read_b64_tr_b16 a[228:229],v184 offset:37120\n"
"v_mfma_f32_16x16x32_bf16 v[36:39],a[244:247],v[84:87],v[36:39]\n"
"ds_read_b64_tr_b16 a[230:231],v184 offset:38688\n"
"buffer_load_dwordx4 v206,s[72:75],s39 offen offset:1024 lds\n"
"v_mfma_f32_16x16x32_bf16 v[136:139],a[240:243],v[232:235],v[136:139]\n"
"v_mfma_f32_16x16x32_bf16 v[136:139],a[244:247],v[232:235],v[136:139]\n"
"s_waitcnt lgkmcnt(12)\n"
"v_mfma_f32_16x16x32_bf16 v[40:43],a[248:251],v[72:75],v[40:43]\n"
"ds_read_b64_tr_b16 a[232:233],v184 offset:24864\n"
"v_mfma_f32_16x16x32_bf16 v[40:43],a[252:255],v[80:83],v[40:43]\n"
"ds_read_b64_tr_b16 a[234:235],v184 offset:26368\n"
"buffer_load_dwordx4 v207,s[72:75],s39 offen offset:2048 lds\n"
"v_mfma_f32_16x16x32_bf16 v[44:47],a[248:251],v[76:79],v[44:47]\n"
"ds_read_b64_tr_b16 a[236:237],v184 offset:37152\n"
"v_mfma_f32_16x16x32_bf16 v[44:47],a[252:255],v[84:87],v[44:47]\n"
"ds_read_b64_tr_b16 a[238:239],v184 offset:38656\n"
"buffer_load_dwordx4 v208,s[72:75],s39 offen offset:3072 lds\n"
"v_mfma_f32_16x16x32_bf16 v[140:143],a[248:251],v[232:235],v[140:143]\n"
"v_mfma_f32_16x16x32_bf16 v[140:143],a[252:255],v[232:235],v[140:143]\n"
"s_waitcnt lgkmcnt(12)\n"
"v_mfma_f32_16x16x32_bf16 v[0:3],a[208:211],v[88:91],v[0:3]\n"
"ds_read_b64_tr_b16 a[240:241],v184 offset:25088\n"
"v_mfma_f32_16x16x32_bf16 v[0:3],a[212:215],v[96:99],v[0:3]\n"
"ds_read_b64_tr_b16 a[242:243],v184 offset:26656\n"
"v_mfma_f32_16x16x32_bf16 v[4:7],a[208:211],v[92:95],v[4:7]\n"
"ds_read_b64_tr_b16 a[244:245],v184 offset:37376\n"
"v_mfma_f32_16x16x32_bf16 v[4:7],a[212:215],v[100:103],v[4:7]\n"
"ds_read_b64_tr_b16 a[246:247],v184 offset:38944\n"
"v_mfma_f32_16x16x32_bf16 v[120:123],a[208:211],v[232:235],v[120:123]\n"
"v_mfma_f32_16x16x32_bf16 v[120:123],a[212:215],v[232:235],v[120:123]\n"
"s_waitcnt lgkmcnt(12)\n"
"v_mfma_f32_16x16x32_bf16 v[8:11],a[216:219],v[88:91],v[8:11]\n"
"ds_read_b64_tr_b16 a[248:249],v184 offset:25120\n"
"v_mfma_f32_16x16x32_bf16 v[8:11],a[220:223],v[96:99],v[8:11]\n"
"ds_read_b64_tr_b16 a[250:251],v184 offset:26624\n"
"v_mfma_f32_16x16x32_bf16 v[12:15],a[216:219],v[92:95],v[12:15]\n"
"ds_read_b64_tr_b16 a[252:253],v184 offset:37408\n"
"v_mfma_f32_16x16x32_bf16 v[12:15],a[220:223],v[100:103],v[12:15]\n"
"ds_read_b64_tr_b16 a[254:255],v184 offset:38912\n"
"v_mfma_f32_16x16x32_bf16 v[124:127],a[216:219],v[232:235],v[124:127]\n"
"v_mfma_f32_16x16x32_bf16 v[124:127],a[220:223],v[232:235],v[124:127]\n"
"s_waitcnt vmcnt(17) lgkmcnt(12)\n"
"s_barrier\n"
"v_mfma_f32_16x16x32_bf16 v[16:19],a[224:227],v[88:91],v[16:19]\n"
"ds_read_b64_tr_b16 a[208:209],v186 offset:24576\n"
"v_mfma_f32_16x16x32_bf16 v[16:19],a[228:231],v[96:99],v[16:19]\n"
"ds_read_b64_tr_b16 a[210:211],v186 offset:26144\n"
"v_mfma_f32_16x16x32_bf16 v[20:23],a[224:227],v[92:95],v[20:23]\n"
"ds_read_b64_tr_b16 a[212:213],v186 offset:36864\n"
"v_mfma_f32_16x16x32_bf16 v[20:23],a[228:231],v[100:103],v[20:23]\n"
"ds_read_b64_tr_b16 a[214:215],v186 offset:38432\n"
"v_mfma_f32_16x16x32_bf16 v[128:131],a[224:227],v[232:235],v[128:131]\n"
"v_mfma_f32_16x16x32_bf16 v[128:131],a[228:231],v[232:235],v[128:131]\n"
"s_waitcnt lgkmcnt(12)\n"
"v_mfma_f32_16x16x32_bf16 v[24:27],a[232:235],v[88:91],v[24:27]\n"
"ds_read_b64_tr_b16 a[216:217],v186 offset:24608\n"
"v_mfma_f32_16x16x32_bf16 v[24:27],a[236:239],v[96:99],v[24:27]\n"
"ds_read_b64_tr_b16 a[218:219],v186 offset:26112\n"
"v_mfma_f32_16x16x32_bf16 v[28:31],a[232:235],v[92:95],v[28:31]\n"
"ds_read_b64_tr_b16 a[220:221],v186 offset:36896\n"
"v_mfma_f32_16x16x32_bf16 v[28:31],a[236:239],v[100:103],v[28:31]\n"
"ds_read_b64_tr_b16 a[222:223],v186 offset:38400\n"
"v_mfma_f32_16x16x32_bf16 v[132:135],a[232:235],v[232:235],v[132:135]\n"
"v_mfma_f32_16x16x32_bf16 v[132:135],a[236:239],v[232:235],v[132:135]\n"
"s_waitcnt lgkmcnt(12)\n"
"v_mfma_f32_16x16x32_bf16 v[32:35],a[240:243],v[88:91],v[32:35]\n"
"ds_read_b64_tr_b16 a[224:225],v186 offset:24832\n"
"v_mfma_f32_16x16x32_bf16 v[32:35],a[244:247],v[96:99],v[32:35]\n"
"ds_read_b64_tr_b16 a[226:227],v186 offset:26400\n"
"v_mfma_f32_16x16x32_bf16 v[36:39],a[240:243],v[92:95],v[36:39]\n"
"ds_read_b64_tr_b16 a[228:229],v186 offset:37120\n"
"v_mfma_f32_16x16x32_bf16 v[36:39],a[244:247],v[100:103],v[36:39]\n"
"ds_read_b64_tr_b16 a[230:231],v186 offset:38688\n"
"v_mfma_f32_16x16x32_bf16 v[136:139],a[240:243],v[232:235],v[136:139]\n"
"v_mfma_f32_16x16x32_bf16 v[136:139],a[244:247],v[232:235],v[136:139]\n"
"s_waitcnt lgkmcnt(12)\n"
"v_mfma_f32_16x16x32_bf16 v[40:43],a[248:251],v[88:91],v[40:43]\n"
"ds_read_b64_tr_b16 a[232:233],v186 offset:24864\n"
"v_mfma_f32_16x16x32_bf16 v[40:43],a[252:255],v[96:99],v[40:43]\n"
"ds_read_b64_tr_b16 a[234:235],v186 offset:26368\n"
"v_mfma_f32_16x16x32_bf16 v[44:47],a[248:251],v[92:95],v[44:47]\n"
"ds_read_b64_tr_b16 a[236:237],v186 offset:37152\n"
"v_mfma_f32_16x16x32_bf16 v[44:47],a[252:255],v[100:103],v[44:47]\n"
"ds_read_b64_tr_b16 a[238:239],v186 offset:38656\n"
"v_mfma_f32_16x16x32_bf16 v[140:143],a[248:251],v[232:235],v[140:143]\n"
"v_mfma_f32_16x16x32_bf16 v[140:143],a[252:255],v[232:235],v[140:143]\n"
"s_waitcnt lgkmcnt(12)\n"
"v_mfma_f32_16x16x32_bf16 v[0:3],a[208:211],v[104:107],v[0:3]\n"
"ds_read_b64_tr_b16 a[240:241],v186 offset:25088\n"
"v_mfma_f32_16x16x32_bf16 v[0:3],a[212:215],v[112:115],v[0:3]\n"
"ds_read_b64_tr_b16 a[242:243],v186 offset:26656\n"
"s_mov_b32 s55,0x40000\n"
"s_waitcnt vmcnt(8)\n"
"LM_POLL3:\n"
"v_cmp_ne_u64 vcc,v[226:227],v[224:225]\n"
"s_nop 1\n"
"s_and_b64 s[44:45],vcc,s[46:47]\n"
"s_cbranch_scc0 LM_POK4\n"
"s_sleep 1\n"
"buffer_load_dwordx2 v[226:227],v223,s[68:71],0 offen sc1\n"
"s_waitcnt vmcnt(0)\n"
"s_sub_u32 s55,s55,1\n"
"s_cbranch_scc0 LM_POLL3\n"
"LM_POK4:\n"
"v_and_b32 v187,0xf0,v209\n"
"v_lshrrev_b32 v187,1,v187\n"
"buffer_load_dwordx2 v[188:189],v187,s[64:67],s59 offen sc1\n"
"buffer_load_dwordx2 v[190:191],v187,s[64:67],s59 offen offset:128 sc1\n"
"s_add_u32 s40,s53,0\n"
"buffer_load_dwordx4 a[0:3],v209,s[64:67],s40 offen sc1\n"
"v_mfma_f32_16x16x32_bf16 v[4:7],a[208:211],v[108:111],v[4:7]\n"
"ds_read_b64_tr_b16 a[244:245],v186 offset:37376\n"
"v_mfma_f32_16x16x32_bf16 v[4:7],a[212:215],v[116:119],v[4:7]\n"
"ds_read_b64_tr_b16 a[246:247],v186 offset:38944\n"
"buffer_load_dwordx4 a[4:7],v209,s[64:67],s40 offen offset:1024 sc1\n"
"v_mfma_f32_16x16x32_bf16 v[120:123],a[208:211],v[232:235],v[120:123]\n"
"v_mfma_f32_16x16x32_bf16 v[120:123],a[212:215],v[232:235],v[120:123]\n"
"s_waitcnt lgkmcnt(12)\n"
"v_mfma_f32_16x16x32_bf16 v[8:11],a[216:219],v[104:107],v[8:11]\n"
"ds_read_b64_tr_b16 a[248:249],v186 offset:25120\n"
"v_mfma_f32_16x16x32_bf16 v[8:11],a[220:223],v[112:115],v[8:11]\n"
"ds_read_b64_tr_b16 a[250:251],v186 offset:26624\n"
"buffer_load_dwordx4 a[8:11],v209,s[64:67],s40 offen offset:2048 sc1\n"
"v_mfma_f32_16x16x32_bf16 v[12:15],a[216:219],v[108:111],v[12:15]\n"
"ds_read_b64_tr_b16 a[252:253],v186 offset:37408\n"
"v_mfma_f32_16x16x32_bf16 v[12:15],a[220:223],v[116:119],v[12:15]\n"
"ds_read_b64_tr_b16 a[254:255],v186 offset:38912\n"
"buffer_load_dwordx4 a[12:15],v209,s[64:67],s40 offen offset:3072 sc1\n"
"v_mfma_f32_16x16x32_bf16 v[124:127],a[216:219],v[232:235],v[124:127]\n"
"v_mfma_f32_16x16x32_bf16 v[124:127],a[220:223],v[232:235],v[124:127]\n"
"s_waitcnt lgkmcnt(12)\n"
"v_mfma_f32_16x16x32_bf16 v[16:19],a[224:227],v[104:107],v[16:19]\n"
"v_mfma_f32_16x16x32_bf16 v[16:19],a[228:231],v[112:115],v[16:19]\n"
"s_add_u32 s40,s53,4096\n"
"buffer_load_dwordx4 a[16:19],v209,s[64:67],s40 offen sc1\n"
"v_mfma_f32_16x16x32_bf16 v[20:23],a[224:227],v[108:111],v[20:23]\n"
"v_mfma_f32_16x16x32_bf16 v[20:23],a[228:231],v[116:119],v[20:23]\n"
"buffer_load_dwordx4 a[20:23],v209,s[64:67],s40 offen offset:1024 sc1\n"
"v_mfma_f32_16x16x32_bf16 v[128:131],a[224:227],v[232:235],v[128:131]\n"
"v_mfma_f32_16x16x32_bf16 v[128:131],a[228:231],v[232:235],v[128:131]\n"
"s_waitcnt lgkmcnt(8)\n"
"v_mfma_f32_16x16x32_bf16 v[24:27],a[232:235],v[104:107],v[24:27]\n"
"v_mfma_f32_16x16x32_bf16 v[24:27],a[236:239],v[112:115],v[24:27]\n"
"buffer_load_dwordx4 a[24:27],v209,s[64:67],s40 offen offset:2048 sc1\n"
"v_mfma_f32_16x16x32_bf16 v[28:31],a[232:235],v[108:111],v[28:31]\n"
"v_mfma_f32_16x16x32_bf16 v[28:31],a[236:239],v[116:119],v[28:31]\n"
"buffer_load_dwordx4 a[28:31],v209,s[64:67],s40 offen offset:3072 sc1\n"
"v_mfma_f32_16x16x32_bf16 v[132:135],a[232:235],v[232:235],v[132:135]\n"
"v_mfma_f32_16x16x32_bf16 v[132:135],a[236:239],v[232:235],v[132:135]\n"
"s_waitcnt lgkmcnt(4)\n"
"v_mfma_f32_16x16x32_bf16 v[32:35],a[240:243],v[104:107],v[32:35]\n"
"v_mfma_f32_16x16x32_bf16 v[32:35],a[244:247],v[112:115],v[32:35]\n"
"v_mfma_f32_16x16x32_bf16 v[36:39],a[240:243],v[108:111],v[36:39]\n"
"v_mfma_f32_16x16x32_bf16 v[36:39],a[244:247],v[116:119],v[36:39]\n"
"v_mfma_f32_16x16x32_bf16 v[136:139],a[240:243],v[232:235],v[136:139]\n"
"v_mfma_f32_16x16x32_bf16 v[136:139],a[244:247],v[232:235],v[136:139]\n"
"s_waitcnt lgkmcnt(0)\n"
"v_mfma_f32_16x16x32_bf16 v[40:43],a[248:251],v[104:107],v[40:43]\n"
"v_mfma_f32_16x16x32_bf16 v[40:43],a[252:255],v[112:115],v[40:43]\n"
"v_mfma_f32_16x16x32_bf16 v[44:47],a[248:251],v[108:111],v[44:47]\n"
"v_mfma_f32_16x16x32_bf16 v[44:47],a[252:255],v[116:119],v[44:47]\n"
"v_mfma_f32_16x16x32_bf16 v[140:143],a[248:251],v[232:235],v[140:143]\n"
"v_mfma_f32_16x16x32_bf16 v[140:143],a[252:255],v[232:235],v[140:143]\n"
"s_waitcnt vmcnt(8)\n"
"s_barrier\n"
"s_mov_b32 s56,0xbdd2d3e8\n"
"s_mov_b32 s57,0xbdd2d3e8\n"
"v_mov_b32 v184,0xc0135761\n"
"v_mov_b32 v185,0xc0135761\n"
"v_mov_b32 v186,1.0\n"
"v_mov_b32 v187,1.0\n"
"v_mul_f32 v0,v0,v188\n"
"v_mul_f32 v1,v1,v188\n"
"v_mul_f32 v2,v2,v188\n"
"v_mul_f32 v3,v3,v188\n"
"v_mul_f32 v4,v4,v190\n"
"v_mul_f32 v5,v5,v190\n"
"v_mul_f32 v6,v6,v190\n"
"v_mul_f32 v7,v7,v190\n"
"v_fmac_f32 v0,v120,v189\n"
"v_fmac_f32 v1,v121,v189\n"
"v_fmac_f32 v2,v122,v189\n"
"v_fmac_f32 v3,v123,v189\n"
"v_fmac_f32 v4,v120,v191\n"
"v_fmac_f32 v5,v121,v191\n"
"v_fmac_f32 v6,v122,v191\n"
"v_fmac_f32 v7,v123,v191\n"
"s_add_u32 s40,s53,8192\n"
"buffer_load_dwordx4 a[32:35],v209,s[64:67],s40 offen sc1\n"
"buffer_load_dwordx4 a[36:39],v209,s[64:67],s40 offen offset:1024 sc1\n"
"v_mul_f32 v8,v8,v188\n"
"v_mul_f32 v9,v9,v188\n"
"v_mul_f32 v10,v10,v188\n"
"v_mul_f32 v11,v11,v188\n"
"v_mul_f32 v12,v12,v190\n"
"v_mul_f32 v13,v13,v190\n"
"v_mul_f32 v14,v14,v190\n"
"v_mul_f32 v15,v15,v190\n"
"v_fmac_f32 v8,v124,v189\n"
"v_fmac_f32 v9,v125,v189\n"
"v_fmac_f32 v10,v126,v189\n"
"v_fmac_f32 v11,v127,v189\n"
"v_fmac_f32 v12,v124,v191\n"
"v_fmac_f32 v13,v125,v191\n"
"v_fmac_f32 v14,v126,v191\n"
"v_fmac_f32 v15,v127,v191\n"
"buffer_load_dwordx4 a[40:43],v209,s[64:67],s40 offen offset:2048 sc1\n"
"buffer_load_dwordx4 a[44:47],v209,s[64:67],s40 offen offset:3072 sc1\n"
"v_mul_f32 v16,v16,v188\n"
"v_mul_f32 v17,v17,v188\n"
"v_mul_f32 v18,v18,v188\n"
"v_mul_f32 v19,v19,v188\n"
"v_mul_f32 v20,v20,v190\n"
"v_mul_f32 v21,v21,v190\n"
"v_mul_f32 v22,v22,v190\n"
"v_mul_f32 v23,v23,v190\n"
"v_fmac_f32 v16,v128,v189\n"
"v_fmac_f32 v17,v129,v189\n"
"v_fmac_f32 v18,v130,v189\n"
"v_fmac_f32 v19,v131,v189\n"
"v_fmac_f32 v20,v128,v191\n"
"v_fmac_f32 v21,v129,v191\n"
"v_fmac_f32 v22,v130,v191\n"
"v_fmac_f32 v23,v131,v191\n"
"s_add_u32 s40,s53,12288\n"
"buffer_load_dwordx4 a[48:51],v209,s[64:67],s40 offen sc1\n"
"buffer_load_dwordx4 a[52:55],v209,s[64:67],s40 offen offset:1024 sc1\n"
"v_mul_f32 v24,v24,v188\n"
"v_mul_f32 v25,v25,v188\n"
"v_mul_f32 v26,v26,v188\n"
"v_mul_f32 v27,v27,v188\n"
"v_mul_f32 v28,v28,v190\n"
"v_mul_f32 v29,v29,v190\n"
"v_mul_f32 v30,v30,v190\n"
"v_mul_f32 v31,v31,v190\n"
"v_fmac_f32 v24,v132,v189\n"
"v_fmac_f32 v25,v133,v189\n"
"v_fmac_f32 v26,v134,v189\n"
"v_fmac_f32 v27,v135,v189\n"
"v_fmac_f32 v28,v132,v191\n"
"v_fmac_f32 v29,v133,v191\n"
"v_fmac_f32 v30,v134,v191\n"
"v_fmac_f32 v31,v135,v191\n"
"buffer_load_dwordx4 a[56:59],v209,s[64:67],s40 offen offset:2048 sc1\n"
"buffer_load_dwordx4 a[60:63],v209,s[64:67],s40 offen offset:3072 sc1\n"
"v_mul_f32 v32,v32,v188\n"
"v_mul_f32 v33,v33,v188\n"
"v_mul_f32 v34,v34,v188\n"
"v_mul_f32 v35,v35,v188\n"
"v_mul_f32 v36,v36,v190\n"
"v_mul_f32 v37,v37,v190\n"
"v_mul_f32 v38,v38,v190\n"
"v_mul_f32 v39,v39,v190\n"
"v_fmac_f32 v32,v136,v189\n"
"v_fmac_f32 v33,v137,v189\n"
"v_fmac_f32 v34,v138,v189\n"
"v_fmac_f32 v35,v139,v189\n"
"v_fmac_f32 v36,v136,v191\n"
"v_fmac_f32 v37,v137,v191\n"
"v_fmac_f32 v38,v138,v191\n"
"v_fmac_f32 v39,v139,v191\n"
"v_mul_f32 v40,v40,v188\n"
"v_mul_f32 v41,v41,v188\n"
"v_mul_f32 v42,v42,v188\n"
"v_mul_f32 v43,v43,v188\n"
"v_mul_f32 v44,v44,v190\n"
"v_mul_f32 v45,v45,v190\n"
"v_mul_f32 v46,v46,v190\n"
"v_mul_f32 v47,v47,v190\n"
"v_fmac_f32 v40,v140,v189\n"
"v_fmac_f32 v41,v141,v189\n"
"v_fmac_f32 v42,v142,v189\n"
"v_fmac_f32 v43,v143,v189\n"
"v_fmac_f32 v44,v140,v191\n"
"v_fmac_f32 v45,v141,v191\n"
"v_fmac_f32 v46,v142,v191\n"
"v_fmac_f32 v47,v143,v191\n"
"s_nop 7\n"
"v_pk_mul_f32 v[144:145],v[0:1],v[0:1]\n"
"v_pk_mul_f32 v[146:147],v[2:3],v[2:3]\n"
"v_pk_mul_f32 v[148:149],v[8:9],v[8:9]\n"
"v_pk_mul_f32 v[150:151],v[10:11],v[10:11]\n"
"v_pk_fma_f32 v[144:145],v[144:145],s[56:57],v[184:185]\n"
"v_pk_fma_f32 v[146:147],v[146:147],s[56:57],v[184:185]\n"
"v_pk_fma_f32 v[148:149],v[148:149],s[56:57],v[184:185]\n"
"v_pk_fma_f32 v[150:151],v[150:151],s[56:57],v[184:185]\n"
"v_pk_mul_f32 v[144:145],v[144:145],v[0:1]\n"
"v_pk_mul_f32 v[146:147],v[146:147],v[2:3]\n"
"v_pk_mul_f32 v[148:149],v[148:149],v[8:9]\n"
"v_pk_mul_f32 v[150:151],v[150:151],v[10:11]\n"
"v_exp_f32 v144,v144\n"
"v_exp_f32 v145,v145\n"
"v_exp_f32 v146,v146\n"
"v_exp_f32 v147,v147\n"
"v_exp_f32 v148,v148\n"
"v_exp_f32 v149,v149\n"
"v_exp_f32 v150,v150\n"
"v_exp_f32 v151,v151\n"
"s_nop 0\n"
"v_pk_add_f32 v[144:145],v[144:145],v[186:187]\n"
"v_pk_add_f32 v[146:147],v[146:147],v[186:187]\n"
"v_pk_add_f32 v[148:149],v[148:149],v[186:187]\n"
"v_pk_add_f32 v[150:151],v[150:151],v[186:187]\n"
"v_rcp_f32 v144,v144\n"
"v_rcp_f32 v145,v145\n"
"v_rcp_f32 v146,v146\n"
"v_rcp_f32 v147,v147\n"
"v_rcp_f32 v148,v148\n"
"v_rcp_f32 v149,v149\n"
"v_rcp_f32 v150,v150\n"
"v_rcp_f32 v151,v151\n"
"s_nop 0\n"
"v_pk_mul_f32 v[144:145],v[144:145],v[0:1]\n"
"v_pk_mul_f32 v[146:147],v[146:147],v[2:3]\n"
"v_pk_mul_f32 v[148:149],v[148:149],v[8:9]\n"
"v_pk_mul_f32 v[150:151],v[150:151],v[10:11]\n"
"v_cvt_pk_bf16_f32 v160,v144,v145\n"
"v_cvt_pk_bf16_f32 v161,v146,v147\n"
"v_cvt_pk_bf16_f32 v162,v148,v149\n"
"v_cvt_pk_bf16_f32 v163,v150,v151\n"
"s_add_u32 s42,s58,2359296\n"
"buffer_store_dwordx4 v[160:163],v209,s[64:67],s42 offen sc1\n"
"v_pk_mul_f32 v[144:145],v[4:5],v[4:5]\n"
"v_pk_mul_f32 v[146:147],v[6:7],v[6:7]\n"
"v_pk_mul_f32 v[148:149],v[12:13],v[12:13]\n"
"v_pk_mul_f32 v[150:151],v[14:15],v[14:15]\n"
"v_pk_fma_f32 v[144:145],v[144:145],s[56:57],v[184:185]\n"
"v_pk_fma_f32 v[146:147],v[146:147],s[56:57],v[184:185]\n"
"v_pk_fma_f32 v[148:149],v[148:149],s[56:57],v[184:185]\n"
"v_pk_fma_f32 v[150:151],v[150:151],s[56:57],v[184:185]\n"
"v_pk_mul_f32 v[144:145],v[144:145],v[4:5]\n"
"v_pk_mul_f32 v[146:147],v[146:147],v[6:7]\n"
"v_pk_mul_f32 v[148:149],v[148:149],v[12:13]\n"
"v_pk_mul_f32 v[150:151],v[150:151],v[14:15]\n"
"v_exp_f32 v144,v144\n"
"v_exp_f32 v145,v145\n"
"v_exp_f32 v146,v146\n"
"v_exp_f32 v147,v147\n"
"v_exp_f32 v148,v148\n"
"v_exp_f32 v149,v149\n"
"v_exp_f32 v150,v150\n"
"v_exp_f32 v151,v151\n"
"s_nop 0\n"
"v_pk_add_f32 v[144:145],v[144:145],v[186:187]\n"
"v_pk_add_f32 v[146:147],v[146:147],v[186:187]\n"
"v_pk_add_f32 v[148:149],v[148:149],v[186:187]\n"
"v_pk_add_f32 v[150:151],v[150:151],v[186:187]\n"
"v_rcp_f32 v144,v144\n"
"v_rcp_f32 v145,v145\n"
"v_rcp_f32 v146,v146\n"
"v_rcp_f32 v147,v147\n"
"v_rcp_f32 v148,v148\n"
"v_rcp_f32 v149,v149\n"
"v_rcp_f32 v150,v150\n"
"v_rcp_f32 v151,v151\n"
"s_nop 0\n"
"v_pk_mul_f32 v[144:145],v[144:145],v[4:5]\n"
"v_pk_mul_f32 v[146:147],v[146:147],v[6:7]\n"
"v_pk_mul_f32 v[148:149],v[148:149],v[12:13]\n"
"v_pk_mul_f32 v[150:151],v[150:151],v[14:15]\n"
"v_cvt_pk_bf16_f32 v164,v144,v145\n"
"v_cvt_pk_bf16_f32 v165,v146,v147\n"
"v_cvt_pk_bf16_f32 v166,v148,v149\n"
"v_cvt_pk_bf16_f32 v167,v150,v151\n"
"s_add_u32 s42,s58,2359296\n"
"buffer_store_dwordx4 v[164:167],v209,s[64:67],s42 offen offset:1024 sc1\n"
"v_pk_mul_f32 v[144:145],v[16:17],v[16:17]\n"
"v_pk_mul_f32 v[146:147],v[18:19],v[18:19]\n"
"v_pk_mul_f32 v[148:149],v[24:25],v[24:25]\n"
"v_pk_mul_f32 v[150:151],v[26:27],v[26:27]\n"
"v_pk_fma_f32 v[144:145],v[144:145],s[56:57],v[184:185]\n"
"v_pk_fma_f32 v[146:147],v[146:147],s[56:57],v[184:185]\n"
"v_pk_fma_f32 v[148:149],v[148:149],s[56:57],v[184:185]\n"
"v_pk_fma_f32 v[150:151],v[150:151],s[56:57],v[184:185]\n"
"v_pk_mul_f32 v[144:145],v[144:145],v[16:17]\n"
"v_pk_mul_f32 v[146:147],v[146:147],v[18:19]\n"
"v_pk_mul_f32 v[148:149],v[148:149],v[24:25]\n"
"v_pk_mul_f32 v[150:151],v[150:151],v[26:27]\n"
"v_exp_f32 v144,v144\n"
"v_exp_f32 v145,v145\n"
"v_exp_f32 v146,v146\n"
"v_exp_f32 v147,v147\n"
"v_exp_f32 v148,v148\n"
"v_exp_f32 v149,v149\n"
"v_exp_f32 v150,v150\n"
"v_exp_f32 v151,v151\n"
"s_nop 0\n"
"v_pk_add_f32 v[144:145],v[144:145],v[186:187]\n"
"v_pk_add_f32 v[146:147],v[146:147],v[186:187]\n"
"v_pk_add_f32 v[148:149],v[148:149],v[186:187]\n"
"v_pk_add_f32 v[150:151],v[150:151],v[186:187]\n"
"v_rcp_f32 v144,v144\n"
"v_rcp_f32 v145,v145\n"
"v_rcp_f32 v146,v146\n"
"v_rcp_f32 v147,v147\n"
"v_rcp_f32 v148,v148\n"
"v_rcp_f32 v149,v149\n"
"v_rcp_f32 v150,v150\n"
"v_rcp_f32 v151,v151\n"
"s_nop 0\n"
"v_pk_mul_f32 v[144:145],v[144:145],v[16:17]\n"
"v_pk_mul_f32 v[146:147],v[146:147],v[18:19]\n"
"v_pk_mul_f32 v[148:149],v[148:149],v[24:25]\n"
"v_pk_mul_f32 v[150:151],v[150:151],v[26:27]\n"
"v_cvt_pk_bf16_f32 v168,v144,v145\n"
"v_cvt_pk_bf16_f32 v169,v146,v147\n"
"v_cvt_pk_bf16_f32 v170,v148,v149\n"
"v_cvt_pk_bf16_f32 v171,v150,v151\n"
"s_add_u32 s42,s58,2363392\n"
"buffer_store_dwordx4 v[168:171],v209,s[64:67],s42 offen sc1\n"
"v_pk_mul_f32 v[144:145],v[20:21],v[20:21]\n"
"v_pk_mul_f32 v[146:147],v[22:23],v[22:23]\n"
"v_pk_mul_f32 v[148:149],v[28:29],v[28:29]\n"
"v_pk_mul_f32 v[150:151],v[30:31],v[30:31]\n"
"v_pk_fma_f32 v[144:145],v[144:145],s[56:57],v[184:185]\n"
"v_pk_fma_f32 v[146:147],v[146:147],s[56:57],v[184:185]\n"
"v_pk_fma_f32 v[148:149],v[148:149],s[56:57],v[184:185]\n"
"v_pk_fma_f32 v[150:151],v[150:151],s[56:57],v[184:185]\n"
"v_pk_mul_f32 v[144:145],v[144:145],v[20:21]\n"
"v_pk_mul_f32 v[146:147],v[146:147],v[22:23]\n"
"v_pk_mul_f32 v[148:149],v[148:149],v[28:29]\n"
"v_pk_mul_f32 v[150:151],v[150:151],v[30:31]\n"
"v_exp_f32 v144,v144\n"
"v_exp_f32 v145,v145\n"
"v_exp_f32 v146,v146\n"
"v_exp_f32 v147,v147\n"
"v_exp_f32 v148,v148\n"
"v_exp_f32 v149,v149\n"
"v_exp_f32 v150,v150\n"
"v_exp_f32 v151,v151\n"
"s_nop 0\n"
"v_pk_add_f32 v[144:145],v[144:145],v[186:187]\n"
"v_pk_add_f32 v[146:147],v[146:147],v[186:187]\n"
"v_pk_add_f32 v[148:149],v[148:149],v[186:187]\n"
"v_pk_add_f32 v[150:151],v[150:151],v[186:187]\n"
"v_rcp_f32 v144,v144\n"
"v_rcp_f32 v145,v145\n"
"v_rcp_f32 v146,v146\n"
"v_rcp_f32 v147,v147\n"
"v_rcp_f32 v148,v148\n"
"v_rcp_f32 v149,v149\n"
"v_rcp_f32 v150,v150\n"
"v_rcp_f32 v151,v151\n"
"s_nop 0\n"
"v_pk_mul_f32 v[144:145],v[144:145],v[20:21]\n"
"v_pk_mul_f32 v[146:147],v[146:147],v[22:23]\n"
"v_pk_mul_f32 v[148:149],v[148:149],v[28:29]\n"
"v_pk_mul_f32 v[150:151],v[150:151],v[30:31]\n"
"v_cvt_pk_bf16_f32 v172,v144,v145\n"
"v_cvt_pk_bf16_f32 v173,v146,v147\n"
"v_cvt_pk_bf16_f32 v174,v148,v149\n"
"v_cvt_pk_bf16_f32 v175,v150,v151\n"
"s_add_u32 s42,s58,2363392\n"
"buffer_store_dwordx4 v[172:175],v209,s[64:67],s42 offen offset:1024 sc1\n"
"v_pk_mul_f32 v[144:145],v[32:33],v[32:33]\n"
"v_pk_mul_f32 v[146:147],v[34:35],v[34:35]\n"
"v_pk_mul_f32 v[148:149],v[40:41],v[40:41]\n"
"v_pk_mul_f32 v[150:151],v[42:43],v[42:43]\n"
"v_pk_fma_f32 v[144:145],v[144:145],s[56:57],v[184:185]\n"
"v_pk_fma_f32 v[146:147],v[146:147],s[56:57],v[184:185]\n"
"v_pk_fma_f32 v[148:149],v[148:149],s[56:57],v[184:185]\n"
"v_pk_fma_f32 v[150:151],v[150:151],s[56:57],v[184:185]\n"
"v_pk_mul_f32 v[144:145],v[144:145],v[32:33]\n"
"v_pk_mul_f32 v[146:147],v[146:147],v[34:35]\n"
"v_pk_mul_f32 v[148:149],v[148:149],v[40:41]\n"
"v_pk_mul_f32 v[150:151],v[150:151],v[42:43]\n"
"v_exp_f32 v144,v144\n"
"v_exp_f32 v145,v145\n"
"v_exp_f32 v146,v146\n"
"v_exp_f32 v147,v147\n"
"v_exp_f32 v148,v148\n"
"v_exp_f32 v149,v149\n"
"v_exp_f32 v150,v150\n"
"v_exp_f32 v151,v151\n"
"s_nop 0\n"
"v_pk_add_f32 v[144:145],v[144:145],v[186:187]\n"
"v_pk_add_f32 v[146:147],v[146:147],v[186:187]\n"
"v_pk_add_f32 v[148:149],v[148:149],v[186:187]\n"
"v_pk_add_f32 v[150:151],v[150:151],v[186:187]\n"
"v_rcp_f32 v144,v144\n"
"v_rcp_f32 v145,v145\n"
"v_rcp_f32 v146,v146\n"
"v_rcp_f32 v147,v147\n"
"v_rcp_f32 v148,v148\n"
"v_rcp_f32 v149,v149\n"
"v_rcp_f32 v150,v150\n"
"v_rcp_f32 v151,v151\n"
"s_nop 0\n"
"v_pk_mul_f32 v[144:145],v[144:145],v[32:33]\n"
"v_pk_mul_f32 v[146:147],v[146:147],v[34:35]\n"
"v_pk_mul_f32 v[148:149],v[148:149],v[40:41]\n"
"v_pk_mul_f32 v[150:151],v[150:151],v[42:43]\n"
"v_cvt_pk_bf16_f32 v176,v144,v145\n"
"v_cvt_pk_bf16_f32 v177,v146,v147\n"
"v_cvt_pk_bf16_f32 v178,v148,v149\n"
"v_cvt_pk_bf16_f32 v179,v150,v151\n"
"s_add_u32 s42,s58,2367488\n"
"buffer_store_dwordx4 v[176:179],v209,s[64:67],s42 offen sc1\n"
"v_pk_mul_f32 v[144:145],v[36:37],v[36:37]\n"
"v_pk_mul_f32 v[146:147],v[38:39],v[38:39]\n"
"v_pk_mul_f32 v[148:149],v[44:45],v[44:45]\n"
"v_pk_mul_f32 v[150:151],v[46:47],v[46:47]\n"
"v_pk_fma_f32 v[144:145],v[144:145],s[56:57],v[184:185]\n"
"v_pk_fma_f32 v[146:147],v[146:147],s[56:57],v[184:185]\n"
"v_pk_fma_f32 v[148:149],v[148:149],s[56:57],v[184:185]\n"
"v_pk_fma_f32 v[150:151],v[150:151],s[56:57],v[184:185]\n"
"v_pk_mul_f32 v[144:145],v[144:145],v[36:37]\n"
"v_pk_mul_f32 v[146:147],v[146:147],v[38:39]\n"
"v_pk_mul_f32 v[148:149],v[148:149],v[44:45]\n"
"v_pk_mul_f32 v[150:151],v[150:151],v[46:47]\n"
"v_exp_f32 v144,v144\n"
"v_exp_f32 v145,v145\n"
"v_exp_f32 v146,v146\n"
"v_exp_f32 v147,v147\n"
"v_exp_f32 v148,v148\n"
"v_exp_f32 v149,v149\n"
"v_exp_f32 v150,v150\n"
"v_exp_f32 v151,v151\n"
"s_nop 0\n"
"v_pk_add_f32 v[144:145],v[144:145],v[186:187]\n"
"v_pk_add_f32 v[146:147],v[146:147],v[186:187]\n"
"v_pk_add_f32 v[148:149],v[148:149],v[186:187]\n"
"v_pk_add_f32 v[150:151],v[150:151],v[186:187]\n"
"v_rcp_f32 v144,v144\n"
"v_rcp_f32 v145,v145\n"
"v_rcp_f32 v146,v146\n"
"v_rcp_f32 v147,v147\n"
"v_rcp_f32 v148,v148\n"
"v_rcp_f32 v149,v149\n"
"v_rcp_f32 v150,v150\n"
"v_rcp_f32 v151,v151\n"
"s_nop 0\n"
"v_pk_mul_f32 v[144:145],v[144:145],v[36:37]\n"
"v_pk_mul_f32 v[146:147],v[146:147],v[38:39]\n"
"v_pk_mul_f32 v[148:149],v[148:149],v[44:45]\n"
"v_pk_mul_f32 v[150:151],v[150:151],v[46:47]\n"
"v_cvt_pk_bf16_f32 v180,v144,v145\n"
"v_cvt_pk_bf16_f32 v181,v146,v147\n"
"v_cvt_pk_bf16_f32 v182,v148,v149\n"
"v_cvt_pk_bf16_f32 v183,v150,v151\n"
"s_add_u32 s42,s58,2367488\n"
"buffer_store_dwordx4 v[180:183],v209,s[64:67],s42 offen offset:1024 sc1\n"
"LM_QP:\n"
"s_mov_b32 s56,0x3E0293EE\n"
"s_mov_b32 s57,0x3E0293EE\n"
"ds_read_b64_tr_b16 a[208:209],v204\n"
"ds_read_b64_tr_b16 a[210:211],v204 offset:4096\n"
"ds_read_b64_tr_b16 a[212:213],v204 offset:8192\n"
"ds_read_b64_tr_b16 a[214:215],v204 offset:12288\n"
"ds_read_b64_tr_b16 a[216:217],v204 offset:8\n"
"ds_read_b64_tr_b16 a[218:219],v204 offset:4104\n"
"ds_read_b64_tr_b16 a[220:221],v204 offset:8200\n"
"ds_read_b64_tr_b16 a[222:223],v204 offset:12296\n"
"ds_read_b64_tr_b16 a[224:225],v204 offset:256\n"
"ds_read_b64_tr_b16 a[226:227],v204 offset:4352\n"
"ds_read_b64_tr_b16 a[228:229],v204 offset:8448\n"
"ds_read_b64_tr_b16 a[230:231],v204 offset:12544\n"
"s_waitcnt vmcnt(8)\n"
"s_waitcnt lgkmcnt(8)\n"
"v_mfma_f32_16x16x32_bf16 v[0:3],a[208:211],a[0:3],0\n"
"ds_read_b64_tr_b16 a[232:233],v204 offset:264\n"
"v_mfma_f32_16x16x32_bf16 v[0:3],a[212:215],a[16:19],v[0:3]\n"
"v_mfma_f32_16x16x32_bf16 v[4:7],a[208:211],a[4:7],0\n"
"s_add_u32 s40,s53,16384\n"
"ds_read_b64_tr_b16 a[234:235],v204 offset:4360\n"
"v_mfma_f32_16x16x32_bf16 v[4:7],a[212:215],a[20:23],v[4:7]\n"
"buffer_load_dwordx4 a[64:67],v209,s[64:67],s40 offen sc1\n"
"v_mfma_f32_16x16x32_bf16 v[8:11],a[208:211],a[8:11],0\n"
"ds_read_b64_tr_b16 a[236:237],v204 offset:8456\n"
"v_mfma_f32_16x16x32_bf16 v[8:11],a[212:215],a[24:27],v[8:11]\n"
"v_mfma_f32_16x16x32_bf16 v[12:15],a[208:211],a[12:15],0\n"
"ds_read_b64_tr_b16 a[238:239],v204 offset:12552\n"
"v_mfma_f32_16x16x32_bf16 v[12:15],a[212:215],a[28:31],v[12:15]\n"
"buffer_load_dwordx4 a[68:71],v209,s[64:67],s40 offen offset:1024 sc1\n"
"s_waitcnt lgkmcnt(8)\n"
"v_mfma_f32_16x16x32_bf16 v[16:19],a[216:219],a[0:3],0\n"
"ds_read_b64_tr_b16 a[240:241],v204 offset:512\n"
"v_mfma_f32_16x16x32_bf16 v[16:19],a[220:223],a[16:19],v[16:19]\n"
"v_mfma_f32_16x16x32_bf16 v[20:23],a[216:219],a[4:7],0\n"
"ds_read_b64_tr_b16 a[242:243],v204 offset:4608\n"
"v_mfma_f32_16x16x32_bf16 v[20:23],a[220:223],a[20:23],v[20:23]\n"
"buffer_load_dwordx4 a[72:75],v209,s[64:67],s40 offen offset:2048 sc1\n"
"v_mfma_f32_16x16x32_bf16 v[24:27],a[216:219],a[8:11],0\n"
"ds_read_b64_tr_b16 a[244:245],v204 offset:8704\n"
"v_mfma_f32_16x16x32_bf16 v[24:27],a[220:223],a[24:27],v[24:27]\n"
"v_mfma_f32_16x16x32_bf16 v[28:31],a[216:219],a[12:15],0\n"
"ds_read_b64_tr_b16 a[246:247],v204 offset:12800\n"
"v_mfma_f32_16x16x32_bf16 v[28:31],a[220:223],a[28:31],v[28:31]\n"
"buffer_load_dwordx4 a[76:79],v209,s[64:67],s40 offen offset:3072 sc1\n"
"s_waitcnt lgkmcnt(8)\n"
"v_mfma_f32_16x16x32_bf16 v[32:35],a[224:227],a[0:3],0\n"
"ds_read_b64_tr_b16 a[248:249],v204 offset:520\n"
"v_mfma_f32_16x16x32_bf16 v[32:35],a[228:231],a[16:19],v[32:35]\n"
"v_mfma_f32_16x16x32_bf16 v[36:39],a[224:227],a[4:7],0\n"
"s_add_u32 s40,s53,20480\n"
"ds_read_b64_tr_b16 a[250:251],v204 offset:4616\n"
"v_mfma_f32_16x16x32_bf16 v[36:39],a[228:231],a[20:23],v[36:39]\n"
"buffer_load_dwordx4 a[80:83],v209,s[64:67],s40 offen sc1\n"
"v_mfma_f32_16x16x32_bf16 v[40:43],a[224:227],a[8:11],0\n"
"ds_read_b64_tr_b16 a[252:253],v204 offset:8712\n"
"v_mfma_f32_16x16x32_bf16 v[40:43],a[228:231],a[24:27],v[40:43]\n"
"v_mfma_f32_16x16x32_bf16 v[44:47],a[224:227],a[12:15],0\n"
"ds_read_b64_tr_b16 a[254:255],v204 offset:12808\n"
"v_mfma_f32_16x16x32_bf16 v[44:47],a[228:231],a[28:31],v[44:47]\n"
"buffer_load_dwordx4 a[84:87],v209,s[64:67],s40 offen offset:1024 sc1\n"
"s_waitcnt lgkmcnt(8)\n"
"v_mfma_f32_16x16x32_bf16 v[48:51],a[232:235],a[0:3],0\n"
"ds_read_b64_tr_b16 a[208:209],v204 offset:768\n"
"v_mfma_f32_16x16x32_bf16 v[48:51],a[236:239],a[16:19],v[48:51]\n"
"v_mfma_f32_16x16x32_bf16 v[52:55],a[232:235],a[4:7],0\n"
"ds_read_b64_tr_b16 a[210:211],v204 offset:4864\n"
"v_mfma_f32_16x16x32_bf16 v[52:55],a[236:239],a[20:23],v[52:55]\n"
"buffer_load_dwordx4 a[88:91],v209,s[64:67],s40 offen offset:2048 sc1\n"
"v_mfma_f32_16x16x32_bf16 v[56:59],a[232:235],a[8:11],0\n"
"ds_read_b64_tr_b16 a[212:213],v204 offset:8960\n"
"v_mfma_f32_16x16x32_bf16 v[56:59],a[236:239],a[24:27],v[56:59]\n"
"v_mfma_f32_16x16x32_bf16 v[60:63],a[232:235],a[12:15],0\n"
"ds_read_b64_tr_b16 a[214:215],v204 offset:13056\n"
"v_mfma_f32_16x16x32_bf16 v[60:63],a[236:239],a[28:31],v[60:63]\n"
"buffer_load_dwordx4 a[92:95],v209,s[64:67],s40 offen offset:3072 sc1\n"
"s_waitcnt lgkmcnt(8)\n"
"v_mfma_f32_16x16x32_bf16 v[64:67],a[240:243],a[0:3],0\n"
"ds_read_b64_tr_b16 a[216:217],v204 offset:776\n"
"v_mfma_f32_16x16x32_bf16 v[64:67],a[244:247],a[16:19],v[64:67]\n"
"ds_read_b64_tr_b16 a[218:219],v204 offset:4872\n"
"v_mfma_f32_16x16x32_bf16 v[68:71],a[240:243],a[4:7],0\n"
"s_add_u32 s40,s53,24576\n"
"ds_read_b64_tr_b16 a[220:221],v204 offset:8968\n"
"v_mfma_f32_16x16x32_bf16 v[68:71],a[244:247],a[20:23],v[68:71]\n"
"ds_read_b64_tr_b16 a[222:223],v204 offset:13064\n"
"buffer_load_dwordx4 a[96:99],v209,s[64:67],s40 offen sc1\n"
"v_mfma_f32_16x16x32_bf16 v[72:75],a[240:243],a[8:11],0\n"
"v_mfma_f32_16x16x32_bf16 v[72:75],a[244:247],a[24:27],v[72:75]\n"
"v_mfma_f32_16x16x32_bf16 v[76:79],a[240:243],a[12:15],0\n"
"v_mfma_f32_16x16x32_bf16 v[76:79],a[244:247],a[28:31],v[76:79]\n"
"buffer_load_dwordx4 a[100:103],v209,s[64:67],s40 offen offset:1024 sc1\n"
"s_waitcnt vmcnt(10) lgkmcnt(0)\n"
"s_barrier\n"
"v_readlane_b32 s43,v210,46\n"
"s_nop 0\n"
"s_lshl_b32 s43,s43,6\n"
"v_lshl_add_u32 v223,v209,2,s43\n"
"s_cmp_lg_u32 s48,0\n"
"s_cbranch_scc1 LM_FS5\n"
"s_mov_b64 exec,1\n"
"buffer_store_dwordx2 v[224:225],v223,s[68:71],0 offen sc1\n"
"s_mov_b64 exec,-1\n"
"LM_FS5:\n"
"v_mfma_f32_16x16x32_bf16 v[80:83],a[248:251],a[0:3],0\n"
"ds_read_b64_tr_b16 a[224:225],v204 offset:16384\n"
"v_mfma_f32_16x16x32_bf16 v[80:83],a[252:255],a[16:19],v[80:83]\n"
"v_mfma_f32_16x16x32_bf16 v[84:87],a[248:251],a[4:7],0\n"
"ds_read_b64_tr_b16 a[226:227],v204 offset:20480\n"
"v_mfma_f32_16x16x32_bf16 v[84:87],a[252:255],a[20:23],v[84:87]\n"
"buffer_load_dwordx4 a[104:107],v209,s[64:67],s40 offen offset:2048 sc1\n"
"v_mfma_f32_16x16x32_bf16 v[88:91],a[248:251],a[8:11],0\n"
"ds_read_b64_tr_b16 a[228:229],v204 offset:24576\n"
"v_mfma_f32_16x16x32_bf16 v[88:91],a[252:255],a[24:27],v[88:91]\n"
"v_mfma_f32_16x16x32_bf16 v[92:95],a[248:251],a[12:15],0\n"
"ds_read_b64_tr_b16 a[230:231],v204 offset:28672\n"
"v_mfma_f32_16x16x32_bf16 v[92:95],a[252:255],a[28:31],v[92:95]\n"
"buffer_load_dwordx4 a[108:111],v209,s[64:67],s40 offen offset:3072 sc1\n"
"v_mfma_f32_16x16x32_bf16 v[96:99],a[208:211],a[0:3],0\n"
"ds_read_b64_tr_b16 a[232:233],v204 offset:16392\n"
"v_mfma_f32_16x16x32_bf16 v[96:99],a[212:215],a[16:19],v[96:99]\n"
"v_mfma_f32_16x16x32_bf16 v[100:103],a[208:211],a[4:7],0\n"
"s_add_u32 s40,s53,28672\n"
"ds_read_b64_tr_b16 a[234:235],v204 offset:20488\n"
"v_mfma_f32_16x16x32_bf16 v[100:103],a[212:215],a[20:23],v[100:103]\n"
"buffer_load_dwordx4 a[112:115],v209,s[64:67],s40 offen sc1\n"
"v_mfma_f32_16x16x32_bf16 v[104:107],a[208:211],a[8:11],0\n"
"ds_read_b64_tr_b16 a[236:237],v204 offset:24584\n"
"v_mfma_f32_16x16x32_bf16 v[104:107],a[212:215],a[24:27],v[104:107]\n"
"v_mfma_f32_16x16x32_bf16 v[108:111],a[208:211],a[12:15],0\n"
"ds_read_b64_tr_b16 a[238:239],v204 offset:28680\n"
"v_mfma_f32_16x16x32_bf16 v[108:111],a[212:215],a[28:31],v[108:111]\n"
"buffer_load_dwordx4 a[116:119],v209,s[64:67],s40 offen offset:1024 sc1\n"
"v_mfma_f32_16x16x32_bf16 v[112:115],a[216:219],a[0:3],0\n"
"ds_read_b64_tr_b16 a[240:241],v204 offset:16640\n"
"v_mfma_f32_16x16x32_bf16 v[112:115],a[220:223],a[16:19],v[112:115]\n"
"v_mfma_f32_16x16x32_bf16 v[116:119],a[216:219],a[4:7],0\n"
"ds_read_b64_tr_b16 a[242:243],v204 offset:20736\n"
"v_mfma_f32_16x16x32_bf16 v[116:119],a[220:223],a[20:23],v[116:119]\n"
"buffer_load_dwordx4 a[120:123],v209,s[64:67],s40 offen offset:2048 sc1\n"
"v_mfma_f32_16x16x32_bf16 v[120:123],a[216:219],a[8:11],0\n"
"ds_read_b64_tr_b16 a[244:245],v204 offset:24832\n"
"v_mfma_f32_16x16x32_bf16 v[120:123],a[220:223],a[24:27],v[120:123]\n"
"v_mfma_f32_16x16x32_bf16 v[124:127],a[216:219],a[12:15],0\n"
"ds_read_b64_tr_b16 a[246:247],v204 offset:28928\n"
"v_mfma_f32_16x16x32_bf16 v[124:127],a[220:223],a[28:31],v[124:127]\n"
"buffer_load_dwordx4 a[124:127],v209,s[64:67],s40 offen offset:3072 sc1\n"
"s_waitcnt lgkmcnt(8)\n"
"v_mfma_f32_16x16x32_bf16 v[0:3],a[224:227],a[32:35],v[0:3]\n"
"ds_read_b64_tr_b16 a[248:249],v204 offset:16648\n"
"v_mfma_f32_16x16x32_bf16 v[0:3],a[228:231],a[48:51],v[0:3]\n"
"v_mfma_f32_16x16x32_bf16 v[4:7],a[224:227],a[36:39],v[4:7]\n"
"s_add_u32 m0,s50,0\n"
"s_add_u32 s39,s52,2097152\n"
"ds_read_b64_tr_b16 a[250:251],v204 offset:20744\n"
"v_mfma_f32_16x16x32_bf16 v[4:7],a[228:231],a[52:55],v[4:7]\n"
"buffer_load_dwordx4 v205,s[72:75],s39 offen lds\n"
"v_mfma_f32_16x16x32_bf16 v[8:11],a[224:227],a[40:43],v[8:11]\n"
"ds_read_b64_tr_b16 a[252:253],v204 offset:24840\n"
"v_mfma_f32_16x16x32_bf16 v[8:11],a[228:231],a[56:59],v[8:11]\n"
"v_mfma_f32_16x16x32_bf16 v[12:15],a[224:227],a[44:47],v[12:15]\n"
"ds_read_b64_tr_b16 a[254:255],v204 offset:28936\n"
"v_mfma_f32_16x16x32_bf16 v[12:15],a[228:231],a[60:63],v[12:15]\n"
"s_waitcnt lgkmcnt(8)\n"
"v_mfma_f32_16x16x32_bf16 v[16:19],a[232:235],a[32:35],v[16:19]\n"
"ds_read_b64_tr_b16 a[208:209],v204 offset:16896\n"
"v_mfma_f32_16x16x32_bf16 v[16:19],a[236:239],a[48:51],v[16:19]\n"
"buffer_load_dwordx4 v206,s[72:75],s39 offen offset:1024 lds\n"
"v_mfma_f32_16x16x32_bf16 v[20:23],a[232:235],a[36:39],v[20:23]\n"
"ds_read_b64_tr_b16 a[210:211],v204 offset:20992\n"
"v_mfma_f32_16x16x32_bf16 v[20:23],a[236:239],a[52:55],v[20:23]\n"
"v_mfma_f32_16x16x32_bf16 v[24:27],a[232:235],a[40:43],v[24:27]\n"
"ds_read_b64_tr_b16 a[212:213],v204 offset:25088\n"
"v_mfma_f32_16x16x32_bf16 v[24:27],a[236:239],a[56:59],v[24:27]\n"
"buffer_load_dwordx4 v207,s[72:75],s39 offen offset:2048 lds\n"
"v_mfma_f32_16x16x32_bf16 v[28:31],a[232:235],a[44:47],v[28:31]\n"
"ds_read_b64_tr_b16 a[214:215],v204 offset:29184\n"
"v_mfma_f32_16x16x32_bf16 v[28:31],a[236:239],a[60:63],v[28:31]\n"
"s_waitcnt lgkmcnt(8)\n"
"v_mfma_f32_16x16x32_bf16 v[32:35],a[240:243],a[32:35],v[32:35]\n"
"ds_read_b64_tr_b16 a[216:217],v204 offset:16904\n"
"v_mfma_f32_16x16x32_bf16 v[32:35],a[244:247],a[48:51],v[32:35]\n"
"v_mfma_f32_16x16x32_bf16 v[36:39],a[240:243],a[36:39],v[36:39]\n"
"ds_read_b64_tr_b16 a[218:219],v204 offset:21000\n"
"v_mfma_f32_16x16x32_bf16 v[36:39],a[244:247],a[52:55],v[36:39]\n"
"buffer_load_dwordx4 v208,s[72:75],s39 offen offset:3072 lds\n"
"v_mfma_f32_16x16x32_bf16 v[40:43],a[240:243],a[40:43],v[40:43]\n"
"ds_read_b64_tr_b16 a[220:221],v204 offset:25096\n"
"v_mfma_f32_16x16x32_bf16 v[40:43],a[244:247],a[56:59],v[40:43]\n"
"v_mfma_f32_16x16x32_bf16 v[44:47],a[240:243],a[44:47],v[44:47]\n"
"ds_read_b64_tr_b16 a[222:223],v204 offset:29192\n"
"v_mfma_f32_16x16x32_bf16 v[44:47],a[244:247],a[60:63],v[44:47]\n"
"s_waitcnt lgkmcnt(8)\n"
"v_mfma_f32_16x16x32_bf16 v[48:51],a[248:251],a[32:35],v[48:51]\n"
"s_add_u32 s40,s53,32768\n"
"ds_read_b64_tr_b16 a[224:225],v204 offset:17152\n"
"v_mfma_f32_16x16x32_bf16 v[48:51],a[252:255],a[48:51],v[48:51]\n"
"buffer_load_dwordx4 a[0:3],v209,s[64:67],s40 offen sc1\n"
"v_mfma_f32_16x16x32_bf16 v[52:55],a[248:251],a[36:39],v[52:55]\n"
"ds_read_b64_tr_b16 a[226:227],v204 offset:21248\n"
"v_mfma_f32_16x16x32_bf16 v[52:55],a[252:255],a[52:55],v[52:55]\n"
"v_mfma_f32_16x16x32_bf16 v[56:59],a[248:251],a[40:43],v[56:59]\n"
"ds_read_b64_tr_b16 a[228:229],v204 offset:25344\n"
"v_mfma_f32_16x16x32_bf16 v[56:59],a[252:255],a[56:59],v[56:59]\n"
"buffer_load_dwordx4 a[4:7],v209,s[64:67],s40 offen offset:1024 sc1\n"
"v_mfma_f32_16x16x32_bf16 v[60:63],a[248:251],a[44:47],v[60:63]\n"
"ds_read_b64_tr_b16 a[230:231],v204 offset:29440\n"
"v_mfma_f32_16x16x32_bf16 v[60:63],a[252:255],a[60:63],v[60:63]\n"
"s_waitcnt lgkmcnt(8)\n"
"v_mfma_f32_16x16x32_bf16 v[64:67],a[208:211],a[32:35],v[64:67]\n"
"ds_read_b64_tr_b16 a[232:233],v204 offset:17160\n"
"v_mfma_f32_16x16x32_bf16 v[64:67],a[212:215],a[48:51],v[64:67]\n"
"ds_read_b64_tr_b16 a[234:235],v204 offset:21256\n"
"v_mfma_f32_16x16x32_bf16 v[68:71],a[208:211],a[36:39],v[68:71]\n"
"ds_read_b64_tr_b16 a[236:237],v204 offset:25352\n"
"v_mfma_f32_16x16x32_bf16 v[68:71],a[212:215],a[52:55],v[68:71]\n"
"ds_read_b64_tr_b16 a[238:239],v204 offset:29448\n"
"buffer_load_dwordx4 a[8:11],v209,s[64:67],s40 offen offset:2048 sc1\n"
"v_mfma_f32_16x16x32_bf16 v[72:75],a[208:211],a[40:43],v[72:75]\n"
"v_mfma_f32_16x16x32_bf16 v[72:75],a[212:215],a[56:59],v[72:75]\n"
"v_mfma_f32_16x16x32_bf16 v[76:79],a[208:211],a[44:47],v[76:79]\n"
"v_mfma_f32_16x16x32_bf16 v[76:79],a[212:215],a[60:63],v[76:79]\n"
"s_waitcnt vmcnt(15) lgkmcnt(0)\n"
"s_barrier\n"
"v_mfma_f32_16x16x32_bf16 v[80:83],a[216:219],a[32:35],v[80:83]\n"
"ds_read_b64_tr_b16 a[240:241],v204 offset:32768\n"
"v_mfma_f32_16x16x32_bf16 v[80:83],a[220:223],a[48:51],v[80:83]\n"
"buffer_load_dwordx4 a[12:15],v209,s[64:67],s40 offen offset:3072 sc1\n"
"v_mfma_f32_16x16x32_bf16 v[84:87],a[216:219],a[36:39],v[84:87]\n"
"ds_read_b64_tr_b16 a[242:243],v204 offset:36864\n"
"v_mfma_f32_16x16x32_bf16 v[84:87],a[220:223],a[52:55],v[84:87]\n"
"v_mfma_f32_16x16x32_bf16 v[88:91],a[216:219],a[40:43],v[88:91]\n"
"s_add_u32 s40,s53,36864\n"
"ds_read_b64_tr_b16 a[244:245],v204 offset:40960\n"
"v_mfma_f32_16x16x32_bf16 v[88:91],a[220:223],a[56:59],v[88:91]\n"
"buffer_load_dwordx4 a[16:19],v209,s[64:67],s40 offen sc1\n"
"v_mfma_f32_16x16x32_bf16 v[92:95],a[216:219],a[44:47],v[92:95]\n"
"ds_read_b64_tr_b16 a[246:247],v204 offset:45056\n"
"v_mfma_f32_16x16x32_bf16 v[92:95],a[220:223],a[60:63],v[92:95]\n"
"v_mfma_f32_16x16x32_bf16 v[96:99],a[224:227],a[32:35],v[96:99]\n"
"ds_read_b64_tr_b16 a[248:249],v204 offset:32776\n"
"v_mfma_f32_16x16x32_bf16 v[96:99],a[228:231],a[48:51],v[96:99]\n"
"v_mfma_f32_16x16x32_bf16 v[100:103],a[224:227],a[36:39],v[100:103]\n"
"ds_read_b64_tr_b16 a[250:251],v204 offset:36872\n"
"v_mfma_f32_16x16x32_bf16 v[100:103],a[228:231],a[52:55],v[100:103]\n"
"buffer_load_dwordx4 a[20:23],v209,s[64:67],s40 offen offset:1024 sc1\n"
"v_mfma_f32_16x16x32_bf16 v[104:107],a[224:227],a[40:43],v[104:107]\n"
"ds_read_b64_tr_b16 a[252:253],v204 offset:40968\n"
"v_mfma_f32_16x16x32_bf16 v[104:107],a[228:231],a[56:59],v[104:107]\n"
"v_mfma_f32_16x16x32_bf16 v[108:111],a[224:227],a[44:47],v[108:111]\n"
"ds_read_b64_tr_b16 a[254:255],v204 offset:45064\n"
"v_mfma_f32_16x16x32_bf16 v[108:111],a[228:231],a[60:63],v[108:111]\n"
"v_mfma_f32_16x16x32_bf16 v[112:115],a[232:235],a[32:35],v[112:115]\n"
"ds_read_b64_tr_b16 a[208:209],v204 offset:33024\n"
"v_mfma_f32_16x16x32_bf16 v[112:115],a[236:239],a[48:51],v[112:115]\n"
"buffer_load_dwordx4 a[24:27],v209,s[64:67],s40 offen offset:2048 sc1\n"
"v_mfma_f32_16x16x32_bf16 v[116:119],a[232:235],a[36:39],v[116:119]\n"
"ds_read_b64_tr_b16 a[210:211],v204 offset:37120\n"
"v_mfma_f32_16x16x32_bf16 v[116:119],a[236:239],a[52:55],v[116:119]\n"
"v_mfma_f32_16x16x32_bf16 v[120:123],a[232:235],a[40:43],v[120:123]\n"
"ds_read_b64_tr_b16 a[212:213],v204 offset:41216\n"
"v_mfma_f32_16x16x32_bf16 v[120:123],a[236:239],a[56:59],v[120:123]\n"
"buffer_load_dwordx4 a[28:31],v209,s[64:67],s40 offen offset:3072 sc1\n"
"v_mfma_f32_16x16x32_bf16 v[124:127],a[232:235],a[44:47],v[124:127]\n"
"ds_read_b64_tr_b16 a[214:215],v204 offset:45312\n"
"v_mfma_f32_16x16x32_bf16 v[124:127],a[236:239],a[60:63],v[124:127]\n"
"s_waitcnt lgkmcnt(8)\n"
"v_mfma_f32_16x16x32_bf16 v[0:3],a[240:243],a[64:67],v[0:3]\n"
"ds_read_b64_tr_b16 a[216:217],v204 offset:33032\n"
"v_mfma_f32_16x16x32_bf16 v[0:3],a[244:247],a[80:83],v[0:3]\n"
"v_mfma_f32_16x16x32_bf16 v[4:7],a[240:243],a[68:71],v[4:7]\n"
"s_add_u32 m0,s50,16384\n"
"s_add_u32 s39,s52,2621440\n"
"ds_read_b64_tr_b16 a[218:219],v204 offset:37128\n"
"v_mfma_f32_16x16x32_bf16 v[4:7],a[244:247],a[84:87],v[4:7]\n"
"buffer_load_dwordx4 v205,s[72:75],s39 offen lds\n"
"v_mfma_f32_16x16x32_bf16 v[8:11],a[240:243],a[72:75],v[8:11]\n"
"ds_read_b64_tr_b16 a[220:221],v204 offset:41224\n"
"v_mfma_f32_16x16x32_bf16 v[8:11],a[244:247],a[88:91],v[8:11]\n"
"v_mfma_f32_16x16x32_bf16 v[12:15],a[240:243],a[76:79],v[12:15]\n"
"ds_read_b64_tr_b16 a[222:223],v204 offset:45320\n"
"v_mfma_f32_16x16x32_bf16 v[12:15],a[244:247],a[92:95],v[12:15]\n"
"s_waitcnt lgkmcnt(8)\n"
"v_mfma_f32_16x16x32_bf16 v[16:19],a[248:251],a[64:67],v[16:19]\n"
"ds_read_b64_tr_b16 a[224:225],v204 offset:33280\n"
"v_mfma_f32_16x16x32_bf16 v[16:19],a[252:255],a[80:83],v[16:19]\n"
"buffer_load_dwordx4 v206,s[72:75],s39 offen offset:1024 lds\n"
"v_mfma_f32_16x16x32_bf16 v[20:23],a[248:251],a[68:71],v[20:23]\n"
"ds_read_b64_tr_b16 a[226:227],v204 offset:37376\n"
"v_mfma_f32_16x16x32_bf16 v[20:23],a[252:255],a[84:87],v[20:23]\n"
"v_mfma_f32_16x16x32_bf16 v[24:27],a[248:251],a[72:75],v[24:27]\n"
"ds_read_b64_tr_b16 a[228:229],v204 offset:41472\n"
"v_mfma_f32_16x16x32_bf16 v[24:27],a[252:255],a[88:91],v[24:27]\n"
"buffer_load_dwordx4 v207,s[72:75],s39 offen offset:2048 lds\n"
"v_mfma_f32_16x16x32_bf16 v[28:31],a[248:251],a[76:79],v[28:31]\n"
"ds_read_b64_tr_b16 a[230:231],v204 offset:45568\n"
"v_mfma_f32_16x16x32_bf16 v[28:31],a[252:255],a[92:95],v[28:31]\n"
"s_waitcnt lgkmcnt(8)\n"
"v_mfma_f32_16x16x32_bf16 v[32:35],a[208:211],a[64:67],v[32:35]\n"
"ds_read_b64_tr_b16 a[232:233],v204 offset:33288\n"
"v_mfma_f32_16x16x32_bf16 v[32:35],a[212:215],a[80:83],v[32:35]\n"
"v_mfma_f32_16x16x32_bf16 v[36:39],a[208:211],a[68:71],v[36:39]\n"
"ds_read_b64_tr_b16 a[234:235],v204 offset:37384\n"
"v_mfma_f32_16x16x32_bf16 v[36:39],a[212:215],a[84:87],v[36:39]\n"
"buffer_load_dwordx4 v208,s[72:75],s39 offen offset:3072 lds\n"
"v_mfma_f32_16x16x32_bf16 v[40:43],a[208:211],a[72:75],v[40:43]\n"
"ds_read_b64_tr_b16 a[236:237],v204 offset:41480\n"
"v_mfma_f32_16x16x32_bf16 v[40:43],a[212:215],a[88:91],v[40:43]\n"
"v_mfma_f32_16x16x32_bf16 v[44:47],a[208:211],a[76:79],v[44:47]\n"
"ds_read_b64_tr_b16 a[238:239],v204 offset:45576\n"
"v_mfma_f32_16x16x32_bf16 v[44:47],a[212:215],a[92:95],v[44:47]\n"
"s_waitcnt lgkmcnt(8)\n"
"v_mfma_f32_16x16x32_bf16 v[48:51],a[216:219],a[64:67],v[48:51]\n"
"s_add_u32 s40,s53,40960\n"
"ds_read_b64_tr_b16 a[240:241],v204 offset:33536\n"
"v_mfma_f32_16x16x32_bf16 v[48:51],a[220:223],a[80:83],v[48:51]\n"
"buffer_load_dwordx4 a[32:35],v209,s[64:67],s40 offen sc1\n"
"v_mfma_f32_16x16x32_bf16 v[52:55],a[216:219],a[68:71],v[52:55]\n"
"ds_read_b64_tr_b16 a[242:243],v204 offset:37632\n"
"v_mfma_f32_16x16x32_bf16 v[52:55],a[220:223],a[84:87],v[52:55]\n"
"v_mfma_f32_16x16x32_bf16 v[56:59],a[216:219],a[72:75],v[56:59]\n"
"ds_read_b64_tr_b16 a[244:245],v204 offset:41728\n"
"v_mfma_f32_16x16x32_bf16 v[56:59],a[220:223],a[88:91],v[56:59]\n"
"buffer_load_dwordx4 a[36:39],v209,s[64:67],s40 offen offset:1024 sc1\n"
"v_mfma_f32_16x16x32_bf16 v[60:63],a[216:219],a[76:79],v[60:63]\n"
"ds_read_b64_tr_b16 a[246:247],v204 offset:45824\n"
"v_mfma_f32_16x16x32_bf16 v[60:63],a[220:223],a[92:95],v[60:63]\n"
"s_waitcnt lgkmcnt(8)\n"
"v_mfma_f32_16x16x32_bf16 v[64:67],a[224:227],a[64:67],v[64:67]\n"
"ds_read_b64_tr_b16 a[248:249],v204 offset:33544\n"
"v_mfma_f32_16x16x32_bf16 v[64:67],a[228:231],a[80:83],v[64:67]\n"
"ds_read_b64_tr_b16 a[250:251],v204 offset:37640\n"
"v_mfma_f32_16x16x32_bf16 v[68:71],a[224:227],a[68:71],v[68:71]\n"
"ds_read_b64_tr_b16 a[252:253],v204 offset:41736\n"
"v_mfma_f32_16x16x32_bf16 v[68:71],a[228:231],a[84:87],v[68:71]\n"
"ds_read_b64_tr_b16 a[254:255],v204 offset:45832\n"
"buffer_load_dwordx4 a[40:43],v209,s[64:67],s40 offen offset:2048 sc1\n"
"v_mfma_f32_16x16x32_bf16 v[72:75],a[224:227],a[72:75],v[72:75]\n"
"v_mfma_f32_16x16x32_bf16 v[72:75],a[228:231],a[88:91],v[72:75]\n"
"v_mfma_f32_16x16x32_bf16 v[76:79],a[224:227],a[76:79],v[76:79]\n"
"v_mfma_f32_16x16x32_bf16 v[76:79],a[228:231],a[92:95],v[76:79]\n"
"s_waitcnt vmcnt(19) lgkmcnt(0)\n"
"s_barrier\n"
"v_mfma_f32_16x16x32_bf16 v[80:83],a[232:235],a[64:67],v[80:83]\n"
"ds_read_b64_tr_b16 a[208:209],v204 offset:49152\n"
"v_mfma_f32_16x16x32_bf16 v[80:83],a[236:239],a[80:83],v[80:83]\n"
"buffer_load_dwordx4 a[44:47],v209,s[64:67],s40 offen offset:3072 sc1\n"
"v_mfma_f32_16x16x32_bf16 v[84:87],a[232:235],a[68:71],v[84:87]\n"
"ds_read_b64_tr_b16 a[210:211],v204 offset:53248\n"
"v_mfma_f32_16x16x32_bf16 v[84:87],a[236:239],a[84:87],v[84:87]\n"
"v_mfma_f32_16x16x32_bf16 v[88:91],a[232:235],a[72:75],v[88:91]\n"
"s_add_u32 s40,s53,45056\n"
"ds_read_b64_tr_b16 a[212:213],v204 offset:57344\n"
"v_mfma_f32_16x16x32_bf16 v[88:91],a[236:239],a[88:91],v[88:91]\n"
"buffer_load_dwordx4 a[48:51],v209,s[64:67],s40 offen sc1\n"
"v_mfma_f32_16x16x32_bf16 v[92:95],a[232:235],a[76:79],v[92:95]\n"
"ds_read_b64_tr_b16 a[214:215],v204 offset:61440\n"
"v_mfma_f32_16x16x32_bf16 v[92:95],a[236:239],a[92:95],v[92:95]\n"
"v_mfma_f32_16x16x32_bf16 v[96:99],a[240:243],a[64:67],v[96:99]\n"
"ds_read_b64_tr_b16 a[216:217],v204 offset:49160\n"
"v_mfma_f32_16x16x32_bf16 v[96:99],a[244:247],a[80:83],v[96:99]\n"
"v_mfma_f32_16x16x32_bf16 v[100:103],a[240:243],a[68:71],v[100:103]\n"
"ds_read_b64_tr_b16 a[218:219],v204 offset:53256\n"
"v_mfma_f32_16x16x32_bf16 v[100:103],a[244:247],a[84:87],v[100:103]\n"
"buffer_load_dwordx4 a[52:55],v209,s[64:67],s40 offen offset:1024 sc1\n"
"v_mfma_f32_16x16x32_bf16 v[104:107],a[240:243],a[72:75],v[104:107]\n"
"ds_read_b64_tr_b16 a[220:221],v204 offset:57352\n"
"v_mfma_f32_16x16x32_bf16 v[104:107],a[244:247],a[88:91],v[104:107]\n"
"v_mfma_f32_16x16x32_bf16 v[108:111],a[240:243],a[76:79],v[108:111]\n"
"ds_read_b64_tr_b16 a[222:223],v204 offset:61448\n"
"v_mfma_f32_16x16x32_bf16 v[108:111],a[244:247],a[92:95],v[108:111]\n"
"v_mfma_f32_16x16x32_bf16 v[112:115],a[248:251],a[64:67],v[112:115]\n"
"ds_read_b64_tr_b16 a[224:225],v204 offset:49408\n"
"v_mfma_f32_16x16x32_bf16 v[112:115],a[252:255],a[80:83],v[112:115]\n"
"buffer_load_dwordx4 a[56:59],v209,s[64:67],s40 offen offset:2048 sc1\n"
"v_mfma_f32_16x16x32_bf16 v[116:119],a[248:251],a[68:71],v[116:119]\n"
"ds_read_b64_tr_b16 a[226:227],v204 offset:53504\n"
"v_mfma_f32_16x16x32_bf16 v[116:119],a[252:255],a[84:87],v[116:119]\n"
"v_mfma_f32_16x16x32_bf16 v[120:123],a[248:251],a[72:75],v[120:123]\n"
"ds_read_b64_tr_b16 a[228:229],v204 offset:57600\n"
"v_mfma_f32_16x16x32_bf16 v[120:123],a[252:255],a[88:91],v[120:123]\n"
"buffer_load_dwordx4 a[60:63],v209,s[64:67],s40 offen offset:3072 sc1\n"
"v_mfma_f32_16x16x32_bf16 v[124:127],a[248:251],a[76:79],v[124:127]\n"
"ds_read_b64_tr_b16 a[230:231],v204 offset:61696\n"
"v_mfma_f32_16x16x32_bf16 v[124:127],a[252:255],a[92:95],v[124:127]\n"
"s_waitcnt lgkmcnt(8)\n"
"v_mfma_f32_16x16x32_bf16 v[0:3],a[208:211],a[96:99],v[0:3]\n"
"ds_read_b64_tr_b16 a[232:233],v204 offset:49416\n"
"v_mfma_f32_16x16x32_bf16 v[0:3],a[212:215],a[112:115],v[0:3]\n"
"v_mfma_f32_16x16x32_bf16 v[4:7],a[208:211],a[100:103],v[4:7]\n"
"s_add_u32 m0,s50,32768\n"
"s_add_u32 s39,s52,3145728\n"
"ds_read_b64_tr_b16 a[234:235],v204 offset:53512\n"
"v_mfma_f32_16x16x32_bf16 v[4:7],a[212:215],a[116:119],v[4:7]\n"
"buffer_load_dwordx4 v205,s[72:75],s39 offen lds\n"
"v_mfma_f32_16x16x32_bf16 v[8:11],a[208:211],a[104:107],v[8:11]\n"
"ds_read_b64_tr_b16 a[236:237],v204 offset:57608\n"
"v_mfma_f32_16x16x32_bf16 v[8:11],a[212:215],a[120:123],v[8:11]\n"
"v_mfma_f32_16x16x32_bf16 v[12:15],a[208:211],a[108:111],v[12:15]\n"
"ds_read_b64_tr_b16 a[238:239],v204 offset:61704\n"
"v_mfma_f32_16x16x32_bf16 v[12:15],a[212:215],a[124:127],v[12:15]\n"
"s_waitcnt lgkmcnt(8)\n"
"v_mfma_f32_16x16x32_bf16 v[16:19],a[216:219],a[96:99],v[16:19]\n"
"ds_read_b64_tr_b16 a[240:241],v204 offset:49664\n"
"v_mfma_f32_16x16x32_bf16 v[16:19],a[220:223],a[112:115],v[16:19]\n"
"buffer_load_dwordx4 v206,s[72:75],s39 offen offset:1024 lds\n"
"v_mfma_f32_16x16x32_bf16 v[20:23],a[216:219],a[100:103],v[20:23]\n"
"ds_read_b64_tr_b16 a[242:243],v204 offset:53760\n"
"v_mfma_f32_16x16x32_bf16 v[20:23],a[220:223],a[116:119],v[20:23]\n"
"v_mfma_f32_16x16x32_bf16 v[24:27],a[216:219],a[104:107],v[24:27]\n"
"ds_read_b64_tr_b16 a[244:245],v204 offset:57856\n"
"v_mfma_f32_16x16x32_bf16 v[24:27],a[220:223],a[120:123],v[24:27]\n"
"buffer_load_dwordx4 v207,s[72:75],s39 offen offset:2048 lds\n"
"v_mfma_f32_16x16x32_bf16 v[28:31],a[216:219],a[108:111],v[28:31]\n"
"ds_read_b64_tr_b16 a[246:247],v204 offset:61952\n"
"v_mfma_f32_16x16x32_bf16 v[28:31],a[220:223],a[124:127],v[28:31]\n"
"s_waitcnt lgkmcnt(8)\n"
"v_mfma_f32_16x16x32_bf16 v[32:35],a[224:227],a[96:99],v[32:35]\n"
"ds_read_b64_tr_b16 a[248:249],v204 offset:49672\n"
"v_mfma_f32_16x16x32_bf16 v[32:35],a[228:231],a[112:115],v[32:35]\n"
"v_mfma_f32_16x16x32_bf16 v[36:39],a[224:227],a[100:103],v[36:39]\n"
"ds_read_b64_tr_b16 a[250:251],v204 offset:53768\n"
"v_mfma_f32_16x16x32_bf16 v[36:39],a[228:231],a[116:119],v[36:39]\n"
"buffer_load_dwordx4 v208,s[72:75],s39 offen offset:3072 lds\n"
"v_mfma_f32_16x16x32_bf16 v[40:43],a[224:227],a[104:107],v[40:43]\n"
"ds_read_b64_tr_b16 a[252:253],v204 offset:57864\n"
"v_mfma_f32_16x16x32_bf16 v[40:43],a[228:231],a[120:123],v[40:43]\n"
"v_mfma_f32_16x16x32_bf16 v[44:47],a[224:227],a[108:111],v[44:47]\n"
"ds_read_b64_tr_b16 a[254:255],v204 offset:61960\n"
"v_mfma_f32_16x16x32_bf16 v[44:47],a[228:231],a[124:127],v[44:47]\n"
"s_waitcnt lgkmcnt(8)\n"
"v_mfma_f32_16x16x32_bf16 v[48:51],a[232:235],a[96:99],v[48:51]\n"
"s_add_u32 s40,s53,49152\n"
"ds_read_b64_tr_b16 a[208:209],v204 offset:49920\n"
"v_mfma_f32_16x16x32_bf16 v[48:51],a[236:239],a[112:115],v[48:51]\n"
"buffer_load_dwordx4 a[64:67],v209,s[64:67],s40 offen sc1\n"
"v_mfma_f32_16x16x32_bf16 v[52:55],a[232:235],a[100:103],v[52:55]\n"
"ds_read_b64_tr_b16 a[210:211],v204 offset:54016\n"
"v_mfma_f32_16x16x32_bf16 v[52:55],a[236:239],a[116:119],v[52:55]\n"
"v_mfma_f32_16x16x32_bf16 v[56:59],a[232:235],a[104:107],v[56:59]\n"
"ds_read_b64_tr_b16 a[212:213],v204 offset:58112\n"
"v_mfma_f32_16x16x32_bf16 v[56:59],a[236:239],a[120:123],v[56:59]\n"
"buffer_load_dwordx4 a[68:71],v209,s[64:67],s40 offen offset:1024 sc1\n"
"v_mfma_f32_16x16x32_bf16 v[60:63],a[232:235],a[108:111],v[60:63]\n"
"ds_read_b64_tr_b16 a[214:215],v204 offset:62208\n"
"v_mfma_f32_16x16x32_bf16 v[60:63],a[236:239],a[124:127],v[60:63]\n"
"s_waitcnt lgkmcnt(8)\n"
"v_mfma_f32_16x16x32_bf16 v[64:67],a[240:243],a[96:99],v[64:67]\n"
"ds_read_b64_tr_b16 a[216:217],v204 offset:49928\n"
"v_mfma_f32_16x16x32_bf16 v[64:67],a[244:247],a[112:115],v[64:67]\n"
"ds_read_b64_tr_b16 a[218:219],v204 offset:54024\n"
"v_mfma_f32_16x16x32_bf16 v[68:71],a[240:243],a[100:103],v[68:71]\n"
"ds_read_b64_tr_b16 a[220:221],v204 offset:58120\n"
"v_mfma_f32_16x16x32_bf16 v[68:71],a[244:247],a[116:119],v[68:71]\n"
"ds_read_b64_tr_b16 a[222:223],v204 offset:62216\n"
"buffer_load_dwordx4 a[72:75],v209,s[64:67],s40 offen offset:2048 sc1\n"
"v_mfma_f32_16x16x32_bf16 v[72:75],a[240:243],a[104:107],v[72:75]\n"
"v_mfma_f32_16x16x32_bf16 v[72:75],a[244:247],a[120:123],v[72:75]\n"
"v_mfma_f32_16x16x32_bf16 v[76:79],a[240:243],a[108:111],v[76:79]\n"
"v_mfma_f32_16x16x32_bf16 v[76:79],a[244:247],a[124:127],v[76:79]\n"
"s_waitcnt vmcnt(19) lgkmcnt(0)\n"
"s_barrier\n"
"v_mfma_f32_16x16x32_bf16 v[80:83],a[248:251],a[96:99],v[80:83]\n"
"ds_read_b64_tr_b16 a[224:225],v204\n"
"v_mfma_f32_16x16x32_bf16 v[80:83],a[252:255],a[112:115],v[80:83]\n"
"buffer_load_dwordx4 a[76:79],v209,s[64:67],s40 offen offset:3072 sc1\n"
"v_mfma_f32_16x16x32_bf16 v[84:87],a[248:251],a[100:103],v[84:87]\n"
"ds_read_b64_tr_b16 a[226:227],v204 offset:4096\n"
"v_mfma_f32_16x16x32_bf16 v[84:87],a[252:255],a[116:119],v[84:87]\n"
"v_mfma_f32_16x16x32_bf16 v[88:91],a[248:251],a[104:107],v[88:91]\n"
"s_add_u32 s40,s53,53248\n"
"ds_read_b64_tr_b16 a[228:229],v204 offset:8192\n"
"v_mfma_f32_16x16x32_bf16 v[88:91],a[252:255],a[120:123],v[88:91]\n"
"buffer_load_dwordx4 a[80:83],v209,s[64:67],s40 offen sc1\n"
"v_mfma_f32_16x16x32_bf16 v[92:95],a[248:251],a[108:111],v[92:95]\n"
"ds_read_b64_tr_b16 a[230:231],v204 offset:12288\n"
"v_mfma_f32_16x16x32_bf16 v[92:95],a[252:255],a[124:127],v[92:95]\n"
"v_mfma_f32_16x16x32_bf16 v[96:99],a[208:211],a[96:99],v[96:99]\n"
"ds_read_b64_tr_b16 a[232:233],v204 offset:8\n"
"v_mfma_f32_16x16x32_bf16 v[96:99],a[212:215],a[112:115],v[96:99]\n"
"v_mfma_f32_16x16x32_bf16 v[100:103],a[208:211],a[100:103],v[100:103]\n"
"ds_read_b64_tr_b16 a[234:235],v204 offset:4104\n"
"v_mfma_f32_16x16x32_bf16 v[100:103],a[212:215],a[116:119],v[100:103]\n"
"buffer_load_dwordx4 a[84:87],v209,s[64:67],s40 offen offset:1024 sc1\n"
"v_mfma_f32_16x16x32_bf16 v[104:107],a[208:211],a[104:107],v[104:107]\n"
"ds_read_b64_tr_b16 a[236:237],v204 offset:8200\n"
"v_mfma_f32_16x16x32_bf16 v[104:107],a[212:215],a[120:123],v[104:107]\n"
"v_mfma_f32_16x16x32_bf16 v[108:111],a[208:211],a[108:111],v[108:111]\n"
"ds_read_b64_tr_b16 a[238:239],v204 offset:12296\n"
"v_mfma_f32_16x16x32_bf16 v[108:111],a[212:215],a[124:127],v[108:111]\n"
"v_mfma_f32_16x16x32_bf16 v[112:115],a[216:219],a[96:99],v[112:115]\n"
"ds_read_b64_tr_b16 a[240:241],v204 offset:256\n"
"v_mfma_f32_16x16x32_bf16 v[112:115],a[220:223],a[112:115],v[112:115]\n"
"buffer_load_dwordx4 a[88:91],v209,s[64:67],s40 offen offset:2048 sc1\n"
"v_mfma_f32_16x16x32_bf16 v[116:119],a[216:219],a[100:103],v[116:119]\n"
"ds_read_b64_tr_b16 a[242:243],v204 offset:4352\n"
"v_mfma_f32_16x16x32_bf16 v[116:119],a[220:223],a[116:119],v[116:119]\n"
"v_mfma_f32_16x16x32_bf16 v[120:123],a[216:219],a[104:107],v[120:123]\n"
"ds_read_b64_tr_b16 a[244:245],v204 offset:8448\n"
"v_mfma_f32_16x16x32_bf16 v[120:123],a[220:223],a[120:123],v[120:123]\n"
"buffer_load_dwordx4 a[92:95],v209,s[64:67],s40 offen offset:3072 sc1\n"
"v_mfma_f32_16x16x32_bf16 v[124:127],a[216:219],a[108:111],v[124:127]\n"
"ds_read_b64_tr_b16 a[246:247],v204 offset:12544\n"
"v_mfma_f32_16x16x32_bf16 v[124:127],a[220:223],a[124:127],v[124:127]\n"
"s_waitcnt lgkmcnt(8)\n"
"v_mfma_f32_16x16x32_bf16 v[0:3],a[224:227],a[0:3],v[0:3]\n"
"ds_read_b64_tr_b16 a[248:249],v204 offset:264\n"
"v_mfma_f32_16x16x32_bf16 v[0:3],a[228:231],a[16:19],v[0:3]\n"
"v_mfma_f32_16x16x32_bf16 v[4:7],a[224:227],a[4:7],v[4:7]\n"
"s_add_u32 m0,s50,49152\n"
"s_add_u32 s39,s52,3670016\n"
"ds_read_b64_tr_b16 a[250:251],v204 offset:4360\n"
"v_mfma_f32_16x16x32_bf16 v[4:7],a[228:231],a[20:23],v[4:7]\n"
"buffer_load_dwordx4 v205,s[72:75],s39 offen lds\n"
"v_mfma_f32_16x16x32_bf16 v[8:11],a[224:227],a[8:11],v[8:11]\n"
"ds_read_b64_tr_b16 a[252:253],v204 offset:8456\n"
"v_mfma_f32_16x16x32_bf16 v[8:11],a[228:231],a[24:27],v[8:11]\n"
"v_mfma_f32_16x16x32_bf16 v[12:15],a[224:227],a[12:15],v[12:15]\n"
"ds_read_b64_tr_b16 a[254:255],v204 offset:12552\n"
"v_mfma_f32_16x16x32_bf16 v[12:15],a[228:231],a[28:31],v[12:15]\n"
"s_waitcnt lgkmcnt(8)\n"
"v_mfma_f32_16x16x32_bf16 v[16:19],a[232:235],a[0:3],v[16:19]\n"
"ds_read_b64_tr_b16 a[208:209],v204 offset:512\n"
"v_mfma_f32_16x16x32_bf16 v[16:19],a[236:239],a[16:19],v[16:19]\n"
"buffer_load_dwordx4 v206,s[72:75],s39 offen offset:1024 lds\n"
"v_mfma_f32_16x16x32_bf16 v[20:23],a[232:235],a[4:7],v[20:23]\n"
"ds_read_b64_tr_b16 a[210:211],v204 offset:4608\n"
"v_mfma_f32_16x16x32_bf16 v[20:23],a[236:239],a[20:23],v[20:23]\n"
"v_mfma_f32_16x16x32_bf16 v[24:27],a[232:235],a[8:11],v[24:27]\n"
"ds_read_b64_tr_b16 a[212:213],v204 offset:8704\n"
"v_mfma_f32_16x16x32_bf16 v[24:27],a[236:239],a[24:27],v[24:27]\n"
"buffer_load_dwordx4 v207,s[72:75],s39 offen offset:2048 lds\n"
"v_mfma_f32_16x16x32_bf16 v[28:31],a[232:235],a[12:15],v[28:31]\n"
"ds_read_b64_tr_b16 a[214:215],v204 offset:12800\n"
"v_mfma_f32_16x16x32_bf16 v[28:31],a[236:239],a[28:31],v[28:31]\n"
"s_waitcnt lgkmcnt(8)\n"
"v_mfma_f32_16x16x32_bf16 v[32:35],a[240:243],a[0:3],v[32:35]\n"
"ds_read_b64_tr_b16 a[216:217],v204 offset:520\n"
"v_mfma_f32_16x16x32_bf16 v[32:35],a[244:247],a[16:19],v[32:35]\n"
"v_mfma_f32_16x16x32_bf16 v[36:39],a[240:243],a[4:7],v[36:39]\n"
"ds_read_b64_tr_b16 a[218:219],v204 offset:4616\n"
"v_mfma_f32_16x16x32_bf16 v[36:39],a[244:247],a[20:23],v[36:39]\n"
"buffer_load_dwordx4 v208,s[72:75],s39 offen offset:3072 lds\n"
"v_mfma_f32_16x16x32_bf16 v[40:43],a[240:243],a[8:11],v[40:43]\n"
"ds_read_b64_tr_b16 a[220:221],v204 offset:8712\n"
"v_mfma_f32_16x16x32_bf16 v[40:43],a[244:247],a[24:27],v[40:43]\n"
"v_mfma_f32_16x16x32_bf16 v[44:47],a[240:243],a[12:15],v[44:47]\n"
"ds_read_b64_tr_b16 a[222:223],v204 offset:12808\n"
"v_mfma_f32_16x16x32_bf16 v[44:47],a[244:247],a[28:31],v[44:47]\n"
"s_waitcnt lgkmcnt(8)\n"
"v_mfma_f32_16x16x32_bf16 v[48:51],a[248:251],a[0:3],v[48:51]\n"
"s_add_u32 s40,s53,57344\n"
"ds_read_b64_tr_b16 a[224:225],v204 offset:768\n"
"v_mfma_f32_16x16x32_bf16 v[48:51],a[252:255],a[16:19],v[48:51]\n"
"buffer_load_dwordx4 a[96:99],v209,s[64:67],s40 offen sc1\n"
"v_mfma_f32_16x16x32_bf16 v[52:55],a[248:251],a[4:7],v[52:55]\n"
"ds_read_b64_tr_b16 a[226:227],v204 offset:4864\n"
"v_mfma_f32_16x16x32_bf16 v[52:55],a[252:255],a[20:23],v[52:55]\n"
"v_mfma_f32_16x16x32_bf16 v[56:59],a[248:251],a[8:11],v[56:59]\n"
"ds_read_b64_tr_b16 a[228:229],v204 offset:8960\n"
"v_mfma_f32_16x16x32_bf16 v[56:59],a[252:255],a[24:27],v[56:59]\n"
"buffer_load_dwordx4 a[100:103],v209,s[64:67],s40 offen offset:1024 sc1\n"
"v_mfma_f32_16x16x32_bf16 v[60:63],a[248:251],a[12:15],v[60:63]\n"
"ds_read_b64_tr_b16 a[230:231],v204 offset:13056\n"
"v_mfma_f32_16x16x32_bf16 v[60:63],a[252:255],a[28:31],v[60:63]\n"
"s_waitcnt lgkmcnt(8)\n"
"v_mfma_f32_16x16x32_bf16 v[64:67],a[208:211],a[0:3],v[64:67]\n"
"ds_read_b64_tr_b16 a[232:233],v204 offset:776\n"
"v_mfma_f32_16x16x32_bf16 v[64:67],a[212:215],a[16:19],v[64:67]\n"
"ds_read_b64_tr_b16 a[234:235],v204 offset:4872\n"
"v_mfma_f32_16x16x32_bf16 v[68:71],a[208:211],a[4:7],v[68:71]\n"
"ds_read_b64_tr_b16 a[236:237],v204 offset:8968\n"
"v_mfma_f32_16x16x32_bf16 v[68:71],a[212:215],a[20:23],v[68:71]\n"
"ds_read_b64_tr_b16 a[238:239],v204 offset:13064\n"
"buffer_load_dwordx4 a[104:107],v209,s[64:67],s40 offen offset:2048 sc1\n"
"v_mfma_f32_16x16x32_bf16 v[72:75],a[208:211],a[8:11],v[72:75]\n"
"v_mfma_f32_16x16x32_bf16 v[72:75],a[212:215],a[24:27],v[72:75]\n"
"v_mfma_f32_16x16x32_bf16 v[76:79],a[208:211],a[12:15],v[76:79]\n"
"v_mfma_f32_16x16x32_bf16 v[76:79],a[212:215],a[28:31],v[76:79]\n"
"s_waitcnt vmcnt(19) lgkmcnt(0)\n"
"s_barrier\n"
"v_mfma_f32_16x16x32_bf16 v[80:83],a[216:219],a[0:3],v[80:83]\n"
"ds_read_b64_tr_b16 a[240:241],v204 offset:16384\n"
"v_mfma_f32_16x16x32_bf16 v[80:83],a[220:223],a[16:19],v[80:83]\n"
"buffer_load_dwordx4 a[108:111],v209,s[64:67],s40 offen offset:3072 sc1\n"
"v_mfma_f32_16x16x32_bf16 v[84:87],a[216:219],a[4:7],v[84:87]\n"
"ds_read_b64_tr_b16 a[242:243],v204 offset:20480\n"
"v_mfma_f32_16x16x32_bf16 v[84:87],a[220:223],a[20:23],v[84:87]\n"
"v_mfma_f32_16x16x32_bf16 v[88:91],a[216:219],a[8:11],v[88:91]\n"
"s_add_u32 s40,s53,61440\n"
"ds_read_b64_tr_b16 a[244:245],v204 offset:24576\n"
"v_mfma_f32_16x16x32_bf16 v[88:91],a[220:223],a[24:27],v[88:91]\n"
"buffer_load_dwordx4 a[112:115],v209,s[64:67],s40 offen sc1\n"
"v_mfma_f32_16x16x32_bf16 v[92:95],a[216:219],a[12:15],v[92:95]\n"
"ds_read_b64_tr_b16 a[246:247],v204 offset:28672\n"
"v_mfma_f32_16x16x32_bf16 v[92:95],a[220:223],a[28:31],v[92:95]\n"
"v_mfma_f32_16x16x32_bf16 v[96:99],a[224:227],a[0:3],v[96:99]\n"
"ds_read_b64_tr_b16 a[248:249],v204 offset:16392\n"
"v_mfma_f32_16x16x32_bf16 v[96:99],a[228:231],a[16:19],v[96:99]\n"
"v_mfma_f32_16x16x32_bf16 v[100:103],a[224:227],a[4:7],v[100:103]\n"
"ds_read_b64_tr_b16 a[250:251],v204 offset:20488\n"
"v_mfma_f32_16x16x32_bf16 v[100:103],a[228:231],a[20:23],v[100:103]\n"
"buffer_load_dwordx4 a[116:119],v209,s[64:67],s40 offen offset:1024 sc1\n"
"v_mfma_f32_16x16x32_bf16 v[104:107],a[224:227],a[8:11],v[104:107]\n"
"ds_read_b64_tr_b16 a[252:253],v204 offset:24584\n"
"v_mfma_f32_16x16x32_bf16 v[104:107],a[228:231],a[24:27],v[104:107]\n"
"v_mfma_f32_16x16x32_bf16 v[108:111],a[224:227],a[12:15],v[108:111]\n"
"ds_read_b64_tr_b16 a[254:255],v204 offset:28680\n"
"v_mfma_f32_16x16x32_bf16 v[108:111],a[228:231],a[28:31],v[108:111]\n"
"v_mfma_f32_16x16x32_bf16 v[112:115],a[232:235],a[0:3],v[112:115]\n"
"ds_read_b64_tr_b16 a[208:209],v204 offset:16640\n"
"v_mfma_f32_16x16x32_bf16 v[112:115],a[236:239],a[16:19],v[112:115]\n"
"buffer_load_dwordx4 a[120:123],v209,s[64:67],s40 offen offset:2048 sc1\n"
"v_mfma_f32_16x16x32_bf16 v[116:119],a[232:235],a[4:7],v[116:119]\n"
"ds_read_b64_tr_b16 a[210:211],v204 offset:20736\n"
"v_mfma_f32_16x16x32_bf16 v[116:119],a[236:239],a[20:23],v[116:119]\n"
"v_mfma_f32_16x16x32_bf16 v[120:123],a[232:235],a[8:11],v[120:123]\n"
"ds_read_b64_tr_b16 a[212:213],v204 offset:24832\n"
"v_mfma_f32_16x16x32_bf16 v[120:123],a[236:239],a[24:27],v[120:123]\n"
"buffer_load_dwordx4 a[124:127],v209,s[64:67],s40 offen offset:3072 sc1\n"
"v_mfma_f32_16x16x32_bf16 v[124:127],a[232:235],a[12:15],v[124:127]\n"
"ds_read_b64_tr_b16 a[214:215],v204 offset:28928\n"
"v_mfma_f32_16x16x32_bf16 v[124:127],a[236:239],a[28:31],v[124:127]\n"
"s_waitcnt lgkmcnt(8)\n"
"v_mfma_f32_16x16x32_bf16 v[0:3],a[240:243],a[32:35],v[0:3]\n"
"ds_read_b64_tr_b16 a[216:217],v204 offset:16648\n"
"v_mfma_f32_16x16x32_bf16 v[0:3],a[244:247],a[48:51],v[0:3]\n"
"v_mfma_f32_16x16x32_bf16 v[4:7],a[240:243],a[36:39],v[4:7]\n"
"s_add_u32 m0,s50,0\n"
"s_add_u32 s39,s52,4194304\n"
"ds_read_b64_tr_b16 a[218:219],v204 offset:20744\n"
"v_mfma_f32_16x16x32_bf16 v[4:7],a[244:247],a[52:55],v[4:7]\n"
"buffer_load_dwordx4 v205,s[72:75],s39 offen lds\n"
"v_mfma_f32_16x16x32_bf16 v[8:11],a[240:243],a[40:43],v[8:11]\n"
"ds_read_b64_tr_b16 a[220:221],v204 offset:24840\n"
"v_mfma_f32_16x16x32_bf16 v[8:11],a[244:247],a[56:59],v[8:11]\n"
"v_mfma_f32_16x16x32_bf16 v[12:15],a[240:243],a[44:47],v[12:15]\n"
"ds_read_b64_tr_b16 a[222:223],v204 offset:28936\n"
"v_mfma_f32_16x16x32_bf16 v[12:15],a[244:247],a[60:63],v[12:15]\n"
"s_waitcnt lgkmcnt(8)\n"
"v_mfma_f32_16x16x32_bf16 v[16:19],a[248:251],a[32:35],v[16:19]\n"
"ds_read_b64_tr_b16 a[224:225],v204 offset:16896\n"
"v_mfma_f32_16x16x32_bf16 v[16:19],a[252:255],a[48:51],v[16:19]\n"
"buffer_load_dwordx4 v206,s[72:75],s39 offen offset:1024 lds\n"
"v_mfma_f32_16x16x32_bf16 v[20:23],a[248:251],a[36:39],v[20:23]\n"
"ds_read_b64_tr_b16 a[226:227],v204 offset:20992\n"
"v_mfma_f32_16x16x32_bf16 v[20:23],a[252:255],a[52:55],v[20:23]\n"
"v_mfma_f32_16x16x32_bf16 v[24:27],a[248:251],a[40:43],v[24:27]\n"
"ds_read_b64_tr_b16 a[228:229],v204 offset:25088\n"
"v_mfma_f32_16x16x32_bf16 v[24:27],a[252:255],a[56:59],v[24:27]\n"
"buffer_load_dwordx4 v207,s[72:75],s39 offen offset:2048 lds\n"
"v_mfma_f32_16x16x32_bf16 v[28:31],a[248:251],a[44:47],v[28:31]\n"
"ds_read_b64_tr_b16 a[230:231],v204 offset:29184\n"
"v_mfma_f32_16x16x32_bf16 v[28:31],a[252:255],a[60:63],v[28:31]\n"
"s_waitcnt lgkmcnt(8)\n"
"v_mfma_f32_16x16x32_bf16 v[32:35],a[208:211],a[32:35],v[32:35]\n"
"ds_read_b64_tr_b16 a[232:233],v204 offset:16904\n"
"v_mfma_f32_16x16x32_bf16 v[32:35],a[212:215],a[48:51],v[32:35]\n"
"v_mfma_f32_16x16x32_bf16 v[36:39],a[208:211],a[36:39],v[36:39]\n"
"ds_read_b64_tr_b16 a[234:235],v204 offset:21000\n"
"v_mfma_f32_16x16x32_bf16 v[36:39],a[212:215],a[52:55],v[36:39]\n"
"buffer_load_dwordx4 v208,s[72:75],s39 offen offset:3072 lds\n"
"v_mfma_f32_16x16x32_bf16 v[40:43],a[208:211],a[40:43],v[40:43]\n"
"ds_read_b64_tr_b16 a[236:237],v204 offset:25096\n"
"v_mfma_f32_16x16x32_bf16 v[40:43],a[212:215],a[56:59],v[40:43]\n"
"v_mfma_f32_16x16x32_bf16 v[44:47],a[208:211],a[44:47],v[44:47]\n"
"ds_read_b64_tr_b16 a[238:239],v204 offset:29192\n"
"v_mfma_f32_16x16x32_bf16 v[44:47],a[212:215],a[60:63],v[44:47]\n"
"s_waitcnt lgkmcnt(8)\n"
"v_mfma_f32_16x16x32_bf16 v[48:51],a[216:219],a[32:35],v[48:51]\n"
"s_add_u32 s40,s53,65536\n"
"ds_read_b64_tr_b16 a[240:241],v204 offset:17152\n"
"v_mfma_f32_16x16x32_bf16 v[48:51],a[220:223],a[48:51],v[48:51]\n"
"buffer_load_dwordx4 a[0:3],v209,s[64:67],s40 offen sc1\n"
"v_mfma_f32_16x16x32_bf16 v[52:55],a[216:219],a[36:39],v[52:55]\n"
"ds_read_b64_tr_b16 a[242:243],v204 offset:21248\n"
"v_mfma_f32_16x16x32_bf16 v[52:55],a[220:223],a[52:55],v[52:55]\n"
"v_mfma_f32_16x16x32_bf16 v[56:59],a[216:219],a[40:43],v[56:59]\n"
"ds_read_b64_tr_b16 a[244:245],v204 offset:25344\n"
"v_mfma_f32_16x16x32_bf16 v[56:59],a[220:223],a[56:59],v[56:59]\n"
"buffer_load_dwordx4 a[4:7],v209,s[64:67],s40 offen offset:1024 sc1\n"
"v_mfma_f32_16x16x32_bf16 v[60:63],a[216:219],a[44:47],v[60:63]\n"
"ds_read_b64_tr_b16 a[246:247],v204 offset:29440\n"
"v_mfma_f32_16x16x32_bf16 v[60:63],a[220:223],a[60:63],v[60:63]\n"
"s_waitcnt lgkmcnt(8)\n"
"v_mfma_f32_16x16x32_bf16 v[64:67],a[224:227],a[32:35],v[64:67]\n"
"ds_read_b64_tr_b16 a[248:249],v204 offset:17160\n"
"v_mfma_f32_16x16x32_bf16 v[64:67],a[228:231],a[48:51],v[64:67]\n"
"ds_read_b64_tr_b16 a[250:251],v204 offset:21256\n"
"v_mfma_f32_16x16x32_bf16 v[68:71],a[224:227],a[36:39],v[68:71]\n"
"ds_read_b64_tr_b16 a[252:253],v204 offset:25352\n"
"v_mfma_f32_16x16x32_bf16 v[68:71],a[228:231],a[52:55],v[68:71]\n"
"ds_read_b64_tr_b16 a[254:255],v204 offset:29448\n"
"buffer_load_dwordx4 a[8:11],v209,s[64:67],s40 offen offset:2048 sc1\n"
"v_mfma_f32_16x16x32_bf16 v[72:75],a[224:227],a[40:43],v[72:75]\n"
"v_mfma_f32_16x16x32_bf16 v[72:75],a[228:231],a[56:59],v[72:75]\n"
"v_mfma_f32_16x16x32_bf16 v[76:79],a[224:227],a[44:47],v[76:79]\n"
"v_mfma_f32_16x16x32_bf16 v[76:79],a[228:231],a[60:63],v[76:79]\n"
"s_waitcnt vmcnt(19) lgkmcnt(0)\n"
"s_barrier\n"
"v_mfma_f32_16x16x32_bf16 v[80:83],a[232:235],a[32:35],v[80:83]\n"
"ds_read_b64_tr_b16 a[208:209],v204 offset:32768\n"
"v_mfma_f32_16x16x32_bf16 v[80:83],a[236:239],a[48:51],v[80:83]\n"
"buffer_load_dwordx4 a[12:15],v209,s[64:67],s40 offen offset:3072 sc1\n"
"v_mfma_f32_16x16x32_bf16 v[84:87],a[232:235],a[36:39],v[84:87]\n"
"ds_read_b64_tr_b16 a[210:211],v204 offset:36864\n"
"v_mfma_f32_16x16x32_bf16 v[84:87],a[236:239],a[52:55],v[84:87]\n"
"v_mfma_f32_16x16x32_bf16 v[88:91],a[232:235],a[40:43],v[88:91]\n"
"s_add_u32 s40,s53,69632\n"
"ds_read_b64_tr_b16 a[212:213],v204 offset:40960\n"
"v_mfma_f32_16x16x32_bf16 v[88:91],a[236:239],a[56:59],v[88:91]\n"
"buffer_load_dwordx4 a[16:19],v209,s[64:67],s40 offen sc1\n"
"v_mfma_f32_16x16x32_bf16 v[92:95],a[232:235],a[44:47],v[92:95]\n"
"ds_read_b64_tr_b16 a[214:215],v204 offset:45056\n"
"v_mfma_f32_16x16x32_bf16 v[92:95],a[236:239],a[60:63],v[92:95]\n"
"v_mfma_f32_16x16x32_bf16 v[96:99],a[240:243],a[32:35],v[96:99]\n"
"ds_read_b64_tr_b16 a[216:217],v204 offset:32776\n"
"v_mfma_f32_16x16x32_bf16 v[96:99],a[244:247],a[48:51],v[96:99]\n"
"v_mfma_f32_16x16x32_bf16 v[100:103],a[240:243],a[36:39],v[100:103]\n"
"ds_read_b64_tr_b16 a[218:219],v204 offset:36872\n"
"v_mfma_f32_16x16x32_bf16 v[100:103],a[244:247],a[52:55],v[100:103]\n"
"buffer_load_dwordx4 a[20:23],v209,s[64:67],s40 offen offset:1024 sc1\n"
"v_mfma_f32_16x16x32_bf16 v[104:107],a[240:243],a[40:43],v[104:107]\n"
"ds_read_b64_tr_b16 a[220:221],v204 offset:40968\n"
"v_mfma_f32_16x16x32_bf16 v[104:107],a[244:247],a[56:59],v[104:107]\n"
"v_mfma_f32_16x16x32_bf16 v[108:111],a[240:243],a[44:47],v[108:111]\n"
"ds_read_b64_tr_b16 a[222:223],v204 offset:45064\n"
"v_mfma_f32_16x16x32_bf16 v[108:111],a[244:247],a[60:63],v[108:111]\n"
"v_mfma_f32_16x16x32_bf16 v[112:115],a[248:251],a[32:35],v[112:115]\n"
"ds_read_b64_tr_b16 a[224:225],v204 offset:33024\n"
"v_mfma_f32_16x16x32_bf16 v[112:115],a[252:255],a[48:51],v[112:115]\n"
"buffer_load_dwordx4 a[24:27],v209,s[64:67],s40 offen offset:2048 sc1\n"
"v_mfma_f32_16x16x32_bf16 v[116:119],a[248:251],a[36:39],v[116:119]\n"
"ds_read_b64_tr_b16 a[226:227],v204 offset:37120\n"
"v_mfma_f32_16x16x32_bf16 v[116:119],a[252:255],a[52:55],v[116:119]\n"
"v_mfma_f32_16x16x32_bf16 v[120:123],a[248:251],a[40:43],v[120:123]\n"
"ds_read_b64_tr_b16 a[228:229],v204 offset:41216\n"
"v_mfma_f32_16x16x32_bf16 v[120:123],a[252:255],a[56:59],v[120:123]\n"
"buffer_load_dwordx4 a[28:31],v209,s[64:67],s40 offen offset:3072 sc1\n"
"v_mfma_f32_16x16x32_bf16 v[124:127],a[248:251],a[44:47],v[124:127]\n"
"ds_read_b64_tr_b16 a[230:231],v204 offset:45312\n"
"v_mfma_f32_16x16x32_bf16 v[124:127],a[252:255],a[60:63],v[124:127]\n"
"s_cmp_eq_u32 s35,2\n"
"s_cbranch_scc0 LM_NOB\n"
"s_waitcnt lgkmcnt(8)\n"
"v_mfma_f32_16x16x32_bf16 v[0:3],a[208:211],a[64:67],v[0:3]\n"
"ds_read_b64_tr_b16 a[232:233],v204 offset:33032\n"
"v_mfma_f32_16x16x32_bf16 v[0:3],a[212:215],a[80:83],v[0:3]\n"
"v_mfma_f32_16x16x32_bf16 v[4:7],a[208:211],a[68:71],v[4:7]\n"
"ds_read_b64_tr_b16 a[234:235],v204 offset:37128\n"
"v_mfma_f32_16x16x32_bf16 v[4:7],a[212:215],a[84:87],v[4:7]\n"
"v_mfma_f32_16x16x32_bf16 v[8:11],a[208:211],a[72:75],v[8:11]\n"
"ds_read_b64_tr_b16 a[236:237],v204 offset:41224\n"
"v_mfma_f32_16x16x32_bf16 v[8:11],a[212:215],a[88:91],v[8:11]\n"
"v_mfma_f32_16x16x32_bf16 v[12:15],a[208:211],a[76:79],v[12:15]\n"
"ds_read_b64_tr_b16 a[238:239],v204 offset:45320\n"
"v_mfma_f32_16x16x32_bf16 v[12:15],a[212:215],a[92:95],v[12:15]\n"
"s_waitcnt lgkmcnt(8)\n"
"v_mfma_f32_16x16x32_bf16 v[16:19],a[216:219],a[64:67],v[16:19]\n"
"ds_read_b64_tr_b16 a[240:241],v204 offset:33280\n"
"v_mfma_f32_16x16x32_bf16 v[16:19],a[220:223],a[80:83],v[16:19]\n"
"v_mfma_f32_16x16x32_bf16 v[20:23],a[216:219],a[68:71],v[20:23]\n"
"ds_read_b64_tr_b16 a[242:243],v204 offset:37376\n"
"v_mfma_f32_16x16x32_bf16 v[20:23],a[220:223],a[84:87],v[20:23]\n"
"v_mfma_f32_16x16x32_bf16 v[24:27],a[216:219],a[72:75],v[24:27]\n"
"ds_read_b64_tr_b16 a[244:245],v204 offset:41472\n"
"v_mfma_f32_16x16x32_bf16 v[24:27],a[220:223],a[88:91],v[24:27]\n"
"v_mfma_f32_16x16x32_bf16 v[28:31],a[216:219],a[76:79],v[28:31]\n"
"ds_read_b64_tr_b16 a[246:247],v204 offset:45568\n"
"v_mfma_f32_16x16x32_bf16 v[28:31],a[220:223],a[92:95],v[28:31]\n"
"s_waitcnt lgkmcnt(8)\n"
"v_mfma_f32_16x16x32_bf16 v[32:35],a[224:227],a[64:67],v[32:35]\n"
"ds_read_b64_tr_b16 a[248:249],v204 offset:33288\n"
"v_mfma_f32_16x16x32_bf16 v[32:35],a[228:231],a[80:83],v[32:35]\n"
"v_mfma_f32_16x16x32_bf16 v[36:39],a[224:227],a[68:71],v[36:39]\n"
"ds_read_b64_tr_b16 a[250:251],v204 offset:37384\n"
"v_mfma_f32_16x16x32_bf16 v[36:39],a[228:231],a[84:87],v[36:39]\n"
"v_mfma_f32_16x16x32_bf16 v[40:43],a[224:227],a[72:75],v[40:43]\n"
"ds_read_b64_tr_b16 a[252:253],v204 offset:41480\n"
"v_mfma_f32_16x16x32_bf16 v[40:43],a[228:231],a[88:91],v[40:43]\n"
"v_mfma_f32_16x16x32_bf16 v[44:47],a[224:227],a[76:79],v[44:47]\n"
"ds_read_b64_tr_b16 a[254:255],v204 offset:45576\n"
"v_mfma_f32_16x16x32_bf16 v[44:47],a[228:231],a[92:95],v[44:47]\n"
"s_waitcnt lgkmcnt(8)\n"
"v_mfma_f32_16x16x32_bf16 v[48:51],a[232:235],a[64:67],v[48:51]\n"
"ds_read_b64_tr_b16 a[208:209],v204 offset:33536\n"
"v_mfma_f32_16x16x32_bf16 v[48:51],a[236:239],a[80:83],v[48:51]\n"
"v_mfma_f32_16x16x32_bf16 v[52:55],a[232:235],a[68:71],v[52:55]\n"
"ds_read_b64_tr_b16 a[210:211],v204 offset:37632\n"
"v_mfma_f32_16x16x32_bf16 v[52:55],a[236:239],a[84:87],v[52:55]\n"
"v_mfma_f32_16x16x32_bf16 v[56:59],a[232:235],a[72:75],v[56:59]\n"
"ds_read_b64_tr_b16 a[212:213],v204 offset:41728\n"
"v_mfma_f32_16x16x32_bf16 v[56:59],a[236:239],a[88:91],v[56:59]\n"
"v_mfma_f32_16x16x32_bf16 v[60:63],a[232:235],a[76:79],v[60:63]\n"
"ds_read_b64_tr_b16 a[214:215],v204 offset:45824\n"
"v_mfma_f32_16x16x32_bf16 v[60:63],a[236:239],a[92:95],v[60:63]\n"
"s_waitcnt lgkmcnt(8)\n"
"v_mfma_f32_16x16x32_bf16 v[64:67],a[240:243],a[64:67],v[64:67]\n"
"ds_read_b64_tr_b16 a[216:217],v204 offset:33544\n"
"v_mfma_f32_16x16x32_bf16 v[64:67],a[244:247],a[80:83],v[64:67]\n"
"ds_read_b64_tr_b16 a[218:219],v204 offset:37640\n"
"v_mfma_f32_16x16x32_bf16 v[68:71],a[240:243],a[68:71],v[68:71]\n"
"ds_read_b64_tr_b16 a[220:221],v204 offset:41736\n"
"v_mfma_f32_16x16x32_bf16 v[68:71],a[244:247],a[84:87],v[68:71]\n"
"ds_read_b64_tr_b16 a[222:223],v204 offset:45832\n"
"v_mfma_f32_16x16x32_bf16 v[72:75],a[240:243],a[72:75],v[72:75]\n"
"v_mfma_f32_16x16x32_bf16 v[72:75],a[244:247],a[88:91],v[72:75]\n"
"v_mfma_f32_16x16x32_bf16 v[76:79],a[240:243],a[76:79],v[76:79]\n"
"v_mfma_f32_16x16x32_bf16 v[76:79],a[244:247],a[92:95],v[76:79]\n"
"s_waitcnt vmcnt(12) lgkmcnt(0)\n"
"s_barrier\n"
"v_mfma_f32_16x16x32_bf16 v[80:83],a[248:251],a[64:67],v[80:83]\n"
"ds_read_b64_tr_b16 a[224:225],v204 offset:49152\n"
"v_mfma_f32_16x16x32_bf16 v[80:83],a[252:255],a[80:83],v[80:83]\n"
"v_mfma_f32_16x16x32_bf16 v[84:87],a[248:251],a[68:71],v[84:87]\n"
"ds_read_b64_tr_b16 a[226:227],v204 offset:53248\n"
"v_mfma_f32_16x16x32_bf16 v[84:87],a[252:255],a[84:87],v[84:87]\n"
"v_mfma_f32_16x16x32_bf16 v[88:91],a[248:251],a[72:75],v[88:91]\n"
"ds_read_b64_tr_b16 a[228:229],v204 offset:57344\n"
"v_mfma_f32_16x16x32_bf16 v[88:91],a[252:255],a[88:91],v[88:91]\n"
"v_mfma_f32_16x16x32_bf16 v[92:95],a[248:251],a[76:79],v[92:95]\n"
"ds_read_b64_tr_b16 a[230:231],v204 offset:61440\n"
"v_mfma_f32_16x16x32_bf16 v[92:95],a[252:255],a[92:95],v[92:95]\n"
"v_readlane_b32 s43,v210,47\n"
"s_nop 0\n"
"s_lshl_b32 s43,s43,6\n"
"v_lshl_add_u32 v223,v209,2,s43\n"
"v_readlane_b32 s46,v210,48\n"
"s_mov_b32 s47,0\n"
"buffer_load_dwordx2 v[226:227],v223,s[68:71],0 offen sc1\n"
"v_mfma_f32_16x16x32_bf16 v[96:99],a[208:211],a[64:67],v[96:99]\n"
"ds_read_b64_tr_b16 a[232:233],v204 offset:49160\n"
"v_mfma_f32_16x16x32_bf16 v[96:99],a[212:215],a[80:83],v[96:99]\n"
"v_mfma_f32_16x16x32_bf16 v[100:103],a[208:211],a[68:71],v[100:103]\n"
"ds_read_b64_tr_b16 a[234:235],v204 offset:53256\n"
"v_mfma_f32_16x16x32_bf16 v[100:103],a[212:215],a[84:87],v[100:103]\n"
"v_mfma_f32_16x16x32_bf16 v[104:107],a[208:211],a[72:75],v[104:107]\n"
"ds_read_b64_tr_b16 a[236:237],v204 offset:57352\n"
"v_mfma_f32_16x16x32_bf16 v[104:107],a[212:215],a[88:91],v[104:107]\n"
"v_mfma_f32_16x16x32_bf16 v[108:111],a[208:211],a[76:79],v[108:111]\n"
"ds_read_b64_tr_b16 a[238:239],v204 offset:61448\n"
"v_mfma_f32_16x16x32_bf16 v[108:111],a[212:215],a[92:95],v[108:111]\n"
"v_mfma_f32_16x16x32_bf16 v[112:115],a[216:219],a[64:67],v[112:115]\n"
"ds_read_b64_tr_b16 a[240:241],v204 offset:49408\n"
"v_mfma_f32_16x16x32_bf16 v[112:115],a[220:223],a[80:83],v[112:115]\n"
"v_mfma_f32_16x16x32_bf16 v[116:119],a[216:219],a[68:71],v[116:119]\n"
"ds_read_b64_tr_b16 a[242:243],v204 offset:53504\n"
"v_mfma_f32_16x16x32_bf16 v[116:119],a[220:223],a[84:87],v[116:119]\n"
"v_mfma_f32_16x16x32_bf16 v[120:123],a[216:219],a[72:75],v[120:123]\n"
"ds_read_b64_tr_b16 a[244:245],v204 offset:57600\n"
"v_mfma_f32_16x16x32_bf16 v[120:123],a[220:223],a[88:91],v[120:123]\n"
"v_mfma_f32_16x16x32_bf16 v[124:127],a[216:219],a[76:79],v[124:127]\n"
"ds_read_b64_tr_b16 a[246:247],v204 offset:61696\n"
"v_mfma_f32_16x16x32_bf16 v[124:127],a[220:223],a[92:95],v[124:127]\n"
"s_waitcnt lgkmcnt(8)\n"
"v_mfma_f32_16x16x32_bf16 v[0:3],a[224:227],a[96:99],v[0:3]\n"
"ds_read_b64_tr_b16 a[248:249],v204 offset:49416\n"
"v_mfma_f32_16x16x32_bf16 v[0:3],a[228:231],a[112:115],v[0:3]\n"
"v_mfma_f32_16x16x32_bf16 v[4:7],a[224:227],a[100:103],v[4:7]\n"
"s_add_u32 m0,s51,65536\n"
"s_add_u32 s42,s60,294912\n"
"ds_read_b64_tr_b16 a[250:251],v204 offset:53512\n"
"v_mfma_f32_16x16x32_bf16 v[4:7],a[228:231],a[116:119],v[4:7]\n"
"buffer_load_dwordx4 v213,s[92:95],s42 offen lds\n"
"v_mfma_f32_16x16x32_bf16 v[8:11],a[224:227],a[104:107],v[8:11]\n"
"ds_read_b64_tr_b16 a[252:253],v204 offset:57608\n"
"v_mfma_f32_16x16x32_bf16 v[8:11],a[228:231],a[120:123],v[8:11]\n"
"v_mfma_f32_16x16x32_bf16 v[12:15],a[224:227],a[108:111],v[12:15]\n"
"ds_read_b64_tr_b16 a[254:255],v204 offset:61704\n"
"v_mfma_f32_16x16x32_bf16 v[12:15],a[228:231],a[124:127],v[12:15]\n"
"buffer_load_dwordx4 v214,s[92:95],s42 offen offset:1024 lds\n"
"s_waitcnt lgkmcnt(8)\n"
"v_mfma_f32_16x16x32_bf16 v[16:19],a[232:235],a[96:99],v[16:19]\n"
"ds_read_b64_tr_b16 a[208:209],v204 offset:49664\n"
"v_mfma_f32_16x16x32_bf16 v[16:19],a[236:239],a[112:115],v[16:19]\n"
"v_mfma_f32_16x16x32_bf16 v[20:23],a[232:235],a[100:103],v[20:23]\n"
"ds_read_b64_tr_b16 a[210:211],v204 offset:53760\n"
"v_mfma_f32_16x16x32_bf16 v[20:23],a[236:239],a[116:119],v[20:23]\n"
"buffer_load_dwordx4 v215,s[92:95],s42 offen offset:2048 lds\n"
"v_mfma_f32_16x16x32_bf16 v[24:27],a[232:235],a[104:107],v[24:27]\n"
"ds_read_b64_tr_b16 a[212:213],v204 offset:57856\n"
"v_mfma_f32_16x16x32_bf16 v[24:27],a[236:239],a[120:123],v[24:27]\n"
"v_mfma_f32_16x16x32_bf16 v[28:31],a[232:235],a[108:111],v[28:31]\n"
"ds_read_b64_tr_b16 a[214:215],v204 offset:61952\n"
"v_mfma_f32_16x16x32_bf16 v[28:31],a[236:239],a[124:127],v[28:31]\n"
"s_waitcnt lgkmcnt(8)\n"
"v_mfma_f32_16x16x32_bf16 v[32:35],a[240:243],a[96:99],v[32:35]\n"
"s_add_u32 m0,s51,68608\n"
"s_add_u32 s42,s60,304128\n"
"ds_read_b64_tr_b16 a[216:217],v204 offset:49672\n"
"v_mfma_f32_16x16x32_bf16 v[32:35],a[244:247],a[112:115],v[32:35]\n"
"buffer_load_dwordx4 v213,s[92:95],s42 offen lds\n"
"v_mfma_f32_16x16x32_bf16 v[36:39],a[240:243],a[100:103],v[36:39]\n"
"ds_read_b64_tr_b16 a[218:219],v204 offset:53768\n"
"v_mfma_f32_16x16x32_bf16 v[36:39],a[244:247],a[116:119],v[36:39]\n"
"v_mfma_f32_16x16x32_bf16 v[40:43],a[240:243],a[104:107],v[40:43]\n"
"ds_read_b64_tr_b16 a[220:221],v204 offset:57864\n"
"v_mfma_f32_16x16x32_bf16 v[40:43],a[244:247],a[120:123],v[40:43]\n"
"buffer_load_dwordx4 v214,s[92:95],s42 offen offset:1024 lds\n"
"v_mfma_f32_16x16x32_bf16 v[44:47],a[240:243],a[108:111],v[44:47]\n"
"ds_read_b64_tr_b16 a[222:223],v204 offset:61960\n"
"v_mfma_f32_16x16x32_bf16 v[44:47],a[244:247],a[124:127],v[44:47]\n"
"s_waitcnt lgkmcnt(8)\n"
"v_mfma_f32_16x16x32_bf16 v[48:51],a[248:251],a[96:99],v[48:51]\n"
"ds_read_b64_tr_b16 a[224:225],v204 offset:49920\n"
"v_mfma_f32_16x16x32_bf16 v[48:51],a[252:255],a[112:115],v[48:51]\n"
"buffer_load_dwordx4 v215,s[92:95],s42 offen offset:2048 lds\n"
"v_mfma_f32_16x16x32_bf16 v[52:55],a[248:251],a[100:103],v[52:55]\n"
"ds_read_b64_tr_b16 a[226:227],v204 offset:54016\n"
"v_mfma_f32_16x16x32_bf16 v[52:55],a[252:255],a[116:119],v[52:55]\n"
"v_mfma_f32_16x16x32_bf16 v[56:59],a[248:251],a[104:107],v[56:59]\n"
"s_mov_b32 s55,0x40000\n"
"s_waitcnt vmcnt(6)\n"
"LM_POLL6:\n"
"v_cmp_ne_u64 vcc,v[226:227],v[224:225]\n"
"s_nop 1\n"
"s_and_b64 s[44:45],vcc,s[46:47]\n"
"s_cbranch_scc0 LM_POK7\n"
"s_sleep 1\n"
"buffer_load_dwordx2 v[226:227],v223,s[68:71],0 offen sc1\n"
"s_waitcnt vmcnt(0)\n"
"s_sub_u32 s55,s55,1\n"
"s_cbranch_scc0 LM_POLL6\n"
"LM_POK7:\n"
"s_mov_b32 s43,2359296\n"
"ds_read_b64_tr_b16 a[228:229],v204 offset:58112\n"
"v_mfma_f32_16x16x32_bf16 v[56:59],a[252:255],a[120:123],v[56:59]\n"
"buffer_load_dwordx4 a[128:131],v219,s[88:91],s43 offen sc1\n"
"v_mfma_f32_16x16x32_bf16 v[60:63],a[248:251],a[108:111],v[60:63]\n"
"ds_read_b64_tr_b16 a[230:231],v204 offset:62208\n"
"v_mfma_f32_16x16x32_bf16 v[60:63],a[252:255],a[124:127],v[60:63]\n"
"s_waitcnt lgkmcnt(8)\n"
"v_mfma_f32_16x16x32_bf16 v[64:67],a[208:211],a[96:99],v[64:67]\n"
"ds_read_b64_tr_b16 a[232:233],v204 offset:49928\n"
"v_mfma_f32_16x16x32_bf16 v[64:67],a[212:215],a[112:115],v[64:67]\n"
"ds_read_b64_tr_b16 a[234:235],v204 offset:54024\n"
"v_mfma_f32_16x16x32_bf16 v[68:71],a[208:211],a[100:103],v[68:71]\n"
"ds_read_b64_tr_b16 a[236:237],v204 offset:58120\n"
"v_mfma_f32_16x16x32_bf16 v[68:71],a[212:215],a[116:119],v[68:71]\n"
"ds_read_b64_tr_b16 a[238:239],v204 offset:62216\n"
"buffer_load_dwordx4 a[132:135],v228,s[88:91],s43 offen sc1\n"
"v_mfma_f32_16x16x32_bf16 v[72:75],a[208:211],a[104:107],v[72:75]\n"
"v_mfma_f32_16x16x32_bf16 v[72:75],a[212:215],a[120:123],v[72:75]\n"
"v_mfma_f32_16x16x32_bf16 v[76:79],a[208:211],a[108:111],v[76:79]\n"
"s_mov_b32 s43,2363392\n"
"v_mfma_f32_16x16x32_bf16 v[76:79],a[212:215],a[124:127],v[76:79]\n"
"buffer_load_dwordx4 a[136:139],v219,s[88:91],s43 offen sc1\n"
"s_waitcnt vmcnt(10) lgkmcnt(0)\n"
"s_barrier\n"
"v_mfma_f32_16x16x32_bf16 v[80:83],a[216:219],a[96:99],v[80:83]\n"
"ds_read_b64_tr_b16 a[240:241],v204\n"
"v_mfma_f32_16x16x32_bf16 v[80:83],a[220:223],a[112:115],v[80:83]\n"
"v_mfma_f32_16x16x32_bf16 v[84:87],a[216:219],a[100:103],v[84:87]\n"
"ds_read_b64_tr_b16 a[242:243],v204 offset:4096\n"
"v_mfma_f32_16x16x32_bf16 v[84:87],a[220:223],a[116:119],v[84:87]\n"
"buffer_load_dwordx4 a[140:143],v228,s[88:91],s43 offen sc1\n"
"v_mfma_f32_16x16x32_bf16 v[88:91],a[216:219],a[104:107],v[88:91]\n"
"ds_read_b64_tr_b16 a[244:245],v204 offset:8192\n"
"v_mfma_f32_16x16x32_bf16 v[88:91],a[220:223],a[120:123],v[88:91]\n"
"v_mfma_f32_16x16x32_bf16 v[92:95],a[216:219],a[108:111],v[92:95]\n"
"ds_read_b64_tr_b16 a[246:247],v204 offset:12288\n"
"v_mfma_f32_16x16x32_bf16 v[92:95],a[220:223],a[124:127],v[92:95]\n"
"v_mfma_f32_16x16x32_bf16 v[96:99],a[224:227],a[96:99],v[96:99]\n"
"s_mov_b32 s43,2367488\n"
"ds_read_b64_tr_b16 a[248:249],v204 offset:8\n"
"v_mfma_f32_16x16x32_bf16 v[96:99],a[228:231],a[112:115],v[96:99]\n"
"buffer_load_dwordx4 a[144:147],v219,s[88:91],s43 offen sc1\n"
"v_mfma_f32_16x16x32_bf16 v[100:103],a[224:227],a[100:103],v[100:103]\n"
"ds_read_b64_tr_b16 a[250:251],v204 offset:4104\n"
"v_mfma_f32_16x16x32_bf16 v[100:103],a[228:231],a[116:119],v[100:103]\n"
"v_mfma_f32_16x16x32_bf16 v[104:107],a[224:227],a[104:107],v[104:107]\n"
"ds_read_b64_tr_b16 a[252:253],v204 offset:8200\n"
"v_mfma_f32_16x16x32_bf16 v[104:107],a[228:231],a[120:123],v[104:107]\n"
"buffer_load_dwordx4 a[148:151],v228,s[88:91],s43 offen sc1\n"
"v_mfma_f32_16x16x32_bf16 v[108:111],a[224:227],a[108:111],v[108:111]\n"
"ds_read_b64_tr_b16 a[254:255],v204 offset:12296\n"
"v_mfma_f32_16x16x32_bf16 v[108:111],a[228:231],a[124:127],v[108:111]\n"
"v_mfma_f32_16x16x32_bf16 v[112:115],a[232:235],a[96:99],v[112:115]\n"
"s_mov_b32 s43,2371584\n"
"ds_read_b64_tr_b16 a[208:209],v204 offset:256\n"
"v_mfma_f32_16x16x32_bf16 v[112:115],a[236:239],a[112:115],v[112:115]\n"
"buffer_load_dwordx4 a[152:155],v219,s[88:91],s43 offen sc1\n"
"v_mfma_f32_16x16x32_bf16 v[116:119],a[232:235],a[100:103],v[116:119]\n"
"ds_read_b64_tr_b16 a[210:211],v204 offset:4352\n"
"v_mfma_f32_16x16x32_bf16 v[116:119],a[236:239],a[116:119],v[116:119]\n"
"v_mfma_f32_16x16x32_bf16 v[120:123],a[232:235],a[104:107],v[120:123]\n"
"ds_read_b64_tr_b16 a[212:213],v204 offset:8448\n"
"v_mfma_f32_16x16x32_bf16 v[120:123],a[236:239],a[120:123],v[120:123]\n"
"buffer_load_dwordx4 a[156:159],v228,s[88:91],s43 offen sc1\n"
"v_mfma_f32_16x16x32_bf16 v[124:127],a[232:235],a[108:111],v[124:127]\n"
"ds_read_b64_tr_b16 a[214:215],v204 offset:12544\n"
"v_mfma_f32_16x16x32_bf16 v[124:127],a[236:239],a[124:127],v[124:127]\n"
"s_waitcnt lgkmcnt(8)\n"
"v_mfma_f32_16x16x32_bf16 v[0:3],a[240:243],a[0:3],v[0:3]\n"
"ds_read_b64_tr_b16 a[216:217],v204 offset:264\n"
"v_mfma_f32_16x16x32_bf16 v[0:3],a[244:247],a[16:19],v[0:3]\n"
"v_mfma_f32_16x16x32_bf16 v[4:7],a[240:243],a[4:7],v[4:7]\n"
"s_mov_b32 s43,2375680\n"
"ds_read_b64_tr_b16 a[218:219],v204 offset:4360\n"
"v_mfma_f32_16x16x32_bf16 v[4:7],a[244:247],a[20:23],v[4:7]\n"
"buffer_load_dwordx2 a[160:161],v219,s[88:91],s43 offen sc1\n"
"v_mfma_f32_16x16x32_bf16 v[8:11],a[240:243],a[8:11],v[8:11]\n"
"ds_read_b64_tr_b16 a[220:221],v204 offset:8456\n"
"v_mfma_f32_16x16x32_bf16 v[8:11],a[244:247],a[24:27],v[8:11]\n"
"v_mfma_f32_16x16x32_bf16 v[12:15],a[240:243],a[12:15],v[12:15]\n"
"ds_read_b64_tr_b16 a[222:223],v204 offset:12552\n"
"v_mfma_f32_16x16x32_bf16 v[12:15],a[244:247],a[28:31],v[12:15]\n"
"buffer_load_dwordx2 a[162:163],v220,s[88:91],s43 offen offset:8 sc1\n"
"s_waitcnt lgkmcnt(8)\n"
"v_mfma_f32_16x16x32_bf16 v[16:19],a[248:251],a[0:3],v[16:19]\n"
"ds_read_b64_tr_b16 a[224:225],v204 offset:512\n"
"v_mfma_f32_16x16x32_bf16 v[16:19],a[252:255],a[16:19],v[16:19]\n"
"v_mfma_f32_16x16x32_bf16 v[20:23],a[248:251],a[4:7],v[20:23]\n"
"ds_read_b64_tr_b16 a[226:227],v204 offset:4608\n"
"v_mfma_f32_16x16x32_bf16 v[20:23],a[252:255],a[20:23],v[20:23]\n"
"buffer_load_dwordx2 a[164:165],v228,s[88:91],s43 offen sc1\n"
"v_mfma_f32_16x16x32_bf16 v[24:27],a[248:251],a[8:11],v[24:27]\n"
"ds_read_b64_tr_b16 a[228:229],v204 offset:8704\n"
"v_mfma_f32_16x16x32_bf16 v[24:27],a[252:255],a[24:27],v[24:27]\n"
"v_mfma_f32_16x16x32_bf16 v[28:31],a[248:251],a[12:15],v[28:31]\n"
"ds_read_b64_tr_b16 a[230:231],v204 offset:12800\n"
"v_mfma_f32_16x16x32_bf16 v[28:31],a[252:255],a[28:31],v[28:31]\n"
"buffer_load_dwordx2 a[166:167],v229,s[88:91],s43 offen offset:8 sc1\n"
"s_waitcnt lgkmcnt(8)\n"
"v_mfma_f32_16x16x32_bf16 v[32:35],a[208:211],a[0:3],v[32:35]\n"
"ds_read_b64_tr_b16 a[232:233],v204 offset:520\n"
"v_mul_f32 v0,s56,v0\n"
"v_mfma_f32_16x16x32_bf16 v[32:35],a[212:215],a[16:19],v[32:35]\n"
"v_mul_f32 v1,s56,v1\n"
"v_mul_f32 v2,s56,v2\n"
"v_mfma_f32_16x16x32_bf16 v[36:39],a[208:211],a[4:7],v[36:39]\n"
"s_mov_b32 s43,2379776\n"
"ds_read_b64_tr_b16 a[234:235],v204 offset:4616\n"
"v_mul_f32 v3,s56,v3\n"
"v_mfma_f32_16x16x32_bf16 v[36:39],a[212:215],a[20:23],v[36:39]\n"
"buffer_load_dwordx4 a[168:171],v220,s[88:91],s43 offen sc1\n"
"v_mfma_f32_16x16x32_bf16 v[40:43],a[208:211],a[8:11],v[40:43]\n"
"ds_read_b64_tr_b16 a[236:237],v204 offset:8712\n"
"v_mul_f32 v4,s56,v4\n"
"v_mfma_f32_16x16x32_bf16 v[40:43],a[212:215],a[24:27],v[40:43]\n"
"v_mul_f32 v5,s56,v5\n"
"v_mul_f32 v6,s56,v6\n"
"v_mfma_f32_16x16x32_bf16 v[44:47],a[208:211],a[12:15],v[44:47]\n"
"ds_read_b64_tr_b16 a[238:239],v204 offset:12808\n"
"v_mul_f32 v7,s56,v7\n"
"v_mfma_f32_16x16x32_bf16 v[44:47],a[212:215],a[28:31],v[44:47]\n"
"buffer_load_dwordx4 a[172:175],v229,s[88:91],s43 offen sc1\n"
"s_waitcnt lgkmcnt(8)\n"
"v_mfma_f32_16x16x32_bf16 v[48:51],a[216:219],a[0:3],v[48:51]\n"
"ds_read_b64_tr_b16 a[240:241],v204 offset:768\n"
"v_mul_f32 v8,s56,v8\n"
"v_mfma_f32_16x16x32_bf16 v[48:51],a[220:223],a[16:19],v[48:51]\n"
"v_mul_f32 v9,s56,v9\n"
"v_mul_f32 v10,s56,v10\n"
"v_mfma_f32_16x16x32_bf16 v[52:55],a[216:219],a[4:7],v[52:55]\n"
"s_mov_b32 s43,2383872\n"
"ds_read_b64_tr_b16 a[242:243],v204 offset:4864\n"
"v_mul_f32 v11,s56,v11\n"
"v_mfma_f32_16x16x32_bf16 v[52:55],a[220:223],a[20:23],v[52:55]\n"
"buffer_load_dwordx4 a[176:179],v220,s[88:91],s43 offen sc1\n"
"v_mfma_f32_16x16x32_bf16 v[56:59],a[216:219],a[8:11],v[56:59]\n"
"ds_read_b64_tr_b16 a[244:245],v204 offset:8960\n"
"v_mul_f32 v12,s56,v12\n"
"v_mfma_f32_16x16x32_bf16 v[56:59],a[220:223],a[24:27],v[56:59]\n"
"v_mul_f32 v13,s56,v13\n"
"v_mul_f32 v14,s56,v14\n"
"v_mfma_f32_16x16x32_bf16 v[60:63],a[216:219],a[12:15],v[60:63]\n"
"ds_read_b64_tr_b16 a[246:247],v204 offset:13056\n"
"v_mul_f32 v15,s56,v15\n"
"v_mfma_f32_16x16x32_bf16 v[60:63],a[220:223],a[28:31],v[60:63]\n"
"buffer_load_dwordx4 a[180:183],v229,s[88:91],s43 offen sc1\n"
"s_waitcnt lgkmcnt(8)\n"
"v_mfma_f32_16x16x32_bf16 v[64:67],a[224:227],a[0:3],v[64:67]\n"
"ds_read_b64_tr_b16 a[248:249],v204 offset:776\n"
"v_cvt_pk_bf16_f32 v128,v0,v1\n"
"v_mfma_f32_16x16x32_bf16 v[64:67],a[228:231],a[16:19],v[64:67]\n"
"ds_read_b64_tr_b16 a[250:251],v204 offset:4872\n"
"v_cvt_pk_bf16_f32 v129,v2,v3\n"
"v_mfma_f32_16x16x32_bf16 v[68:71],a[224:227],a[4:7],v[68:71]\n"
"s_mov_b32 s43,2387968\n"
"ds_read_b64_tr_b16 a[252:253],v204 offset:8968\n"
"v_cvt_pk_bf16_f32 v144,v4,v5\n"
"v_mfma_f32_16x16x32_bf16 v[68:71],a[228:231],a[20:23],v[68:71]\n"
"ds_read_b64_tr_b16 a[254:255],v204 offset:13064\n"
"buffer_load_dwordx4 a[184:187],v220,s[88:91],s43 offen sc1\n"
"v_mfma_f32_16x16x32_bf16 v[72:75],a[224:227],a[8:11],v[72:75]\n"
"v_cvt_pk_bf16_f32 v145,v6,v7\n"
"v_cvt_pk_bf16_f32 v160,v8,v9\n"
"v_mfma_f32_16x16x32_bf16 v[72:75],a[228:231],a[24:27],v[72:75]\n"
"v_cvt_pk_bf16_f32 v161,v10,v11\n"
"v_cvt_pk_bf16_f32 v176,v12,v13\n"
"v_mfma_f32_16x16x32_bf16 v[76:79],a[224:227],a[12:15],v[76:79]\n"
"v_cvt_pk_bf16_f32 v177,v14,v15\n"
"v_mul_f32 v16,s56,v16\n"
"v_mfma_f32_16x16x32_bf16 v[76:79],a[228:231],a[28:31],v[76:79]\n"
"buffer_load_dwordx4 a[188:191],v229,s[88:91],s43 offen sc1\n"
"s_waitcnt lgkmcnt(0)\n"
"s_barrier\n"
"v_mfma_f32_16x16x32_bf16 v[80:83],a[232:235],a[0:3],v[80:83]\n"
"v_mul_f32 v17,s56,v17\n"
"v_mul_f32 v18,s56,v18\n"
"v_mfma_f32_16x16x32_bf16 v[80:83],a[236:239],a[16:19],v[80:83]\n"
"v_mul_f32 v19,s56,v19\n"
"v_mul_f32 v20,s56,v20\n"
"v_mfma_f32_16x16x32_bf16 v[84:87],a[232:235],a[4:7],v[84:87]\n"
"s_add_u32 m0,s51,90112\n"
"s_add_u32 s42,s60,368640\n"
"v_mul_f32 v21,s56,v21\n"
"v_mul_f32 v22,s56,v22\n"
"v_mfma_f32_16x16x32_bf16 v[84:87],a[236:239],a[20:23],v[84:87]\n"
"buffer_load_dwordx4 v213,s[92:95],s42 offen lds\n"
"v_mfma_f32_16x16x32_bf16 v[88:91],a[232:235],a[8:11],v[88:91]\n"
"v_mul_f32 v23,s56,v23\n"
"v_mul_f32 v24,s56,v24\n"
"v_mfma_f32_16x16x32_bf16 v[88:91],a[236:239],a[24:27],v[88:91]\n"
"v_mul_f32 v25,s56,v25\n"
"v_mul_f32 v26,s56,v26\n"
"v_mfma_f32_16x16x32_bf16 v[92:95],a[232:235],a[12:15],v[92:95]\n"
"v_mul_f32 v27,s56,v27\n"
"v_mul_f32 v28,s56,v28\n"
"v_mfma_f32_16x16x32_bf16 v[92:95],a[236:239],a[28:31],v[92:95]\n"
"buffer_load_dwordx4 v214,s[92:95],s42 offen offset:1024 lds\n"
"v_mfma_f32_16x16x32_bf16 v[96:99],a[240:243],a[0:3],v[96:99]\n"
"v_mul_f32 v29,s56,v29\n"
"v_mul_f32 v30,s56,v30\n"
"v_mfma_f32_16x16x32_bf16 v[96:99],a[244:247],a[16:19],v[96:99]\n"
"v_mul_f32 v31,s56,v31\n"
"v_cvt_pk_bf16_f32 v130,v16,v17\n"
"v_mfma_f32_16x16x32_bf16 v[100:103],a[240:243],a[4:7],v[100:103]\n"
"v_cvt_pk_bf16_f32 v131,v18,v19\n"
"v_cvt_pk_bf16_f32 v146,v20,v21\n"
"v_mfma_f32_16x16x32_bf16 v[100:103],a[244:247],a[20:23],v[100:103]\n"
"buffer_load_dwordx4 v215,s[92:95],s42 offen offset:2048 lds\n"
"v_mfma_f32_16x16x32_bf16 v[104:107],a[240:243],a[8:11],v[104:107]\n"
"v_cvt_pk_bf16_f32 v147,v22,v23\n"
"v_cvt_pk_bf16_f32 v162,v24,v25\n"
"v_mfma_f32_16x16x32_bf16 v[104:107],a[244:247],a[24:27],v[104:107]\n"
"v_cvt_pk_bf16_f32 v163,v26,v27\n"
"v_cvt_pk_bf16_f32 v178,v28,v29\n"
"v_mfma_f32_16x16x32_bf16 v[108:111],a[240:243],a[12:15],v[108:111]\n"
"s_add_u32 m0,s51,93184\n"
"s_add_u32 s42,s60,377856\n"
"v_cvt_pk_bf16_f32 v179,v30,v31\n"
"v_mul_f32 v32,s56,v32\n"
"v_mfma_f32_16x16x32_bf16 v[108:111],a[244:247],a[28:31],v[108:111]\n"
"buffer_load_dwordx4 v213,s[92:95],s42 offen lds\n"
"v_mfma_f32_16x16x32_bf16 v[112:115],a[248:251],a[0:3],v[112:115]\n"
"v_mul_f32 v33,s56,v33\n"
"v_mul_f32 v34,s56,v34\n"
"v_mfma_f32_16x16x32_bf16 v[112:115],a[252:255],a[16:19],v[112:115]\n"
"v_mul_f32 v35,s56,v35\n"
"v_mul_f32 v36,s56,v36\n"
"v_mfma_f32_16x16x32_bf16 v[116:119],a[248:251],a[4:7],v[116:119]\n"
"v_mul_f32 v37,s56,v37\n"
"v_mul_f32 v38,s56,v38\n"
"v_mfma_f32_16x16x32_bf16 v[116:119],a[252:255],a[20:23],v[116:119]\n"
"buffer_load_dwordx4 v214,s[92:95],s42 offen offset:1024 lds\n"
"v_mfma_f32_16x16x32_bf16 v[120:123],a[248:251],a[8:11],v[120:123]\n"
"v_mul_f32 v39,s56,v39\n"
"v_mul_f32 v40,s56,v40\n"
"v_mfma_f32_16x16x32_bf16 v[120:123],a[252:255],a[24:27],v[120:123]\n"
"v_mul_f32 v41,s56,v41\n"
"v_mul_f32 v42,s56,v42\n"
"v_mfma_f32_16x16x32_bf16 v[124:127],a[248:251],a[12:15],v[124:127]\n"
"v_mul_f32 v43,s56,v43\n"
"v_mul_f32 v44,s56,v44\n"
"v_mfma_f32_16x16x32_bf16 v[124:127],a[252:255],a[28:31],v[124:127]\n"
"buffer_load_dwordx4 v215,s[92:95],s42 offen offset:2048 lds\n"
"s_add_u32 m0,s63,49152\n"
"s_add_u32 s41,s61,0\n"
"buffer_load_dwordx4 v202,s[80:83],s41 offen lds\n"
"buffer_load_dwordx4 v203,s[80:83],s41 offen offset:1024 lds\n"
"buffer_load_dwordx4 v202,s[80:83],s41 offen offset:2048 lds\n"
"buffer_load_dwordx4 v203,s[80:83],s41 offen offset:3072 lds\n"
"v_mul_f32 v45,s56,v45\n"
"v_mul_f32 v46,s56,v46\n"
"v_mul_f32 v47,s56,v47\n"
"v_cvt_pk_bf16_f32 v132,v32,v33\n"
"v_cvt_pk_bf16_f32 v133,v34,v35\n"
"v_cvt_pk_bf16_f32 v148,v36,v37\n"
"v_cvt_pk_bf16_f32 v149,v38,v39\n"
"v_cvt_pk_bf16_f32 v164,v40,v41\n"
"v_cvt_pk_bf16_f32 v165,v42,v43\n"
"v_cvt_pk_bf16_f32 v180,v44,v45\n"
"v_cvt_pk_bf16_f32 v181,v46,v47\n"
"ds_read_b64_tr_b16 a[208:209],v236\n"
"ds_read_b64_tr_b16 a[210:211],v236 offset:6144\n"
"ds_read_b64_tr_b16 a[212:213],v236 offset:12288\n"
"ds_read_b64_tr_b16 a[214:215],v236 offset:18432\n"
"ds_read_b64_tr_b16 a[216:217],v237\n"
"ds_read_b64_tr_b16 a[218:219],v237 offset:6144\n"
"ds_read_b64_tr_b16 a[220:221],v237 offset:12288\n"
"ds_read_b64_tr_b16 a[222:223],v237 offset:18432\n"
"ds_read_b64_tr_b16 a[224:225],v236 offset:256\n"
"ds_read_b64_tr_b16 a[226:227],v236 offset:6400\n"
"ds_read_b64_tr_b16 a[228:229],v236 offset:12544\n"
"ds_read_b64_tr_b16 a[230:231],v236 offset:18688\n"
"ds_read_b64_tr_b16 a[232:233],v237 offset:256\n"
"ds_read_b64_tr_b16 a[234:235],v237 offset:6400\n"
"ds_read_b64_tr_b16 a[236:237],v237 offset:12544\n"
"ds_read_b64_tr_b16 a[238:239],v237 offset:18688\n"
"s_waitcnt vmcnt(24) lgkmcnt(12)\n"
"v_mfma_f32_16x16x32_bf16 v[0:3],a[208:211],a[128:131],0\n"
"ds_read_b64_tr_b16 a[240:241],v236 offset:512\n"
"v_mul_f32 v48,s56,v48\n"
"v_mfma_f32_16x16x32_bf16 v[0:3],a[212:215],a[136:139],v[0:3]\n"
"ds_read_b64_tr_b16 a[242:243],v236 offset:6656\n"
"s_mov_b32 s43,2392064\n"
"buffer_load_dwordx4 a[192:195],v220,s[88:91],s43 offen sc1\n"
"v_mfma_f32_16x16x32_bf16 v[4:7],a[208:211],a[132:135],0\n"
"ds_read_b64_tr_b16 a[244:245],v236 offset:12800\n"
"v_mul_f32 v49,s56,v49\n"
"v_mfma_f32_16x16x32_bf16 v[4:7],a[212:215],a[140:143],v[4:7]\n"
"ds_read_b64_tr_b16 a[246:247],v236 offset:18944\n"
"buffer_load_dwordx4 a[196:199],v229,s[88:91],s43 offen sc1\n"
"s_waitcnt lgkmcnt(12)\n"
"v_mfma_f32_16x16x32_bf16 v[8:11],a[216:219],a[128:131],0\n"
"ds_read_b64_tr_b16 a[248:249],v237 offset:512\n"
"v_mul_f32 v50,s56,v50\n"
"v_mfma_f32_16x16x32_bf16 v[8:11],a[220:223],a[136:139],v[8:11]\n"
"ds_read_b64_tr_b16 a[250:251],v237 offset:6656\n"
"s_mov_b32 s43,2396160\n"
"buffer_load_dwordx4 a[32:35],v221,s[88:91],s43 offen sc1\n"
"v_mfma_f32_16x16x32_bf16 v[12:15],a[216:219],a[132:135],0\n"
"ds_read_b64_tr_b16 a[252:253],v237 offset:12800\n"
"v_mul_f32 v51,s56,v51\n"
"v_mfma_f32_16x16x32_bf16 v[12:15],a[220:223],a[140:143],v[12:15]\n"
"ds_read_b64_tr_b16 a[254:255],v237 offset:18944\n"
"buffer_load_dwordx4 a[36:39],v230,s[88:91],s43 offen sc1\n"
"s_waitcnt lgkmcnt(12)\n"
"v_mfma_f32_16x16x32_bf16 v[16:19],a[224:227],a[128:131],0\n"
"ds_read_b64_tr_b16 a[208:209],v236 offset:24576\n"
"v_mul_f32 v52,s56,v52\n"
"v_mfma_f32_16x16x32_bf16 v[16:19],a[228:231],a[136:139],v[16:19]\n"
"ds_read_b64_tr_b16 a[210:211],v236 offset:30720\n"
"v_mul_f32 v53,s56,v53\n"
"v_mfma_f32_16x16x32_bf16 v[20:23],a[224:227],a[132:135],0\n"
"ds_read_b64_tr_b16 a[212:213],v236 offset:36864\n"
"v_mul_f32 v54,s56,v54\n"
"v_mfma_f32_16x16x32_bf16 v[20:23],a[228:231],a[140:143],v[20:23]\n"
"ds_read_b64_tr_b16 a[214:215],v236 offset:43008\n"
"v_mul_f32 v55,s56,v55\n"
"s_waitcnt lgkmcnt(12)\n"
"v_mfma_f32_16x16x32_bf16 v[24:27],a[232:235],a[128:131],0\n"
"ds_read_b64_tr_b16 a[216:217],v237 offset:24576\n"
"v_mul_f32 v56,s56,v56\n"
"v_mfma_f32_16x16x32_bf16 v[24:27],a[236:239],a[136:139],v[24:27]\n"
"ds_read_b64_tr_b16 a[218:219],v237 offset:30720\n"
"v_mul_f32 v57,s56,v57\n"
"v_mfma_f32_16x16x32_bf16 v[28:31],a[232:235],a[132:135],0\n"
"ds_read_b64_tr_b16 a[220:221],v237 offset:36864\n"
"v_mul_f32 v58,s56,v58\n"
"v_mfma_f32_16x16x32_bf16 v[28:31],a[236:239],a[140:143],v[28:31]\n"
"ds_read_b64_tr_b16 a[222:223],v237 offset:43008\n"
"v_mul_f32 v59,s56,v59\n"
"s_waitcnt lgkmcnt(12)\n"
"v_mfma_f32_16x16x32_bf16 v[32:35],a[240:243],a[128:131],0\n"
"ds_read_b64_tr_b16 a[224:225],v236 offset:24832\n"
"v_mul_f32 v60,s56,v60\n"
"v_mfma_f32_16x16x32_bf16 v[32:35],a[244:247],a[136:139],v[32:35]\n"
"ds_read_b64_tr_b16 a[226:227],v236 offset:30976\n"
"v_mul_f32 v61,s56,v61\n"
"v_mfma_f32_16x16x32_bf16 v[36:39],a[240:243],a[132:135],0\n"
"ds_read_b64_tr_b16 a[228:229],v236 offset:37120\n"
"v_mul_f32 v62,s56,v62\n"
"v_mfma_f32_16x16x32_bf16 v[36:39],a[244:247],a[140:143],v[36:39]\n"
"ds_read_b64_tr_b16 a[230:231],v236 offset:43264\n"
"v_mul_f32 v63,s56,v63\n"
"s_waitcnt lgkmcnt(12)\n"
"v_mfma_f32_16x16x32_bf16 v[40:43],a[248:251],a[128:131],0\n"
"ds_read_b64_tr_b16 a[232:233],v237 offset:24832\n"
"v_cvt_pk_bf16_f32 v134,v48,v49\n"
"v_mfma_f32_16x16x32_bf16 v[40:43],a[252:255],a[136:139],v[40:43]\n"
"ds_read_b64_tr_b16 a[234:235],v237 offset:30976\n"
"v_cvt_pk_bf16_f32 v135,v50,v51\n"
"v_mfma_f32_16x16x32_bf16 v[44:47],a[248:251],a[132:135],0\n"
"ds_read_b64_tr_b16 a[236:237],v237 offset:37120\n"
"v_cvt_pk_bf16_f32 v150,v52,v53\n"
"v_mfma_f32_16x16x32_bf16 v[44:47],a[252:255],a[140:143],v[44:47]\n"
"ds_read_b64_tr_b16 a[238:239],v237 offset:43264\n"
"v_cvt_pk_bf16_f32 v151,v54,v55\n"
"s_waitcnt vmcnt(24) lgkmcnt(12)\n"
"v_mfma_f32_16x16x32_bf16 v[0:3],a[208:211],a[144:147],v[0:3]\n"
"ds_read_b64_tr_b16 a[240:241],v236 offset:25088\n"
"v_cvt_pk_bf16_f32 v166,v56,v57\n"
"v_mfma_f32_16x16x32_bf16 v[0:3],a[212:215],a[152:155],v[0:3]\n"
"ds_read_b64_tr_b16 a[242:243],v236 offset:31232\n"
"s_mov_b32 s43,2400256\n"
"buffer_load_dwordx4 a[40:43],v221,s[88:91],s43 offen sc1\n"
"v_mfma_f32_16x16x32_bf16 v[4:7],a[208:211],a[148:151],v[4:7]\n"
"ds_read_b64_tr_b16 a[244:245],v236 offset:37376\n"
"v_cvt_pk_bf16_f32 v167,v58,v59\n"
"v_mfma_f32_16x16x32_bf16 v[4:7],a[212:215],a[156:159],v[4:7]\n"
"ds_read_b64_tr_b16 a[246:247],v236 offset:43520\n"
"buffer_load_dwordx4 a[44:47],v230,s[88:91],s43 offen sc1\n"
"s_waitcnt lgkmcnt(12)\n"
"v_mfma_f32_16x16x32_bf16 v[8:11],a[216:219],a[144:147],v[8:11]\n"
"ds_read_b64_tr_b16 a[248:249],v237 offset:25088\n"
"v_cvt_pk_bf16_f32 v182,v60,v61\n"
"v_mfma_f32_16x16x32_bf16 v[8:11],a[220:223],a[152:155],v[8:11]\n"
"ds_read_b64_tr_b16 a[250:251],v237 offset:31232\n"
"s_mov_b32 s43,2404352\n"
"buffer_load_dwordx4 a[48:51],v221,s[88:91],s43 offen sc1\n"
"v_mfma_f32_16x16x32_bf16 v[12:15],a[216:219],a[148:151],v[12:15]\n"
"ds_read_b64_tr_b16 a[252:253],v237 offset:37376\n"
"v_cvt_pk_bf16_f32 v183,v62,v63\n"
"v_mfma_f32_16x16x32_bf16 v[12:15],a[220:223],a[156:159],v[12:15]\n"
"ds_read_b64_tr_b16 a[254:255],v237 offset:43520\n"
"buffer_load_dwordx4 a[52:55],v230,s[88:91],s43 offen sc1\n"
"s_waitcnt lgkmcnt(12)\n"
"s_barrier\n"
"v_mfma_f32_16x16x32_bf16 v[16:19],a[224:227],a[144:147],v[16:19]\n"
"ds_read_b64_tr_b16 a[208:209],v240\n"
"v_mul_f32 v64,s56,v64\n"
"v_mfma_f32_16x16x32_bf16 v[16:19],a[228:231],a[152:155],v[16:19]\n"
"ds_read_b64_tr_b16 a[210:211],v240 offset:6144\n"
"s_add_u32 m0,s51,0\n"
"s_add_u32 s42,s60,442368\n"
"buffer_load_dwordx4 v213,s[92:95],s42 offen lds\n"
"v_mfma_f32_16x16x32_bf16 v[20:23],a[224:227],a[148:151],v[20:23]\n"
"ds_read_b64_tr_b16 a[212:213],v240 offset:12288\n"
"v_mul_f32 v65,s56,v65\n"
"v_mfma_f32_16x16x32_bf16 v[20:23],a[228:231],a[156:159],v[20:23]\n"
"ds_read_b64_tr_b16 a[214:215],v240 offset:18432\n"
"buffer_load_dwordx4 v214,s[92:95],s42 offen offset:1024 lds\n"
"s_waitcnt lgkmcnt(12)\n"
"v_mfma_f32_16x16x32_bf16 v[24:27],a[232:235],a[144:147],v[24:27]\n"
"ds_read_b64_tr_b16 a[216:217],v241\n"
"v_mul_f32 v66,s56,v66\n"
"v_mfma_f32_16x16x32_bf16 v[24:27],a[236:239],a[152:155],v[24:27]\n"
"ds_read_b64_tr_b16 a[218:219],v241 offset:6144\n"
"buffer_load_dwordx4 v215,s[92:95],s42 offen offset:2048 lds\n"
"v_mfma_f32_16x16x32_bf16 v[28:31],a[232:235],a[148:151],v[28:31]\n"
"ds_read_b64_tr_b16 a[220:221],v241 offset:12288\n"
"v_mul_f32 v67,s56,v67\n"
"v_mfma_f32_16x16x32_bf16 v[28:31],a[236:239],a[156:159],v[28:31]\n"
"ds_read_b64_tr_b16 a[222:223],v241 offset:18432\n"
"s_add_u32 m0,s51,3072\n"
"s_add_u32 s42,s60,451584\n"
"buffer_load_dwordx4 v213,s[92:95],s42 offen lds\n"
"s_waitcnt lgkmcnt(12)\n"
"v_mfma_f32_16x16x32_bf16 v[32:35],a[240:243],a[144:147],v[32:35]\n"
"ds_read_b64_tr_b16 a[224:225],v240 offset:256\n"
"v_mul_f32 v68,s56,v68\n"
"v_mfma_f32_16x16x32_bf16 v[32:35],a[244:247],a[152:155],v[32:35]\n"
"ds_read_b64_tr_b16 a[226:227],v240 offset:6400\n"
"buffer_load_dwordx4 v214,s[92:95],s42 offen offset:1024 lds\n"
"v_mfma_f32_16x16x32_bf16 v[36:39],a[240:243],a[148:151],v[36:39]\n"
"ds_read_b64_tr_b16 a[228:229],v240 offset:12544\n"
"v_mul_f32 v69,s56,v69\n"
"v_mfma_f32_16x16x32_bf16 v[36:39],a[244:247],a[156:159],v[36:39]\n"
"ds_read_b64_tr_b16 a[230:231],v240 offset:18688\n"
"buffer_load_dwordx4 v215,s[92:95],s42 offen offset:2048 lds\n"
"s_waitcnt lgkmcnt(12)\n"
"v_mfma_f32_16x16x32_bf16 v[40:43],a[248:251],a[144:147],v[40:43]\n"
"ds_read_b64_tr_b16 a[232:233],v241 offset:256\n"
"v_mul_f32 v70,s56,v70\n"
"v_mfma_f32_16x16x32_bf16 v[40:43],a[252:255],a[152:155],v[40:43]\n"
"ds_read_b64_tr_b16 a[234:235],v241 offset:6400\n"
"v_mul_f32 v71,s56,v71\n"
"v_mfma_f32_16x16x32_bf16 v[44:47],a[248:251],a[148:151],v[44:47]\n"
"ds_read_b64_tr_b16 a[236:237],v241 offset:12544\n"
"v_mul_f32 v72,s56,v72\n"
"v_mfma_f32_16x16x32_bf16 v[44:47],a[252:255],a[156:159],v[44:47]\n"
"ds_read_b64_tr_b16 a[238:239],v241 offset:18688\n"
"v_mul_f32 v73,s56,v73\n"
"s_waitcnt vmcnt(28) lgkmcnt(12)\n"
"v_mfma_f32_16x16x32_bf16 v[0:3],a[208:211],a[160:163],v[0:3]\n"
"ds_read_b64_tr_b16 a[240:241],v240 offset:512\n"
"v_mul_f32 v74,s56,v74\n"
"v_mfma_f32_16x16x32_bf16 v[0:3],a[212:215],a[168:171],v[0:3]\n"
"ds_read_b64_tr_b16 a[242:243],v240 offset:6656\n"
"s_mov_b32 s43,2408448\n"
"buffer_load_dwordx4 a[56:59],v221,s[88:91],s43 offen sc1\n"
"v_mfma_f32_16x16x32_bf16 v[4:7],a[208:211],a[164:167],v[4:7]\n"
"ds_read_b64_tr_b16 a[244:245],v240 offset:12800\n"
"v_mul_f32 v75,s56,v75\n"
"v_mfma_f32_16x16x32_bf16 v[4:7],a[212:215],a[172:175],v[4:7]\n"
"ds_read_b64_tr_b16 a[246:247],v240 offset:18944\n"
"buffer_load_dwordx4 a[60:63],v230,s[88:91],s43 offen sc1\n"
"s_waitcnt lgkmcnt(12)\n"
"v_mfma_f32_16x16x32_bf16 v[8:11],a[216:219],a[160:163],v[8:11]\n"
"ds_read_b64_tr_b16 a[248:249],v241 offset:512\n"
"v_mul_f32 v76,s56,v76\n"
"v_mfma_f32_16x16x32_bf16 v[8:11],a[220:223],a[168:171],v[8:11]\n"
"ds_read_b64_tr_b16 a[250:251],v241 offset:6656\n"
"s_mov_b32 s43,2412544\n"
"buffer_load_dwordx2 a[64:65],v221,s[88:91],s43 offen sc1\n"
"v_mfma_f32_16x16x32_bf16 v[12:15],a[216:219],a[164:167],v[12:15]\n"
"ds_read_b64_tr_b16 a[252:253],v241 offset:12800\n"
"v_mul_f32 v77,s56,v77\n"
"v_mfma_f32_16x16x32_bf16 v[12:15],a[220:223],a[172:175],v[12:15]\n"
"ds_read_b64_tr_b16 a[254:255],v241 offset:18944\n"
"buffer_load_dwordx2 a[66:67],v222,s[88:91],s43 offen offset:8 sc1\n"
"s_waitcnt lgkmcnt(12)\n"
"s_barrier\n"
"v_mfma_f32_16x16x32_bf16 v[16:19],a[224:227],a[160:163],v[16:19]\n"
"ds_read_b64_tr_b16 a[208:209],v240 offset:24576\n"
"v_mul_f32 v78,s56,v78\n"
"v_mfma_f32_16x16x32_bf16 v[16:19],a[228:231],a[168:171],v[16:19]\n"
"ds_read_b64_tr_b16 a[210:211],v240 offset:30720\n"
"buffer_load_dwordx2 a[68:69],v230,s[88:91],s43 offen sc1\n"
"v_mfma_f32_16x16x32_bf16 v[20:23],a[224:227],a[164:167],v[20:23]\n"
"ds_read_b64_tr_b16 a[212:213],v240 offset:36864\n"
"v_mul_f32 v79,s56,v79\n"
"v_mfma_f32_16x16x32_bf16 v[20:23],a[228:231],a[172:175],v[20:23]\n"
"ds_read_b64_tr_b16 a[214:215],v240 offset:43008\n"
"buffer_load_dwordx2 a[70:71],v231,s[88:91],s43 offen offset:8 sc1\n"
"s_waitcnt lgkmcnt(12)\n"
"v_mfma_f32_16x16x32_bf16 v[24:27],a[232:235],a[160:163],v[24:27]\n"
"ds_read_b64_tr_b16 a[216:217],v241 offset:24576\n"
"v_cvt_pk_bf16_f32 v136,v64,v65\n"
"v_mfma_f32_16x16x32_bf16 v[24:27],a[236:239],a[168:171],v[24:27]\n"
"ds_read_b64_tr_b16 a[218:219],v241 offset:30720\n"
"s_add_u32 m0,s51,24576\n"
"s_add_u32 s42,s60,516096\n"
"buffer_load_dwordx4 v213,s[92:95],s42 offen lds\n"
"v_mfma_f32_16x16x32_bf16 v[28:31],a[232:235],a[164:167],v[28:31]\n"
"ds_read_b64_tr_b16 a[220:221],v241 offset:36864\n"
"v_cvt_pk_bf16_f32 v137,v66,v67\n"
"v_mfma_f32_16x16x32_bf16 v[28:31],a[236:239],a[172:175],v[28:31]\n"
"ds_read_b64_tr_b16 a[222:223],v241 offset:43008\n"
"buffer_load_dwordx4 v214,s[92:95],s42 offen offset:1024 lds\n"
"s_waitcnt lgkmcnt(12)\n"
"v_mfma_f32_16x16x32_bf16 v[32:35],a[240:243],a[160:163],v[32:35]\n"
"ds_read_b64_tr_b16 a[224:225],v240 offset:24832\n"
"v_cvt_pk_bf16_f32 v152,v68,v69\n"
"v_mfma_f32_16x16x32_bf16 v[32:35],a[244:247],a[168:171],v[32:35]\n"
"ds_read_b64_tr_b16 a[226:227],v240 offset:30976\n"
"buffer_load_dwordx4 v215,s[92:95],s42 offen offset:2048 lds\n"
"v_mfma_f32_16x16x32_bf16 v[36:39],a[240:243],a[164:167],v[36:39]\n"
"ds_read_b64_tr_b16 a[228:229],v240 offset:37120\n"
"v_cvt_pk_bf16_f32 v153,v70,v71\n"
"v_mfma_f32_16x16x32_bf16 v[36:39],a[244:247],a[172:175],v[36:39]\n"
"ds_read_b64_tr_b16 a[230:231],v240 offset:43264\n"
"s_add_u32 m0,s51,27648\n"
"s_add_u32 s42,s60,525312\n"
"buffer_load_dwordx4 v213,s[92:95],s42 offen lds\n"
"s_waitcnt lgkmcnt(12)\n"
"v_mfma_f32_16x16x32_bf16 v[40:43],a[248:251],a[160:163],v[40:43]\n"
"ds_read_b64_tr_b16 a[232:233],v241 offset:24832\n"
"v_cvt_pk_bf16_f32 v168,v72,v73\n"
"v_mfma_f32_16x16x32_bf16 v[40:43],a[252:255],a[168:171],v[40:43]\n"
"ds_read_b64_tr_b16 a[234:235],v241 offset:30976\n"
"buffer_load_dwordx4 v214,s[92:95],s42 offen offset:1024 lds\n"
"v_mfma_f32_16x16x32_bf16 v[44:47],a[248:251],a[164:167],v[44:47]\n"
"ds_read_b64_tr_b16 a[236:237],v241 offset:37120\n"
"v_cvt_pk_bf16_f32 v169,v74,v75\n"
"v_mfma_f32_16x16x32_bf16 v[44:47],a[252:255],a[172:175],v[44:47]\n"
"ds_read_b64_tr_b16 a[238:239],v241 offset:43264\n"
"buffer_load_dwordx4 v215,s[92:95],s42 offen offset:2048 lds\n"
"s_waitcnt vmcnt(36) lgkmcnt(12)\n"
"v_mfma_f32_16x16x32_bf16 v[0:3],a[208:211],a[176:179],v[0:3]\n"
"ds_read_b64_tr_b16 a[240:241],v240 offset:25088\n"
"v_cvt_pk_bf16_f32 v184,v76,v77\n"
"v_mfma_f32_16x16x32_bf16 v[0:3],a[212:215],a[184:187],v[0:3]\n"
"ds_read_b64_tr_b16 a[242:243],v240 offset:31232\n"
"s_mov_b32 s43,2416640\n"
"buffer_load_dwordx4 a[72:75],v222,s[88:91],s43 offen sc1\n"
"v_mfma_f32_16x16x32_bf16 v[4:7],a[208:211],a[180:183],v[4:7]\n"
"ds_read_b64_tr_b16 a[244:245],v240 offset:37376\n"
"v_cvt_pk_bf16_f32 v185,v78,v79\n"
"v_mfma_f32_16x16x32_bf16 v[4:7],a[212:215],a[188:191],v[4:7]\n"
"ds_read_b64_tr_b16 a[246:247],v240 offset:43520\n"
"buffer_load_dwordx4 a[76:79],v231,s[88:91],s43 offen sc1\n"
"s_waitcnt lgkmcnt(12)\n"
"v_mfma_f32_16x16x32_bf16 v[8:11],a[216:219],a[176:179],v[8:11]\n"
"ds_read_b64_tr_b16 a[248:249],v241 offset:25088\n"
"v_mul_f32 v80,s56,v80\n"
"v_mfma_f32_16x16x32_bf16 v[8:11],a[220:223],a[184:187],v[8:11]\n"
"ds_read_b64_tr_b16 a[250:251],v241 offset:31232\n"
"s_mov_b32 s43,2420736\n"
"buffer_load_dwordx4 a[80:83],v222,s[88:91],s43 offen sc1\n"
"v_mfma_f32_16x16x32_bf16 v[12:15],a[216:219],a[180:183],v[12:15]\n"
"ds_read_b64_tr_b16 a[252:253],v241 offset:37376\n"
"v_mul_f32 v81,s56,v81\n"
"v_mfma_f32_16x16x32_bf16 v[12:15],a[220:223],a[188:191],v[12:15]\n"
"ds_read_b64_tr_b16 a[254:255],v241 offset:43520\n"
"buffer_load_dwordx4 a[84:87],v231,s[88:91],s43 offen sc1\n"
"s_waitcnt vmcnt(58) lgkmcnt(12)\n"
"s_barrier\n"
"v_mfma_f32_16x16x32_bf16 v[16:19],a[224:227],a[176:179],v[16:19]\n"
"ds_read_b64_tr_b16 a[208:209],v238 offset:16384\n"
"v_mul_f32 v82,s56,v82\n"
"v_mfma_f32_16x16x32_bf16 v[16:19],a[228:231],a[184:187],v[16:19]\n"
"ds_read_b64_tr_b16 a[210:211],v238 offset:22528\n"
"s_add_u32 m0,s51,114688\n"
"s_add_u32 s42,s60,589824\n"
"buffer_load_dwordx4 v213,s[92:95],s42 offen lds\n"
"v_mfma_f32_16x16x32_bf16 v[20:23],a[224:227],a[180:183],v[20:23]\n"
"ds_read_b64_tr_b16 a[212:213],v238 offset:28672\n"
"v_mul_f32 v83,s56,v83\n"
"v_mfma_f32_16x16x32_bf16 v[20:23],a[228:231],a[188:191],v[20:23]\n"
"ds_read_b64_tr_b16 a[214:215],v238 offset:34816\n"
"buffer_load_dwordx4 v214,s[92:95],s42 offen offset:1024 lds\n"
"s_waitcnt lgkmcnt(12)\n"
"v_mfma_f32_16x16x32_bf16 v[24:27],a[232:235],a[176:179],v[24:27]\n"
"ds_read_b64_tr_b16 a[216:217],v239 offset:16384\n"
"v_mul_f32 v84,s56,v84\n"
"v_mfma_f32_16x16x32_bf16 v[24:27],a[236:239],a[184:187],v[24:27]\n"
"ds_read_b64_tr_b16 a[218:219],v239 offset:22528\n"
"buffer_load_dwordx4 v215,s[92:95],s42 offen offset:2048 lds\n"
"v_mfma_f32_16x16x32_bf16 v[28:31],a[232:235],a[180:183],v[28:31]\n"
"ds_read_b64_tr_b16 a[220:221],v239 offset:28672\n"
"v_mul_f32 v85,s56,v85\n"
"v_mfma_f32_16x16x32_bf16 v[28:31],a[236:239],a[188:191],v[28:31]\n"
"ds_read_b64_tr_b16 a[222:223],v239 offset:34816\n"
"s_add_u32 m0,s51,117760\n"
"s_add_u32 s42,s60,599040\n"
"buffer_load_dwordx4 v213,s[92:95],s42 offen lds\n"
"s_waitcnt lgkmcnt(12)\n"
"v_mfma_f32_16x16x32_bf16 v[32:35],a[240:243],a[176:179],v[32:35]\n"
"ds_read_b64_tr_b16 a[224:225],v238 offset:16640\n"
"v_mul_f32 v86,s56,v86\n"
"v_mfma_f32_16x16x32_bf16 v[32:35],a[244:247],a[184:187],v[32:35]\n"
"ds_read_b64_tr_b16 a[226:227],v238 offset:22784\n"
"buffer_load_dwordx4 v214,s[92:95],s42 offen offset:1024 lds\n"
"v_mfma_f32_16x16x32_bf16 v[36:39],a[240:243],a[180:183],v[36:39]\n"
"ds_read_b64_tr_b16 a[228:229],v238 offset:28928\n"
"v_mul_f32 v87,s56,v87\n"
"v_mfma_f32_16x16x32_bf16 v[36:39],a[244:247],a[188:191],v[36:39]\n"
"ds_read_b64_tr_b16 a[230:231],v238 offset:35072\n"
"buffer_load_dwordx4 v215,s[92:95],s42 offen offset:2048 lds\n"
"s_waitcnt lgkmcnt(12)\n"
"v_mfma_f32_16x16x32_bf16 v[40:43],a[248:251],a[176:179],v[40:43]\n"
"ds_read_b64_tr_b16 a[232:233],v239 offset:16640\n"
"v_mul_f32 v88,s56,v88\n"
"v_mfma_f32_16x16x32_bf16 v[40:43],a[252:255],a[184:187],v[40:43]\n"
"ds_read_b64_tr_b16 a[234:235],v239 offset:22784\n"
"v_mul_f32 v89,s56,v89\n"
"v_mfma_f32_16x16x32_bf16 v[44:47],a[248:251],a[180:183],v[44:47]\n"
"ds_read_b64_tr_b16 a[236:237],v239 offset:28928\n"
"v_mul_f32 v90,s56,v90\n"
"v_mfma_f32_16x16x32_bf16 v[44:47],a[252:255],a[188:191],v[44:47]\n"
"ds_read_b64_tr_b16 a[238:239],v239 offset:35072\n"
"v_mul_f32 v91,s56,v91\n"
"s_waitcnt vmcnt(32) lgkmcnt(12)\n"
"v_mfma_f32_16x16x32_bf16 v[0:3],a[208:211],a[192:195],v[0:3]\n"
"ds_read_b64_tr_b16 a[240:241],v238 offset:16896\n"
"v_mul_f32 v92,s56,v92\n"
"v_mfma_f32_16x16x32_bf16 v[0:3],a[212:215],a[32:35],v[0:3]\n"
"ds_read_b64_tr_b16 a[242:243],v238 offset:23040\n"
"s_mov_b32 s43,2424832\n"
"buffer_load_dwordx4 a[88:91],v222,s[88:91],s43 offen sc1\n"
"v_mfma_f32_16x16x32_bf16 v[4:7],a[208:211],a[196:199],v[4:7]\n"
"ds_read_b64_tr_b16 a[244:245],v238 offset:29184\n"
"v_mul_f32 v93,s56,v93\n"
"v_mfma_f32_16x16x32_bf16 v[4:7],a[212:215],a[36:39],v[4:7]\n"
"ds_read_b64_tr_b16 a[246:247],v238 offset:35328\n"
"buffer_load_dwordx4 a[92:95],v231,s[88:91],s43 offen sc1\n"
"s_waitcnt lgkmcnt(12)\n"
"v_mfma_f32_16x16x32_bf16 v[8:11],a[216:219],a[192:195],v[8:11]\n"
"ds_read_b64_tr_b16 a[248:249],v239 offset:16896\n"
"v_mul_f32 v94,s56,v94\n"
"v_mfma_f32_16x16x32_bf16 v[8:11],a[220:223],a[32:35],v[8:11]\n"
"ds_read_b64_tr_b16 a[250:251],v239 offset:23040\n"
"s_mov_b32 s43,2428928\n"
"buffer_load_dwordx4 a[96:99],v222,s[88:91],s43 offen sc1\n"
"v_mfma_f32_16x16x32_bf16 v[12:15],a[216:219],a[196:199],v[12:15]\n"
"ds_read_b64_tr_b16 a[252:253],v239 offset:29184\n"
"v_mul_f32 v95,s56,v95\n"
"v_mfma_f32_16x16x32_bf16 v[12:15],a[220:223],a[36:39],v[12:15]\n"
"ds_read_b64_tr_b16 a[254:255],v239 offset:35328\n"
"buffer_load_dwordx4 a[100:103],v231,s[88:91],s43 offen sc1\n"
"s_waitcnt vmcnt(44) lgkmcnt(12)\n"
"s_barrier\n"
"v_mfma_f32_16x16x32_bf16 v[16:19],a[224:227],a[192:195],v[16:19]\n"
"ds_read_b64_tr_b16 a[208:209],v238 offset:40960\n"
"v_cvt_pk_bf16_f32 v138,v80,v81\n"
"v_mfma_f32_16x16x32_bf16 v[16:19],a[228:231],a[32:35],v[16:19]\n"
"ds_read_b64_tr_b16 a[210:211],v238 offset:47104\n"
"v_cvt_pk_bf16_f32 v139,v82,v83\n"
"v_mfma_f32_16x16x32_bf16 v[20:23],a[224:227],a[196:199],v[20:23]\n"
"ds_read_b64_tr_b16 a[212:213],v238 offset:53248\n"
"v_cvt_pk_bf16_f32 v154,v84,v85\n"
"v_mfma_f32_16x16x32_bf16 v[20:23],a[228:231],a[36:39],v[20:23]\n"
"ds_read_b64_tr_b16 a[214:215],v238 offset:59392\n"
"v_cvt_pk_bf16_f32 v155,v86,v87\n"
"s_waitcnt lgkmcnt(12)\n"
"v_mfma_f32_16x16x32_bf16 v[24:27],a[232:235],a[192:195],v[24:27]\n"
"ds_read_b64_tr_b16 a[216:217],v239 offset:40960\n"
"v_cvt_pk_bf16_f32 v170,v88,v89\n"
"v_mfma_f32_16x16x32_bf16 v[24:27],a[236:239],a[32:35],v[24:27]\n"
"ds_read_b64_tr_b16 a[218:219],v239 offset:47104\n"
"v_cvt_pk_bf16_f32 v171,v90,v91\n"
"v_mfma_f32_16x16x32_bf16 v[28:31],a[232:235],a[196:199],v[28:31]\n"
"ds_read_b64_tr_b16 a[220:221],v239 offset:53248\n"
"v_cvt_pk_bf16_f32 v186,v92,v93\n"
"v_mfma_f32_16x16x32_bf16 v[28:31],a[236:239],a[36:39],v[28:31]\n"
"ds_read_b64_tr_b16 a[222:223],v239 offset:59392\n"
"v_cvt_pk_bf16_f32 v187,v94,v95\n"
"s_waitcnt lgkmcnt(12)\n"
"v_mfma_f32_16x16x32_bf16 v[32:35],a[240:243],a[192:195],v[32:35]\n"
"ds_read_b64_tr_b16 a[224:225],v238 offset:41216\n"
"v_mul_f32 v96,s56,v96\n"
"v_mfma_f32_16x16x32_bf16 v[32:35],a[244:247],a[32:35],v[32:35]\n"
"ds_read_b64_tr_b16 a[226:227],v238 offset:47360\n"
"v_mul_f32 v97,s56,v97\n"
"v_mfma_f32_16x16x32_bf16 v[36:39],a[240:243],a[196:199],v[36:39]\n"
"ds_read_b64_tr_b16 a[228:229],v238 offset:53504\n"
"v_mul_f32 v98,s56,v98\n"
"v_mfma_f32_16x16x32_bf16 v[36:39],a[244:247],a[36:39],v[36:39]\n"
"ds_read_b64_tr_b16 a[230:231],v238 offset:59648\n"
"v_mul_f32 v99,s56,v99\n"
"s_waitcnt lgkmcnt(12)\n"
"v_mfma_f32_16x16x32_bf16 v[40:43],a[248:251],a[192:195],v[40:43]\n"
"ds_read_b64_tr_b16 a[232:233],v239 offset:41216\n"
"v_mul_f32 v100,s56,v100\n"
"v_mfma_f32_16x16x32_bf16 v[40:43],a[252:255],a[32:35],v[40:43]\n"
"ds_read_b64_tr_b16 a[234:235],v239 offset:47360\n"
"v_mul_f32 v101,s56,v101\n"
"v_mfma_f32_16x16x32_bf16 v[44:47],a[248:251],a[196:199],v[44:47]\n"
"ds_read_b64_tr_b16 a[236:237],v239 offset:53504\n"
"v_mul_f32 v102,s56,v102\n"
"v_mfma_f32_16x16x32_bf16 v[44:47],a[252:255],a[36:39],v[44:47]\n"
"ds_read_b64_tr_b16 a[238:239],v239 offset:59648\n"
"v_mul_f32 v103,s56,v103\n"
"s_waitcnt vmcnt(32) lgkmcnt(12)\n"
"v_mfma_f32_16x16x32_bf16 v[0:3],a[208:211],a[40:43],v[0:3]\n"
"ds_read_b64_tr_b16 a[240:241],v238 offset:41472\n"
"v_mul_f32 v104,s56,v104\n"
"v_mfma_f32_16x16x32_bf16 v[0:3],a[212:215],a[48:51],v[0:3]\n"
"ds_read_b64_tr_b16 a[242:243],v238 offset:47616\n"
"v_mul_f32 v105,s56,v105\n"
"v_mfma_f32_16x16x32_bf16 v[4:7],a[208:211],a[44:47],v[4:7]\n"
"ds_read_b64_tr_b16 a[244:245],v238 offset:53760\n"
"v_mul_f32 v106,s56,v106\n"
"v_mfma_f32_16x16x32_bf16 v[4:7],a[212:215],a[52:55],v[4:7]\n"
"ds_read_b64_tr_b16 a[246:247],v238 offset:59904\n"
"v_mul_f32 v107,s56,v107\n"
"s_waitcnt lgkmcnt(12)\n"
"v_mfma_f32_16x16x32_bf16 v[8:11],a[216:219],a[40:43],v[8:11]\n"
"ds_read_b64_tr_b16 a[248:249],v239 offset:41472\n"
"v_mul_f32 v108,s56,v108\n"
"v_mfma_f32_16x16x32_bf16 v[8:11],a[220:223],a[48:51],v[8:11]\n"
"ds_read_b64_tr_b16 a[250:251],v239 offset:47616\n"
"v_mul_f32 v109,s56,v109\n"
"v_mfma_f32_16x16x32_bf16 v[12:15],a[216:219],a[44:47],v[12:15]\n"
"ds_read_b64_tr_b16 a[252:253],v239 offset:53760\n"
"v_mul_f32 v110,s56,v110\n"
"v_mfma_f32_16x16x32_bf16 v[12:15],a[220:223],a[52:55],v[12:15]\n"
"ds_read_b64_tr_b16 a[254:255],v239 offset:59904\n"
"v_mul_f32 v111,s56,v111\n"
"s_waitcnt vmcnt(26) lgkmcnt(12)\n"
"s_barrier\n"
"v_mfma_f32_16x16x32_bf16 v[16:19],a[224:227],a[40:43],v[16:19]\n"
"ds_read_b64_tr_b16 a[208:209],v236\n"
"v_cvt_pk_bf16_f32 v140,v96,v97\n"
"v_mfma_f32_16x16x32_bf16 v[16:19],a[228:231],a[48:51],v[16:19]\n"
"ds_read_b64_tr_b16 a[210:211],v236 offset:6144\n"
"v_cvt_pk_bf16_f32 v141,v98,v99\n"
"v_mfma_f32_16x16x32_bf16 v[20:23],a[224:227],a[44:47],v[20:23]\n"
"ds_read_b64_tr_b16 a[212:213],v236 offset:12288\n"
"v_cvt_pk_bf16_f32 v156,v100,v101\n"
"v_mfma_f32_16x16x32_bf16 v[20:23],a[228:231],a[52:55],v[20:23]\n"
"ds_read_b64_tr_b16 a[214:215],v236 offset:18432\n"
"v_cvt_pk_bf16_f32 v157,v102,v103\n"
"s_waitcnt lgkmcnt(12)\n"
"v_mfma_f32_16x16x32_bf16 v[24:27],a[232:235],a[40:43],v[24:27]\n"
"ds_read_b64_tr_b16 a[216:217],v237\n"
"v_cvt_pk_bf16_f32 v172,v104,v105\n"
"v_mfma_f32_16x16x32_bf16 v[24:27],a[236:239],a[48:51],v[24:27]\n"
"ds_read_b64_tr_b16 a[218:219],v237 offset:6144\n"
"v_cvt_pk_bf16_f32 v173,v106,v107\n"
"v_mfma_f32_16x16x32_bf16 v[28:31],a[232:235],a[44:47],v[28:31]\n"
"ds_read_b64_tr_b16 a[220:221],v237 offset:12288\n"
"v_cvt_pk_bf16_f32 v188,v108,v109\n"
"v_mfma_f32_16x16x32_bf16 v[28:31],a[236:239],a[52:55],v[28:31]\n"
"ds_read_b64_tr_b16 a[222:223],v237 offset:18432\n"
"v_cvt_pk_bf16_f32 v189,v110,v111\n"
"s_waitcnt lgkmcnt(12)\n"
"v_mfma_f32_16x16x32_bf16 v[32:35],a[240:243],a[40:43],v[32:35]\n"
"ds_read_b64_tr_b16 a[224:225],v236 offset:256\n"
"v_mul_f32 v112,s56,v112\n"
"v_mfma_f32_16x16x32_bf16 v[32:35],a[244:247],a[48:51],v[32:35]\n"
"ds_read_b64_tr_b16 a[226:227],v236 offset:6400\n"
"v_mul_f32 v113,s56,v113\n"
"v_mfma_f32_16x16x32_bf16 v[36:39],a[240:243],a[44:47],v[36:39]\n"
"ds_read_b64_tr_b16 a[228:229],v236 offset:12544\n"
"v_mul_f32 v114,s56,v114\n"
"v_mfma_f32_16x16x32_bf16 v[36:39],a[244:247],a[52:55],v[36:39]\n"
"ds_read_b64_tr_b16 a[230:231],v236 offset:18688\n"
"v_mul_f32 v115,s56,v115\n"
"s_waitcnt lgkmcnt(12)\n"
"v_mfma_f32_16x16x32_bf16 v[40:43],a[248:251],a[40:43],v[40:43]\n"
"ds_read_b64_tr_b16 a[232:233],v237 offset:256\n"
"v_mul_f32 v116,s56,v116\n"
"v_mfma_f32_16x16x32_bf16 v[40:43],a[252:255],a[48:51],v[40:43]\n"
"ds_read_b64_tr_b16 a[234:235],v237 offset:6400\n"
"v_mul_f32 v117,s56,v117\n"
"v_mfma_f32_16x16x32_bf16 v[44:47],a[248:251],a[44:47],v[44:47]\n"
"ds_read_b64_tr_b16 a[236:237],v237 offset:12544\n"
"v_mul_f32 v118,s56,v118\n"
"v_mfma_f32_16x16x32_bf16 v[44:47],a[252:255],a[52:55],v[44:47]\n"
"ds_read_b64_tr_b16 a[238:239],v237 offset:18688\n"
"v_mul_f32 v119,s56,v119\n"
"s_waitcnt vmcnt(20) lgkmcnt(12)\n"
"v_mfma_f32_16x16x32_bf16 v[0:3],a[208:211],a[56:59],v[0:3]\n"
"ds_read_b64_tr_b16 a[240:241],v236 offset:512\n"
"v_mul_f32 v120,s56,v120\n"
"v_mfma_f32_16x16x32_bf16 v[0:3],a[212:215],a[64:67],v[0:3]\n"
"ds_read_b64_tr_b16 a[242:243],v236 offset:6656\n"
"s_add_u32 s43,s58,0\n"
"buffer_load_dwordx4 v[48:51],v209,s[64:67],s43 offen sc1\n"
"v_mfma_f32_16x16x32_bf16 v[4:7],a[208:211],a[60:63],v[4:7]\n"
"ds_read_b64_tr_b16 a[244:245],v236 offset:12800\n"
"v_mul_f32 v121,s56,v121\n"
"v_mfma_f32_16x16x32_bf16 v[4:7],a[212:215],a[68:71],v[4:7]\n"
"ds_read_b64_tr_b16 a[246:247],v236 offset:18944\n"
"s_add_u32 s43,s58,0\n"
"buffer_load_dwordx4 v[52:55],v209,s[64:67],s43 offen offset:1024 sc1\n"
"s_waitcnt lgkmcnt(12)\n"
"v_mfma_f32_16x16x32_bf16 v[8:11],a[216:219],a[56:59],v[8:11]\n"
"ds_read_b64_tr_b16 a[248:249],v237 offset:512\n"
"v_mul_f32 v122,s56,v122\n"
"v_mfma_f32_16x16x32_bf16 v[8:11],a[220:223],a[64:67],v[8:11]\n"
"ds_read_b64_tr_b16 a[250:251],v237 offset:6656\n"
"s_add_u32 s43,s58,4096\n"
"buffer_load_dwordx4 v[56:59],v209,s[64:67],s43 offen sc1\n"
"v_mfma_f32_16x16x32_bf16 v[12:15],a[216:219],a[60:63],v[12:15]\n"
"ds_read_b64_tr_b16 a[252:253],v237 offset:12800\n"
"v_mul_f32 v123,s56,v123\n"
"v_mfma_f32_16x16x32_bf16 v[12:15],a[220:223],a[68:71],v[12:15]\n"
"ds_read_b64_tr_b16 a[254:255],v237 offset:18944\n"
"s_add_u32 s43,s58,4096\n"
"buffer_load_dwordx4 v[60:63],v209,s[64:67],s43 offen offset:1024 sc1\n"
"s_waitcnt vmcnt(18) lgkmcnt(12)\n"
"s_barrier\n"
"v_mfma_f32_16x16x32_bf16 v[16:19],a[224:227],a[56:59],v[16:19]\n"
"ds_read_b64_tr_b16 a[208:209],v236 offset:24576\n"
"v_mul_f32 v124,s56,v124\n"
"v_mfma_f32_16x16x32_bf16 v[16:19],a[228:231],a[64:67],v[16:19]\n"
"ds_read_b64_tr_b16 a[210:211],v236 offset:30720\n"
"s_add_u32 s43,s58,8192\n"
"buffer_load_dwordx4 v[64:67],v209,s[64:67],s43 offen sc1\n"
"v_mfma_f32_16x16x32_bf16 v[20:23],a[224:227],a[60:63],v[20:23]\n"
"ds_read_b64_tr_b16 a[212:213],v236 offset:36864\n"
"v_mul_f32 v125,s56,v125\n"
"v_mfma_f32_16x16x32_bf16 v[20:23],a[228:231],a[68:71],v[20:23]\n"
"ds_read_b64_tr_b16 a[214:215],v236 offset:43008\n"
"s_add_u32 s43,s58,8192\n"
"buffer_load_dwordx4 v[68:71],v209,s[64:67],s43 offen offset:1024 sc1\n"
"s_waitcnt lgkmcnt(12)\n"
"v_mfma_f32_16x16x32_bf16 v[24:27],a[232:235],a[56:59],v[24:27]\n"
"ds_read_b64_tr_b16 a[216:217],v237 offset:24576\n"
"v_mul_f32 v126,s56,v126\n"
"v_mfma_f32_16x16x32_bf16 v[24:27],a[236:239],a[64:67],v[24:27]\n"
"ds_read_b64_tr_b16 a[218:219],v237 offset:30720\n"
"s_add_u32 m0,s62,0\n"
"s_add_u32 s41,s61,0\n"
"buffer_load_dwordx4 v198,s[76:79],s41 offen lds\n"
"v_mfma_f32_16x16x32_bf16 v[28:31],a[232:235],a[60:63],v[28:31]\n"
"ds_read_b64_tr_b16 a[220:221],v237 offset:36864\n"
"v_mul_f32 v127,s56,v127\n"
"v_mfma_f32_16x16x32_bf16 v[28:31],a[236:239],a[68:71],v[28:31]\n"
"ds_read_b64_tr_b16 a[222:223],v237 offset:43008\n"
"buffer_load_dwordx4 v199,s[76:79],s41 offen offset:1024 lds\n"
"s_waitcnt lgkmcnt(12)\n"
"v_mfma_f32_16x16x32_bf16 v[32:35],a[240:243],a[56:59],v[32:35]\n"
"ds_read_b64_tr_b16 a[224:225],v236 offset:24832\n"
"v_cvt_pk_bf16_f32 v142,v112,v113\n"
"v_mfma_f32_16x16x32_bf16 v[32:35],a[244:247],a[64:67],v[32:35]\n"
"ds_read_b64_tr_b16 a[226:227],v236 offset:30976\n"
"buffer_load_dwordx4 v200,s[76:79],s41 offen offset:2048 lds\n"
"v_mfma_f32_16x16x32_bf16 v[36:39],a[240:243],a[60:63],v[36:39]\n"
"ds_read_b64_tr_b16 a[228:229],v236 offset:37120\n"
"v_cvt_pk_bf16_f32 v143,v114,v115\n"
"v_mfma_f32_16x16x32_bf16 v[36:39],a[244:247],a[68:71],v[36:39]\n"
"ds_read_b64_tr_b16 a[230:231],v236 offset:43264\n"
"buffer_load_dwordx4 v201,s[76:79],s41 offen offset:3072 lds\n"
"s_waitcnt lgkmcnt(12)\n"
"v_mfma_f32_16x16x32_bf16 v[40:43],a[248:251],a[56:59],v[40:43]\n"
"ds_read_b64_tr_b16 a[232:233],v237 offset:24832\n"
"v_cvt_pk_bf16_f32 v158,v116,v117\n"
"v_mfma_f32_16x16x32_bf16 v[40:43],a[252:255],a[64:67],v[40:43]\n"
"ds_read_b64_tr_b16 a[234:235],v237 offset:30976\n"
"s_add_u32 m0,s62,16384\n"
"s_add_u32 s41,s61,16384\n"
"buffer_load_dwordx4 v198,s[76:79],s41 offen lds\n"
"v_mfma_f32_16x16x32_bf16 v[44:47],a[248:251],a[60:63],v[44:47]\n"
"ds_read_b64_tr_b16 a[236:237],v237 offset:37120\n"
"v_cvt_pk_bf16_f32 v159,v118,v119\n"
"v_mfma_f32_16x16x32_bf16 v[44:47],a[252:255],a[68:71],v[44:47]\n"
"ds_read_b64_tr_b16 a[238:239],v237 offset:43264\n"
"buffer_load_dwordx4 v199,s[76:79],s41 offen offset:1024 lds\n"
"s_waitcnt vmcnt(22) lgkmcnt(12)\n"
"v_mfma_f32_16x16x32_bf16 v[0:3],a[208:211],a[72:75],v[0:3]\n"
"ds_read_b64_tr_b16 a[240:241],v236 offset:25088\n"
"v_cvt_pk_bf16_f32 v174,v120,v121\n"
"v_mfma_f32_16x16x32_bf16 v[0:3],a[212:215],a[80:83],v[0:3]\n"
"ds_read_b64_tr_b16 a[242:243],v236 offset:31232\n"
"buffer_load_dwordx4 v200,s[76:79],s41 offen offset:2048 lds\n"
"v_mfma_f32_16x16x32_bf16 v[4:7],a[208:211],a[76:79],v[4:7]\n"
"ds_read_b64_tr_b16 a[244:245],v236 offset:37376\n"
"v_cvt_pk_bf16_f32 v175,v122,v123\n"
"v_mfma_f32_16x16x32_bf16 v[4:7],a[212:215],a[84:87],v[4:7]\n"
"ds_read_b64_tr_b16 a[246:247],v236 offset:43520\n"
"buffer_load_dwordx4 v201,s[76:79],s41 offen offset:3072 lds\n"
"s_waitcnt lgkmcnt(12)\n"
"v_mfma_f32_16x16x32_bf16 v[8:11],a[216:219],a[72:75],v[8:11]\n"
"ds_read_b64_tr_b16 a[248:249],v237 offset:25088\n"
"v_cvt_pk_bf16_f32 v190,v124,v125\n"
"v_mfma_f32_16x16x32_bf16 v[8:11],a[220:223],a[80:83],v[8:11]\n"
"ds_read_b64_tr_b16 a[250:251],v237 offset:31232\n"
"s_add_u32 m0,s62,32768\n"
"s_add_u32 s41,s61,32768\n"
"buffer_load_dwordx4 v198,s[76:79],s41 offen lds\n"
"v_mfma_f32_16x16x32_bf16 v[12:15],a[216:219],a[76:79],v[12:15]\n"
"ds_read_b64_tr_b16 a[252:253],v237 offset:37376\n"
"v_cvt_pk_bf16_f32 v191,v126,v127\n"
"v_mfma_f32_16x16x32_bf16 v[12:15],a[220:223],a[84:87],v[12:15]\n"
"ds_read_b64_tr_b16 a[254:255],v237 offset:43520\n"
"buffer_load_dwordx4 v199,s[76:79],s41 offen offset:1024 lds\n"
"s_waitcnt vmcnt(20) lgkmcnt(12)\n"
"s_barrier\n"
"v_mfma_f32_16x16x32_bf16 v[16:19],a[224:227],a[72:75],v[16:19]\n"
"ds_read_b64_tr_b16 a[208:209],v240\n"
"v_mfma_f32_16x16x32_bf16 v[16:19],a[228:231],a[80:83],v[16:19]\n"
"ds_read_b64_tr_b16 a[210:211],v240 offset:6144\n"
"buffer_load_dwordx4 v200,s[76:79],s41 offen offset:2048 lds\n"
"v_mfma_f32_16x16x32_bf16 v[20:23],a[224:227],a[76:79],v[20:23]\n"
"ds_read_b64_tr_b16 a[212:213],v240 offset:12288\n"
"v_mfma_f32_16x16x32_bf16 v[20:23],a[228:231],a[84:87],v[20:23]\n"
"ds_read_b64_tr_b16 a[214:215],v240 offset:18432\n"
"buffer_load_dwordx4 v201,s[76:79],s41 offen offset:3072 lds\n"
"s_waitcnt lgkmcnt(12)\n"
"v_mfma_f32_16x16x32_bf16 v[24:27],a[232:235],a[72:75],v[24:27]\n"
"ds_read_b64_tr_b16 a[216:217],v241\n"
"v_mfma_f32_16x16x32_bf16 v[24:27],a[236:239],a[80:83],v[24:27]\n"
"ds_read_b64_tr_b16 a[218:219],v241 offset:6144\n"
"s_add_u32 m0,s63,0\n"
"s_add_u32 s41,s61,16384\n"
"buffer_load_dwordx4 v202,s[80:83],s41 offen lds\n"
"v_mfma_f32_16x16x32_bf16 v[28:31],a[232:235],a[76:79],v[28:31]\n"
"ds_read_b64_tr_b16 a[220:221],v241 offset:12288\n"
"v_mfma_f32_16x16x32_bf16 v[28:31],a[236:239],a[84:87],v[28:31]\n"
"ds_read_b64_tr_b16 a[222:223],v241 offset:18432\n"
"buffer_load_dwordx4 v203,s[80:83],s41 offen offset:1024 lds\n"
"s_waitcnt lgkmcnt(12)\n"
"v_mfma_f32_16x16x32_bf16 v[32:35],a[240:243],a[72:75],v[32:35]\n"
"ds_read_b64_tr_b16 a[224:225],v240 offset:256\n"
"v_mfma_f32_16x16x32_bf16 v[32:35],a[244:247],a[80:83],v[32:35]\n"
"ds_read_b64_tr_b16 a[226:227],v240 offset:6400\n"
"buffer_load_dwordx4 v202,s[80:83],s41 offen offset:2048 lds\n"
"v_mfma_f32_16x16x32_bf16 v[36:39],a[240:243],a[76:79],v[36:39]\n"
"ds_read_b64_tr_b16 a[228:229],v240 offset:12544\n"
"v_mfma_f32_16x16x32_bf16 v[36:39],a[244:247],a[84:87],v[36:39]\n"
"ds_read_b64_tr_b16 a[230:231],v240 offset:18688\n"
"buffer_load_dwordx4 v203,s[80:83],s41 offen offset:3072 lds\n"
"s_waitcnt lgkmcnt(12)\n"
"v_mfma_f32_16x16x32_bf16 v[40:43],a[248:251],a[72:75],v[40:43]\n"
"ds_read_b64_tr_b16 a[232:233],v241 offset:256\n"
"v_mfma_f32_16x16x32_bf16 v[40:43],a[252:255],a[80:83],v[40:43]\n"
"ds_read_b64_tr_b16 a[234:235],v241 offset:6400\n"
"v_mfma_f32_16x16x32_bf16 v[44:47],a[248:251],a[76:79],v[44:47]\n"
"ds_read_b64_tr_b16 a[236:237],v241 offset:12544\n"
"v_mfma_f32_16x16x32_bf16 v[44:47],a[252:255],a[84:87],v[44:47]\n"
"ds_read_b64_tr_b16 a[238:239],v241 offset:18688\n"
"s_waitcnt vmcnt(22) lgkmcnt(12)\n"
"v_mfma_f32_16x16x32_bf16 v[0:3],a[208:211],a[88:91],v[0:3]\n"
"ds_read_b64_tr_b16 a[240:241],v240 offset:512\n"
"v_mfma_f32_16x16x32_bf16 v[0:3],a[212:215],a[96:99],v[0:3]\n"
"ds_read_b64_tr_b16 a[242:243],v240 offset:6656\n"
"v_mfma_f32_16x16x32_bf16 v[4:7],a[208:211],a[92:95],v[4:7]\n"
"ds_read_b64_tr_b16 a[244:245],v240 offset:12800\n"
"v_mfma_f32_16x16x32_bf16 v[4:7],a[212:215],a[100:103],v[4:7]\n"
"ds_read_b64_tr_b16 a[246:247],v240 offset:18944\n"
"s_waitcnt lgkmcnt(12)\n"
"v_mfma_f32_16x16x32_bf16 v[8:11],a[216:219],a[88:91],v[8:11]\n"
"ds_read_b64_tr_b16 a[248:249],v241 offset:512\n"
"v_mfma_f32_16x16x32_bf16 v[8:11],a[220:223],a[96:99],v[8:11]\n"
"ds_read_b64_tr_b16 a[250:251],v241 offset:6656\n"
"v_mfma_f32_16x16x32_bf16 v[12:15],a[216:219],a[92:95],v[12:15]\n"
"ds_read_b64_tr_b16 a[252:253],v241 offset:12800\n"
"v_mfma_f32_16x16x32_bf16 v[12:15],a[220:223],a[100:103],v[12:15]\n"
"ds_read_b64_tr_b16 a[254:255],v241 offset:18944\n"
"s_waitcnt lgkmcnt(12)\n"
"v_mfma_f32_16x16x32_bf16 v[16:19],a[224:227],a[88:91],v[16:19]\n"
"v_mfma_f32_16x16x32_bf16 v[16:19],a[228:231],a[96:99],v[16:19]\n"
"v_mfma_f32_16x16x32_bf16 v[20:23],a[224:227],a[92:95],v[20:23]\n"
"v_mfma_f32_16x16x32_bf16 v[20:23],a[228:231],a[100:103],v[20:23]\n"
"s_waitcnt lgkmcnt(8)\n"
"v_mfma_f32_16x16x32_bf16 v[24:27],a[232:235],a[88:91],v[24:27]\n"
"v_mfma_f32_16x16x32_bf16 v[24:27],a[236:239],a[96:99],v[24:27]\n"
"v_mfma_f32_16x16x32_bf16 v[28:31],a[232:235],a[92:95],v[28:31]\n"
"v_mfma_f32_16x16x32_bf16 v[28:31],a[236:239],a[100:103],v[28:31]\n"
"s_waitcnt lgkmcnt(4)\n"
"v_mfma_f32_16x16x32_bf16 v[32:35],a[240:243],a[88:91],v[32:35]\n"
"v_mfma_f32_16x16x32_bf16 v[32:35],a[244:247],a[96:99],v[32:35]\n"
"v_mfma_f32_16x16x32_bf16 v[36:39],a[240:243],a[92:95],v[36:39]\n"
"v_mfma_f32_16x16x32_bf16 v[36:39],a[244:247],a[100:103],v[36:39]\n"
"s_waitcnt lgkmcnt(0)\n"
"v_mfma_f32_16x16x32_bf16 v[40:43],a[248:251],a[88:91],v[40:43]\n"
"v_mfma_f32_16x16x32_bf16 v[40:43],a[252:255],a[96:99],v[40:43]\n"
"v_mfma_f32_16x16x32_bf16 v[44:47],a[248:251],a[92:95],v[44:47]\n"
"v_mfma_f32_16x16x32_bf16 v[44:47],a[252:255],a[100:103],v[44:47]\n"
"s_nop 7\n"
"s_mov_b32 s43,0xffff0000\n"
"s_waitcnt vmcnt(16)\n"
"v_lshlrev_b32 v72,16,v48\n"
"v_and_b32 v73,s43,v48\n"
"v_pk_add_f32 v[72:73],v[72:73],v[0:1]\n"
"v_lshlrev_b32 v74,16,v49\n"
"v_and_b32 v75,s43,v49\n"
"v_pk_add_f32 v[74:75],v[74:75],v[2:3]\n"
"v_lshlrev_b32 v76,16,v50\n"
"v_and_b32 v77,s43,v50\n"
"v_pk_add_f32 v[76:77],v[76:77],v[8:9]\n"
"v_lshlrev_b32 v78,16,v51\n"
"v_and_b32 v79,s43,v51\n"
"v_pk_add_f32 v[78:79],v[78:79],v[10:11]\n"
"v_cvt_pk_bf16_f32 v96,v72,v73\n"
"v_cvt_pk_bf16_f32 v97,v74,v75\n"
"v_cvt_pk_bf16_f32 v98,v76,v77\n"
"v_cvt_pk_bf16_f32 v99,v78,v79\n"
"s_add_u32 s42,s58,4718592\n"
"buffer_store_dwordx4 v[96:99],v209,s[64:67],s42 offen sc1\n"
"v_lshlrev_b32 v80,16,v52\n"
"v_and_b32 v81,s43,v52\n"
"v_pk_add_f32 v[80:81],v[80:81],v[4:5]\n"
"v_lshlrev_b32 v82,16,v53\n"
"v_and_b32 v83,s43,v53\n"
"v_pk_add_f32 v[82:83],v[82:83],v[6:7]\n"
"v_lshlrev_b32 v84,16,v54\n"
"v_and_b32 v85,s43,v54\n"
"v_pk_add_f32 v[84:85],v[84:85],v[12:13]\n"
"v_lshlrev_b32 v86,16,v55\n"
"v_and_b32 v87,s43,v55\n"
"v_pk_add_f32 v[86:87],v[86:87],v[14:15]\n"
"v_cvt_pk_bf16_f32 v100,v80,v81\n"
"v_cvt_pk_bf16_f32 v101,v82,v83\n"
"v_cvt_pk_bf16_f32 v102,v84,v85\n"
"v_cvt_pk_bf16_f32 v103,v86,v87\n"
"s_add_u32 s42,s58,4718592\n"
"buffer_store_dwordx4 v[100:103],v209,s[64:67],s42 offen offset:1024 sc1\n"
"v_lshlrev_b32 v72,16,v56\n"
"v_and_b32 v73,s43,v56\n"
"v_pk_add_f32 v[72:73],v[72:73],v[16:17]\n"
"v_lshlrev_b32 v74,16,v57\n"
"v_and_b32 v75,s43,v57\n"
"v_pk_add_f32 v[74:75],v[74:75],v[18:19]\n"
"v_lshlrev_b32 v76,16,v58\n"
"v_and_b32 v77,s43,v58\n"
"v_pk_add_f32 v[76:77],v[76:77],v[24:25]\n"
"v_lshlrev_b32 v78,16,v59\n"
"v_and_b32 v79,s43,v59\n"
"v_pk_add_f32 v[78:79],v[78:79],v[26:27]\n"
"v_cvt_pk_bf16_f32 v104,v72,v73\n"
"v_cvt_pk_bf16_f32 v105,v74,v75\n"
"v_cvt_pk_bf16_f32 v106,v76,v77\n"
"v_cvt_pk_bf16_f32 v107,v78,v79\n"
"s_add_u32 s42,s58,4722688\n"
"buffer_store_dwordx4 v[104:107],v209,s[64:67],s42 offen sc1\n"
"v_lshlrev_b32 v80,16,v60\n"
"v_and_b32 v81,s43,v60\n"
"v_pk_add_f32 v[80:81],v[80:81],v[20:21]\n"
"v_lshlrev_b32 v82,16,v61\n"
"v_and_b32 v83,s43,v61\n"
"v_pk_add_f32 v[82:83],v[82:83],v[22:23]\n"
"v_lshlrev_b32 v84,16,v62\n"
"v_and_b32 v85,s43,v62\n"
"v_pk_add_f32 v[84:85],v[84:85],v[28:29]\n"
"v_lshlrev_b32 v86,16,v63\n"
"v_and_b32 v87,s43,v63\n"
"v_pk_add_f32 v[86:87],v[86:87],v[30:31]\n"
"v_cvt_pk_bf16_f32 v108,v80,v81\n"
"v_cvt_pk_bf16_f32 v109,v82,v83\n"
"v_cvt_pk_bf16_f32 v110,v84,v85\n"
"v_cvt_pk_bf16_f32 v111,v86,v87\n"
"s_add_u32 s42,s58,4722688\n"
"buffer_store_dwordx4 v[108:111],v209,s[64:67],s42 offen offset:1024 sc1\n"
"v_lshlrev_b32 v72,16,v64\n"
"v_and_b32 v73,s43,v64\n"
"v_pk_add_f32 v[72:73],v[72:73],v[32:33]\n"
"v_lshlrev_b32 v74,16,v65\n"
"v_and_b32 v75,s43,v65\n"
"v_pk_add_f32 v[74:75],v[74:75],v[34:35]\n"
"v_lshlrev_b32 v76,16,v66\n"
"v_and_b32 v77,s43,v66\n"
"v_pk_add_f32 v[76:77],v[76:77],v[40:41]\n"
"v_lshlrev_b32 v78,16,v67\n"
"v_and_b32 v79,s43,v67\n"
"v_pk_add_f32 v[78:79],v[78:79],v[42:43]\n"
"v_cvt_pk_bf16_f32 v112,v72,v73\n"
"v_cvt_pk_bf16_f32 v113,v74,v75\n"
"v_cvt_pk_bf16_f32 v114,v76,v77\n"
"v_cvt_pk_bf16_f32 v115,v78,v79\n"
"s_add_u32 s42,s58,4726784\n"
"buffer_store_dwordx4 v[112:115],v209,s[64:67],s42 offen sc1\n"
"v_lshlrev_b32 v80,16,v68\n"
"v_and_b32 v81,s43,v68\n"
"v_pk_add_f32 v[80:81],v[80:81],v[36:37]\n"
"v_lshlrev_b32 v82,16,v69\n"
"v_and_b32 v83,s43,v69\n"
"v_pk_add_f32 v[82:83],v[82:83],v[38:39]\n"
"v_lshlrev_b32 v84,16,v70\n"
"v_and_b32 v85,s43,v70\n"
"v_pk_add_f32 v[84:85],v[84:85],v[44:45]\n"
"v_lshlrev_b32 v86,16,v71\n"
"v_and_b32 v87,s43,v71\n"
"v_pk_add_f32 v[86:87],v[86:87],v[46:47]\n"
"v_cvt_pk_bf16_f32 v116,v80,v81\n"
"v_cvt_pk_bf16_f32 v117,v82,v83\n"
"v_cvt_pk_bf16_f32 v118,v84,v85\n"
"v_cvt_pk_bf16_f32 v119,v86,v87\n"
"s_add_u32 s42,s58,4726784\n"
"buffer_store_dwordx4 v[116:119],v209,s[64:67],s42 offen offset:1024 sc1\n"
"s_waitcnt vmcnt(18)\n"
"s_barrier\n"
"s_add_u32 m0,s62,49152\n"
"s_add_u32 s41,s61,49152\n"
"buffer_load_dwordx4 v198,s[76:79],s41 offen lds\n"
"buffer_load_dwordx4 v199,s[76:79],s41 offen offset:1024 lds\n"
"buffer_load_dwordx4 v200,s[76:79],s41 offen offset:2048 lds\n"
"buffer_load_dwordx4 v201,s[76:79],s41 offen offset:3072 lds\n"
"s_branch LM_ATT2\n"
"LM_NOB:\n"
"s_waitcnt lgkmcnt(8)\n"
"v_mfma_f32_16x16x32_bf16 v[0:3],a[208:211],a[64:67],v[0:3]\n"
"ds_read_b64_tr_b16 a[232:233],v204 offset:33032\n"
"v_mfma_f32_16x16x32_bf16 v[0:3],a[212:215],a[80:83],v[0:3]\n"
"v_mfma_f32_16x16x32_bf16 v[4:7],a[208:211],a[68:71],v[4:7]\n"
"ds_read_b64_tr_b16 a[234:235],v204 offset:37128\n"
"v_mfma_f32_16x16x32_bf16 v[4:7],a[212:215],a[84:87],v[4:7]\n"
"v_mfma_f32_16x16x32_bf16 v[8:11],a[208:211],a[72:75],v[8:11]\n"
"s_add_u32 m0,s62,0\n"
"s_add_u32 s41,s61,0\n"
"ds_read_b64_tr_b16 a[236:237],v204 offset:41224\n"
"v_mfma_f32_16x16x32_bf16 v[8:11],a[212:215],a[88:91],v[8:11]\n"
"buffer_load_dwordx4 v198,s[76:79],s41 offen lds\n"
"v_mfma_f32_16x16x32_bf16 v[12:15],a[208:211],a[76:79],v[12:15]\n"
"ds_read_b64_tr_b16 a[238:239],v204 offset:45320\n"
"v_mfma_f32_16x16x32_bf16 v[12:15],a[212:215],a[92:95],v[12:15]\n"
"s_waitcnt lgkmcnt(8)\n"
"v_mfma_f32_16x16x32_bf16 v[16:19],a[216:219],a[64:67],v[16:19]\n"
"ds_read_b64_tr_b16 a[240:241],v204 offset:33280\n"
"v_mfma_f32_16x16x32_bf16 v[16:19],a[220:223],a[80:83],v[16:19]\n"
"v_mfma_f32_16x16x32_bf16 v[20:23],a[216:219],a[68:71],v[20:23]\n"
"ds_read_b64_tr_b16 a[242:243],v204 offset:37376\n"
"v_mfma_f32_16x16x32_bf16 v[20:23],a[220:223],a[84:87],v[20:23]\n"
"v_mfma_f32_16x16x32_bf16 v[24:27],a[216:219],a[72:75],v[24:27]\n"
"ds_read_b64_tr_b16 a[244:245],v204 offset:41472\n"
"v_mfma_f32_16x16x32_bf16 v[24:27],a[220:223],a[88:91],v[24:27]\n"
"buffer_load_dwordx4 v199,s[76:79],s41 offen offset:1024 lds\n"
"v_mfma_f32_16x16x32_bf16 v[28:31],a[216:219],a[76:79],v[28:31]\n"
"ds_read_b64_tr_b16 a[246:247],v204 offset:45568\n"
"v_mfma_f32_16x16x32_bf16 v[28:31],a[220:223],a[92:95],v[28:31]\n"
"s_waitcnt lgkmcnt(8)\n"
"v_mfma_f32_16x16x32_bf16 v[32:35],a[224:227],a[64:67],v[32:35]\n"
"ds_read_b64_tr_b16 a[248:249],v204 offset:33288\n"
"v_mfma_f32_16x16x32_bf16 v[32:35],a[228:231],a[80:83],v[32:35]\n"
"v_mfma_f32_16x16x32_bf16 v[36:39],a[224:227],a[68:71],v[36:39]\n"
"ds_read_b64_tr_b16 a[250:251],v204 offset:37384\n"
"v_mfma_f32_16x16x32_bf16 v[36:39],a[228:231],a[84:87],v[36:39]\n"
"v_mfma_f32_16x16x32_bf16 v[40:43],a[224:227],a[72:75],v[40:43]\n"
"ds_read_b64_tr_b16 a[252:253],v204 offset:41480\n"
"v_mfma_f32_16x16x32_bf16 v[40:43],a[228:231],a[88:91],v[40:43]\n"
"buffer_load_dwordx4 v200,s[76:79],s41 offen offset:2048 lds\n"
"v_mfma_f32_16x16x32_bf16 v[44:47],a[224:227],a[76:79],v[44:47]\n"
"ds_read_b64_tr_b16 a[254:255],v204 offset:45576\n"
"v_mfma_f32_16x16x32_bf16 v[44:47],a[228:231],a[92:95],v[44:47]\n"
"s_waitcnt lgkmcnt(8)\n"
"v_mfma_f32_16x16x32_bf16 v[48:51],a[232:235],a[64:67],v[48:51]\n"
"ds_read_b64_tr_b16 a[208:209],v204 offset:33536\n"
"v_mfma_f32_16x16x32_bf16 v[48:51],a[236:239],a[80:83],v[48:51]\n"
"v_mfma_f32_16x16x32_bf16 v[52:55],a[232:235],a[68:71],v[52:55]\n"
"ds_read_b64_tr_b16 a[210:211],v204 offset:37632\n"
"v_mfma_f32_16x16x32_bf16 v[52:55],a[236:239],a[84:87],v[52:55]\n"
"v_mfma_f32_16x16x32_bf16 v[56:59],a[232:235],a[72:75],v[56:59]\n"
"ds_read_b64_tr_b16 a[212:213],v204 offset:41728\n"
"v_mfma_f32_16x16x32_bf16 v[56:59],a[236:239],a[88:91],v[56:59]\n"
"buffer_load_dwordx4 v201,s[76:79],s41 offen offset:3072 lds\n"
"v_mfma_f32_16x16x32_bf16 v[60:63],a[232:235],a[76:79],v[60:63]\n"
"ds_read_b64_tr_b16 a[214:215],v204 offset:45824\n"
"v_mfma_f32_16x16x32_bf16 v[60:63],a[236:239],a[92:95],v[60:63]\n"
"s_waitcnt lgkmcnt(8)\n"
"v_mfma_f32_16x16x32_bf16 v[64:67],a[240:243],a[64:67],v[64:67]\n"
"ds_read_b64_tr_b16 a[216:217],v204 offset:33544\n"
"v_mfma_f32_16x16x32_bf16 v[64:67],a[244:247],a[80:83],v[64:67]\n"
"ds_read_b64_tr_b16 a[218:219],v204 offset:37640\n"
"v_mfma_f32_16x16x32_bf16 v[68:71],a[240:243],a[68:71],v[68:71]\n"
"ds_read_b64_tr_b16 a[220:221],v204 offset:41736\n"
"v_mfma_f32_16x16x32_bf16 v[68:71],a[244:247],a[84:87],v[68:71]\n"
"ds_read_b64_tr_b16 a[222:223],v204 offset:45832\n"
"v_mfma_f32_16x16x32_bf16 v[72:75],a[240:243],a[72:75],v[72:75]\n"
"s_add_u32 m0,s63,0\n"
"s_add_u32 s41,s61,16384\n"
"v_mfma_f32_16x16x32_bf16 v[72:75],a[244:247],a[88:91],v[72:75]\n"
"buffer_load_dwordx4 v202,s[80:83],s41 offen lds\n"
"v_mfma_f32_16x16x32_bf16 v[76:79],a[240:243],a[76:79],v[76:79]\n"
"v_mfma_f32_16x16x32_bf16 v[76:79],a[244:247],a[92:95],v[76:79]\n"
"s_waitcnt vmcnt(17) lgkmcnt(0)\n"
"s_barrier\n"
"v_mfma_f32_16x16x32_bf16 v[80:83],a[248:251],a[64:67],v[80:83]\n"
"ds_read_b64_tr_b16 a[224:225],v204 offset:49152\n"
"v_mfma_f32_16x16x32_bf16 v[80:83],a[252:255],a[80:83],v[80:83]\n"
"v_mfma_f32_16x16x32_bf16 v[84:87],a[248:251],a[68:71],v[84:87]\n"
"ds_read_b64_tr_b16 a[226:227],v204 offset:53248\n"
"v_mfma_f32_16x16x32_bf16 v[84:87],a[252:255],a[84:87],v[84:87]\n"
"v_mfma_f32_16x16x32_bf16 v[88:91],a[248:251],a[72:75],v[88:91]\n"
"ds_read_b64_tr_b16 a[228:229],v204 offset:57344\n"
"v_mfma_f32_16x16x32_bf16 v[88:91],a[252:255],a[88:91],v[88:91]\n"
"buffer_load_dwordx4 v203,s[80:83],s41 offen offset:1024 lds\n"
"v_mfma_f32_16x16x32_bf16 v[92:95],a[248:251],a[76:79],v[92:95]\n"
"ds_read_b64_tr_b16 a[230:231],v204 offset:61440\n"
"v_mfma_f32_16x16x32_bf16 v[92:95],a[252:255],a[92:95],v[92:95]\n"
"v_mfma_f32_16x16x32_bf16 v[96:99],a[208:211],a[64:67],v[96:99]\n"
"ds_read_b64_tr_b16 a[232:233],v204 offset:49160\n"
"v_mfma_f32_16x16x32_bf16 v[96:99],a[212:215],a[80:83],v[96:99]\n"
"v_mfma_f32_16x16x32_bf16 v[100:103],a[208:211],a[68:71],v[100:103]\n"
"ds_read_b64_tr_b16 a[234:235],v204 offset:53256\n"
"v_mfma_f32_16x16x32_bf16 v[100:103],a[212:215],a[84:87],v[100:103]\n"
"v_mfma_f32_16x16x32_bf16 v[104:107],a[208:211],a[72:75],v[104:107]\n"
"ds_read_b64_tr_b16 a[236:237],v204 offset:57352\n"
"v_mfma_f32_16x16x32_bf16 v[104:107],a[212:215],a[88:91],v[104:107]\n"
"buffer_load_dwordx4 v202,s[80:83],s41 offen offset:2048 lds\n"
"v_mfma_f32_16x16x32_bf16 v[108:111],a[208:211],a[76:79],v[108:111]\n"
"ds_read_b64_tr_b16 a[238:239],v204 offset:61448\n"
"v_mfma_f32_16x16x32_bf16 v[108:111],a[212:215],a[92:95],v[108:111]\n"
"v_mfma_f32_16x16x32_bf16 v[112:115],a[216:219],a[64:67],v[112:115]\n"
"ds_read_b64_tr_b16 a[240:241],v204 offset:49408\n"
"v_mfma_f32_16x16x32_bf16 v[112:115],a[220:223],a[80:83],v[112:115]\n"
"v_mfma_f32_16x16x32_bf16 v[116:119],a[216:219],a[68:71],v[116:119]\n"
"ds_read_b64_tr_b16 a[242:243],v204 offset:53504\n"
"v_mfma_f32_16x16x32_bf16 v[116:119],a[220:223],a[84:87],v[116:119]\n"
"v_mfma_f32_16x16x32_bf16 v[120:123],a[216:219],a[72:75],v[120:123]\n"
"ds_read_b64_tr_b16 a[244:245],v204 offset:57600\n"
"v_mfma_f32_16x16x32_bf16 v[120:123],a[220:223],a[88:91],v[120:123]\n"
"buffer_load_dwordx4 v203,s[80:83],s41 offen offset:3072 lds\n"
"v_mfma_f32_16x16x32_bf16 v[124:127],a[216:219],a[76:79],v[124:127]\n"
"ds_read_b64_tr_b16 a[246:247],v204 offset:61696\n"
"v_mfma_f32_16x16x32_bf16 v[124:127],a[220:223],a[92:95],v[124:127]\n"
"s_waitcnt lgkmcnt(8)\n"
"v_mfma_f32_16x16x32_bf16 v[0:3],a[224:227],a[96:99],v[0:3]\n"
"ds_read_b64_tr_b16 a[248:249],v204 offset:49416\n"
"v_mfma_f32_16x16x32_bf16 v[0:3],a[228:231],a[112:115],v[0:3]\n"
"v_mfma_f32_16x16x32_bf16 v[4:7],a[224:227],a[100:103],v[4:7]\n"
"ds_read_b64_tr_b16 a[250:251],v204 offset:53512\n"
"v_mfma_f32_16x16x32_bf16 v[4:7],a[228:231],a[116:119],v[4:7]\n"
"v_mfma_f32_16x16x32_bf16 v[8:11],a[224:227],a[104:107],v[8:11]\n"
"ds_read_b64_tr_b16 a[252:253],v204 offset:57608\n"
"v_mfma_f32_16x16x32_bf16 v[8:11],a[228:231],a[120:123],v[8:11]\n"
"v_mfma_f32_16x16x32_bf16 v[12:15],a[224:227],a[108:111],v[12:15]\n"
"ds_read_b64_tr_b16 a[254:255],v204 offset:61704\n"
"v_mfma_f32_16x16x32_bf16 v[12:15],a[228:231],a[124:127],v[12:15]\n"
"s_waitcnt lgkmcnt(8)\n"
"v_mfma_f32_16x16x32_bf16 v[16:19],a[232:235],a[96:99],v[16:19]\n"
"s_add_u32 m0,s62,16384\n"
"s_add_u32 s41,s61,16384\n"
"ds_read_b64_tr_b16 a[208:209],v204 offset:49664\n"
"v_mfma_f32_16x16x32_bf16 v[16:19],a[236:239],a[112:115],v[16:19]\n"
"buffer_load_dwordx4 v198,s[76:79],s41 offen lds\n"
"v_mfma_f32_16x16x32_bf16 v[20:23],a[232:235],a[100:103],v[20:23]\n"
"ds_read_b64_tr_b16 a[210:211],v204 offset:53760\n"
"v_mfma_f32_16x16x32_bf16 v[20:23],a[236:239],a[116:119],v[20:23]\n"
"v_mfma_f32_16x16x32_bf16 v[24:27],a[232:235],a[104:107],v[24:27]\n"
"ds_read_b64_tr_b16 a[212:213],v204 offset:57856\n"
"v_mfma_f32_16x16x32_bf16 v[24:27],a[236:239],a[120:123],v[24:27]\n"
"v_mfma_f32_16x16x32_bf16 v[28:31],a[232:235],a[108:111],v[28:31]\n"
"ds_read_b64_tr_b16 a[214:215],v204 offset:61952\n"
"v_mfma_f32_16x16x32_bf16 v[28:31],a[236:239],a[124:127],v[28:31]\n"
"s_waitcnt lgkmcnt(8)\n"
"v_mfma_f32_16x16x32_bf16 v[32:35],a[240:243],a[96:99],v[32:35]\n"
"ds_read_b64_tr_b16 a[216:217],v204 offset:49672\n"
"v_mfma_f32_16x16x32_bf16 v[32:35],a[244:247],a[112:115],v[32:35]\n"
"v_mfma_f32_16x16x32_bf16 v[36:39],a[240:243],a[100:103],v[36:39]\n"
"ds_read_b64_tr_b16 a[218:219],v204 offset:53768\n"
"v_mfma_f32_16x16x32_bf16 v[36:39],a[244:247],a[116:119],v[36:39]\n"
"v_mfma_f32_16x16x32_bf16 v[40:43],a[240:243],a[104:107],v[40:43]\n"
"ds_read_b64_tr_b16 a[220:221],v204 offset:57864\n"
"v_mfma_f32_16x16x32_bf16 v[40:43],a[244:247],a[120:123],v[40:43]\n"
"v_mfma_f32_16x16x32_bf16 v[44:47],a[240:243],a[108:111],v[44:47]\n"
"ds_read_b64_tr_b16 a[222:223],v204 offset:61960\n"
"v_mfma_f32_16x16x32_bf16 v[44:47],a[244:247],a[124:127],v[44:47]\n"
"s_waitcnt lgkmcnt(8)\n"
"v_mfma_f32_16x16x32_bf16 v[48:51],a[248:251],a[96:99],v[48:51]\n"
"ds_read_b64_tr_b16 a[224:225],v204 offset:49920\n"
"v_mfma_f32_16x16x32_bf16 v[48:51],a[252:255],a[112:115],v[48:51]\n"
"buffer_load_dwordx4 v199,s[76:79],s41 offen offset:1024 lds\n"
"v_mfma_f32_16x16x32_bf16 v[52:55],a[248:251],a[100:103],v[52:55]\n"
"ds_read_b64_tr_b16 a[226:227],v204 offset:54016\n"
"v_mfma_f32_16x16x32_bf16 v[52:55],a[252:255],a[116:119],v[52:55]\n"
"v_mfma_f32_16x16x32_bf16 v[56:59],a[248:251],a[104:107],v[56:59]\n"
"ds_read_b64_tr_b16 a[228:229],v204 offset:58112\n"
"v_mfma_f32_16x16x32_bf16 v[56:59],a[252:255],a[120:123],v[56:59]\n"
"v_mfma_f32_16x16x32_bf16 v[60:63],a[248:251],a[108:111],v[60:63]\n"
"ds_read_b64_tr_b16 a[230:231],v204 offset:62208\n"
"v_mfma_f32_16x16x32_bf16 v[60:63],a[252:255],a[124:127],v[60:63]\n"
"s_waitcnt lgkmcnt(8)\n"
"v_mfma_f32_16x16x32_bf16 v[64:67],a[208:211],a[96:99],v[64:67]\n"
"ds_read_b64_tr_b16 a[232:233],v204 offset:49928\n"
"v_mfma_f32_16x16x32_bf16 v[64:67],a[212:215],a[112:115],v[64:67]\n"
"ds_read_b64_tr_b16 a[234:235],v204 offset:54024\n"
"v_mfma_f32_16x16x32_bf16 v[68:71],a[208:211],a[100:103],v[68:71]\n"
"ds_read_b64_tr_b16 a[236:237],v204 offset:58120\n"
"v_mfma_f32_16x16x32_bf16 v[68:71],a[212:215],a[116:119],v[68:71]\n"
"ds_read_b64_tr_b16 a[238:239],v204 offset:62216\n"
"v_mfma_f32_16x16x32_bf16 v[72:75],a[208:211],a[104:107],v[72:75]\n"
"v_mfma_f32_16x16x32_bf16 v[72:75],a[212:215],a[120:123],v[72:75]\n"
"v_mfma_f32_16x16x32_bf16 v[76:79],a[208:211],a[108:111],v[76:79]\n"
"v_mfma_f32_16x16x32_bf16 v[76:79],a[212:215],a[124:127],v[76:79]\n"
"s_waitcnt vmcnt(10) lgkmcnt(0)\n"
"s_barrier\n"
"v_mfma_f32_16x16x32_bf16 v[80:83],a[216:219],a[96:99],v[80:83]\n"
"ds_read_b64_tr_b16 a[240:241],v204\n"
"v_mfma_f32_16x16x32_bf16 v[80:83],a[220:223],a[112:115],v[80:83]\n"
"buffer_load_dwordx4 v200,s[76:79],s41 offen offset:2048 lds\n"
"v_mfma_f32_16x16x32_bf16 v[84:87],a[216:219],a[100:103],v[84:87]\n"
"ds_read_b64_tr_b16 a[242:243],v204 offset:4096\n"
"v_mfma_f32_16x16x32_bf16 v[84:87],a[220:223],a[116:119],v[84:87]\n"
"v_mfma_f32_16x16x32_bf16 v[88:91],a[216:219],a[104:107],v[88:91]\n"
"ds_read_b64_tr_b16 a[244:245],v204 offset:8192\n"
"v_mfma_f32_16x16x32_bf16 v[88:91],a[220:223],a[120:123],v[88:91]\n"
"v_mfma_f32_16x16x32_bf16 v[92:95],a[216:219],a[108:111],v[92:95]\n"
"ds_read_b64_tr_b16 a[246:247],v204 offset:12288\n"
"v_mfma_f32_16x16x32_bf16 v[92:95],a[220:223],a[124:127],v[92:95]\n"
"v_mfma_f32_16x16x32_bf16 v[96:99],a[224:227],a[96:99],v[96:99]\n"
"ds_read_b64_tr_b16 a[248:249],v204 offset:8\n"
"v_mfma_f32_16x16x32_bf16 v[96:99],a[228:231],a[112:115],v[96:99]\n"
"v_mfma_f32_16x16x32_bf16 v[100:103],a[224:227],a[100:103],v[100:103]\n"
"ds_read_b64_tr_b16 a[250:251],v204 offset:4104\n"
"v_mfma_f32_16x16x32_bf16 v[100:103],a[228:231],a[116:119],v[100:103]\n"
"v_mfma_f32_16x16x32_bf16 v[104:107],a[224:227],a[104:107],v[104:107]\n"
"ds_read_b64_tr_b16 a[252:253],v204 offset:8200\n"
"v_mfma_f32_16x16x32_bf16 v[104:107],a[228:231],a[120:123],v[104:107]\n"
"v_mfma_f32_16x16x32_bf16 v[108:111],a[224:227],a[108:111],v[108:111]\n"
"ds_read_b64_tr_b16 a[254:255],v204 offset:12296\n"
"v_mfma_f32_16x16x32_bf16 v[108:111],a[228:231],a[124:127],v[108:111]\n"
"v_mfma_f32_16x16x32_bf16 v[112:115],a[232:235],a[96:99],v[112:115]\n"
"ds_read_b64_tr_b16 a[208:209],v204 offset:256\n"
"v_mfma_f32_16x16x32_bf16 v[112:115],a[236:239],a[112:115],v[112:115]\n"
"buffer_load_dwordx4 v201,s[76:79],s41 offen offset:3072 lds\n"
"v_mfma_f32_16x16x32_bf16 v[116:119],a[232:235],a[100:103],v[116:119]\n"
"ds_read_b64_tr_b16 a[210:211],v204 offset:4352\n"
"v_mfma_f32_16x16x32_bf16 v[116:119],a[236:239],a[116:119],v[116:119]\n"
"v_mfma_f32_16x16x32_bf16 v[120:123],a[232:235],a[104:107],v[120:123]\n"
"ds_read_b64_tr_b16 a[212:213],v204 offset:8448\n"
"v_mfma_f32_16x16x32_bf16 v[120:123],a[236:239],a[120:123],v[120:123]\n"
"v_mfma_f32_16x16x32_bf16 v[124:127],a[232:235],a[108:111],v[124:127]\n"
"ds_read_b64_tr_b16 a[214:215],v204 offset:12544\n"
"v_mfma_f32_16x16x32_bf16 v[124:127],a[236:239],a[124:127],v[124:127]\n"
"s_waitcnt lgkmcnt(8)\n"
"v_mfma_f32_16x16x32_bf16 v[0:3],a[240:243],a[0:3],v[0:3]\n"
"ds_read_b64_tr_b16 a[216:217],v204 offset:264\n"
"v_mfma_f32_16x16x32_bf16 v[0:3],a[244:247],a[16:19],v[0:3]\n"
"v_mfma_f32_16x16x32_bf16 v[4:7],a[240:243],a[4:7],v[4:7]\n"
"ds_read_b64_tr_b16 a[218:219],v204 offset:4360\n"
"v_mfma_f32_16x16x32_bf16 v[4:7],a[244:247],a[20:23],v[4:7]\n"
"v_mfma_f32_16x16x32_bf16 v[8:11],a[240:243],a[8:11],v[8:11]\n"
"ds_read_b64_tr_b16 a[220:221],v204 offset:8456\n"
"v_mfma_f32_16x16x32_bf16 v[8:11],a[244:247],a[24:27],v[8:11]\n"
"v_mfma_f32_16x16x32_bf16 v[12:15],a[240:243],a[12:15],v[12:15]\n"
"ds_read_b64_tr_b16 a[222:223],v204 offset:12552\n"
"v_mfma_f32_16x16x32_bf16 v[12:15],a[244:247],a[28:31],v[12:15]\n"
"s_waitcnt lgkmcnt(8)\n"
"v_mfma_f32_16x16x32_bf16 v[16:19],a[248:251],a[0:3],v[16:19]\n"
"s_add_u32 m0,s62,32768\n"
"s_add_u32 s41,s61,32768\n"
"ds_read_b64_tr_b16 a[224:225],v204 offset:512\n"
"v_mfma_f32_16x16x32_bf16 v[16:19],a[252:255],a[16:19],v[16:19]\n"
"buffer_load_dwordx4 v198,s[76:79],s41 offen lds\n"
"v_mfma_f32_16x16x32_bf16 v[20:23],a[248:251],a[4:7],v[20:23]\n"
"ds_read_b64_tr_b16 a[226:227],v204 offset:4608\n"
"v_mfma_f32_16x16x32_bf16 v[20:23],a[252:255],a[20:23],v[20:23]\n"
"v_mfma_f32_16x16x32_bf16 v[24:27],a[248:251],a[8:11],v[24:27]\n"
"ds_read_b64_tr_b16 a[228:229],v204 offset:8704\n"
"v_mfma_f32_16x16x32_bf16 v[24:27],a[252:255],a[24:27],v[24:27]\n"
"v_mfma_f32_16x16x32_bf16 v[28:31],a[248:251],a[12:15],v[28:31]\n"
"ds_read_b64_tr_b16 a[230:231],v204 offset:12800\n"
"v_mfma_f32_16x16x32_bf16 v[28:31],a[252:255],a[28:31],v[28:31]\n"
"s_waitcnt lgkmcnt(8)\n"
"v_mfma_f32_16x16x32_bf16 v[32:35],a[208:211],a[0:3],v[32:35]\n"
"ds_read_b64_tr_b16 a[232:233],v204 offset:520\n"
"v_mul_f32 v0,s56,v0\n"
"v_mfma_f32_16x16x32_bf16 v[32:35],a[212:215],a[16:19],v[32:35]\n"
"v_mul_f32 v1,s56,v1\n"
"v_mul_f32 v2,s56,v2\n"
"v_mfma_f32_16x16x32_bf16 v[36:39],a[208:211],a[4:7],v[36:39]\n"
"ds_read_b64_tr_b16 a[234:235],v204 offset:4616\n"
"v_mul_f32 v3,s56,v3\n"
"v_mfma_f32_16x16x32_bf16 v[36:39],a[212:215],a[20:23],v[36:39]\n"
"v_mul_f32 v4,s56,v4\n"
"v_mul_f32 v5,s56,v5\n"
"v_mfma_f32_16x16x32_bf16 v[40:43],a[208:211],a[8:11],v[40:43]\n"
"ds_read_b64_tr_b16 a[236:237],v204 offset:8712\n"
"v_mul_f32 v6,s56,v6\n"
"v_mfma_f32_16x16x32_bf16 v[40:43],a[212:215],a[24:27],v[40:43]\n"
"v_mul_f32 v7,s56,v7\n"
"v_mul_f32 v8,s56,v8\n"
"v_mfma_f32_16x16x32_bf16 v[44:47],a[208:211],a[12:15],v[44:47]\n"
"ds_read_b64_tr_b16 a[238:239],v204 offset:12808\n"
"v_mul_f32 v9,s56,v9\n"
"v_mfma_f32_16x16x32_bf16 v[44:47],a[212:215],a[28:31],v[44:47]\n"
"v_mul_f32 v10,s56,v10\n"
"v_mul_f32 v11,s56,v11\n"
"s_waitcnt lgkmcnt(8)\n"
"v_mfma_f32_16x16x32_bf16 v[48:51],a[216:219],a[0:3],v[48:51]\n"
"ds_read_b64_tr_b16 a[240:241],v204 offset:768\n"
"v_mul_f32 v12,s56,v12\n"
"v_mfma_f32_16x16x32_bf16 v[48:51],a[220:223],a[16:19],v[48:51]\n"
"buffer_load_dwordx4 v199,s[76:79],s41 offen offset:1024 lds\n"
"v_mfma_f32_16x16x32_bf16 v[52:55],a[216:219],a[4:7],v[52:55]\n"
"ds_read_b64_tr_b16 a[242:243],v204 offset:4864\n"
"v_mul_f32 v13,s56,v13\n"
"v_mfma_f32_16x16x32_bf16 v[52:55],a[220:223],a[20:23],v[52:55]\n"
"v_mul_f32 v14,s56,v14\n"
"v_mul_f32 v15,s56,v15\n"
"v_mfma_f32_16x16x32_bf16 v[56:59],a[216:219],a[8:11],v[56:59]\n"
"ds_read_b64_tr_b16 a[244:245],v204 offset:8960\n"
"v_cvt_pk_bf16_f32 v128,v0,v1\n"
"v_mfma_f32_16x16x32_bf16 v[56:59],a[220:223],a[24:27],v[56:59]\n"
"v_cvt_pk_bf16_f32 v129,v2,v3\n"
"v_cvt_pk_bf16_f32 v144,v4,v5\n"
"v_mfma_f32_16x16x32_bf16 v[60:63],a[216:219],a[12:15],v[60:63]\n"
"ds_read_b64_tr_b16 a[246:247],v204 offset:13056\n"
"v_cvt_pk_bf16_f32 v145,v6,v7\n"
"v_mfma_f32_16x16x32_bf16 v[60:63],a[220:223],a[28:31],v[60:63]\n"
"v_cvt_pk_bf16_f32 v160,v8,v9\n"
"v_cvt_pk_bf16_f32 v161,v10,v11\n"
"s_waitcnt lgkmcnt(8)\n"
"v_mfma_f32_16x16x32_bf16 v[64:67],a[224:227],a[0:3],v[64:67]\n"
"ds_read_b64_tr_b16 a[248:249],v204 offset:776\n"
"v_cvt_pk_bf16_f32 v176,v12,v13\n"
"v_mfma_f32_16x16x32_bf16 v[64:67],a[228:231],a[16:19],v[64:67]\n"
"ds_read_b64_tr_b16 a[250:251],v204 offset:4872\n"
"v_cvt_pk_bf16_f32 v177,v14,v15\n"
"v_mfma_f32_16x16x32_bf16 v[68:71],a[224:227],a[4:7],v[68:71]\n"
"ds_read_b64_tr_b16 a[252:253],v204 offset:8968\n"
"v_mul_f32 v16,s56,v16\n"
"v_mfma_f32_16x16x32_bf16 v[68:71],a[228:231],a[20:23],v[68:71]\n"
"ds_read_b64_tr_b16 a[254:255],v204 offset:13064\n"
"v_mul_f32 v17,s56,v17\n"
"v_mfma_f32_16x16x32_bf16 v[72:75],a[224:227],a[8:11],v[72:75]\n"
"v_mul_f32 v18,s56,v18\n"
"v_mul_f32 v19,s56,v19\n"
"v_mfma_f32_16x16x32_bf16 v[72:75],a[228:231],a[24:27],v[72:75]\n"
"v_mul_f32 v20,s56,v20\n"
"v_mul_f32 v21,s56,v21\n"
"v_mfma_f32_16x16x32_bf16 v[76:79],a[224:227],a[12:15],v[76:79]\n"
"v_mul_f32 v22,s56,v22\n"
"v_mul_f32 v23,s56,v23\n"
"v_mfma_f32_16x16x32_bf16 v[76:79],a[228:231],a[28:31],v[76:79]\n"
"v_mul_f32 v24,s56,v24\n"
"v_mul_f32 v25,s56,v25\n"
"s_waitcnt lgkmcnt(0)\n"
"s_barrier\n"
"v_mfma_f32_16x16x32_bf16 v[80:83],a[232:235],a[0:3],v[80:83]\n"
"v_mul_f32 v26,s56,v26\n"
"v_mul_f32 v27,s56,v27\n"
"v_mfma_f32_16x16x32_bf16 v[80:83],a[236:239],a[16:19],v[80:83]\n"
"buffer_load_dwordx4 v200,s[76:79],s41 offen offset:2048 lds\n"
"v_mfma_f32_16x16x32_bf16 v[84:87],a[232:235],a[4:7],v[84:87]\n"
"v_mul_f32 v28,s56,v28\n"
"v_mul_f32 v29,s56,v29\n"
"v_mfma_f32_16x16x32_bf16 v[84:87],a[236:239],a[20:23],v[84:87]\n"
"v_mul_f32 v30,s56,v30\n"
"v_mul_f32 v31,s56,v31\n"
"v_mfma_f32_16x16x32_bf16 v[88:91],a[232:235],a[8:11],v[88:91]\n"
"v_cvt_pk_bf16_f32 v130,v16,v17\n"
"v_cvt_pk_bf16_f32 v131,v18,v19\n"
"v_mfma_f32_16x16x32_bf16 v[88:91],a[236:239],a[24:27],v[88:91]\n"
"v_cvt_pk_bf16_f32 v146,v20,v21\n"
"v_cvt_pk_bf16_f32 v147,v22,v23\n"
"v_mfma_f32_16x16x32_bf16 v[92:95],a[232:235],a[12:15],v[92:95]\n"
"v_cvt_pk_bf16_f32 v162,v24,v25\n"
"v_cvt_pk_bf16_f32 v163,v26,v27\n"
"v_mfma_f32_16x16x32_bf16 v[92:95],a[236:239],a[28:31],v[92:95]\n"
"v_cvt_pk_bf16_f32 v178,v28,v29\n"
"v_cvt_pk_bf16_f32 v179,v30,v31\n"
"v_mfma_f32_16x16x32_bf16 v[96:99],a[240:243],a[0:3],v[96:99]\n"
"v_mul_f32 v32,s56,v32\n"
"v_mul_f32 v33,s56,v33\n"
"v_mfma_f32_16x16x32_bf16 v[96:99],a[244:247],a[16:19],v[96:99]\n"
"v_mul_f32 v34,s56,v34\n"
"v_mul_f32 v35,s56,v35\n"
"v_mfma_f32_16x16x32_bf16 v[100:103],a[240:243],a[4:7],v[100:103]\n"
"v_mul_f32 v36,s56,v36\n"
"v_mul_f32 v37,s56,v37\n"
"v_mfma_f32_16x16x32_bf16 v[100:103],a[244:247],a[20:23],v[100:103]\n"
"v_mul_f32 v38,s56,v38\n"
"v_mul_f32 v39,s56,v39\n"
"v_mfma_f32_16x16x32_bf16 v[104:107],a[240:243],a[8:11],v[104:107]\n"
"v_mul_f32 v40,s56,v40\n"
"v_mul_f32 v41,s56,v41\n"
"v_mfma_f32_16x16x32_bf16 v[104:107],a[244:247],a[24:27],v[104:107]\n"
"v_mul_f32 v42,s56,v42\n"
"v_mul_f32 v43,s56,v43\n"
"v_mfma_f32_16x16x32_bf16 v[108:111],a[240:243],a[12:15],v[108:111]\n"
"v_mul_f32 v44,s56,v44\n"
"v_mul_f32 v45,s56,v45\n"
"v_mfma_f32_16x16x32_bf16 v[108:111],a[244:247],a[28:31],v[108:111]\n"
"v_mul_f32 v46,s56,v46\n"
"v_mul_f32 v47,s56,v47\n"
"v_mfma_f32_16x16x32_bf16 v[112:115],a[248:251],a[0:3],v[112:115]\n"
"v_cvt_pk_bf16_f32 v132,v32,v33\n"
"v_cvt_pk_bf16_f32 v133,v34,v35\n"
"v_mfma_f32_16x16x32_bf16 v[112:115],a[252:255],a[16:19],v[112:115]\n"
"buffer_load_dwordx4 v201,s[76:79],s41 offen offset:3072 lds\n"
"v_mfma_f32_16x16x32_bf16 v[116:119],a[248:251],a[4:7],v[116:119]\n"
"v_cvt_pk_bf16_f32 v148,v36,v37\n"
"v_cvt_pk_bf16_f32 v149,v38,v39\n"
"v_mfma_f32_16x16x32_bf16 v[116:119],a[252:255],a[20:23],v[116:119]\n"
"v_cvt_pk_bf16_f32 v164,v40,v41\n"
"v_cvt_pk_bf16_f32 v165,v42,v43\n"
"v_mfma_f32_16x16x32_bf16 v[120:123],a[248:251],a[8:11],v[120:123]\n"
"v_cvt_pk_bf16_f32 v180,v44,v45\n"
"v_cvt_pk_bf16_f32 v181,v46,v47\n"
"v_mfma_f32_16x16x32_bf16 v[120:123],a[252:255],a[24:27],v[120:123]\n"
"v_mul_f32 v48,s56,v48\n"
"v_mul_f32 v49,s56,v49\n"
"v_mfma_f32_16x16x32_bf16 v[124:127],a[248:251],a[12:15],v[124:127]\n"
"v_mul_f32 v50,s56,v50\n"
"v_mul_f32 v51,s56,v51\n"
"v_mfma_f32_16x16x32_bf16 v[124:127],a[252:255],a[28:31],v[124:127]\n"
"v_mul_f32 v52,s56,v52\n"
"v_mul_f32 v53,s56,v53\n"
"s_add_u32 m0,s63,49152\n"
"s_add_u32 s41,s61,0\n"
"buffer_load_dwordx4 v202,s[80:83],s41 offen lds\n"
"buffer_load_dwordx4 v203,s[80:83],s41 offen offset:1024 lds\n"
"buffer_load_dwordx4 v202,s[80:83],s41 offen offset:2048 lds\n"
"buffer_load_dwordx4 v203,s[80:83],s41 offen offset:3072 lds\n"
"s_add_u32 m0,s62,49152\n"
"s_add_u32 s41,s61,49152\n"
"buffer_load_dwordx4 v198,s[76:79],s41 offen lds\n"
"buffer_load_dwordx4 v199,s[76:79],s41 offen offset:1024 lds\n"
"buffer_load_dwordx4 v200,s[76:79],s41 offen offset:2048 lds\n"
"buffer_load_dwordx4 v201,s[76:79],s41 offen offset:3072 lds\n"
"v_mul_f32 v54,s56,v54\n"
"v_mul_f32 v55,s56,v55\n"
"v_mul_f32 v56,s56,v56\n"
"v_mul_f32 v57,s56,v57\n"
"v_mul_f32 v58,s56,v58\n"
"v_mul_f32 v59,s56,v59\n"
"v_mul_f32 v60,s56,v60\n"
"v_mul_f32 v61,s56,v61\n"
"v_mul_f32 v62,s56,v62\n"
"v_mul_f32 v63,s56,v63\n"
"v_cvt_pk_bf16_f32 v134,v48,v49\n"
"v_cvt_pk_bf16_f32 v135,v50,v51\n"
"v_cvt_pk_bf16_f32 v150,v52,v53\n"
"v_cvt_pk_bf16_f32 v151,v54,v55\n"
"v_cvt_pk_bf16_f32 v166,v56,v57\n"
"v_cvt_pk_bf16_f32 v167,v58,v59\n"
"v_cvt_pk_bf16_f32 v182,v60,v61\n"
"v_cvt_pk_bf16_f32 v183,v62,v63\n"
"v_mul_f32 v64,s56,v64\n"
"v_mul_f32 v65,s56,v65\n"
"v_mul_f32 v66,s56,v66\n"
"v_mul_f32 v67,s56,v67\n"
"v_mul_f32 v68,s56,v68\n"
"v_mul_f32 v69,s56,v69\n"
"v_mul_f32 v70,s56,v70\n"
"v_mul_f32 v71,s56,v71\n"
"v_mul_f32 v72,s56,v72\n"
"v_mul_f32 v73,s56,v73\n"
"v_mul_f32 v74,s56,v74\n"
"v_mul_f32 v75,s56,v75\n"
"v_mul_f32 v76,s56,v76\n"
"v_mul_f32 v77,s56,v77\n"
"v_mul_f32 v78,s56,v78\n"
"v_mul_f32 v79,s56,v79\n"
"v_cvt_pk_bf16_f32 v136,v64,v65\n"
"v_cvt_pk_bf16_f32 v137,v66,v67\n"
"v_cvt_pk_bf16_f32 v152,v68,v69\n"
"v_cvt_pk_bf16_f32 v153,v70,v71\n"
"v_cvt_pk_bf16_f32 v168,v72,v73\n"
"v_cvt_pk_bf16_f32 v169,v74,v75\n"
"v_cvt_pk_bf16_f32 v184,v76,v77\n"
"v_cvt_pk_bf16_f32 v185,v78,v79\n"
"v_mul_f32 v80,s56,v80\n"
"v_mul_f32 v81,s56,v81\n"
"v_mul_f32 v82,s56,v82\n"
"v_mul_f32 v83,s56,v83\n"
"v_mul_f32 v84,s56,v84\n"
"v_mul_f32 v85,s56,v85\n"
"v_mul_f32 v86,s56,v86\n"
"v_mul_f32 v87,s56,v87\n"
"v_mul_f32 v88,s56,v88\n"
"v_mul_f32 v89,s56,v89\n"
"v_mul_f32 v90,s56,v90\n"
"v_mul_f32 v91,s56,v91\n"
"v_mul_f32 v92,s56,v92\n"
"v_mul_f32 v93,s56,v93\n"
"v_mul_f32 v94,s56,v94\n"
"v_mul_f32 v95,s56,v95\n"
"v_cvt_pk_bf16_f32 v138,v80,v81\n"
"v_cvt_pk_bf16_f32 v139,v82,v83\n"
"v_cvt_pk_bf16_f32 v154,v84,v85\n"
"v_cvt_pk_bf16_f32 v155,v86,v87\n"
"v_cvt_pk_bf16_f32 v170,v88,v89\n"
"v_cvt_pk_bf16_f32 v171,v90,v91\n"
"v_cvt_pk_bf16_f32 v186,v92,v93\n"
"v_cvt_pk_bf16_f32 v187,v94,v95\n"
"v_mul_f32 v96,s56,v96\n"
"v_mul_f32 v97,s56,v97\n"
"v_mul_f32 v98,s56,v98\n"
"v_mul_f32 v99,s56,v99\n"
"v_mul_f32 v100,s56,v100\n"
"v_mul_f32 v101,s56,v101\n"
"v_mul_f32 v102,s56,v102\n"
"v_mul_f32 v103,s56,v103\n"
"v_mul_f32 v104,s56,v104\n"
"v_mul_f32 v105,s56,v105\n"
"v_mul_f32 v106,s56,v106\n"
"v_mul_f32 v107,s56,v107\n"
"v_mul_f32 v108,s56,v108\n"
"v_mul_f32 v109,s56,v109\n"
"v_mul_f32 v110,s56,v110\n"
"v_mul_f32 v111,s56,v111\n"
"v_cvt_pk_bf16_f32 v140,v96,v97\n"
"v_cvt_pk_bf16_f32 v141,v98,v99\n"
"v_cvt_pk_bf16_f32 v156,v100,v101\n"
"v_cvt_pk_bf16_f32 v157,v102,v103\n"
"v_cvt_pk_bf16_f32 v172,v104,v105\n"
"v_cvt_pk_bf16_f32 v173,v106,v107\n"
"v_cvt_pk_bf16_f32 v188,v108,v109\n"
"v_cvt_pk_bf16_f32 v189,v110,v111\n"
"v_mul_f32 v112,s56,v112\n"
"v_mul_f32 v113,s56,v113\n"
"v_mul_f32 v114,s56,v114\n"
"v_mul_f32 v115,s56,v115\n"
"v_mul_f32 v116,s56,v116\n"
"v_mul_f32 v117,s56,v117\n"
"v_mul_f32 v118,s56,v118\n"
"v_mul_f32 v119,s56,v119\n"
"v_mul_f32 v120,s56,v120\n"
"v_mul_f32 v121,s56,v121\n"
"v_mul_f32 v122,s56,v122\n"
"v_mul_f32 v123,s56,v123\n"
"v_mul_f32 v124,s56,v124\n"
"v_mul_f32 v125,s56,v125\n"
"v_mul_f32 v126,s56,v126\n"
"v_mul_f32 v127,s56,v127\n"
"v_cvt_pk_bf16_f32 v142,v112,v113\n"
"v_cvt_pk_bf16_f32 v143,v114,v115\n"
"v_cvt_pk_bf16_f32 v158,v116,v117\n"
"v_cvt_pk_bf16_f32 v159,v118,v119\n"
"v_cvt_pk_bf16_f32 v174,v120,v121\n"
"v_cvt_pk_bf16_f32 v175,v122,v123\n"
"v_cvt_pk_bf16_f32 v190,v124,v125\n"
"v_cvt_pk_bf16_f32 v191,v126,v127\n"
"LM_ATT:\n"
"s_waitcnt vmcnt(20)\n"
"s_barrier\n"
"LM_ATT2:\n"
"ds_read_b128 a[128:131],v192\n"
"ds_read_b128 a[132:135],v193\n"
"ds_read_b128 a[136:139],v194\n"
"ds_read_b128 a[140:143],v195\n"
"ds_read_b128 a[144:147],v192 offset:4096\n"
"ds_read_b128 a[148:151],v193 offset:4096\n"
"ds_read_b128 a[152:155],v194 offset:4096\n"
"ds_read_b128 a[156:159],v195 offset:4096\n"
"s_add_u32 s36,s61,65536\n"
"s_add_u32 s37,s61,32768\n"
"s_mov_b32 s38,7\n"
"v_readlane_b32 s92,v248,9\n"
"v_readlane_b32 s93,v248,10\n"
"v_readlane_b32 s94,v248,11\n"
"s_mov_b32 s95,0x20000\n"
"s_mov_b32 s39,0\n"
"s_mov_b32 s40,1024\n"
"v_lshlrev_b32 v63,2,v209\n"
"v_cmp_gt_u32 vcc,1024,v63\n"
"v_mov_b32 v62,0x80000000\n"
"s_nop 0\n"
"v_cndmask_b32 v63,v62,v63,vcc\n"
"v_mov_b32 v232,0x3f803f80\n"
"v_mov_b32 v233,0x3f803f80\n"
"v_mov_b32 v234,0x3f803f80\n"
"v_mov_b32 v235,0x3f803f80\n"
"v_bfe_u32 v61,v209,4,1\n"
"v_mul_u32_u24 v61,0xaaaa,v61\n"
"v_or_b32 v61,0x4444,v61\n"
"v_lshl_or_b32 v61,v61,16,v61\n"
"v_accvgpr_write_b32 a0,0\n"
"v_accvgpr_write_b32 a1,0\n"
"v_accvgpr_write_b32 a2,0\n"
"v_accvgpr_write_b32 a3,0\n"
"v_accvgpr_write_b32 a4,0\n"
"v_accvgpr_write_b32 a5,0\n"
"v_accvgpr_write_b32 a6,0\n"
"v_accvgpr_write_b32 a7,0\n"
"v_accvgpr_write_b32 a8,0\n"
"v_accvgpr_write_b32 a9,0\n"
"v_accvgpr_write_b32 a10,0\n"
"v_accvgpr_write_b32 a11,0\n"
"v_accvgpr_write_b32 a12,0\n"
"v_accvgpr_write_b32 a13,0\n"
"v_accvgpr_write_b32 a14,0\n"
"v_accvgpr_write_b32 a15,0\n"
"v_accvgpr_write_b32 a16,0\n"
"v_accvgpr_write_b32 a17,0\n"
"v_accvgpr_write_b32 a18,0\n"
"v_accvgpr_write_b32 a19,0\n"
"v_accvgpr_write_b32 a20,0\n"
"v_accvgpr_write_b32 a21,0\n"
"v_accvgpr_write_b32 a22,0\n"
"v_accvgpr_write_b32 a23,0\n"
"s_waitcnt lgkmcnt(0)\n"
"v_mfma_f32_16x16x32_bf16 v[0:3],a[128:131],v[128:131],0\n"
"v_accvgpr_write_b32 a24,0\n"
"v_accvgpr_write_b32 a25,0\n"
"v_mfma_f32_16x16x32_bf16 v[0:3],a[132:135],v[132:135],v[0:3]\n"
"v_accvgpr_write_b32 a26,0\n"
"v_accvgpr_write_b32 a27,0\n"
"v_mfma_f32_16x16x32_bf16 v[0:3],a[136:139],v[136:139],v[0:3]\n"
"v_accvgpr_write_b32 a28,0\n"
"v_accvgpr_write_b32 a29,0\n"
"v_mfma_f32_16x16x32_bf16 v[0:3],a[140:143],v[140:143],v[0:3]\n"
"v_accvgpr_write_b32 a30,0\n"
"v_accvgpr_write_b32 a31,0\n"
"v_mfma_f32_16x16x32_bf16 v[4:7],a[128:131],v[144:147],0\n"
"v_accvgpr_write_b32 a32,0\n"
"v_accvgpr_write_b32 a33,0\n"
"v_mfma_f32_16x16x32_bf16 v[4:7],a[132:135],v[148:151],v[4:7]\n"
"v_accvgpr_write_b32 a34,0\n"
"v_accvgpr_write_b32 a35,0\n"
"v_mfma_f32_16x16x32_bf16 v[4:7],a[136:139],v[152:155],v[4:7]\n"
"v_accvgpr_write_b32 a36,0\n"
"v_accvgpr_write_b32 a37,0\n"
"v_mfma_f32_16x16x32_bf16 v[4:7],a[140:143],v[156:159],v[4:7]\n"
"v_accvgpr_write_b32 a38,0\n"
"v_accvgpr_write_b32 a39,0\n"
"v_mfma_f32_16x16x32_bf16 v[8:11],a[128:131],v[160:163],0\n"
"v_accvgpr_write_b32 a40,0\n"
"v_accvgpr_write_b32 a41,0\n"
"v_mfma_f32_16x16x32_bf16 v[8:11],a[132:135],v[164:167],v[8:11]\n"
"v_accvgpr_write_b32 a42,0\n"
"v_accvgpr_write_b32 a43,0\n"
"v_mfma_f32_16x16x32_bf16 v[8:11],a[136:139],v[168:171],v[8:11]\n"
"v_accvgpr_write_b32 a44,0\n"
"v_accvgpr_write_b32 a45,0\n"
"v_mfma_f32_16x16x32_bf16 v[8:11],a[140:143],v[172:175],v[8:11]\n"
"v_accvgpr_write_b32 a46,0\n"
"v_accvgpr_write_b32 a47,0\n"
"v_mfma_f32_16x16x32_bf16 v[12:15],a[128:131],v[176:179],0\n"
"v_accvgpr_write_b32 a48,0\n"
"v_accvgpr_write_b32 a49,0\n"
"v_mfma_f32_16x16x32_bf16 v[12:15],a[132:135],v[180:183],v[12:15]\n"
"v_accvgpr_write_b32 a50,0\n"
"v_accvgpr_write_b32 a51,0\n"
"v_mfma_f32_16x16x32_bf16 v[12:15],a[136:139],v[184:187],v[12:15]\n"
"v_accvgpr_write_b32 a52,0\n"
"v_accvgpr_write_b32 a53,0\n"
"v_mfma_f32_16x16x32_bf16 v[12:15],a[140:143],v[188:191],v[12:15]\n"
"v_accvgpr_write_b32 a54,0\n"
"v_accvgpr_write_b32 a55,0\n"
"v_accvgpr_write_b32 a56,0\n"
"v_accvgpr_write_b32 a57,0\n"
"v_accvgpr_write_b32 a58,0\n"
"v_accvgpr_write_b32 a59,0\n"
"v_accvgpr_write_b32 a60,0\n"
"v_accvgpr_write_b32 a61,0\n"
"v_accvgpr_write_b32 a62,0\n"
"v_accvgpr_write_b32 a63,0\n"
"s_cmp_lg_u32 s54,0\n"
"s_cbranch_scc1 LM_HAVEM\n"
"v_max3_f32 v236,v0,v1,v2\n"
"v_max3_f32 v238,v4,v5,v6\n"
"v_max3_f32 v240,v8,v9,v10\n"
"v_max3_f32 v242,v12,v13,v14\n"
"v_max_f32 v236,v236,v3\n"
"v_max_f32 v238,v238,v7\n"
"v_max_f32 v240,v240,v11\n"
"v_max_f32 v242,v242,v15\n"
"v_mov_b32 v237,v236\n"
"v_mov_b32 v239,v238\n"
"v_mov_b32 v241,v240\n"
"v_mov_b32 v243,v242\n"
"s_nop 1\n"
"v_permlane32_swap_b32 v236,v237\n"
"v_permlane32_swap_b32 v238,v239\n"
"v_permlane32_swap_b32 v240,v241\n"
"v_permlane32_swap_b32 v242,v243\n"
"s_nop 1\n"
"v_max_f32 v236,v236,v237\n"
"v_max_f32 v238,v238,v239\n"
"v_max_f32 v240,v240,v241\n"
"v_max_f32 v242,v242,v243\n"
"v_mov_b32 v237,v236\n"
"v_mov_b32 v239,v238\n"
"v_mov_b32 v241,v240\n"
"v_mov_b32 v243,v242\n"
"s_nop 1\n"
"v_permlane16_swap_b32 v236,v237\n"
"v_permlane16_swap_b32 v238,v239\n"
"v_permlane16_swap_b32 v240,v241\n"
"v_permlane16_swap_b32 v242,v243\n"
"s_nop 1\n"
"v_max_f32 v236,v236,v237\n"
"v_max_f32 v238,v238,v239\n"
"v_max_f32 v240,v240,v241\n"
"v_max_f32 v242,v242,v243\n"
"v_sub_f32 v32,0,v236\n"
"v_sub_f32 v33,0,v236\n"
"v_sub_f32 v34,0,v236\n"
"v_sub_f32 v35,0,v236\n"
"v_sub_f32 v36,0,v238\n"
"v_sub_f32 v37,0,v238\n"
"v_sub_f32 v38,0,v238\n"
"v_sub_f32 v39,0,v238\n"
"v_sub_f32 v40,0,v240\n"
"v_sub_f32 v41,0,v240\n"
"v_sub_f32 v42,0,v240\n"
"v_sub_f32 v43,0,v240\n"
"v_sub_f32 v44,0,v242\n"
"v_sub_f32 v45,0,v242\n"
"v_sub_f32 v46,0,v242\n"
"v_sub_f32 v47,0,v242\n"
"LM_HAVEM:\n"
"v_add_f32 v0,v0,v32\n"
"v_add_f32 v1,v1,v32\n"
"v_add_f32 v2,v2,v32\n"
"v_add_f32 v3,v3,v32\n"
"v_add_f32 v4,v4,v36\n"
"v_add_f32 v5,v5,v36\n"
"v_add_f32 v6,v6,v36\n"
"v_add_f32 v7,v7,v36\n"
"v_add_f32 v8,v8,v40\n"
"v_add_f32 v9,v9,v40\n"
"v_add_f32 v10,v10,v40\n"
"v_add_f32 v11,v11,v40\n"
"v_add_f32 v12,v12,v44\n"
"v_add_f32 v13,v13,v44\n"
"v_add_f32 v14,v14,v44\n"
"v_add_f32 v15,v15,v44\n"
"s_nop 1\n"
"v_mfma_f32_16x16x32_bf16 v[16:19],a[144:147],v[128:131],v[32:35]\n"
"v_exp_f32 v0,v0\n"
"ds_read_b128 a[128:131],v192 offset:8192\n"
"v_accvgpr_write_b32 a64,0\n"
"v_accvgpr_write_b32 a65,0\n"
"v_mfma_f32_16x16x32_bf16 v[16:19],a[148:151],v[132:135],v[16:19]\n"
"v_exp_f32 v1,v1\n"
"ds_read_b128 a[132:135],v193 offset:8192\n"
"v_accvgpr_write_b32 a66,0\n"
"v_accvgpr_write_b32 a67,0\n"
"v_mfma_f32_16x16x32_bf16 v[16:19],a[152:155],v[136:139],v[16:19]\n"
"v_exp_f32 v2,v2\n"
"ds_read_b128 a[136:139],v194 offset:8192\n"
"v_cvt_pk_bf16_f32 v64,v0,v1\n"
"v_accvgpr_write_b32 a68,0\n"
"v_accvgpr_write_b32 a69,0\n"
"v_mfma_f32_16x16x32_bf16 v[16:19],a[156:159],v[140:143],v[16:19]\n"
"v_exp_f32 v3,v3\n"
"ds_read_b128 a[140:143],v195 offset:8192\n"
"v_accvgpr_write_b32 a70,0\n"
"v_accvgpr_write_b32 a71,0\n"
"v_mfma_f32_16x16x32_bf16 v[20:23],a[144:147],v[144:147],v[36:39]\n"
"v_exp_f32 v4,v4\n"
"v_cvt_pk_bf16_f32 v65,v2,v3\n"
"v_accvgpr_write_b32 a72,0\n"
"v_accvgpr_write_b32 a73,0\n"
"v_mfma_f32_16x16x32_bf16 v[20:23],a[148:151],v[148:151],v[20:23]\n"
"v_exp_f32 v5,v5\n"
"v_accvgpr_write_b32 a74,0\n"
"v_accvgpr_write_b32 a75,0\n"
"v_mfma_f32_16x16x32_bf16 v[20:23],a[152:155],v[152:155],v[20:23]\n"
"v_exp_f32 v6,v6\n"
"v_accvgpr_write_b32 a76,0\n"
"v_accvgpr_write_b32 a77,0\n"
"v_mfma_f32_16x16x32_bf16 v[20:23],a[156:159],v[156:159],v[20:23]\n"
"v_exp_f32 v7,v7\n"
"v_accvgpr_write_b32 a78,0\n"
"v_accvgpr_write_b32 a79,0\n"
"v_mfma_f32_16x16x32_bf16 v[24:27],a[144:147],v[160:163],v[40:43]\n"
"v_cvt_pk_bf16_f32 v72,v4,v5\n"
"v_exp_f32 v8,v8\n"
"v_accvgpr_write_b32 a80,0\n"
"v_accvgpr_write_b32 a81,0\n"
"v_mfma_f32_16x16x32_bf16 v[24:27],a[148:151],v[164:167],v[24:27]\n"
"v_cvt_pk_bf16_f32 v73,v6,v7\n"
"v_exp_f32 v9,v9\n"
"v_accvgpr_write_b32 a82,0\n"
"v_accvgpr_write_b32 a83,0\n"
"v_mfma_f32_16x16x32_bf16 v[24:27],a[152:155],v[168:171],v[24:27]\n"
"v_exp_f32 v10,v10\n"
"v_accvgpr_write_b32 a84,0\n"
"v_accvgpr_write_b32 a85,0\n"
"v_mfma_f32_16x16x32_bf16 v[24:27],a[156:159],v[172:175],v[24:27]\n"
"v_cvt_pk_bf16_f32 v80,v8,v9\n"
"v_exp_f32 v11,v11\n"
"v_accvgpr_write_b32 a86,0\n"
"v_accvgpr_write_b32 a87,0\n"
"v_mfma_f32_16x16x32_bf16 v[28:31],a[144:147],v[176:179],v[44:47]\n"
"v_exp_f32 v12,v12\n"
"v_accvgpr_write_b32 a88,0\n"
"v_accvgpr_write_b32 a89,0\n"
"v_mfma_f32_16x16x32_bf16 v[28:31],a[148:151],v[180:183],v[28:31]\n"
"v_cvt_pk_bf16_f32 v81,v10,v11\n"
"v_exp_f32 v13,v13\n"
"v_accvgpr_write_b32 a90,0\n"
"v_accvgpr_write_b32 a91,0\n"
"v_mfma_f32_16x16x32_bf16 v[28:31],a[152:155],v[184:187],v[28:31]\n"
"v_cvt_pk_bf16_f32 v88,v12,v13\n"
"v_exp_f32 v14,v14\n"
"v_accvgpr_write_b32 a92,0\n"
"v_accvgpr_write_b32 a93,0\n"
"v_mfma_f32_16x16x32_bf16 v[28:31],a[156:159],v[188:191],v[28:31]\n"
"v_exp_f32 v15,v15\n"
"v_accvgpr_write_b32 a94,0\n"
"v_accvgpr_write_b32 a95,0\n"
"s_waitcnt lgkmcnt(0)\n"
"v_mfma_f32_16x16x32_bf16 v[0:3],a[128:131],v[128:131],v[32:35]\n"
"v_exp_f32 v16,v16\n"
"ds_read_b128 a[144:147],v192 offset:12288\n"
"v_cvt_pk_bf16_f32 v89,v14,v15\n"
"v_accvgpr_write_b32 a96,0\n"
"v_accvgpr_write_b32 a97,0\n"
"v_mfma_f32_16x16x32_bf16 v[0:3],a[132:135],v[132:135],v[0:3]\n"
"v_exp_f32 v17,v17\n"
"ds_read_b128 a[148:151],v193 offset:12288\n"
"v_accvgpr_write_b32 a98,0\n"
"v_accvgpr_write_b32 a99,0\n"
"v_mfma_f32_16x16x32_bf16 v[0:3],a[136:139],v[136:139],v[0:3]\n"
"v_exp_f32 v18,v18\n"
"ds_read_b128 a[152:155],v194 offset:12288\n"
"v_cvt_pk_bf16_f32 v66,v16,v17\n"
"v_accvgpr_write_b32 a100,0\n"
"v_accvgpr_write_b32 a101,0\n"
"v_mfma_f32_16x16x32_bf16 v[0:3],a[140:143],v[140:143],v[0:3]\n"
"v_exp_f32 v19,v19\n"
"ds_read_b128 a[156:159],v195 offset:12288\n"
"v_accvgpr_write_b32 a102,0\n"
"v_accvgpr_write_b32 a103,0\n"
"v_mfma_f32_16x16x32_bf16 v[4:7],a[128:131],v[144:147],v[36:39]\n"
"v_exp_f32 v20,v20\n"
"v_cvt_pk_bf16_f32 v67,v18,v19\n"
"v_accvgpr_write_b32 a104,0\n"
"v_accvgpr_write_b32 a105,0\n"
"v_mfma_f32_16x16x32_bf16 v[4:7],a[132:135],v[148:151],v[4:7]\n"
"v_exp_f32 v21,v21\n"
"v_accvgpr_write_b32 a106,0\n"
"v_accvgpr_write_b32 a107,0\n"
"v_mfma_f32_16x16x32_bf16 v[4:7],a[136:139],v[152:155],v[4:7]\n"
"v_exp_f32 v22,v22\n"
"v_accvgpr_write_b32 a108,0\n"
"v_accvgpr_write_b32 a109,0\n"
"v_mfma_f32_16x16x32_bf16 v[4:7],a[140:143],v[156:159],v[4:7]\n"
"v_exp_f32 v23,v23\n"
"v_accvgpr_write_b32 a110,0\n"
"v_accvgpr_write_b32 a111,0\n"
"v_mfma_f32_16x16x32_bf16 v[8:11],a[128:131],v[160:163],v[40:43]\n"
"v_cvt_pk_bf16_f32 v74,v20,v21\n"
"v_exp_f32 v24,v24\n"
"v_accvgpr_write_b32 a112,0\n"
"v_accvgpr_write_b32 a113,0\n"
"v_mfma_f32_16x16x32_bf16 v[8:11],a[132:135],v[164:167],v[8:11]\n"
"v_cvt_pk_bf16_f32 v75,v22,v23\n"
"v_exp_f32 v25,v25\n"
"v_accvgpr_write_b32 a114,0\n"
"v_accvgpr_write_b32 a115,0\n"
"v_mfma_f32_16x16x32_bf16 v[8:11],a[136:139],v[168:171],v[8:11]\n"
"v_exp_f32 v26,v26\n"
"v_accvgpr_write_b32 a116,0\n"
"v_accvgpr_write_b32 a117,0\n"
"v_mfma_f32_16x16x32_bf16 v[8:11],a[140:143],v[172:175],v[8:11]\n"
"v_cvt_pk_bf16_f32 v82,v24,v25\n"
"v_exp_f32 v27,v27\n"
"v_accvgpr_write_b32 a118,0\n"
"v_accvgpr_write_b32 a119,0\n"
"v_mfma_f32_16x16x32_bf16 v[12:15],a[128:131],v[176:179],v[44:47]\n"
"v_exp_f32 v28,v28\n"
"v_accvgpr_write_b32 a120,0\n"
"v_accvgpr_write_b32 a121,0\n"
"v_mfma_f32_16x16x32_bf16 v[12:15],a[132:135],v[180:183],v[12:15]\n"
"v_cvt_pk_bf16_f32 v83,v26,v27\n"
"v_exp_f32 v29,v29\n"
"v_accvgpr_write_b32 a122,0\n"
"v_accvgpr_write_b32 a123,0\n"
"v_mfma_f32_16x16x32_bf16 v[12:15],a[136:139],v[184:187],v[12:15]\n"
"v_cvt_pk_bf16_f32 v90,v28,v29\n"
"v_exp_f32 v30,v30\n"
"v_accvgpr_write_b32 a124,0\n"
"v_accvgpr_write_b32 a125,0\n"
"v_mfma_f32_16x16x32_bf16 v[12:15],a[140:143],v[188:191],v[12:15]\n"
"v_exp_f32 v31,v31\n"
"v_accvgpr_write_b32 a126,0\n"
"v_accvgpr_write_b32 a127,0\n"
"s_waitcnt vmcnt(4) lgkmcnt(0)\n"
"s_barrier\n"
"v_readlane_b32 s43,v210,49\n"
"s_nop 0\n"
"s_lshl_b32 s43,s43,6\n"
"v_lshl_add_u32 v223,v209,2,s43\n"
"s_cmp_lg_u32 s48,0\n"
"s_cbranch_scc1 LM_FS8\n"
"s_mov_b64 exec,1\n"
"buffer_store_dwordx2 v[224:225],v223,s[68:71],0 offen sc1\n"
"s_mov_b64 exec,-1\n"
"LM_FS8:\n"
"v_mfma_f32_16x16x32_bf16 v[16:19],a[144:147],v[128:131],v[32:35]\n"
"v_exp_f32 v0,v0\n"
"ds_read_b128 a[128:131],v192 offset:16384\n"
"v_cvt_pk_bf16_f32 v91,v30,v31\n"
"v_accvgpr_write_b32 a192,0\n"
"v_accvgpr_write_b32 a193,0\n"
"v_mfma_f32_16x16x32_bf16 v[16:19],a[148:151],v[132:135],v[16:19]\n"
"v_exp_f32 v1,v1\n"
"ds_read_b128 a[132:135],v193 offset:16384\n"
"v_accvgpr_write_b32 a194,0\n"
"v_accvgpr_write_b32 a195,0\n"
"v_mfma_f32_16x16x32_bf16 v[16:19],a[152:155],v[136:139],v[16:19]\n"
"v_exp_f32 v2,v2\n"
"ds_read_b128 a[136:139],v194 offset:16384\n"
"v_cvt_pk_bf16_f32 v68,v0,v1\n"
"v_accvgpr_write_b32 a196,0\n"
"v_accvgpr_write_b32 a197,0\n"
"v_mfma_f32_16x16x32_bf16 v[16:19],a[156:159],v[140:143],v[16:19]\n"
"v_exp_f32 v3,v3\n"
"ds_read_b128 a[140:143],v195 offset:16384\n"
"v_accvgpr_write_b32 a198,0\n"
"v_accvgpr_write_b32 a199,0\n"
"v_mfma_f32_16x16x32_bf16 v[20:23],a[144:147],v[144:147],v[36:39]\n"
"v_exp_f32 v4,v4\n"
"v_cvt_pk_bf16_f32 v69,v2,v3\n"
"v_accvgpr_write_b32 a200,0\n"
"v_accvgpr_write_b32 a201,0\n"
"v_mfma_f32_16x16x32_bf16 v[20:23],a[148:151],v[148:151],v[20:23]\n"
"v_exp_f32 v5,v5\n"
"v_accvgpr_write_b32 a202,0\n"
"v_accvgpr_write_b32 a203,0\n"
"v_mfma_f32_16x16x32_bf16 v[20:23],a[152:155],v[152:155],v[20:23]\n"
"s_add_u32 m0,s63,16384\n"
"v_exp_f32 v6,v6\n"
"buffer_load_dwordx4 v202,s[80:83],s37 offen lds\n"
"v_accvgpr_write_b32 a204,0\n"
"v_accvgpr_write_b32 a205,0\n"
"v_mfma_f32_16x16x32_bf16 v[20:23],a[156:159],v[156:159],v[20:23]\n"
"v_exp_f32 v7,v7\n"
"v_accvgpr_write_b32 a206,0\n"
"v_accvgpr_write_b32 a207,0\n"
"v_mfma_f32_16x16x32_bf16 v[24:27],a[144:147],v[160:163],v[40:43]\n"
"v_cvt_pk_bf16_f32 v76,v4,v5\n"
"v_exp_f32 v8,v8\n"
"v_mfma_f32_16x16x32_bf16 v[24:27],a[148:151],v[164:167],v[24:27]\n"
"v_cvt_pk_bf16_f32 v77,v6,v7\n"
"v_exp_f32 v9,v9\n"
"v_mfma_f32_16x16x32_bf16 v[24:27],a[152:155],v[168:171],v[24:27]\n"
"v_exp_f32 v10,v10\n"
"v_mfma_f32_16x16x32_bf16 v[24:27],a[156:159],v[172:175],v[24:27]\n"
"v_cvt_pk_bf16_f32 v84,v8,v9\n"
"v_exp_f32 v11,v11\n"
"v_mfma_f32_16x16x32_bf16 v[28:31],a[144:147],v[176:179],v[44:47]\n"
"v_exp_f32 v12,v12\n"
"v_mfma_f32_16x16x32_bf16 v[28:31],a[148:151],v[180:183],v[28:31]\n"
"v_cvt_pk_bf16_f32 v85,v10,v11\n"
"v_exp_f32 v13,v13\n"
"v_mfma_f32_16x16x32_bf16 v[28:31],a[152:155],v[184:187],v[28:31]\n"
"v_cvt_pk_bf16_f32 v92,v12,v13\n"
"v_exp_f32 v14,v14\n"
"v_mfma_f32_16x16x32_bf16 v[28:31],a[156:159],v[188:191],v[28:31]\n"
"v_exp_f32 v15,v15\n"
"buffer_load_dwordx4 v203,s[80:83],s37 offen offset:1024 lds\n"
"s_waitcnt lgkmcnt(0)\n"
"v_mfma_f32_16x16x32_bf16 v[0:3],a[128:131],v[128:131],v[32:35]\n"
"v_exp_f32 v16,v16\n"
"ds_read_b128 a[144:147],v192 offset:20480\n"
"v_cvt_pk_bf16_f32 v93,v14,v15\n"
"v_mfma_f32_16x16x32_bf16 v[0:3],a[132:135],v[132:135],v[0:3]\n"
"v_exp_f32 v17,v17\n"
"ds_read_b128 a[148:151],v193 offset:20480\n"
"v_mfma_f32_16x16x32_bf16 v[0:3],a[136:139],v[136:139],v[0:3]\n"
"v_exp_f32 v18,v18\n"
"ds_read_b128 a[152:155],v194 offset:20480\n"
"v_cvt_pk_bf16_f32 v70,v16,v17\n"
"v_mfma_f32_16x16x32_bf16 v[0:3],a[140:143],v[140:143],v[0:3]\n"
"v_exp_f32 v19,v19\n"
"ds_read_b128 a[156:159],v195 offset:20480\n"
"v_mfma_f32_16x16x32_bf16 v[4:7],a[128:131],v[144:147],v[36:39]\n"
"v_exp_f32 v20,v20\n"
"ds_read_b64_tr_b16 a[160:161],v196 offset:49152\n"
"v_cvt_pk_bf16_f32 v71,v18,v19\n"
"v_mfma_f32_16x16x32_bf16 v[4:7],a[132:135],v[148:151],v[4:7]\n"
"v_exp_f32 v21,v21\n"
"ds_read_b64_tr_b16 a[162:163],v196 offset:53248\n"
"v_mfma_f32_16x16x32_bf16 v[4:7],a[136:139],v[152:155],v[4:7]\n"
"v_exp_f32 v22,v22\n"
"buffer_load_dwordx4 v202,s[80:83],s37 offen offset:2048 lds\n"
"v_mfma_f32_16x16x32_bf16 v[4:7],a[140:143],v[156:159],v[4:7]\n"
"v_exp_f32 v23,v23\n"
"v_mfma_f32_16x16x32_bf16 v[8:11],a[128:131],v[160:163],v[40:43]\n"
"ds_read_b64_tr_b16 a[164:165],v196 offset:57344\n"
"v_cvt_pk_bf16_f32 v78,v20,v21\n"
"v_exp_f32 v24,v24\n"
"v_mfma_f32_16x16x32_bf16 v[8:11],a[132:135],v[164:167],v[8:11]\n"
"ds_read_b64_tr_b16 a[166:167],v196 offset:61440\n"
"v_cvt_pk_bf16_f32 v79,v22,v23\n"
"v_exp_f32 v25,v25\n"
"v_mfma_f32_16x16x32_bf16 v[8:11],a[136:139],v[168:171],v[8:11]\n"
"ds_read_b64_tr_b16 a[168:169],v197 offset:49152\n"
"v_exp_f32 v26,v26\n"
"v_mfma_f32_16x16x32_bf16 v[8:11],a[140:143],v[172:175],v[8:11]\n"
"ds_read_b64_tr_b16 a[170:171],v197 offset:53248\n"
"v_cvt_pk_bf16_f32 v86,v24,v25\n"
"v_exp_f32 v27,v27\n"
"v_mfma_f32_16x16x32_bf16 v[12:15],a[128:131],v[176:179],v[44:47]\n"
"ds_read_b64_tr_b16 a[172:173],v197 offset:57344\n"
"v_exp_f32 v28,v28\n"
"v_mfma_f32_16x16x32_bf16 v[12:15],a[132:135],v[180:183],v[12:15]\n"
"ds_read_b64_tr_b16 a[174:175],v197 offset:61440\n"
"v_cvt_pk_bf16_f32 v87,v26,v27\n"
"v_exp_f32 v29,v29\n"
"v_mfma_f32_16x16x32_bf16 v[12:15],a[136:139],v[184:187],v[12:15]\n"
"v_cvt_pk_bf16_f32 v94,v28,v29\n"
"v_exp_f32 v30,v30\n"
"v_mfma_f32_16x16x32_bf16 v[12:15],a[140:143],v[188:191],v[12:15]\n"
"v_exp_f32 v31,v31\n"
"buffer_load_dwordx4 v203,s[80:83],s37 offen offset:3072 lds\n"
"s_waitcnt lgkmcnt(0)\n"
"LM_TOP:\n"
"s_cmp_lg_u32 s49,0\n"
"s_cbranch_scc1 LM_LN\n"
"LM_TOP1:\n"
"s_cmp_le_u32 s38,2\n"
"s_cbranch_scc1 LM_CW\n"
"LM_TOP2:\n"
"LM_BODY:\n"
"v_mfma_f32_16x16x32_bf16 v[16:19],a[144:147],v[128:131],v[32:35]\n"
"v_exp_f32 v0,v0\n"
"v_mfma_f32_16x16x32_bf16 v[16:19],a[148:151],v[132:135],v[16:19]\n"
"ds_read_b128 a[128:131],v192 offset:24576\n"
"v_cvt_pk_bf16_f32 v95,v30,v31\n"
"v_mfma_f32_16x16x32_bf16 v[16:19],a[152:155],v[136:139],v[16:19]\n"
"s_add_u32 s37,s37,16384\n"
"v_exp_f32 v1,v1\n"
"v_mfma_f32_16x16x32_bf16 v[16:19],a[156:159],v[140:143],v[16:19]\n"
"ds_read_b128 a[132:135],v193 offset:24576\n"
"v_mfma_f32_16x16x32_bf16 v[20:23],a[144:147],v[144:147],v[36:39]\n"
"v_exp_f32 v2,v2\n"
"v_mfma_f32_16x16x32_bf16 v[20:23],a[148:151],v[148:151],v[20:23]\n"
"ds_read_b128 a[136:139],v194 offset:24576\n"
"v_cvt_pk_bf16_f32 v96,v0,v1\n"
"v_mfma_f32_16x16x32_bf16 v[20:23],a[152:155],v[152:155],v[20:23]\n"
"v_exp_f32 v3,v3\n"
"v_mfma_f32_16x16x32_bf16 v[20:23],a[156:159],v[156:159],v[20:23]\n"
"ds_read_b128 a[140:143],v195 offset:24576\n"
"v_mfma_f32_16x16x32_bf16 v[24:27],a[144:147],v[160:163],v[40:43]\n"
"v_exp_f32 v4,v4\n"
"v_mfma_f32_16x16x32_bf16 v[24:27],a[148:151],v[164:167],v[24:27]\n"
"ds_read_b64_tr_b16 a[176:177],v196 offset:49408\n"
"v_cvt_pk_bf16_f32 v97,v2,v3\n"
"v_mfma_f32_16x16x32_bf16 v[24:27],a[152:155],v[168:171],v[24:27]\n"
"v_exp_f32 v5,v5\n"
"v_mfma_f32_16x16x32_bf16 v[24:27],a[156:159],v[172:175],v[24:27]\n"
"ds_read_b64_tr_b16 a[178:179],v196 offset:53504\n"
"v_mfma_f32_16x16x32_bf16 v[28:31],a[144:147],v[176:179],v[44:47]\n"
"s_add_u32 m0,s62,0\n"
"v_exp_f32 v6,v6\n"
"v_mfma_f32_16x16x32_bf16 v[28:31],a[148:151],v[180:183],v[28:31]\n"
"buffer_load_dwordx4 v198,s[76:79],s36 offen lds\n"
"v_mfma_f32_16x16x32_bf16 v[28:31],a[152:155],v[184:187],v[28:31]\n"
"v_mfma_f32_16x16x32_bf16 v[28:31],a[156:159],v[188:191],v[28:31]\n"
"v_exp_f32 v7,v7\n"
"v_mfma_f32_16x16x32_bf16 a[0:3],a[160:163],v[64:67],a[0:3]\n"
"ds_read_b64_tr_b16 a[180:181],v196 offset:57600\n"
"v_cvt_pk_bf16_f32 v104,v4,v5\n"
"v_mfma_f32_16x16x32_bf16 a[0:3],a[164:167],v[68:71],a[0:3]\n"
"v_exp_f32 v8,v8\n"
"v_mfma_f32_16x16x32_bf16 a[4:7],a[160:163],v[72:75],a[4:7]\n"
"ds_read_b64_tr_b16 a[182:183],v196 offset:61696\n"
"v_cvt_pk_bf16_f32 v105,v6,v7\n"
"v_mfma_f32_16x16x32_bf16 a[4:7],a[164:167],v[76:79],a[4:7]\n"
"v_exp_f32 v9,v9\n"
"v_mfma_f32_16x16x32_bf16 a[8:11],a[160:163],v[80:83],a[8:11]\n"
"ds_read_b64_tr_b16 a[184:185],v197 offset:49408\n"
"v_mfma_f32_16x16x32_bf16 a[8:11],a[164:167],v[84:87],a[8:11]\n"
"v_exp_f32 v10,v10\n"
"v_mfma_f32_16x16x32_bf16 a[12:15],a[160:163],v[88:91],a[12:15]\n"
"ds_read_b64_tr_b16 a[186:187],v197 offset:53504\n"
"v_cvt_pk_bf16_f32 v112,v8,v9\n"
"v_mfma_f32_16x16x32_bf16 a[12:15],a[164:167],v[92:95],a[12:15]\n"
"v_exp_f32 v11,v11\n"
"v_mfma_f32_16x16x32_bf16 a[16:19],a[168:171],v[64:67],a[16:19]\n"
"ds_read_b64_tr_b16 a[188:189],v197 offset:57600\n"
"v_mfma_f32_16x16x32_bf16 a[16:19],a[172:175],v[68:71],a[16:19]\n"
"v_exp_f32 v12,v12\n"
"v_mfma_f32_16x16x32_bf16 a[20:23],a[168:171],v[72:75],a[20:23]\n"
"ds_read_b64_tr_b16 a[190:191],v197 offset:61696\n"
"v_cvt_pk_bf16_f32 v113,v10,v11\n"
"v_mfma_f32_16x16x32_bf16 a[20:23],a[172:175],v[76:79],a[20:23]\n"
"v_exp_f32 v13,v13\n"
"v_mfma_f32_16x16x32_bf16 a[24:27],a[168:171],v[80:83],a[24:27]\n"
"v_cvt_pk_bf16_f32 v120,v12,v13\n"
"v_mfma_f32_16x16x32_bf16 a[24:27],a[172:175],v[84:87],a[24:27]\n"
"v_exp_f32 v14,v14\n"
"v_mfma_f32_16x16x32_bf16 a[28:31],a[168:171],v[88:91],a[28:31]\n"
"v_exp_f32 v15,v15\n"
"v_mfma_f32_16x16x32_bf16 a[28:31],a[172:175],v[92:95],a[28:31]\n"
"buffer_load_dwordx4 v199,s[76:79],s36 offen offset:1024 lds\n"
"v_smfmac_f32_16x16x64_bf16 a[192:195],v[232:235],v[64:71],v61\n"
"s_waitcnt lgkmcnt(0)\n"
"v_mfma_f32_16x16x32_bf16 v[0:3],a[128:131],v[128:131],v[32:35]\n"
"v_exp_f32 v16,v16\n"
"v_mfma_f32_16x16x32_bf16 v[0:3],a[132:135],v[132:135],v[0:3]\n"
"ds_read_b128 a[144:147],v192 offset:28672\n"
"v_cvt_pk_bf16_f32 v121,v14,v15\n"
"v_mfma_f32_16x16x32_bf16 v[0:3],a[136:139],v[136:139],v[0:3]\n"
"v_exp_f32 v17,v17\n"
"v_mfma_f32_16x16x32_bf16 v[0:3],a[140:143],v[140:143],v[0:3]\n"
"ds_read_b128 a[148:151],v193 offset:28672\n"
"v_mfma_f32_16x16x32_bf16 v[4:7],a[128:131],v[144:147],v[36:39]\n"
"v_exp_f32 v18,v18\n"
"v_mfma_f32_16x16x32_bf16 v[4:7],a[132:135],v[148:151],v[4:7]\n"
"ds_read_b128 a[152:155],v194 offset:28672\n"
"v_cvt_pk_bf16_f32 v98,v16,v17\n"
"v_mfma_f32_16x16x32_bf16 v[4:7],a[136:139],v[152:155],v[4:7]\n"
"v_exp_f32 v19,v19\n"
"v_mfma_f32_16x16x32_bf16 v[4:7],a[140:143],v[156:159],v[4:7]\n"
"ds_read_b128 a[156:159],v195 offset:28672\n"
"v_mfma_f32_16x16x32_bf16 v[8:11],a[128:131],v[160:163],v[40:43]\n"
"v_exp_f32 v20,v20\n"
"v_mfma_f32_16x16x32_bf16 v[8:11],a[132:135],v[164:167],v[8:11]\n"
"ds_read_b64_tr_b16 a[160:161],v196 offset:49664\n"
"v_cvt_pk_bf16_f32 v99,v18,v19\n"
"v_mfma_f32_16x16x32_bf16 v[8:11],a[136:139],v[168:171],v[8:11]\n"
"v_exp_f32 v21,v21\n"
"v_mfma_f32_16x16x32_bf16 v[8:11],a[140:143],v[172:175],v[8:11]\n"
"ds_read_b64_tr_b16 a[162:163],v196 offset:53760\n"
"v_mfma_f32_16x16x32_bf16 v[12:15],a[128:131],v[176:179],v[44:47]\n"
"v_exp_f32 v22,v22\n"
"v_mfma_f32_16x16x32_bf16 v[12:15],a[132:135],v[180:183],v[12:15]\n"
"buffer_load_dwordx4 v200,s[76:79],s36 offen offset:2048 lds\n"
"v_mfma_f32_16x16x32_bf16 v[12:15],a[136:139],v[184:187],v[12:15]\n"
"buffer_load_dword v62,v63,s[92:95],s39 offen sc1\n"
"v_mfma_f32_16x16x32_bf16 v[12:15],a[140:143],v[188:191],v[12:15]\n"
"v_exp_f32 v23,v23\n"
"v_mfma_f32_16x16x32_bf16 a[32:35],a[176:179],v[64:67],a[32:35]\n"
"ds_read_b64_tr_b16 a[164:165],v196 offset:57856\n"
"v_cvt_pk_bf16_f32 v106,v20,v21\n"
"v_mfma_f32_16x16x32_bf16 a[32:35],a[180:183],v[68:71],a[32:35]\n"
"v_exp_f32 v24,v24\n"
"v_mfma_f32_16x16x32_bf16 a[36:39],a[176:179],v[72:75],a[36:39]\n"
"ds_read_b64_tr_b16 a[166:167],v196 offset:61952\n"
"v_cvt_pk_bf16_f32 v107,v22,v23\n"
"v_mfma_f32_16x16x32_bf16 a[36:39],a[180:183],v[76:79],a[36:39]\n"
"v_exp_f32 v25,v25\n"
"v_mfma_f32_16x16x32_bf16 a[40:43],a[176:179],v[80:83],a[40:43]\n"
"ds_read_b64_tr_b16 a[168:169],v197 offset:49664\n"
"v_mfma_f32_16x16x32_bf16 a[40:43],a[180:183],v[84:87],a[40:43]\n"
"v_exp_f32 v26,v26\n"
"v_mfma_f32_16x16x32_bf16 a[44:47],a[176:179],v[88:91],a[44:47]\n"
"ds_read_b64_tr_b16 a[170:171],v197 offset:53760\n"
"v_cvt_pk_bf16_f32 v114,v24,v25\n"
"v_mfma_f32_16x16x32_bf16 a[44:47],a[180:183],v[92:95],a[44:47]\n"
"v_exp_f32 v27,v27\n"
"v_mfma_f32_16x16x32_bf16 a[48:51],a[184:187],v[64:67],a[48:51]\n"
"ds_read_b64_tr_b16 a[172:173],v197 offset:57856\n"
"v_mfma_f32_16x16x32_bf16 a[48:51],a[188:191],v[68:71],a[48:51]\n"
"v_exp_f32 v28,v28\n"
"v_mfma_f32_16x16x32_bf16 a[52:55],a[184:187],v[72:75],a[52:55]\n"
"ds_read_b64_tr_b16 a[174:175],v197 offset:61952\n"
"v_cvt_pk_bf16_f32 v115,v26,v27\n"
"v_mfma_f32_16x16x32_bf16 a[52:55],a[188:191],v[76:79],a[52:55]\n"
"v_exp_f32 v29,v29\n"
"v_mfma_f32_16x16x32_bf16 a[56:59],a[184:187],v[80:83],a[56:59]\n"
"v_cvt_pk_bf16_f32 v122,v28,v29\n"
"v_mfma_f32_16x16x32_bf16 a[56:59],a[188:191],v[84:87],a[56:59]\n"
"s_add_u32 s39,s39,s40\n"
"v_exp_f32 v30,v30\n"
"v_mfma_f32_16x16x32_bf16 a[60:63],a[184:187],v[88:91],a[60:63]\n"
"v_exp_f32 v31,v31\n"
"v_mfma_f32_16x16x32_bf16 a[60:63],a[188:191],v[92:95],a[60:63]\n"
"buffer_load_dwordx4 v201,s[76:79],s36 offen offset:3072 lds\n"
"v_smfmac_f32_16x16x64_bf16 a[196:199],v[232:235],v[72:79],v61\n"
"s_waitcnt vmcnt(12) lgkmcnt(0)\n"
"s_barrier\n"
"v_mfma_f32_16x16x32_bf16 v[16:19],a[144:147],v[128:131],v[32:35]\n"
"v_exp_f32 v0,v0\n"
"v_mfma_f32_16x16x32_bf16 v[16:19],a[148:151],v[132:135],v[16:19]\n"
"ds_read_b128 a[128:131],v192 offset:32768\n"
"v_cvt_pk_bf16_f32 v123,v30,v31\n"
"v_mfma_f32_16x16x32_bf16 v[16:19],a[152:155],v[136:139],v[16:19]\n"
"s_add_u32 s36,s36,16384\n"
"v_exp_f32 v1,v1\n"
"v_mfma_f32_16x16x32_bf16 v[16:19],a[156:159],v[140:143],v[16:19]\n"
"ds_read_b128 a[132:135],v193 offset:32768\n"
"v_mfma_f32_16x16x32_bf16 v[20:23],a[144:147],v[144:147],v[36:39]\n"
"v_exp_f32 v2,v2\n"
"v_mfma_f32_16x16x32_bf16 v[20:23],a[148:151],v[148:151],v[20:23]\n"
"ds_read_b128 a[136:139],v194 offset:32768\n"
"v_cvt_pk_bf16_f32 v100,v0,v1\n"
"v_mfma_f32_16x16x32_bf16 v[20:23],a[152:155],v[152:155],v[20:23]\n"
"v_exp_f32 v3,v3\n"
"v_mfma_f32_16x16x32_bf16 v[20:23],a[156:159],v[156:159],v[20:23]\n"
"ds_read_b128 a[140:143],v195 offset:32768\n"
"v_mfma_f32_16x16x32_bf16 v[24:27],a[144:147],v[160:163],v[40:43]\n"
"v_exp_f32 v4,v4\n"
"v_mfma_f32_16x16x32_bf16 v[24:27],a[148:151],v[164:167],v[24:27]\n"
"ds_read_b64_tr_b16 a[176:177],v196 offset:49920\n"
"v_cvt_pk_bf16_f32 v101,v2,v3\n"
"v_mfma_f32_16x16x32_bf16 v[24:27],a[152:155],v[168:171],v[24:27]\n"
"v_exp_f32 v5,v5\n"
"v_mfma_f32_16x16x32_bf16 v[24:27],a[156:159],v[172:175],v[24:27]\n"
"ds_read_b64_tr_b16 a[178:179],v196 offset:54016\n"
"v_mfma_f32_16x16x32_bf16 v[28:31],a[144:147],v[176:179],v[44:47]\n"
"s_add_u32 m0,s63,32768\n"
"v_exp_f32 v6,v6\n"
"v_mfma_f32_16x16x32_bf16 v[28:31],a[148:151],v[180:183],v[28:31]\n"
"buffer_load_dwordx4 v202,s[80:83],s37 offen lds\n"
"v_mfma_f32_16x16x32_bf16 v[28:31],a[152:155],v[184:187],v[28:31]\n"
"v_mfma_f32_16x16x32_bf16 v[28:31],a[156:159],v[188:191],v[28:31]\n"
"v_exp_f32 v7,v7\n"
"v_mfma_f32_16x16x32_bf16 a[64:67],a[160:163],v[64:67],a[64:67]\n"
"ds_read_b64_tr_b16 a[180:181],v196 offset:58112\n"
"v_cvt_pk_bf16_f32 v108,v4,v5\n"
"v_mfma_f32_16x16x32_bf16 a[64:67],a[164:167],v[68:71],a[64:67]\n"
"v_exp_f32 v8,v8\n"
"v_mfma_f32_16x16x32_bf16 a[68:71],a[160:163],v[72:75],a[68:71]\n"
"ds_read_b64_tr_b16 a[182:183],v196 offset:62208\n"
"v_cvt_pk_bf16_f32 v109,v6,v7\n"
"v_mfma_f32_16x16x32_bf16 a[68:71],a[164:167],v[76:79],a[68:71]\n"
"v_exp_f32 v9,v9\n"
"v_mfma_f32_16x16x32_bf16 a[72:75],a[160:163],v[80:83],a[72:75]\n"
"ds_read_b64_tr_b16 a[184:185],v197 offset:49920\n"
"v_mfma_f32_16x16x32_bf16 a[72:75],a[164:167],v[84:87],a[72:75]\n"
"v_exp_f32 v10,v10\n"
"v_mfma_f32_16x16x32_bf16 a[76:79],a[160:163],v[88:91],a[76:79]\n"
"ds_read_b64_tr_b16 a[186:187],v197 offset:54016\n"
"v_cvt_pk_bf16_f32 v116,v8,v9\n"
"v_mfma_f32_16x16x32_bf16 a[76:79],a[164:167],v[92:95],a[76:79]\n"
"v_exp_f32 v11,v11\n"
"v_mfma_f32_16x16x32_bf16 a[80:83],a[168:171],v[64:67],a[80:83]\n"
"ds_read_b64_tr_b16 a[188:189],v197 offset:58112\n"
"v_mfma_f32_16x16x32_bf16 a[80:83],a[172:175],v[68:71],a[80:83]\n"
"v_exp_f32 v12,v12\n"
"v_mfma_f32_16x16x32_bf16 a[84:87],a[168:171],v[72:75],a[84:87]\n"
"ds_read_b64_tr_b16 a[190:191],v197 offset:62208\n"
"v_cvt_pk_bf16_f32 v117,v10,v11\n"
"v_mfma_f32_16x16x32_bf16 a[84:87],a[172:175],v[76:79],a[84:87]\n"
"v_exp_f32 v13,v13\n"
"v_mfma_f32_16x16x32_bf16 a[88:91],a[168:171],v[80:83],a[88:91]\n"
"v_cvt_pk_bf16_f32 v124,v12,v13\n"
"v_mfma_f32_16x16x32_bf16 a[88:91],a[172:175],v[84:87],a[88:91]\n"
"v_exp_f32 v14,v14\n"
"v_mfma_f32_16x16x32_bf16 a[92:95],a[168:171],v[88:91],a[92:95]\n"
"v_exp_f32 v15,v15\n"
"v_mfma_f32_16x16x32_bf16 a[92:95],a[172:175],v[92:95],a[92:95]\n"
"buffer_load_dwordx4 v203,s[80:83],s37 offen offset:1024 lds\n"
"v_smfmac_f32_16x16x64_bf16 a[200:203],v[232:235],v[80:87],v61\n"
"s_waitcnt lgkmcnt(0)\n"
"v_mfma_f32_16x16x32_bf16 v[0:3],a[128:131],v[128:131],v[32:35]\n"
"v_exp_f32 v16,v16\n"
"v_mfma_f32_16x16x32_bf16 v[0:3],a[132:135],v[132:135],v[0:3]\n"
"ds_read_b128 a[144:147],v192 offset:36864\n"
"v_cvt_pk_bf16_f32 v125,v14,v15\n"
"v_mfma_f32_16x16x32_bf16 v[0:3],a[136:139],v[136:139],v[0:3]\n"
"v_exp_f32 v17,v17\n"
"v_mfma_f32_16x16x32_bf16 v[0:3],a[140:143],v[140:143],v[0:3]\n"
"ds_read_b128 a[148:151],v193 offset:36864\n"
"v_mfma_f32_16x16x32_bf16 v[4:7],a[128:131],v[144:147],v[36:39]\n"
"v_exp_f32 v18,v18\n"
"v_mfma_f32_16x16x32_bf16 v[4:7],a[132:135],v[148:151],v[4:7]\n"
"ds_read_b128 a[152:155],v194 offset:36864\n"
"v_cvt_pk_bf16_f32 v102,v16,v17\n"
"v_mfma_f32_16x16x32_bf16 v[4:7],a[136:139],v[152:155],v[4:7]\n"
"v_exp_f32 v19,v19\n"
"v_mfma_f32_16x16x32_bf16 v[4:7],a[140:143],v[156:159],v[4:7]\n"
"ds_read_b128 a[156:159],v195 offset:36864\n"
"v_mfma_f32_16x16x32_bf16 v[8:11],a[128:131],v[160:163],v[40:43]\n"
"v_exp_f32 v20,v20\n"
"v_mfma_f32_16x16x32_bf16 v[8:11],a[132:135],v[164:167],v[8:11]\n"
"ds_read_b64_tr_b16 a[160:161],v196\n"
"v_cvt_pk_bf16_f32 v103,v18,v19\n"
"v_mfma_f32_16x16x32_bf16 v[8:11],a[136:139],v[168:171],v[8:11]\n"
"v_exp_f32 v21,v21\n"
"v_mfma_f32_16x16x32_bf16 v[8:11],a[140:143],v[172:175],v[8:11]\n"
"ds_read_b64_tr_b16 a[162:163],v196 offset:4096\n"
"v_mfma_f32_16x16x32_bf16 v[12:15],a[128:131],v[176:179],v[44:47]\n"
"v_exp_f32 v22,v22\n"
"v_mfma_f32_16x16x32_bf16 v[12:15],a[132:135],v[180:183],v[12:15]\n"
"buffer_load_dwordx4 v202,s[80:83],s37 offen offset:2048 lds\n"
"v_mfma_f32_16x16x32_bf16 v[12:15],a[136:139],v[184:187],v[12:15]\n"
"v_mfma_f32_16x16x32_bf16 v[12:15],a[140:143],v[188:191],v[12:15]\n"
"v_exp_f32 v23,v23\n"
"v_mfma_f32_16x16x32_bf16 a[96:99],a[176:179],v[64:67],a[96:99]\n"
"ds_read_b64_tr_b16 a[164:165],v196 offset:8192\n"
"v_cvt_pk_bf16_f32 v110,v20,v21\n"
"v_mfma_f32_16x16x32_bf16 a[96:99],a[180:183],v[68:71],a[96:99]\n"
"v_exp_f32 v24,v24\n"
"v_mfma_f32_16x16x32_bf16 a[100:103],a[176:179],v[72:75],a[100:103]\n"
"ds_read_b64_tr_b16 a[166:167],v196 offset:12288\n"
"v_cvt_pk_bf16_f32 v111,v22,v23\n"
"v_mfma_f32_16x16x32_bf16 a[100:103],a[180:183],v[76:79],a[100:103]\n"
"v_exp_f32 v25,v25\n"
"v_mfma_f32_16x16x32_bf16 a[104:107],a[176:179],v[80:83],a[104:107]\n"
"ds_read_b64_tr_b16 a[168:169],v197\n"
"v_mfma_f32_16x16x32_bf16 a[104:107],a[180:183],v[84:87],a[104:107]\n"
"v_exp_f32 v26,v26\n"
"v_mfma_f32_16x16x32_bf16 a[108:111],a[176:179],v[88:91],a[108:111]\n"
"ds_read_b64_tr_b16 a[170:171],v197 offset:4096\n"
"v_cvt_pk_bf16_f32 v118,v24,v25\n"
"v_mfma_f32_16x16x32_bf16 a[108:111],a[180:183],v[92:95],a[108:111]\n"
"v_exp_f32 v27,v27\n"
"v_mfma_f32_16x16x32_bf16 a[112:115],a[184:187],v[64:67],a[112:115]\n"
"ds_read_b64_tr_b16 a[172:173],v197 offset:8192\n"
"v_mfma_f32_16x16x32_bf16 a[112:115],a[188:191],v[68:71],a[112:115]\n"
"v_exp_f32 v28,v28\n"
"v_mfma_f32_16x16x32_bf16 a[116:119],a[184:187],v[72:75],a[116:119]\n"
"ds_read_b64_tr_b16 a[174:175],v197 offset:12288\n"
"v_cvt_pk_bf16_f32 v119,v26,v27\n"
"v_mfma_f32_16x16x32_bf16 a[116:119],a[188:191],v[76:79],a[116:119]\n"
"v_exp_f32 v29,v29\n"
"v_mfma_f32_16x16x32_bf16 a[120:123],a[184:187],v[80:83],a[120:123]\n"
"v_cvt_pk_bf16_f32 v126,v28,v29\n"
"v_mfma_f32_16x16x32_bf16 a[120:123],a[188:191],v[84:87],a[120:123]\n"
"v_exp_f32 v30,v30\n"
"v_mfma_f32_16x16x32_bf16 a[124:127],a[184:187],v[88:91],a[124:127]\n"
"v_exp_f32 v31,v31\n"
"v_mfma_f32_16x16x32_bf16 a[124:127],a[188:191],v[92:95],a[124:127]\n"
"buffer_load_dwordx4 v203,s[80:83],s37 offen offset:3072 lds\n"
"v_smfmac_f32_16x16x64_bf16 a[204:207],v[232:235],v[88:95],v61\n"
"s_waitcnt lgkmcnt(0)\n"
"v_mfma_f32_16x16x32_bf16 v[16:19],a[144:147],v[128:131],v[32:35]\n"
"v_exp_f32 v0,v0\n"
"v_mfma_f32_16x16x32_bf16 v[16:19],a[148:151],v[132:135],v[16:19]\n"
"ds_read_b128 a[128:131],v192 offset:40960\n"
"v_cvt_pk_bf16_f32 v127,v30,v31\n"
"v_mfma_f32_16x16x32_bf16 v[16:19],a[152:155],v[136:139],v[16:19]\n"
"s_add_u32 s37,s37,16384\n"
"v_exp_f32 v1,v1\n"
"v_mfma_f32_16x16x32_bf16 v[16:19],a[156:159],v[140:143],v[16:19]\n"
"ds_read_b128 a[132:135],v193 offset:40960\n"
"v_mfma_f32_16x16x32_bf16 v[20:23],a[144:147],v[144:147],v[36:39]\n"
"v_exp_f32 v2,v2\n"
"v_mfma_f32_16x16x32_bf16 v[20:23],a[148:151],v[148:151],v[20:23]\n"
"ds_read_b128 a[136:139],v194 offset:40960\n"
"v_cvt_pk_bf16_f32 v64,v0,v1\n"
"v_mfma_f32_16x16x32_bf16 v[20:23],a[152:155],v[152:155],v[20:23]\n"
"v_exp_f32 v3,v3\n"
"v_mfma_f32_16x16x32_bf16 v[20:23],a[156:159],v[156:159],v[20:23]\n"
"ds_read_b128 a[140:143],v195 offset:40960\n"
"v_mfma_f32_16x16x32_bf16 v[24:27],a[144:147],v[160:163],v[40:43]\n"
"v_exp_f32 v4,v4\n"
"v_mfma_f32_16x16x32_bf16 v[24:27],a[148:151],v[164:167],v[24:27]\n"
"ds_read_b64_tr_b16 a[176:177],v196 offset:256\n"
"v_cvt_pk_bf16_f32 v65,v2,v3\n"
"v_mfma_f32_16x16x32_bf16 v[24:27],a[152:155],v[168:171],v[24:27]\n"
"v_exp_f32 v5,v5\n"
"v_mfma_f32_16x16x32_bf16 v[24:27],a[156:159],v[172:175],v[24:27]\n"
"ds_read_b64_tr_b16 a[178:179],v196 offset:4352\n"
"v_mfma_f32_16x16x32_bf16 v[28:31],a[144:147],v[176:179],v[44:47]\n"
"s_add_u32 m0,s62,16384\n"
"v_exp_f32 v6,v6\n"
"v_mfma_f32_16x16x32_bf16 v[28:31],a[148:151],v[180:183],v[28:31]\n"
"buffer_load_dwordx4 v198,s[76:79],s36 offen lds\n"
"v_mfma_f32_16x16x32_bf16 v[28:31],a[152:155],v[184:187],v[28:31]\n"
"v_mfma_f32_16x16x32_bf16 v[28:31],a[156:159],v[188:191],v[28:31]\n"
"v_exp_f32 v7,v7\n"
"v_mfma_f32_16x16x32_bf16 a[0:3],a[160:163],v[96:99],a[0:3]\n"
"ds_read_b64_tr_b16 a[180:181],v196 offset:8448\n"
"v_cvt_pk_bf16_f32 v72,v4,v5\n"
"v_mfma_f32_16x16x32_bf16 a[0:3],a[164:167],v[100:103],a[0:3]\n"
"v_exp_f32 v8,v8\n"
"v_mfma_f32_16x16x32_bf16 a[4:7],a[160:163],v[104:107],a[4:7]\n"
"ds_read_b64_tr_b16 a[182:183],v196 offset:12544\n"
"v_cvt_pk_bf16_f32 v73,v6,v7\n"
"v_mfma_f32_16x16x32_bf16 a[4:7],a[164:167],v[108:111],a[4:7]\n"
"v_exp_f32 v9,v9\n"
"v_mfma_f32_16x16x32_bf16 a[8:11],a[160:163],v[112:115],a[8:11]\n"
"ds_read_b64_tr_b16 a[184:185],v197 offset:256\n"
"v_mfma_f32_16x16x32_bf16 a[8:11],a[164:167],v[116:119],a[8:11]\n"
"v_exp_f32 v10,v10\n"
"v_mfma_f32_16x16x32_bf16 a[12:15],a[160:163],v[120:123],a[12:15]\n"
"ds_read_b64_tr_b16 a[186:187],v197 offset:4352\n"
"v_cvt_pk_bf16_f32 v80,v8,v9\n"
"v_mfma_f32_16x16x32_bf16 a[12:15],a[164:167],v[124:127],a[12:15]\n"
"v_exp_f32 v11,v11\n"
"v_mfma_f32_16x16x32_bf16 a[16:19],a[168:171],v[96:99],a[16:19]\n"
"ds_read_b64_tr_b16 a[188:189],v197 offset:8448\n"
"v_mfma_f32_16x16x32_bf16 a[16:19],a[172:175],v[100:103],a[16:19]\n"
"v_exp_f32 v12,v12\n"
"v_mfma_f32_16x16x32_bf16 a[20:23],a[168:171],v[104:107],a[20:23]\n"
"ds_read_b64_tr_b16 a[190:191],v197 offset:12544\n"
"v_cvt_pk_bf16_f32 v81,v10,v11\n"
"v_mfma_f32_16x16x32_bf16 a[20:23],a[172:175],v[108:111],a[20:23]\n"
"v_exp_f32 v13,v13\n"
"v_mfma_f32_16x16x32_bf16 a[24:27],a[168:171],v[112:115],a[24:27]\n"
"v_cvt_pk_bf16_f32 v88,v12,v13\n"
"v_mfma_f32_16x16x32_bf16 a[24:27],a[172:175],v[116:119],a[24:27]\n"
"v_exp_f32 v14,v14\n"
"v_mfma_f32_16x16x32_bf16 a[28:31],a[168:171],v[120:123],a[28:31]\n"
"v_exp_f32 v15,v15\n"
"v_mfma_f32_16x16x32_bf16 a[28:31],a[172:175],v[124:127],a[28:31]\n"
"buffer_load_dwordx4 v199,s[76:79],s36 offen offset:1024 lds\n"
"v_smfmac_f32_16x16x64_bf16 a[192:195],v[232:235],v[96:103],v61\n"
"s_waitcnt lgkmcnt(0)\n"
"v_mfma_f32_16x16x32_bf16 v[0:3],a[128:131],v[128:131],v[32:35]\n"
"v_exp_f32 v16,v16\n"
"v_mfma_f32_16x16x32_bf16 v[0:3],a[132:135],v[132:135],v[0:3]\n"
"ds_read_b128 a[144:147],v192 offset:45056\n"
"v_cvt_pk_bf16_f32 v89,v14,v15\n"
"v_mfma_f32_16x16x32_bf16 v[0:3],a[136:139],v[136:139],v[0:3]\n"
"v_exp_f32 v17,v17\n"
"v_mfma_f32_16x16x32_bf16 v[0:3],a[140:143],v[140:143],v[0:3]\n"
"ds_read_b128 a[148:151],v193 offset:45056\n"
"v_mfma_f32_16x16x32_bf16 v[4:7],a[128:131],v[144:147],v[36:39]\n"
"v_exp_f32 v18,v18\n"
"v_mfma_f32_16x16x32_bf16 v[4:7],a[132:135],v[148:151],v[4:7]\n"
"ds_read_b128 a[152:155],v194 offset:45056\n"
"v_cvt_pk_bf16_f32 v66,v16,v17\n"
"v_mfma_f32_16x16x32_bf16 v[4:7],a[136:139],v[152:155],v[4:7]\n"
"v_exp_f32 v19,v19\n"
"v_mfma_f32_16x16x32_bf16 v[4:7],a[140:143],v[156:159],v[4:7]\n"
"ds_read_b128 a[156:159],v195 offset:45056\n"
"v_mfma_f32_16x16x32_bf16 v[8:11],a[128:131],v[160:163],v[40:43]\n"
"v_exp_f32 v20,v20\n"
"v_mfma_f32_16x16x32_bf16 v[8:11],a[132:135],v[164:167],v[8:11]\n"
"ds_read_b64_tr_b16 a[160:161],v196 offset:512\n"
"v_cvt_pk_bf16_f32 v67,v18,v19\n"
"v_mfma_f32_16x16x32_bf16 v[8:11],a[136:139],v[168:171],v[8:11]\n"
"v_exp_f32 v21,v21\n"
"v_mfma_f32_16x16x32_bf16 v[8:11],a[140:143],v[172:175],v[8:11]\n"
"ds_read_b64_tr_b16 a[162:163],v196 offset:4608\n"
"v_mfma_f32_16x16x32_bf16 v[12:15],a[128:131],v[176:179],v[44:47]\n"
"v_exp_f32 v22,v22\n"
"v_mfma_f32_16x16x32_bf16 v[12:15],a[132:135],v[180:183],v[12:15]\n"
"buffer_load_dwordx4 v200,s[76:79],s36 offen offset:2048 lds\n"
"v_mfma_f32_16x16x32_bf16 v[12:15],a[136:139],v[184:187],v[12:15]\n"
"buffer_load_dword v62,v63,s[92:95],s39 offen sc1\n"
"v_mfma_f32_16x16x32_bf16 v[12:15],a[140:143],v[188:191],v[12:15]\n"
"v_exp_f32 v23,v23\n"
"v_mfma_f32_16x16x32_bf16 a[32:35],a[176:179],v[96:99],a[32:35]\n"
"ds_read_b64_tr_b16 a[164:165],v196 offset:8704\n"
"v_cvt_pk_bf16_f32 v74,v20,v21\n"
"v_mfma_f32_16x16x32_bf16 a[32:35],a[180:183],v[100:103],a[32:35]\n"
"v_exp_f32 v24,v24\n"
"v_mfma_f32_16x16x32_bf16 a[36:39],a[176:179],v[104:107],a[36:39]\n"
"ds_read_b64_tr_b16 a[166:167],v196 offset:12800\n"
"v_cvt_pk_bf16_f32 v75,v22,v23\n"
"v_mfma_f32_16x16x32_bf16 a[36:39],a[180:183],v[108:111],a[36:39]\n"
"v_exp_f32 v25,v25\n"
"v_mfma_f32_16x16x32_bf16 a[40:43],a[176:179],v[112:115],a[40:43]\n"
"ds_read_b64_tr_b16 a[168:169],v197 offset:512\n"
"v_mfma_f32_16x16x32_bf16 a[40:43],a[180:183],v[116:119],a[40:43]\n"
"v_exp_f32 v26,v26\n"
"v_mfma_f32_16x16x32_bf16 a[44:47],a[176:179],v[120:123],a[44:47]\n"
"ds_read_b64_tr_b16 a[170:171],v197 offset:4608\n"
"v_cvt_pk_bf16_f32 v82,v24,v25\n"
"v_mfma_f32_16x16x32_bf16 a[44:47],a[180:183],v[124:127],a[44:47]\n"
"v_exp_f32 v27,v27\n"
"v_mfma_f32_16x16x32_bf16 a[48:51],a[184:187],v[96:99],a[48:51]\n"
"ds_read_b64_tr_b16 a[172:173],v197 offset:8704\n"
"v_mfma_f32_16x16x32_bf16 a[48:51],a[188:191],v[100:103],a[48:51]\n"
"v_exp_f32 v28,v28\n"
"v_mfma_f32_16x16x32_bf16 a[52:55],a[184:187],v[104:107],a[52:55]\n"
"ds_read_b64_tr_b16 a[174:175],v197 offset:12800\n"
"v_cvt_pk_bf16_f32 v83,v26,v27\n"
"v_mfma_f32_16x16x32_bf16 a[52:55],a[188:191],v[108:111],a[52:55]\n"
"v_exp_f32 v29,v29\n"
"v_mfma_f32_16x16x32_bf16 a[56:59],a[184:187],v[112:115],a[56:59]\n"
"v_cvt_pk_bf16_f32 v90,v28,v29\n"
"v_mfma_f32_16x16x32_bf16 a[56:59],a[188:191],v[116:119],a[56:59]\n"
"s_add_u32 s39,s39,s40\n"
"v_exp_f32 v30,v30\n"
"v_mfma_f32_16x16x32_bf16 a[60:63],a[184:187],v[120:123],a[60:63]\n"
"v_exp_f32 v31,v31\n"
"v_mfma_f32_16x16x32_bf16 a[60:63],a[188:191],v[124:127],a[60:63]\n"
"buffer_load_dwordx4 v201,s[76:79],s36 offen offset:3072 lds\n"
"v_smfmac_f32_16x16x64_bf16 a[196:199],v[232:235],v[104:111],v61\n"
"s_waitcnt vmcnt(12) lgkmcnt(0)\n"
"s_barrier\n"
"v_mfma_f32_16x16x32_bf16 v[16:19],a[144:147],v[128:131],v[32:35]\n"
"v_exp_f32 v0,v0\n"
"v_mfma_f32_16x16x32_bf16 v[16:19],a[148:151],v[132:135],v[16:19]\n"
"ds_read_b128 a[128:131],v192 offset:49152\n"
"v_cvt_pk_bf16_f32 v91,v30,v31\n"
"v_mfma_f32_16x16x32_bf16 v[16:19],a[152:155],v[136:139],v[16:19]\n"
"s_add_u32 s36,s36,16384\n"
"v_exp_f32 v1,v1\n"
"v_mfma_f32_16x16x32_bf16 v[16:19],a[156:159],v[140:143],v[16:19]\n"
"ds_read_b128 a[132:135],v193 offset:49152\n"
"v_mfma_f32_16x16x32_bf16 v[20:23],a[144:147],v[144:147],v[36:39]\n"
"v_exp_f32 v2,v2\n"
"v_mfma_f32_16x16x32_bf16 v[20:23],a[148:151],v[148:151],v[20:23]\n"
"ds_read_b128 a[136:139],v194 offset:49152\n"
"v_cvt_pk_bf16_f32 v68,v0,v1\n"
"v_mfma_f32_16x16x32_bf16 v[20:23],a[152:155],v[152:155],v[20:23]\n"
"v_exp_f32 v3,v3\n"
"v_mfma_f32_16x16x32_bf16 v[20:23],a[156:159],v[156:159],v[20:23]\n"
"ds_read_b128 a[140:143],v195 offset:49152\n"
"v_mfma_f32_16x16x32_bf16 v[24:27],a[144:147],v[160:163],v[40:43]\n"
"v_exp_f32 v4,v4\n"
"v_mfma_f32_16x16x32_bf16 v[24:27],a[148:151],v[164:167],v[24:27]\n"
"ds_read_b64_tr_b16 a[176:177],v196 offset:768\n"
"v_cvt_pk_bf16_f32 v69,v2,v3\n"
"v_mfma_f32_16x16x32_bf16 v[24:27],a[152:155],v[168:171],v[24:27]\n"
"v_exp_f32 v5,v5\n"
"v_mfma_f32_16x16x32_bf16 v[24:27],a[156:159],v[172:175],v[24:27]\n"
"ds_read_b64_tr_b16 a[178:179],v196 offset:4864\n"
"v_mfma_f32_16x16x32_bf16 v[28:31],a[144:147],v[176:179],v[44:47]\n"
"s_add_u32 m0,s63,49152\n"
"v_exp_f32 v6,v6\n"
"v_mfma_f32_16x16x32_bf16 v[28:31],a[148:151],v[180:183],v[28:31]\n"
"buffer_load_dwordx4 v202,s[80:83],s37 offen lds\n"
"v_mfma_f32_16x16x32_bf16 v[28:31],a[152:155],v[184:187],v[28:31]\n"
"v_mfma_f32_16x16x32_bf16 v[28:31],a[156:159],v[188:191],v[28:31]\n"
"v_exp_f32 v7,v7\n"
"v_mfma_f32_16x16x32_bf16 a[64:67],a[160:163],v[96:99],a[64:67]\n"
"ds_read_b64_tr_b16 a[180:181],v196 offset:8960\n"
"v_cvt_pk_bf16_f32 v76,v4,v5\n"
"v_mfma_f32_16x16x32_bf16 a[64:67],a[164:167],v[100:103],a[64:67]\n"
"v_exp_f32 v8,v8\n"
"v_mfma_f32_16x16x32_bf16 a[68:71],a[160:163],v[104:107],a[68:71]\n"
"ds_read_b64_tr_b16 a[182:183],v196 offset:13056\n"
"v_cvt_pk_bf16_f32 v77,v6,v7\n"
"v_mfma_f32_16x16x32_bf16 a[68:71],a[164:167],v[108:111],a[68:71]\n"
"v_exp_f32 v9,v9\n"
"v_mfma_f32_16x16x32_bf16 a[72:75],a[160:163],v[112:115],a[72:75]\n"
"ds_read_b64_tr_b16 a[184:185],v197 offset:768\n"
"v_mfma_f32_16x16x32_bf16 a[72:75],a[164:167],v[116:119],a[72:75]\n"
"v_exp_f32 v10,v10\n"
"v_mfma_f32_16x16x32_bf16 a[76:79],a[160:163],v[120:123],a[76:79]\n"
"ds_read_b64_tr_b16 a[186:187],v197 offset:4864\n"
"v_cvt_pk_bf16_f32 v84,v8,v9\n"
"v_mfma_f32_16x16x32_bf16 a[76:79],a[164:167],v[124:127],a[76:79]\n"
"v_exp_f32 v11,v11\n"
"v_mfma_f32_16x16x32_bf16 a[80:83],a[168:171],v[96:99],a[80:83]\n"
"ds_read_b64_tr_b16 a[188:189],v197 offset:8960\n"
"v_mfma_f32_16x16x32_bf16 a[80:83],a[172:175],v[100:103],a[80:83]\n"
"v_exp_f32 v12,v12\n"
"v_mfma_f32_16x16x32_bf16 a[84:87],a[168:171],v[104:107],a[84:87]\n"
"ds_read_b64_tr_b16 a[190:191],v197 offset:13056\n"
"v_cvt_pk_bf16_f32 v85,v10,v11\n"
"v_mfma_f32_16x16x32_bf16 a[84:87],a[172:175],v[108:111],a[84:87]\n"
"v_exp_f32 v13,v13\n"
"v_mfma_f32_16x16x32_bf16 a[88:91],a[168:171],v[112:115],a[88:91]\n"
"v_cvt_pk_bf16_f32 v92,v12,v13\n"
"v_mfma_f32_16x16x32_bf16 a[88:91],a[172:175],v[116:119],a[88:91]\n"
"v_exp_f32 v14,v14\n"
"v_mfma_f32_16x16x32_bf16 a[92:95],a[168:171],v[120:123],a[92:95]\n"
"v_exp_f32 v15,v15\n"
"v_mfma_f32_16x16x32_bf16 a[92:95],a[172:175],v[124:127],a[92:95]\n"
"buffer_load_dwordx4 v203,s[80:83],s37 offen offset:1024 lds\n"
"v_smfmac_f32_16x16x64_bf16 a[200:203],v[232:235],v[112:119],v61\n"
"s_waitcnt lgkmcnt(0)\n"
"v_mfma_f32_16x16x32_bf16 v[0:3],a[128:131],v[128:131],v[32:35]\n"
"v_exp_f32 v16,v16\n"
"v_mfma_f32_16x16x32_bf16 v[0:3],a[132:135],v[132:135],v[0:3]\n"
"ds_read_b128 a[144:147],v192 offset:53248\n"
"v_cvt_pk_bf16_f32 v93,v14,v15\n"
"v_mfma_f32_16x16x32_bf16 v[0:3],a[136:139],v[136:139],v[0:3]\n"
"v_exp_f32 v17,v17\n"
"v_mfma_f32_16x16x32_bf16 v[0:3],a[140:143],v[140:143],v[0:3]\n"
"ds_read_b128 a[148:151],v193 offset:53248\n"
"v_mfma_f32_16x16x32_bf16 v[4:7],a[128:131],v[144:147],v[36:39]\n"
"v_exp_f32 v18,v18\n"
"v_mfma_f32_16x16x32_bf16 v[4:7],a[132:135],v[148:151],v[4:7]\n"
"ds_read_b128 a[152:155],v194 offset:53248\n"
"v_cvt_pk_bf16_f32 v70,v16,v17\n"
"v_mfma_f32_16x16x32_bf16 v[4:7],a[136:139],v[152:155],v[4:7]\n"
"v_exp_f32 v19,v19\n"
"v_mfma_f32_16x16x32_bf16 v[4:7],a[140:143],v[156:159],v[4:7]\n"
"ds_read_b128 a[156:159],v195 offset:53248\n"
"v_mfma_f32_16x16x32_bf16 v[8:11],a[128:131],v[160:163],v[40:43]\n"
"v_exp_f32 v20,v20\n"
"v_mfma_f32_16x16x32_bf16 v[8:11],a[132:135],v[164:167],v[8:11]\n"
"ds_read_b64_tr_b16 a[160:161],v196 offset:16384\n"
"v_cvt_pk_bf16_f32 v71,v18,v19\n"
"v_mfma_f32_16x16x32_bf16 v[8:11],a[136:139],v[168:171],v[8:11]\n"
"v_exp_f32 v21,v21\n"
"v_mfma_f32_16x16x32_bf16 v[8:11],a[140:143],v[172:175],v[8:11]\n"
"ds_read_b64_tr_b16 a[162:163],v196 offset:20480\n"
"v_mfma_f32_16x16x32_bf16 v[12:15],a[128:131],v[176:179],v[44:47]\n"
"v_exp_f32 v22,v22\n"
"v_mfma_f32_16x16x32_bf16 v[12:15],a[132:135],v[180:183],v[12:15]\n"
"buffer_load_dwordx4 v202,s[80:83],s37 offen offset:2048 lds\n"
"v_mfma_f32_16x16x32_bf16 v[12:15],a[136:139],v[184:187],v[12:15]\n"
"v_mfma_f32_16x16x32_bf16 v[12:15],a[140:143],v[188:191],v[12:15]\n"
"v_exp_f32 v23,v23\n"
"v_mfma_f32_16x16x32_bf16 a[96:99],a[176:179],v[96:99],a[96:99]\n"
"ds_read_b64_tr_b16 a[164:165],v196 offset:24576\n"
"v_cvt_pk_bf16_f32 v78,v20,v21\n"
"v_mfma_f32_16x16x32_bf16 a[96:99],a[180:183],v[100:103],a[96:99]\n"
"v_exp_f32 v24,v24\n"
"v_mfma_f32_16x16x32_bf16 a[100:103],a[176:179],v[104:107],a[100:103]\n"
"ds_read_b64_tr_b16 a[166:167],v196 offset:28672\n"
"v_cvt_pk_bf16_f32 v79,v22,v23\n"
"v_mfma_f32_16x16x32_bf16 a[100:103],a[180:183],v[108:111],a[100:103]\n"
"v_exp_f32 v25,v25\n"
"v_mfma_f32_16x16x32_bf16 a[104:107],a[176:179],v[112:115],a[104:107]\n"
"ds_read_b64_tr_b16 a[168:169],v197 offset:16384\n"
"v_mfma_f32_16x16x32_bf16 a[104:107],a[180:183],v[116:119],a[104:107]\n"
"v_exp_f32 v26,v26\n"
"v_mfma_f32_16x16x32_bf16 a[108:111],a[176:179],v[120:123],a[108:111]\n"
"ds_read_b64_tr_b16 a[170:171],v197 offset:20480\n"
"v_cvt_pk_bf16_f32 v86,v24,v25\n"
"v_mfma_f32_16x16x32_bf16 a[108:111],a[180:183],v[124:127],a[108:111]\n"
"v_exp_f32 v27,v27\n"
"v_mfma_f32_16x16x32_bf16 a[112:115],a[184:187],v[96:99],a[112:115]\n"
"ds_read_b64_tr_b16 a[172:173],v197 offset:24576\n"
"v_mfma_f32_16x16x32_bf16 a[112:115],a[188:191],v[100:103],a[112:115]\n"
"v_exp_f32 v28,v28\n"
"v_mfma_f32_16x16x32_bf16 a[116:119],a[184:187],v[104:107],a[116:119]\n"
"ds_read_b64_tr_b16 a[174:175],v197 offset:28672\n"
"v_cvt_pk_bf16_f32 v87,v26,v27\n"
"v_mfma_f32_16x16x32_bf16 a[116:119],a[188:191],v[108:111],a[116:119]\n"
"v_exp_f32 v29,v29\n"
"v_mfma_f32_16x16x32_bf16 a[120:123],a[184:187],v[112:115],a[120:123]\n"
"v_cvt_pk_bf16_f32 v94,v28,v29\n"
"v_mfma_f32_16x16x32_bf16 a[120:123],a[188:191],v[116:119],a[120:123]\n"
"v_exp_f32 v30,v30\n"
"v_mfma_f32_16x16x32_bf16 a[124:127],a[184:187],v[120:123],a[124:127]\n"
"v_exp_f32 v31,v31\n"
"v_mfma_f32_16x16x32_bf16 a[124:127],a[188:191],v[124:127],a[124:127]\n"
"buffer_load_dwordx4 v203,s[80:83],s37 offen offset:3072 lds\n"
"v_smfmac_f32_16x16x64_bf16 a[204:207],v[232:235],v[120:127],v61\n"
"s_waitcnt lgkmcnt(0)\n"
"v_mfma_f32_16x16x32_bf16 v[16:19],a[144:147],v[128:131],v[32:35]\n"
"v_exp_f32 v0,v0\n"
"v_mfma_f32_16x16x32_bf16 v[16:19],a[148:151],v[132:135],v[16:19]\n"
"ds_read_b128 a[128:131],v192 offset:57344\n"
"v_cvt_pk_bf16_f32 v95,v30,v31\n"
"v_mfma_f32_16x16x32_bf16 v[16:19],a[152:155],v[136:139],v[16:19]\n"
"s_add_u32 s37,s37,16384\n"
"v_exp_f32 v1,v1\n"
"v_mfma_f32_16x16x32_bf16 v[16:19],a[156:159],v[140:143],v[16:19]\n"
"ds_read_b128 a[132:135],v193 offset:57344\n"
"v_mfma_f32_16x16x32_bf16 v[20:23],a[144:147],v[144:147],v[36:39]\n"
"v_exp_f32 v2,v2\n"
"v_mfma_f32_16x16x32_bf16 v[20:23],a[148:151],v[148:151],v[20:23]\n"
"ds_read_b128 a[136:139],v194 offset:57344\n"
"v_cvt_pk_bf16_f32 v96,v0,v1\n"
"v_mfma_f32_16x16x32_bf16 v[20:23],a[152:155],v[152:155],v[20:23]\n"
"v_exp_f32 v3,v3\n"
"v_mfma_f32_16x16x32_bf16 v[20:23],a[156:159],v[156:159],v[20:23]\n"
"ds_read_b128 a[140:143],v195 offset:57344\n"
"v_mfma_f32_16x16x32_bf16 v[24:27],a[144:147],v[160:163],v[40:43]\n"
"v_exp_f32 v4,v4\n"
"v_mfma_f32_16x16x32_bf16 v[24:27],a[148:151],v[164:167],v[24:27]\n"
"ds_read_b64_tr_b16 a[176:177],v196 offset:16640\n"
"v_cvt_pk_bf16_f32 v97,v2,v3\n"
"v_mfma_f32_16x16x32_bf16 v[24:27],a[152:155],v[168:171],v[24:27]\n"
"v_exp_f32 v5,v5\n"
"v_mfma_f32_16x16x32_bf16 v[24:27],a[156:159],v[172:175],v[24:27]\n"
"ds_read_b64_tr_b16 a[178:179],v196 offset:20736\n"
"v_mfma_f32_16x16x32_bf16 v[28:31],a[144:147],v[176:179],v[44:47]\n"
"s_add_u32 m0,s62,32768\n"
"v_exp_f32 v6,v6\n"
"v_mfma_f32_16x16x32_bf16 v[28:31],a[148:151],v[180:183],v[28:31]\n"
"buffer_load_dwordx4 v198,s[76:79],s36 offen lds\n"
"v_mfma_f32_16x16x32_bf16 v[28:31],a[152:155],v[184:187],v[28:31]\n"
"v_mfma_f32_16x16x32_bf16 v[28:31],a[156:159],v[188:191],v[28:31]\n"
"v_exp_f32 v7,v7\n"
"v_mfma_f32_16x16x32_bf16 a[0:3],a[160:163],v[64:67],a[0:3]\n"
"ds_read_b64_tr_b16 a[180:181],v196 offset:24832\n"
"v_cvt_pk_bf16_f32 v104,v4,v5\n"
"v_mfma_f32_16x16x32_bf16 a[0:3],a[164:167],v[68:71],a[0:3]\n"
"v_exp_f32 v8,v8\n"
"v_mfma_f32_16x16x32_bf16 a[4:7],a[160:163],v[72:75],a[4:7]\n"
"ds_read_b64_tr_b16 a[182:183],v196 offset:28928\n"
"v_cvt_pk_bf16_f32 v105,v6,v7\n"
"v_mfma_f32_16x16x32_bf16 a[4:7],a[164:167],v[76:79],a[4:7]\n"
"v_exp_f32 v9,v9\n"
"v_mfma_f32_16x16x32_bf16 a[8:11],a[160:163],v[80:83],a[8:11]\n"
"ds_read_b64_tr_b16 a[184:185],v197 offset:16640\n"
"v_mfma_f32_16x16x32_bf16 a[8:11],a[164:167],v[84:87],a[8:11]\n"
"v_exp_f32 v10,v10\n"
"v_mfma_f32_16x16x32_bf16 a[12:15],a[160:163],v[88:91],a[12:15]\n"
"ds_read_b64_tr_b16 a[186:187],v197 offset:20736\n"
"v_cvt_pk_bf16_f32 v112,v8,v9\n"
"v_mfma_f32_16x16x32_bf16 a[12:15],a[164:167],v[92:95],a[12:15]\n"
"v_exp_f32 v11,v11\n"
"v_mfma_f32_16x16x32_bf16 a[16:19],a[168:171],v[64:67],a[16:19]\n"
"ds_read_b64_tr_b16 a[188:189],v197 offset:24832\n"
"v_mfma_f32_16x16x32_bf16 a[16:19],a[172:175],v[68:71],a[16:19]\n"
"v_exp_f32 v12,v12\n"
"v_mfma_f32_16x16x32_bf16 a[20:23],a[168:171],v[72:75],a[20:23]\n"
"ds_read_b64_tr_b16 a[190:191],v197 offset:28928\n"
"v_cvt_pk_bf16_f32 v113,v10,v11\n"
"v_mfma_f32_16x16x32_bf16 a[20:23],a[172:175],v[76:79],a[20:23]\n"
"v_exp_f32 v13,v13\n"
"v_mfma_f32_16x16x32_bf16 a[24:27],a[168:171],v[80:83],a[24:27]\n"
"v_cvt_pk_bf16_f32 v120,v12,v13\n"
"v_mfma_f32_16x16x32_bf16 a[24:27],a[172:175],v[84:87],a[24:27]\n"
"v_exp_f32 v14,v14\n"
"v_mfma_f32_16x16x32_bf16 a[28:31],a[168:171],v[88:91],a[28:31]\n"
"v_exp_f32 v15,v15\n"
"v_mfma_f32_16x16x32_bf16 a[28:31],a[172:175],v[92:95],a[28:31]\n"
"buffer_load_dwordx4 v199,s[76:79],s36 offen offset:1024 lds\n"
"v_smfmac_f32_16x16x64_bf16 a[192:195],v[232:235],v[64:71],v61\n"
"s_waitcnt lgkmcnt(0)\n"
"v_mfma_f32_16x16x32_bf16 v[0:3],a[128:131],v[128:131],v[32:35]\n"
"v_exp_f32 v16,v16\n"
"v_mfma_f32_16x16x32_bf16 v[0:3],a[132:135],v[132:135],v[0:3]\n"
"ds_read_b128 a[144:147],v192 offset:61440\n"
"v_cvt_pk_bf16_f32 v121,v14,v15\n"
"v_mfma_f32_16x16x32_bf16 v[0:3],a[136:139],v[136:139],v[0:3]\n"
"v_exp_f32 v17,v17\n"
"v_mfma_f32_16x16x32_bf16 v[0:3],a[140:143],v[140:143],v[0:3]\n"
"ds_read_b128 a[148:151],v193 offset:61440\n"
"v_mfma_f32_16x16x32_bf16 v[4:7],a[128:131],v[144:147],v[36:39]\n"
"v_exp_f32 v18,v18\n"
"v_mfma_f32_16x16x32_bf16 v[4:7],a[132:135],v[148:151],v[4:7]\n"
"ds_read_b128 a[152:155],v194 offset:61440\n"
"v_cvt_pk_bf16_f32 v98,v16,v17\n"
"v_mfma_f32_16x16x32_bf16 v[4:7],a[136:139],v[152:155],v[4:7]\n"
"v_exp_f32 v19,v19\n"
"v_mfma_f32_16x16x32_bf16 v[4:7],a[140:143],v[156:159],v[4:7]\n"
"ds_read_b128 a[156:159],v195 offset:61440\n"
"v_mfma_f32_16x16x32_bf16 v[8:11],a[128:131],v[160:163],v[40:43]\n"
"v_exp_f32 v20,v20\n"
"v_mfma_f32_16x16x32_bf16 v[8:11],a[132:135],v[164:167],v[8:11]\n"
"ds_read_b64_tr_b16 a[160:161],v196 offset:16896\n"
"v_cvt_pk_bf16_f32 v99,v18,v19\n"
"v_mfma_f32_16x16x32_bf16 v[8:11],a[136:139],v[168:171],v[8:11]\n"
"v_exp_f32 v21,v21\n"
"v_mfma_f32_16x16x32_bf16 v[8:11],a[140:143],v[172:175],v[8:11]\n"
"ds_read_b64_tr_b16 a[162:163],v196 offset:20992\n"
"v_mfma_f32_16x16x32_bf16 v[12:15],a[128:131],v[176:179],v[44:47]\n"
"v_exp_f32 v22,v22\n"
"v_mfma_f32_16x16x32_bf16 v[12:15],a[132:135],v[180:183],v[12:15]\n"
"buffer_load_dwordx4 v200,s[76:79],s36 offen offset:2048 lds\n"
"v_mfma_f32_16x16x32_bf16 v[12:15],a[136:139],v[184:187],v[12:15]\n"
"buffer_load_dword v62,v63,s[92:95],s39 offen sc1\n"
"v_mfma_f32_16x16x32_bf16 v[12:15],a[140:143],v[188:191],v[12:15]\n"
"v_exp_f32 v23,v23\n"
"v_mfma_f32_16x16x32_bf16 a[32:35],a[176:179],v[64:67],a[32:35]\n"
"ds_read_b64_tr_b16 a[164:165],v196 offset:25088\n"
"v_cvt_pk_bf16_f32 v106,v20,v21\n"
"v_mfma_f32_16x16x32_bf16 a[32:35],a[180:183],v[68:71],a[32:35]\n"
"v_exp_f32 v24,v24\n"
"v_mfma_f32_16x16x32_bf16 a[36:39],a[176:179],v[72:75],a[36:39]\n"
"ds_read_b64_tr_b16 a[166:167],v196 offset:29184\n"
"v_cvt_pk_bf16_f32 v107,v22,v23\n"
"v_mfma_f32_16x16x32_bf16 a[36:39],a[180:183],v[76:79],a[36:39]\n"
"v_exp_f32 v25,v25\n"
"v_mfma_f32_16x16x32_bf16 a[40:43],a[176:179],v[80:83],a[40:43]\n"
"ds_read_b64_tr_b16 a[168:169],v197 offset:16896\n"
"v_mfma_f32_16x16x32_bf16 a[40:43],a[180:183],v[84:87],a[40:43]\n"
"v_exp_f32 v26,v26\n"
"v_mfma_f32_16x16x32_bf16 a[44:47],a[176:179],v[88:91],a[44:47]\n"
"ds_read_b64_tr_b16 a[170:171],v197 offset:20992\n"
"v_cvt_pk_bf16_f32 v114,v24,v25\n"
"v_mfma_f32_16x16x32_bf16 a[44:47],a[180:183],v[92:95],a[44:47]\n"
"v_exp_f32 v27,v27\n"
"v_mfma_f32_16x16x32_bf16 a[48:51],a[184:187],v[64:67],a[48:51]\n"
"ds_read_b64_tr_b16 a[172:173],v197 offset:25088\n"
"v_mfma_f32_16x16x32_bf16 a[48:51],a[188:191],v[68:71],a[48:51]\n"
"v_exp_f32 v28,v28\n"
"v_mfma_f32_16x16x32_bf16 a[52:55],a[184:187],v[72:75],a[52:55]\n"
"ds_read_b64_tr_b16 a[174:175],v197 offset:29184\n"
"v_cvt_pk_bf16_f32 v115,v26,v27\n"
"v_mfma_f32_16x16x32_bf16 a[52:55],a[188:191],v[76:79],a[52:55]\n"
"v_exp_f32 v29,v29\n"
"v_mfma_f32_16x16x32_bf16 a[56:59],a[184:187],v[80:83],a[56:59]\n"
"v_cvt_pk_bf16_f32 v122,v28,v29\n"
"v_mfma_f32_16x16x32_bf16 a[56:59],a[188:191],v[84:87],a[56:59]\n"
"s_add_u32 s39,s39,s40\n"
"v_exp_f32 v30,v30\n"
"v_mfma_f32_16x16x32_bf16 a[60:63],a[184:187],v[88:91],a[60:63]\n"
"v_exp_f32 v31,v31\n"
"v_mfma_f32_16x16x32_bf16 a[60:63],a[188:191],v[92:95],a[60:63]\n"
"buffer_load_dwordx4 v201,s[76:79],s36 offen offset:3072 lds\n"
"v_smfmac_f32_16x16x64_bf16 a[196:199],v[232:235],v[72:79],v61\n"
"s_waitcnt vmcnt(12) lgkmcnt(0)\n"
"s_barrier\n"
"v_mfma_f32_16x16x32_bf16 v[16:19],a[144:147],v[128:131],v[32:35]\n"
"v_exp_f32 v0,v0\n"
"v_mfma_f32_16x16x32_bf16 v[16:19],a[148:151],v[132:135],v[16:19]\n"
"ds_read_b128 a[128:131],v192\n"
"v_cvt_pk_bf16_f32 v123,v30,v31\n"
"v_mfma_f32_16x16x32_bf16 v[16:19],a[152:155],v[136:139],v[16:19]\n"
"s_add_u32 s36,s36,16384\n"
"v_exp_f32 v1,v1\n"
"v_mfma_f32_16x16x32_bf16 v[16:19],a[156:159],v[140:143],v[16:19]\n"
"ds_read_b128 a[132:135],v193\n"
"v_mfma_f32_16x16x32_bf16 v[20:23],a[144:147],v[144:147],v[36:39]\n"
"v_exp_f32 v2,v2\n"
"v_mfma_f32_16x16x32_bf16 v[20:23],a[148:151],v[148:151],v[20:23]\n"
"ds_read_b128 a[136:139],v194\n"
"v_cvt_pk_bf16_f32 v100,v0,v1\n"
"v_mfma_f32_16x16x32_bf16 v[20:23],a[152:155],v[152:155],v[20:23]\n"
"v_exp_f32 v3,v3\n"
"v_mfma_f32_16x16x32_bf16 v[20:23],a[156:159],v[156:159],v[20:23]\n"
"ds_read_b128 a[140:143],v195\n"
"v_mfma_f32_16x16x32_bf16 v[24:27],a[144:147],v[160:163],v[40:43]\n"
"v_exp_f32 v4,v4\n"
"v_mfma_f32_16x16x32_bf16 v[24:27],a[148:151],v[164:167],v[24:27]\n"
"ds_read_b64_tr_b16 a[176:177],v196 offset:17152\n"
"v_cvt_pk_bf16_f32 v101,v2,v3\n"
"v_mfma_f32_16x16x32_bf16 v[24:27],a[152:155],v[168:171],v[24:27]\n"
"v_exp_f32 v5,v5\n"
"v_mfma_f32_16x16x32_bf16 v[24:27],a[156:159],v[172:175],v[24:27]\n"
"ds_read_b64_tr_b16 a[178:179],v196 offset:21248\n"
"v_mfma_f32_16x16x32_bf16 v[28:31],a[144:147],v[176:179],v[44:47]\n"
"s_add_u32 m0,s63,0\n"
"v_exp_f32 v6,v6\n"
"v_mfma_f32_16x16x32_bf16 v[28:31],a[148:151],v[180:183],v[28:31]\n"
"buffer_load_dwordx4 v202,s[80:83],s37 offen lds\n"
"v_mfma_f32_16x16x32_bf16 v[28:31],a[152:155],v[184:187],v[28:31]\n"
"v_mfma_f32_16x16x32_bf16 v[28:31],a[156:159],v[188:191],v[28:31]\n"
"v_exp_f32 v7,v7\n"
"v_mfma_f32_16x16x32_bf16 a[64:67],a[160:163],v[64:67],a[64:67]\n"
"ds_read_b64_tr_b16 a[180:181],v196 offset:25344\n"
"v_cvt_pk_bf16_f32 v108,v4,v5\n"
"v_mfma_f32_16x16x32_bf16 a[64:67],a[164:167],v[68:71],a[64:67]\n"
"v_exp_f32 v8,v8\n"
"v_mfma_f32_16x16x32_bf16 a[68:71],a[160:163],v[72:75],a[68:71]\n"
"ds_read_b64_tr_b16 a[182:183],v196 offset:29440\n"
"v_cvt_pk_bf16_f32 v109,v6,v7\n"
"v_mfma_f32_16x16x32_bf16 a[68:71],a[164:167],v[76:79],a[68:71]\n"
"v_exp_f32 v9,v9\n"
"v_mfma_f32_16x16x32_bf16 a[72:75],a[160:163],v[80:83],a[72:75]\n"
"ds_read_b64_tr_b16 a[184:185],v197 offset:17152\n"
"v_mfma_f32_16x16x32_bf16 a[72:75],a[164:167],v[84:87],a[72:75]\n"
"v_exp_f32 v10,v10\n"
"v_mfma_f32_16x16x32_bf16 a[76:79],a[160:163],v[88:91],a[76:79]\n"
"ds_read_b64_tr_b16 a[186:187],v197 offset:21248\n"
"v_cvt_pk_bf16_f32 v116,v8,v9\n"
"v_mfma_f32_16x16x32_bf16 a[76:79],a[164:167],v[92:95],a[76:79]\n"
"v_exp_f32 v11,v11\n"
"v_mfma_f32_16x16x32_bf16 a[80:83],a[168:171],v[64:67],a[80:83]\n"
"ds_read_b64_tr_b16 a[188:189],v197 offset:25344\n"
"v_mfma_f32_16x16x32_bf16 a[80:83],a[172:175],v[68:71],a[80:83]\n"
"v_exp_f32 v12,v12\n"
"v_mfma_f32_16x16x32_bf16 a[84:87],a[168:171],v[72:75],a[84:87]\n"
"ds_read_b64_tr_b16 a[190:191],v197 offset:29440\n"
"v_cvt_pk_bf16_f32 v117,v10,v11\n"
"v_mfma_f32_16x16x32_bf16 a[84:87],a[172:175],v[76:79],a[84:87]\n"
"v_exp_f32 v13,v13\n"
"v_mfma_f32_16x16x32_bf16 a[88:91],a[168:171],v[80:83],a[88:91]\n"
"v_cvt_pk_bf16_f32 v124,v12,v13\n"
"v_mfma_f32_16x16x32_bf16 a[88:91],a[172:175],v[84:87],a[88:91]\n"
"v_exp_f32 v14,v14\n"
"v_mfma_f32_16x16x32_bf16 a[92:95],a[168:171],v[88:91],a[92:95]\n"
"v_exp_f32 v15,v15\n"
"v_mfma_f32_16x16x32_bf16 a[92:95],a[172:175],v[92:95],a[92:95]\n"
"buffer_load_dwordx4 v203,s[80:83],s37 offen offset:1024 lds\n"
"v_smfmac_f32_16x16x64_bf16 a[200:203],v[232:235],v[80:87],v61\n"
"s_waitcnt lgkmcnt(0)\n"
"v_mfma_f32_16x16x32_bf16 v[0:3],a[128:131],v[128:131],v[32:35]\n"
"v_exp_f32 v16,v16\n"
"v_mfma_f32_16x16x32_bf16 v[0:3],a[132:135],v[132:135],v[0:3]\n"
"ds_read_b128 a[144:147],v192 offset:4096\n"
"v_cvt_pk_bf16_f32 v125,v14,v15\n"
"v_mfma_f32_16x16x32_bf16 v[0:3],a[136:139],v[136:139],v[0:3]\n"
"v_exp_f32 v17,v17\n"
"v_mfma_f32_16x16x32_bf16 v[0:3],a[140:143],v[140:143],v[0:3]\n"
"ds_read_b128 a[148:151],v193 offset:4096\n"
"v_mfma_f32_16x16x32_bf16 v[4:7],a[128:131],v[144:147],v[36:39]\n"
"v_exp_f32 v18,v18\n"
"v_mfma_f32_16x16x32_bf16 v[4:7],a[132:135],v[148:151],v[4:7]\n"
"ds_read_b128 a[152:155],v194 offset:4096\n"
"v_cvt_pk_bf16_f32 v102,v16,v17\n"
"v_mfma_f32_16x16x32_bf16 v[4:7],a[136:139],v[152:155],v[4:7]\n"
"v_exp_f32 v19,v19\n"
"v_mfma_f32_16x16x32_bf16 v[4:7],a[140:143],v[156:159],v[4:7]\n"
"ds_read_b128 a[156:159],v195 offset:4096\n"
"v_mfma_f32_16x16x32_bf16 v[8:11],a[128:131],v[160:163],v[40:43]\n"
"v_exp_f32 v20,v20\n"
"v_mfma_f32_16x16x32_bf16 v[8:11],a[132:135],v[164:167],v[8:11]\n"
"ds_read_b64_tr_b16 a[160:161],v196 offset:32768\n"
"v_cvt_pk_bf16_f32 v103,v18,v19\n"
"v_mfma_f32_16x16x32_bf16 v[8:11],a[136:139],v[168:171],v[8:11]\n"
"v_exp_f32 v21,v21\n"
"v_mfma_f32_16x16x32_bf16 v[8:11],a[140:143],v[172:175],v[8:11]\n"
"ds_read_b64_tr_b16 a[162:163],v196 offset:36864\n"
"v_mfma_f32_16x16x32_bf16 v[12:15],a[128:131],v[176:179],v[44:47]\n"
"v_exp_f32 v22,v22\n"
"v_mfma_f32_16x16x32_bf16 v[12:15],a[132:135],v[180:183],v[12:15]\n"
"buffer_load_dwordx4 v202,s[80:83],s37 offen offset:2048 lds\n"
"v_mfma_f32_16x16x32_bf16 v[12:15],a[136:139],v[184:187],v[12:15]\n"
"v_mfma_f32_16x16x32_bf16 v[12:15],a[140:143],v[188:191],v[12:15]\n"
"v_exp_f32 v23,v23\n"
"v_mfma_f32_16x16x32_bf16 a[96:99],a[176:179],v[64:67],a[96:99]\n"
"ds_read_b64_tr_b16 a[164:165],v196 offset:40960\n"
"v_cvt_pk_bf16_f32 v110,v20,v21\n"
"v_mfma_f32_16x16x32_bf16 a[96:99],a[180:183],v[68:71],a[96:99]\n"
"v_exp_f32 v24,v24\n"
"v_mfma_f32_16x16x32_bf16 a[100:103],a[176:179],v[72:75],a[100:103]\n"
"ds_read_b64_tr_b16 a[166:167],v196 offset:45056\n"
"v_cvt_pk_bf16_f32 v111,v22,v23\n"
"v_mfma_f32_16x16x32_bf16 a[100:103],a[180:183],v[76:79],a[100:103]\n"
"v_exp_f32 v25,v25\n"
"v_mfma_f32_16x16x32_bf16 a[104:107],a[176:179],v[80:83],a[104:107]\n"
"ds_read_b64_tr_b16 a[168:169],v197 offset:32768\n"
"v_mfma_f32_16x16x32_bf16 a[104:107],a[180:183],v[84:87],a[104:107]\n"
"v_exp_f32 v26,v26\n"
"v_mfma_f32_16x16x32_bf16 a[108:111],a[176:179],v[88:91],a[108:111]\n"
"ds_read_b64_tr_b16 a[170:171],v197 offset:36864\n"
"v_cvt_pk_bf16_f32 v118,v24,v25\n"
"v_mfma_f32_16x16x32_bf16 a[108:111],a[180:183],v[92:95],a[108:111]\n"
"v_exp_f32 v27,v27\n"
"v_mfma_f32_16x16x32_bf16 a[112:115],a[184:187],v[64:67],a[112:115]\n"
"ds_read_b64_tr_b16 a[172:173],v197 offset:40960\n"
"v_mfma_f32_16x16x32_bf16 a[112:115],a[188:191],v[68:71],a[112:115]\n"
"v_exp_f32 v28,v28\n"
"v_mfma_f32_16x16x32_bf16 a[116:119],a[184:187],v[72:75],a[116:119]\n"
"ds_read_b64_tr_b16 a[174:175],v197 offset:45056\n"
"v_cvt_pk_bf16_f32 v119,v26,v27\n"
"v_mfma_f32_16x16x32_bf16 a[116:119],a[188:191],v[76:79],a[116:119]\n"
"v_exp_f32 v29,v29\n"
"v_mfma_f32_16x16x32_bf16 a[120:123],a[184:187],v[80:83],a[120:123]\n"
"v_cvt_pk_bf16_f32 v126,v28,v29\n"
"v_mfma_f32_16x16x32_bf16 a[120:123],a[188:191],v[84:87],a[120:123]\n"
"v_exp_f32 v30,v30\n"
"v_mfma_f32_16x16x32_bf16 a[124:127],a[184:187],v[88:91],a[124:127]\n"
"v_exp_f32 v31,v31\n"
"v_mfma_f32_16x16x32_bf16 a[124:127],a[188:191],v[92:95],a[124:127]\n"
"buffer_load_dwordx4 v203,s[80:83],s37 offen offset:3072 lds\n"
"v_smfmac_f32_16x16x64_bf16 a[204:207],v[232:235],v[88:95],v61\n"
"s_waitcnt lgkmcnt(0)\n"
"v_mfma_f32_16x16x32_bf16 v[16:19],a[144:147],v[128:131],v[32:35]\n"
"v_exp_f32 v0,v0\n"
"v_mfma_f32_16x16x32_bf16 v[16:19],a[148:151],v[132:135],v[16:19]\n"
"ds_read_b128 a[128:131],v192 offset:8192\n"
"v_cvt_pk_bf16_f32 v127,v30,v31\n"
"v_mfma_f32_16x16x32_bf16 v[16:19],a[152:155],v[136:139],v[16:19]\n"
"s_add_u32 s37,s37,16384\n"
"v_exp_f32 v1,v1\n"
"v_mfma_f32_16x16x32_bf16 v[16:19],a[156:159],v[140:143],v[16:19]\n"
"ds_read_b128 a[132:135],v193 offset:8192\n"
"v_mfma_f32_16x16x32_bf16 v[20:23],a[144:147],v[144:147],v[36:39]\n"
"v_exp_f32 v2,v2\n"
"v_mfma_f32_16x16x32_bf16 v[20:23],a[148:151],v[148:151],v[20:23]\n"
"ds_read_b128 a[136:139],v194 offset:8192\n"
"v_cvt_pk_bf16_f32 v64,v0,v1\n"
"v_mfma_f32_16x16x32_bf16 v[20:23],a[152:155],v[152:155],v[20:23]\n"
"v_exp_f32 v3,v3\n"
"v_mfma_f32_16x16x32_bf16 v[20:23],a[156:159],v[156:159],v[20:23]\n"
"ds_read_b128 a[140:143],v195 offset:8192\n"
"v_mfma_f32_16x16x32_bf16 v[24:27],a[144:147],v[160:163],v[40:43]\n"
"v_exp_f32 v4,v4\n"
"v_mfma_f32_16x16x32_bf16 v[24:27],a[148:151],v[164:167],v[24:27]\n"
"ds_read_b64_tr_b16 a[176:177],v196 offset:33024\n"
"v_cvt_pk_bf16_f32 v65,v2,v3\n"
"v_mfma_f32_16x16x32_bf16 v[24:27],a[152:155],v[168:171],v[24:27]\n"
"v_exp_f32 v5,v5\n"
"v_mfma_f32_16x16x32_bf16 v[24:27],a[156:159],v[172:175],v[24:27]\n"
"ds_read_b64_tr_b16 a[178:179],v196 offset:37120\n"
"v_mfma_f32_16x16x32_bf16 v[28:31],a[144:147],v[176:179],v[44:47]\n"
"s_add_u32 m0,s62,49152\n"
"v_exp_f32 v6,v6\n"
"v_mfma_f32_16x16x32_bf16 v[28:31],a[148:151],v[180:183],v[28:31]\n"
"buffer_load_dwordx4 v198,s[76:79],s36 offen lds\n"
"v_mfma_f32_16x16x32_bf16 v[28:31],a[152:155],v[184:187],v[28:31]\n"
"v_mfma_f32_16x16x32_bf16 v[28:31],a[156:159],v[188:191],v[28:31]\n"
"v_exp_f32 v7,v7\n"
"v_mfma_f32_16x16x32_bf16 a[0:3],a[160:163],v[96:99],a[0:3]\n"
"ds_read_b64_tr_b16 a[180:181],v196 offset:41216\n"
"v_cvt_pk_bf16_f32 v72,v4,v5\n"
"v_mfma_f32_16x16x32_bf16 a[0:3],a[164:167],v[100:103],a[0:3]\n"
"v_exp_f32 v8,v8\n"
"v_mfma_f32_16x16x32_bf16 a[4:7],a[160:163],v[104:107],a[4:7]\n"
"ds_read_b64_tr_b16 a[182:183],v196 offset:45312\n"
"v_cvt_pk_bf16_f32 v73,v6,v7\n"
"v_mfma_f32_16x16x32_bf16 a[4:7],a[164:167],v[108:111],a[4:7]\n"
"v_exp_f32 v9,v9\n"
"v_mfma_f32_16x16x32_bf16 a[8:11],a[160:163],v[112:115],a[8:11]\n"
"ds_read_b64_tr_b16 a[184:185],v197 offset:33024\n"
"v_mfma_f32_16x16x32_bf16 a[8:11],a[164:167],v[116:119],a[8:11]\n"
"v_exp_f32 v10,v10\n"
"v_mfma_f32_16x16x32_bf16 a[12:15],a[160:163],v[120:123],a[12:15]\n"
"ds_read_b64_tr_b16 a[186:187],v197 offset:37120\n"
"v_cvt_pk_bf16_f32 v80,v8,v9\n"
"v_mfma_f32_16x16x32_bf16 a[12:15],a[164:167],v[124:127],a[12:15]\n"
"v_exp_f32 v11,v11\n"
"v_mfma_f32_16x16x32_bf16 a[16:19],a[168:171],v[96:99],a[16:19]\n"
"ds_read_b64_tr_b16 a[188:189],v197 offset:41216\n"
"v_mfma_f32_16x16x32_bf16 a[16:19],a[172:175],v[100:103],a[16:19]\n"
"v_exp_f32 v12,v12\n"
"v_mfma_f32_16x16x32_bf16 a[20:23],a[168:171],v[104:107],a[20:23]\n"
"ds_read_b64_tr_b16 a[190:191],v197 offset:45312\n"
"v_cvt_pk_bf16_f32 v81,v10,v11\n"
"v_mfma_f32_16x16x32_bf16 a[20:23],a[172:175],v[108:111],a[20:23]\n"
"v_exp_f32 v13,v13\n"
"v_mfma_f32_16x16x32_bf16 a[24:27],a[168:171],v[112:115],a[24:27]\n"
"v_cvt_pk_bf16_f32 v88,v12,v13\n"
"v_mfma_f32_16x16x32_bf16 a[24:27],a[172:175],v[116:119],a[24:27]\n"
"v_exp_f32 v14,v14\n"
"v_mfma_f32_16x16x32_bf16 a[28:31],a[168:171],v[120:123],a[28:31]\n"
"v_exp_f32 v15,v15\n"
"v_mfma_f32_16x16x32_bf16 a[28:31],a[172:175],v[124:127],a[28:31]\n"
"buffer_load_dwordx4 v199,s[76:79],s36 offen offset:1024 lds\n"
"v_smfmac_f32_16x16x64_bf16 a[192:195],v[232:235],v[96:103],v61\n"
"s_waitcnt lgkmcnt(0)\n"
"v_mfma_f32_16x16x32_bf16 v[0:3],a[128:131],v[128:131],v[32:35]\n"
"v_exp_f32 v16,v16\n"
"v_mfma_f32_16x16x32_bf16 v[0:3],a[132:135],v[132:135],v[0:3]\n"
"ds_read_b128 a[144:147],v192 offset:12288\n"
"v_cvt_pk_bf16_f32 v89,v14,v15\n"
"v_mfma_f32_16x16x32_bf16 v[0:3],a[136:139],v[136:139],v[0:3]\n"
"v_exp_f32 v17,v17\n"
"v_mfma_f32_16x16x32_bf16 v[0:3],a[140:143],v[140:143],v[0:3]\n"
"ds_read_b128 a[148:151],v193 offset:12288\n"
"v_mfma_f32_16x16x32_bf16 v[4:7],a[128:131],v[144:147],v[36:39]\n"
"v_exp_f32 v18,v18\n"
"v_mfma_f32_16x16x32_bf16 v[4:7],a[132:135],v[148:151],v[4:7]\n"
"ds_read_b128 a[152:155],v194 offset:12288\n"
"v_cvt_pk_bf16_f32 v66,v16,v17\n"
"v_mfma_f32_16x16x32_bf16 v[4:7],a[136:139],v[152:155],v[4:7]\n"
"v_exp_f32 v19,v19\n"
"v_mfma_f32_16x16x32_bf16 v[4:7],a[140:143],v[156:159],v[4:7]\n"
"ds_read_b128 a[156:159],v195 offset:12288\n"
"v_mfma_f32_16x16x32_bf16 v[8:11],a[128:131],v[160:163],v[40:43]\n"
"v_exp_f32 v20,v20\n"
"v_mfma_f32_16x16x32_bf16 v[8:11],a[132:135],v[164:167],v[8:11]\n"
"ds_read_b64_tr_b16 a[160:161],v196 offset:33280\n"
"v_cvt_pk_bf16_f32 v67,v18,v19\n"
"v_mfma_f32_16x16x32_bf16 v[8:11],a[136:139],v[168:171],v[8:11]\n"
"v_exp_f32 v21,v21\n"
"v_mfma_f32_16x16x32_bf16 v[8:11],a[140:143],v[172:175],v[8:11]\n"
"ds_read_b64_tr_b16 a[162:163],v196 offset:37376\n"
"v_mfma_f32_16x16x32_bf16 v[12:15],a[128:131],v[176:179],v[44:47]\n"
"v_exp_f32 v22,v22\n"
"v_mfma_f32_16x16x32_bf16 v[12:15],a[132:135],v[180:183],v[12:15]\n"
"buffer_load_dwordx4 v200,s[76:79],s36 offen offset:2048 lds\n"
"v_mfma_f32_16x16x32_bf16 v[12:15],a[136:139],v[184:187],v[12:15]\n"
"buffer_load_dword v62,v63,s[92:95],s39 offen sc1\n"
"v_mfma_f32_16x16x32_bf16 v[12:15],a[140:143],v[188:191],v[12:15]\n"
"v_exp_f32 v23,v23\n"
"v_mfma_f32_16x16x32_bf16 a[32:35],a[176:179],v[96:99],a[32:35]\n"
"ds_read_b64_tr_b16 a[164:165],v196 offset:41472\n"
"v_cvt_pk_bf16_f32 v74,v20,v21\n"
"v_mfma_f32_16x16x32_bf16 a[32:35],a[180:183],v[100:103],a[32:35]\n"
"v_exp_f32 v24,v24\n"
"v_mfma_f32_16x16x32_bf16 a[36:39],a[176:179],v[104:107],a[36:39]\n"
"ds_read_b64_tr_b16 a[166:167],v196 offset:45568\n"
"v_cvt_pk_bf16_f32 v75,v22,v23\n"
"v_mfma_f32_16x16x32_bf16 a[36:39],a[180:183],v[108:111],a[36:39]\n"
"v_exp_f32 v25,v25\n"
"v_mfma_f32_16x16x32_bf16 a[40:43],a[176:179],v[112:115],a[40:43]\n"
"ds_read_b64_tr_b16 a[168:169],v197 offset:33280\n"
"v_mfma_f32_16x16x32_bf16 a[40:43],a[180:183],v[116:119],a[40:43]\n"
"v_exp_f32 v26,v26\n"
"v_mfma_f32_16x16x32_bf16 a[44:47],a[176:179],v[120:123],a[44:47]\n"
"ds_read_b64_tr_b16 a[170:171],v197 offset:37376\n"
"v_cvt_pk_bf16_f32 v82,v24,v25\n"
"v_mfma_f32_16x16x32_bf16 a[44:47],a[180:183],v[124:127],a[44:47]\n"
"v_exp_f32 v27,v27\n"
"v_mfma_f32_16x16x32_bf16 a[48:51],a[184:187],v[96:99],a[48:51]\n"
"ds_read_b64_tr_b16 a[172:173],v197 offset:41472\n"
"v_mfma_f32_16x16x32_bf16 a[48:51],a[188:191],v[100:103],a[48:51]\n"
"v_exp_f32 v28,v28\n"
"v_mfma_f32_16x16x32_bf16 a[52:55],a[184:187],v[104:107],a[52:55]\n"
"ds_read_b64_tr_b16 a[174:175],v197 offset:45568\n"
"v_cvt_pk_bf16_f32 v83,v26,v27\n"
"v_mfma_f32_16x16x32_bf16 a[52:55],a[188:191],v[108:111],a[52:55]\n"
"v_exp_f32 v29,v29\n"
"v_mfma_f32_16x16x32_bf16 a[56:59],a[184:187],v[112:115],a[56:59]\n"
"v_cvt_pk_bf16_f32 v90,v28,v29\n"
"v_mfma_f32_16x16x32_bf16 a[56:59],a[188:191],v[116:119],a[56:59]\n"
"s_add_u32 s39,s39,s40\n"
"v_exp_f32 v30,v30\n"
"v_mfma_f32_16x16x32_bf16 a[60:63],a[184:187],v[120:123],a[60:63]\n"
"v_exp_f32 v31,v31\n"
"v_mfma_f32_16x16x32_bf16 a[60:63],a[188:191],v[124:127],a[60:63]\n"
"buffer_load_dwordx4 v201,s[76:79],s36 offen offset:3072 lds\n"
"v_smfmac_f32_16x16x64_bf16 a[196:199],v[232:235],v[104:111],v61\n"
"s_waitcnt vmcnt(12) lgkmcnt(0)\n"
"s_barrier\n"
"v_mfma_f32_16x16x32_bf16 v[16:19],a[144:147],v[128:131],v[32:35]\n"
"v_exp_f32 v0,v0\n"
"v_mfma_f32_16x16x32_bf16 v[16:19],a[148:151],v[132:135],v[16:19]\n"
"ds_read_b128 a[128:131],v192 offset:16384\n"
"v_cvt_pk_bf16_f32 v91,v30,v31\n"
"v_mfma_f32_16x16x32_bf16 v[16:19],a[152:155],v[136:139],v[16:19]\n"
"s_add_u32 s36,s36,16384\n"
"v_exp_f32 v1,v1\n"
"v_mfma_f32_16x16x32_bf16 v[16:19],a[156:159],v[140:143],v[16:19]\n"
"ds_read_b128 a[132:135],v193 offset:16384\n"
"v_mfma_f32_16x16x32_bf16 v[20:23],a[144:147],v[144:147],v[36:39]\n"
"v_exp_f32 v2,v2\n"
"v_mfma_f32_16x16x32_bf16 v[20:23],a[148:151],v[148:151],v[20:23]\n"
"ds_read_b128 a[136:139],v194 offset:16384\n"
"v_cvt_pk_bf16_f32 v68,v0,v1\n"
"v_mfma_f32_16x16x32_bf16 v[20:23],a[152:155],v[152:155],v[20:23]\n"
"v_exp_f32 v3,v3\n"
"v_mfma_f32_16x16x32_bf16 v[20:23],a[156:159],v[156:159],v[20:23]\n"
"ds_read_b128 a[140:143],v195 offset:16384\n"
"v_mfma_f32_16x16x32_bf16 v[24:27],a[144:147],v[160:163],v[40:43]\n"
"v_exp_f32 v4,v4\n"
"v_mfma_f32_16x16x32_bf16 v[24:27],a[148:151],v[164:167],v[24:27]\n"
"ds_read_b64_tr_b16 a[176:177],v196 offset:33536\n"
"v_cvt_pk_bf16_f32 v69,v2,v3\n"
"v_mfma_f32_16x16x32_bf16 v[24:27],a[152:155],v[168:171],v[24:27]\n"
"v_exp_f32 v5,v5\n"
"v_mfma_f32_16x16x32_bf16 v[24:27],a[156:159],v[172:175],v[24:27]\n"
"ds_read_b64_tr_b16 a[178:179],v196 offset:37632\n"
"v_mfma_f32_16x16x32_bf16 v[28:31],a[144:147],v[176:179],v[44:47]\n"
"s_add_u32 m0,s63,16384\n"
"v_exp_f32 v6,v6\n"
"v_mfma_f32_16x16x32_bf16 v[28:31],a[148:151],v[180:183],v[28:31]\n"
"buffer_load_dwordx4 v202,s[80:83],s37 offen lds\n"
"v_mfma_f32_16x16x32_bf16 v[28:31],a[152:155],v[184:187],v[28:31]\n"
"v_mfma_f32_16x16x32_bf16 v[28:31],a[156:159],v[188:191],v[28:31]\n"
"v_exp_f32 v7,v7\n"
"v_mfma_f32_16x16x32_bf16 a[64:67],a[160:163],v[96:99],a[64:67]\n"
"ds_read_b64_tr_b16 a[180:181],v196 offset:41728\n"
"v_cvt_pk_bf16_f32 v76,v4,v5\n"
"v_mfma_f32_16x16x32_bf16 a[64:67],a[164:167],v[100:103],a[64:67]\n"
"v_exp_f32 v8,v8\n"
"v_mfma_f32_16x16x32_bf16 a[68:71],a[160:163],v[104:107],a[68:71]\n"
"ds_read_b64_tr_b16 a[182:183],v196 offset:45824\n"
"v_cvt_pk_bf16_f32 v77,v6,v7\n"
"v_mfma_f32_16x16x32_bf16 a[68:71],a[164:167],v[108:111],a[68:71]\n"
"v_exp_f32 v9,v9\n"
"v_mfma_f32_16x16x32_bf16 a[72:75],a[160:163],v[112:115],a[72:75]\n"
"ds_read_b64_tr_b16 a[184:185],v197 offset:33536\n"
"v_mfma_f32_16x16x32_bf16 a[72:75],a[164:167],v[116:119],a[72:75]\n"
"v_exp_f32 v10,v10\n"
"v_mfma_f32_16x16x32_bf16 a[76:79],a[160:163],v[120:123],a[76:79]\n"
"ds_read_b64_tr_b16 a[186:187],v197 offset:37632\n"
"v_cvt_pk_bf16_f32 v84,v8,v9\n"
"v_mfma_f32_16x16x32_bf16 a[76:79],a[164:167],v[124:127],a[76:79]\n"
"v_exp_f32 v11,v11\n"
"v_mfma_f32_16x16x32_bf16 a[80:83],a[168:171],v[96:99],a[80:83]\n"
"ds_read_b64_tr_b16 a[188:189],v197 offset:41728\n"
"v_mfma_f32_16x16x32_bf16 a[80:83],a[172:175],v[100:103],a[80:83]\n"
"v_exp_f32 v12,v12\n"
"v_mfma_f32_16x16x32_bf16 a[84:87],a[168:171],v[104:107],a[84:87]\n"
"ds_read_b64_tr_b16 a[190:191],v197 offset:45824\n"
"v_cvt_pk_bf16_f32 v85,v10,v11\n"
"v_mfma_f32_16x16x32_bf16 a[84:87],a[172:175],v[108:111],a[84:87]\n"
"v_exp_f32 v13,v13\n"
"v_mfma_f32_16x16x32_bf16 a[88:91],a[168:171],v[112:115],a[88:91]\n"
"v_cvt_pk_bf16_f32 v92,v12,v13\n"
"v_mfma_f32_16x16x32_bf16 a[88:91],a[172:175],v[116:119],a[88:91]\n"
"v_exp_f32 v14,v14\n"
"v_mfma_f32_16x16x32_bf16 a[92:95],a[168:171],v[120:123],a[92:95]\n"
"v_exp_f32 v15,v15\n"
"v_mfma_f32_16x16x32_bf16 a[92:95],a[172:175],v[124:127],a[92:95]\n"
"buffer_load_dwordx4 v203,s[80:83],s37 offen offset:1024 lds\n"
"v_smfmac_f32_16x16x64_bf16 a[200:203],v[232:235],v[112:119],v61\n"
"s_waitcnt lgkmcnt(0)\n"
"v_mfma_f32_16x16x32_bf16 v[0:3],a[128:131],v[128:131],v[32:35]\n"
"v_exp_f32 v16,v16\n"
"v_mfma_f32_16x16x32_bf16 v[0:3],a[132:135],v[132:135],v[0:3]\n"
"ds_read_b128 a[144:147],v192 offset:20480\n"
"v_cvt_pk_bf16_f32 v93,v14,v15\n"
"v_mfma_f32_16x16x32_bf16 v[0:3],a[136:139],v[136:139],v[0:3]\n"
"v_exp_f32 v17,v17\n"
"v_mfma_f32_16x16x32_bf16 v[0:3],a[140:143],v[140:143],v[0:3]\n"
"ds_read_b128 a[148:151],v193 offset:20480\n"
"v_mfma_f32_16x16x32_bf16 v[4:7],a[128:131],v[144:147],v[36:39]\n"
"v_exp_f32 v18,v18\n"
"v_mfma_f32_16x16x32_bf16 v[4:7],a[132:135],v[148:151],v[4:7]\n"
"ds_read_b128 a[152:155],v194 offset:20480\n"
"v_cvt_pk_bf16_f32 v70,v16,v17\n"
"v_mfma_f32_16x16x32_bf16 v[4:7],a[136:139],v[152:155],v[4:7]\n"
"v_exp_f32 v19,v19\n"
"v_mfma_f32_16x16x32_bf16 v[4:7],a[140:143],v[156:159],v[4:7]\n"
"ds_read_b128 a[156:159],v195 offset:20480\n"
"v_mfma_f32_16x16x32_bf16 v[8:11],a[128:131],v[160:163],v[40:43]\n"
"v_exp_f32 v20,v20\n"
"v_mfma_f32_16x16x32_bf16 v[8:11],a[132:135],v[164:167],v[8:11]\n"
"ds_read_b64_tr_b16 a[160:161],v196 offset:49152\n"
"v_cvt_pk_bf16_f32 v71,v18,v19\n"
"v_mfma_f32_16x16x32_bf16 v[8:11],a[136:139],v[168:171],v[8:11]\n"
"v_exp_f32 v21,v21\n"
"v_mfma_f32_16x16x32_bf16 v[8:11],a[140:143],v[172:175],v[8:11]\n"
"ds_read_b64_tr_b16 a[162:163],v196 offset:53248\n"
"v_mfma_f32_16x16x32_bf16 v[12:15],a[128:131],v[176:179],v[44:47]\n"
"v_exp_f32 v22,v22\n"
"v_mfma_f32_16x16x32_bf16 v[12:15],a[132:135],v[180:183],v[12:15]\n"
"buffer_load_dwordx4 v202,s[80:83],s37 offen offset:2048 lds\n"
"v_mfma_f32_16x16x32_bf16 v[12:15],a[136:139],v[184:187],v[12:15]\n"
"v_mfma_f32_16x16x32_bf16 v[12:15],a[140:143],v[188:191],v[12:15]\n"
"v_exp_f32 v23,v23\n"
"v_mfma_f32_16x16x32_bf16 a[96:99],a[176:179],v[96:99],a[96:99]\n"
"ds_read_b64_tr_b16 a[164:165],v196 offset:57344\n"
"v_cvt_pk_bf16_f32 v78,v20,v21\n"
"v_mfma_f32_16x16x32_bf16 a[96:99],a[180:183],v[100:103],a[96:99]\n"
"v_exp_f32 v24,v24\n"
"v_mfma_f32_16x16x32_bf16 a[100:103],a[176:179],v[104:107],a[100:103]\n"
"ds_read_b64_tr_b16 a[166:167],v196 offset:61440\n"
"v_cvt_pk_bf16_f32 v79,v22,v23\n"
"v_mfma_f32_16x16x32_bf16 a[100:103],a[180:183],v[108:111],a[100:103]\n"
"v_exp_f32 v25,v25\n"
"v_mfma_f32_16x16x32_bf16 a[104:107],a[176:179],v[112:115],a[104:107]\n"
"ds_read_b64_tr_b16 a[168:169],v197 offset:49152\n"
"v_mfma_f32_16x16x32_bf16 a[104:107],a[180:183],v[116:119],a[104:107]\n"
"v_exp_f32 v26,v26\n"
"v_mfma_f32_16x16x32_bf16 a[108:111],a[176:179],v[120:123],a[108:111]\n"
"ds_read_b64_tr_b16 a[170:171],v197 offset:53248\n"
"v_cvt_pk_bf16_f32 v86,v24,v25\n"
"v_mfma_f32_16x16x32_bf16 a[108:111],a[180:183],v[124:127],a[108:111]\n"
"v_exp_f32 v27,v27\n"
"v_mfma_f32_16x16x32_bf16 a[112:115],a[184:187],v[96:99],a[112:115]\n"
"ds_read_b64_tr_b16 a[172:173],v197 offset:57344\n"
"v_mfma_f32_16x16x32_bf16 a[112:115],a[188:191],v[100:103],a[112:115]\n"
"v_exp_f32 v28,v28\n"
"v_mfma_f32_16x16x32_bf16 a[116:119],a[184:187],v[104:107],a[116:119]\n"
"ds_read_b64_tr_b16 a[174:175],v197 offset:61440\n"
"v_cvt_pk_bf16_f32 v87,v26,v27\n"
"v_mfma_f32_16x16x32_bf16 a[116:119],a[188:191],v[108:111],a[116:119]\n"
"v_exp_f32 v29,v29\n"
"v_mfma_f32_16x16x32_bf16 a[120:123],a[184:187],v[112:115],a[120:123]\n"
"v_cvt_pk_bf16_f32 v94,v28,v29\n"
"v_mfma_f32_16x16x32_bf16 a[120:123],a[188:191],v[116:119],a[120:123]\n"
"s_sub_u32 s38,s38,1\n"
"v_exp_f32 v30,v30\n"
"v_mfma_f32_16x16x32_bf16 a[124:127],a[184:187],v[120:123],a[124:127]\n"
"s_cmp_eq_u32 s38,0\n"
"v_exp_f32 v31,v31\n"
"v_mfma_f32_16x16x32_bf16 a[124:127],a[188:191],v[124:127],a[124:127]\n"
"buffer_load_dwordx4 v203,s[80:83],s37 offen offset:3072 lds\n"
"v_smfmac_f32_16x16x64_bf16 a[204:207],v[232:235],v[120:127],v61\n"
"s_waitcnt lgkmcnt(0)\n"
"s_cbranch_scc0 LM_TOP\n"
"s_branch LM_TAIL\n"
"LM_CW:\n"
"s_cmp_eq_u32 s38,2\n"
"s_cbranch_scc1 LM_CW3\n"
"s_mov_b32 s55,0x40000\n"
"LM_POLL9:\n"
"v_cmp_ne_u64 vcc,v[226:227],v[224:225]\n"
"s_nop 1\n"
"s_and_b64 s[44:45],vcc,s[46:47]\n"
"s_cbranch_scc0 LM_POK10\n"
"s_sleep 1\n"
"buffer_load_dwordx2 v[226:227],v223,s[68:71],0 offen sc1\n"
"s_waitcnt vmcnt(0)\n"
"s_sub_u32 s55,s55,1\n"
"s_cbranch_scc0 LM_POLL9\n"
"LM_POK10:\n"
"v_readlane_b32 s92,v210,0\n"
"v_readlane_b32 s93,v210,1\n"
"v_readlane_b32 s94,v248,12\n"
"s_mov_b32 s95,0x20000\n"
"s_sub_u32 s39,s94,18432\n"
"s_mov_b32 s40,4608\n"
"v_lshrrev_b32 v63,1,v209\n"
"v_mul_u32_u24 v63,9,v63\n"
"s_nop 2\n"
"s_branch LM_TOP2\n"
"LM_CW3:\n"
"v_readlane_b32 s43,v210,52\n"
"s_and_b32 s43,s43,0x0\n"
"s_add_u32 s43,s43,768\n"
"s_lshl_b32 s43,s43,6\n"
"v_lshl_add_u32 v223,v209,2,s43\n"
"s_mov_b64 s[46:47],-1\n"
"buffer_load_dwordx2 v[226:227],v223,s[68:71],0 offen sc1\n"
"s_getpc_b64 s[84:85]\n"
"LM_CW0:\n"
"s_and_b32 s85,s85,0xffff\n"
"s_mov_b32 s86,LM_FIN-LM_CW0\n"
"s_mov_b32 s87,0x20000\n"
"v_readlane_b32 s41,v210,57\n"
"v_readlane_b32 s92,v248,13\n"
"v_readlane_b32 s93,v248,14\n"
"v_readlane_b32 s94,v248,15\n"
"s_mov_b32 s95,0x20000\n"
"s_mov_b32 s39,0\n"
"s_mov_b32 s40,27648\n"
"v_lshlrev_b32 v63,2,v209\n"
"buffer_load_dword v62,v63,s[84:87],s41 offen\n"
"v_lshrrev_b32 v48,4,v209\n"
"v_mul_u32_u24 v49,57,v48\n"
"v_lshrrev_b32 v49,10,v49\n"
"v_mul_u32_u24 v49,8064,v49\n"
"v_add_u32 v63,v63,v49\n"
"s_branch LM_TOP2\n"
"LM_LN:\n"
"s_cmp_eq_u32 s38,6\n"
"s_cbranch_scc1 LM_LN6\n"
"s_cmp_eq_u32 s38,5\n"
"s_cbranch_scc1 LM_LN5\n"
"s_cmp_eq_u32 s38,4\n"
"s_cbranch_scc1 LM_LN4\n"
"s_cmp_eq_u32 s38,3\n"
"s_cbranch_scc0 LM_TOP1\n"
"v_readlane_b32 s43,v210,51\n"
"s_nop 0\n"
"s_lshl_b32 s43,s43,6\n"
"v_lshl_add_u32 v223,v209,2,s43\n"
"s_cmp_lg_u32 s48,0\n"
"s_cbranch_scc1 LM_FS11\n"
"s_mov_b64 exec,1\n"
"buffer_store_dwordx2 v[224:225],v223,s[68:71],0 offen sc1\n"
"s_mov_b64 exec,-1\n"
"LM_FS11:\n"
"s_mov_b32 s49,0\n"
"s_branch LM_TOP1\n"
"LM_LN6:\n"
"v_readlane_b32 s43,v210,50\n"
"s_nop 0\n"
"s_lshl_b32 s43,s43,6\n"
"v_lshl_add_u32 v223,v209,2,s43\n"
"s_mov_b64 s[46:47],0x7\n"
"buffer_load_dwordx2 v[226:227],v223,s[68:71],0 offen sc1\n"
"s_branch LM_BODY\n"
"LM_LN5:\n"
"s_mov_b32 s55,0x40000\n"
"LM_POLL12:\n"
"v_cmp_ne_u64 vcc,v[226:227],v[224:225]\n"
"s_nop 1\n"
"s_and_b64 s[44:45],vcc,s[46:47]\n"
"s_cbranch_scc0 LM_POK13\n"
"s_sleep 1\n"
"buffer_load_dwordx2 v[226:227],v223,s[68:71],0 offen sc1\n"
"s_waitcnt vmcnt(0)\n"
"s_sub_u32 s55,s55,1\n"
"s_cbranch_scc0 LM_POLL12\n"
"LM_POK13:\n"
"v_readlane_b32 s58,v210,41\n"
"s_add_u32 s42,s58,4718592\n"
"buffer_load_dwordx4 a[208:211],v209,s[64:67],s42 offen sc1\n"
"s_add_u32 s42,s58,4722688\n"
"buffer_load_dwordx4 a[212:215],v209,s[64:67],s42 offen sc1\n"
"s_add_u32 s42,s58,4726784\n"
"buffer_load_dwordx4 a[216:219],v209,s[64:67],s42 offen sc1\n"
"s_add_u32 s42,s58,4730880\n"
"buffer_load_dwordx4 a[220:223],v209,s[64:67],s42 offen sc1\n"
"s_add_u32 s42,s58,4734976\n"
"buffer_load_dwordx4 a[224:227],v209,s[64:67],s42 offen sc1\n"
"s_add_u32 s42,s58,4739072\n"
"buffer_load_dwordx4 a[228:231],v209,s[64:67],s42 offen sc1\n"
"s_add_u32 s42,s58,4743168\n"
"buffer_load_dwordx4 a[232:235],v209,s[64:67],s42 offen sc1\n"
"s_add_u32 s42,s58,4747264\n"
"buffer_load_dwordx4 a[236:239],v209,s[64:67],s42 offen sc1\n"
"s_add_u32 s42,s58,4751360\n"
"buffer_load_dwordx4 a[240:243],v209,s[64:67],s42 offen sc1\n"
"s_branch LM_BODY\n"
"LM_LN4:\n"
"s_mov_b32 s43,0xffff0000\n"
"v_lshrrev_b32 v53,2,v209\n"
"v_readlane_b32 s41,v210,37\n"
"s_lshl_b32 s42,s48,8\n"
"s_add_u32 s42,s42,s41\n"
"s_add_u32 s42,s42,131072\n"
"v_add_u32 v53,s42,v53\n"
"v_xor_b32 v54,512,v53\n"
"v_mov_b32 v51,0\n"
"v_mov_b32 v52,0\n"
"v_mov_b32 v48,0\n"
"v_mov_b32 v49,0\n"
"v_accvgpr_read_b32 v236,a208\n"
"v_accvgpr_read_b32 v237,a209\n"
"v_accvgpr_read_b32 v238,a210\n"
"v_accvgpr_read_b32 v239,a211\n"
"v_dot2c_f32_bf16 v51,v236,v232\n"
"v_dot2c_f32_bf16 v52,v237,v232\n"
"v_dot2c_f32_bf16 v48,v238,v232\n"
"v_dot2c_f32_bf16 v49,v239,v232\n"
"v_accvgpr_read_b32 v240,a212\n"
"v_accvgpr_read_b32 v241,a213\n"
"v_accvgpr_read_b32 v242,a214\n"
"v_accvgpr_read_b32 v243,a215\n"
"v_dot2c_f32_bf16 v51,v240,v232\n"
"v_dot2c_f32_bf16 v52,v241,v232\n"
"v_dot2c_f32_bf16 v48,v242,v232\n"
"v_dot2c_f32_bf16 v49,v243,v232\n"
"v_accvgpr_read_b32 v236,a216\n"
"v_accvgpr_read_b32 v237,a217\n"
"v_accvgpr_read_b32 v238,a218\n"
"v_accvgpr_read_b32 v239,a219\n"
"v_dot2c_f32_bf16 v51,v236,v232\n"
"v_dot2c_f32_bf16 v52,v237,v232\n"
"v_dot2c_f32_bf16 v48,v238,v232\n"
"v_dot2c_f32_bf16 v49,v239,v232\n"
"v_accvgpr_read_b32 v240,a220\n"
"v_accvgpr_read_b32 v241,a221\n"
"v_accvgpr_read_b32 v242,a222\n"
"v_accvgpr_read_b32 v243,a223\n"
"v_dot2c_f32_bf16 v51,v240,v232\n"
"v_dot2c_f32_bf16 v52,v241,v232\n"
"v_dot2c_f32_bf16 v48,v242,v232\n"
"v_dot2c_f32_bf16 v49,v243,v232\n"
"v_accvgpr_read_b32 v236,a224\n"
"v_accvgpr_read_b32 v237,a225\n"
"v_accvgpr_read_b32 v238,a226\n"
"v_accvgpr_read_b32 v239,a227\n"
"v_dot2c_f32_bf16 v51,v236,v232\n"
"v_dot2c_f32_bf16 v52,v237,v232\n"
"v_dot2c_f32_bf16 v48,v238,v232\n"
"v_dot2c_f32_bf16 v49,v239,v232\n"
"v_accvgpr_read_b32 v240,a228\n"
"v_accvgpr_read_b32 v241,a229\n"
"v_accvgpr_read_b32 v242,a230\n"
"v_accvgpr_read_b32 v243,a231\n"
"v_dot2c_f32_bf16 v51,v240,v232\n"
"v_dot2c_f32_bf16 v52,v241,v232\n"
"v_dot2c_f32_bf16 v48,v242,v232\n"
"v_dot2c_f32_bf16 v49,v243,v232\n"
"v_accvgpr_read_b32 v236,a232\n"
"v_accvgpr_read_b32 v237,a233\n"
"v_accvgpr_read_b32 v238,a234\n"
"v_accvgpr_read_b32 v239,a235\n"
"v_dot2c_f32_bf16 v51,v236,v232\n"
"v_dot2c_f32_bf16 v52,v237,v232\n"
"v_dot2c_f32_bf16 v48,v238,v232\n"
"v_dot2c_f32_bf16 v49,v239,v232\n"
"v_accvgpr_read_b32 v240,a236\n"
"v_accvgpr_read_b32 v241,a237\n"
"v_accvgpr_read_b32 v242,a238\n"
"v_accvgpr_read_b32 v243,a239\n"
"v_dot2c_f32_bf16 v51,v240,v232\n"
"v_dot2c_f32_bf16 v52,v241,v232\n"
"v_dot2c_f32_bf16 v48,v242,v232\n"
"v_dot2c_f32_bf16 v49,v243,v232\n"
"v_accvgpr_read_b32 v236,a240\n"
"v_accvgpr_read_b32 v237,a241\n"
"v_accvgpr_read_b32 v238,a242\n"
"v_accvgpr_read_b32 v239,a243\n"
"v_dot2c_f32_bf16 v51,v236,v232\n"
"v_dot2c_f32_bf16 v52,v237,v232\n"
"v_dot2c_f32_bf16 v48,v238,v232\n"
"v_dot2c_f32_bf16 v49,v239,v232\n"
"s_nop 2\n"
"v_add_f32 v51,v51,v48\n"
"v_add_f32 v52,v52,v49\n"
"v_add_f32 v51,v51,v52\n"
"v_mov_b32 v52,v51\n"
"s_nop 1\n"
"v_permlane32_swap_b32 v51,v52\n"
"s_nop 1\n"
"v_add_f32 v51,v51,v52\n"
"v_mov_b32 v52,v51\n"
"s_nop 1\n"
"v_permlane16_swap_b32 v51,v52\n"
"s_nop 1\n"
"v_add_f32 v51,v51,v52\n"
"ds_write_b32 v53,v51\n"
"s_waitcnt lgkmcnt(0)\n"
"s_barrier\n"
"ds_read_b32 v52,v54\n"
"s_waitcnt lgkmcnt(0)\n"
"v_add_f32 v51,v51,v52\n"
"v_mul_f32 v51,0x3ae38e39,v51\n"
"v_mov_b32 v55,v51\n"
"v_mov_b32 v51,0\n"
"v_mov_b32 v52,0\n"
"v_mov_b32 v48,0\n"
"v_mov_b32 v49,0\n"
"v_accvgpr_read_b32 v236,a208\n"
"v_accvgpr_read_b32 v237,a209\n"
"v_accvgpr_read_b32 v238,a210\n"
"v_accvgpr_read_b32 v239,a211\n"
"v_lshlrev_b32 v240,16,v236\n"
"v_lshlrev_b32 v241,16,v237\n"
"v_lshlrev_b32 v242,16,v238\n"
"v_lshlrev_b32 v243,16,v239\n"
"v_and_b32 v244,s43,v236\n"
"v_and_b32 v245,s43,v237\n"
"v_and_b32 v246,s43,v238\n"
"v_and_b32 v247,s43,v239\n"
"v_sub_f32 v240,v240,v55\n"
"v_sub_f32 v241,v241,v55\n"
"v_sub_f32 v242,v242,v55\n"
"v_sub_f32 v243,v243,v55\n"
"v_sub_f32 v244,v244,v55\n"
"v_sub_f32 v245,v245,v55\n"
"v_sub_f32 v246,v246,v55\n"
"v_sub_f32 v247,v247,v55\n"
"v_fmac_f32 v51,v240,v240\n"
"v_fmac_f32 v52,v241,v241\n"
"v_fmac_f32 v48,v242,v242\n"
"v_fmac_f32 v49,v243,v243\n"
"v_fmac_f32 v51,v244,v244\n"
"v_fmac_f32 v52,v245,v245\n"
"v_fmac_f32 v48,v246,v246\n"
"v_fmac_f32 v49,v247,v247\n"
"v_accvgpr_read_b32 v236,a212\n"
"v_accvgpr_read_b32 v237,a213\n"
"v_accvgpr_read_b32 v238,a214\n"
"v_accvgpr_read_b32 v239,a215\n"
"v_lshlrev_b32 v240,16,v236\n"
"v_lshlrev_b32 v241,16,v237\n"
"v_lshlrev_b32 v242,16,v238\n"
"v_lshlrev_b32 v243,16,v239\n"
"v_and_b32 v244,s43,v236\n"
"v_and_b32 v245,s43,v237\n"
"v_and_b32 v246,s43,v238\n"
"v_and_b32 v247,s43,v239\n"
"v_sub_f32 v240,v240,v55\n"
"v_sub_f32 v241,v241,v55\n"
"v_sub_f32 v242,v242,v55\n"
"v_sub_f32 v243,v243,v55\n"
"v_sub_f32 v244,v244,v55\n"
"v_sub_f32 v245,v245,v55\n"
"v_sub_f32 v246,v246,v55\n"
"v_sub_f32 v247,v247,v55\n"
"v_fmac_f32 v51,v240,v240\n"
"v_fmac_f32 v52,v241,v241\n"
"v_fmac_f32 v48,v242,v242\n"
"v_fmac_f32 v49,v243,v243\n"
"v_fmac_f32 v51,v244,v244\n"
"v_fmac_f32 v52,v245,v245\n"
"v_fmac_f32 v48,v246,v246\n"
"v_fmac_f32 v49,v247,v247\n"
"v_accvgpr_read_b32 v236,a216\n"
"v_accvgpr_read_b32 v237,a217\n"
"v_accvgpr_read_b32 v238,a218\n"
"v_accvgpr_read_b32 v239,a219\n"
"v_lshlrev_b32 v240,16,v236\n"
"v_lshlrev_b32 v241,16,v237\n"
"v_lshlrev_b32 v242,16,v238\n"
"v_lshlrev_b32 v243,16,v239\n"
"v_and_b32 v244,s43,v236\n"
"v_and_b32 v245,s43,v237\n"
"v_and_b32 v246,s43,v238\n"
"v_and_b32 v247,s43,v239\n"
"v_sub_f32 v240,v240,v55\n"
"v_sub_f32 v241,v241,v55\n"
"v_sub_f32 v242,v242,v55\n"
"v_sub_f32 v243,v243,v55\n"
"v_sub_f32 v244,v244,v55\n"
"v_sub_f32 v245,v245,v55\n"
"v_sub_f32 v246,v246,v55\n"
"v_sub_f32 v247,v247,v55\n"
"v_fmac_f32 v51,v240,v240\n"
"v_fmac_f32 v52,v241,v241\n"
"v_fmac_f32 v48,v242,v242\n"
"v_fmac_f32 v49,v243,v243\n"
"v_fmac_f32 v51,v244,v244\n"
"v_fmac_f32 v52,v245,v245\n"
"v_fmac_f32 v48,v246,v246\n"
"v_fmac_f32 v49,v247,v247\n"
"v_accvgpr_read_b32 v236,a220\n"
"v_accvgpr_read_b32 v237,a221\n"
"v_accvgpr_read_b32 v238,a222\n"
"v_accvgpr_read_b32 v239,a223\n"
"v_lshlrev_b32 v240,16,v236\n"
"v_lshlrev_b32 v241,16,v237\n"
"v_lshlrev_b32 v242,16,v238\n"
"v_lshlrev_b32 v243,16,v239\n"
"v_and_b32 v244,s43,v236\n"
"v_and_b32 v245,s43,v237\n"
"v_and_b32 v246,s43,v238\n"
"v_and_b32 v247,s43,v239\n"
"v_sub_f32 v240,v240,v55\n"
"v_sub_f32 v241,v241,v55\n"
"v_sub_f32 v242,v242,v55\n"
"v_sub_f32 v243,v243,v55\n"
"v_sub_f32 v244,v244,v55\n"
"v_sub_f32 v245,v245,v55\n"
"v_sub_f32 v246,v246,v55\n"
"v_sub_f32 v247,v247,v55\n"
"v_fmac_f32 v51,v240,v240\n"
"v_fmac_f32 v52,v241,v241\n"
"v_fmac_f32 v48,v242,v242\n"
"v_fmac_f32 v49,v243,v243\n"
"v_fmac_f32 v51,v244,v244\n"
"v_fmac_f32 v52,v245,v245\n"
"v_fmac_f32 v48,v246,v246\n"
"v_fmac_f32 v49,v247,v247\n"
"v_accvgpr_read_b32 v236,a224\n"
"v_accvgpr_read_b32 v237,a225\n"
"v_accvgpr_read_b32 v238,a226\n"
"v_accvgpr_read_b32 v239,a227\n"
"v_lshlrev_b32 v240,16,v236\n"
"v_lshlrev_b32 v241,16,v237\n"
"v_lshlrev_b32 v242,16,v238\n"
"v_lshlrev_b32 v243,16,v239\n"
"v_and_b32 v244,s43,v236\n"
"v_and_b32 v245,s43,v237\n"
"v_and_b32 v246,s43,v238\n"
"v_and_b32 v247,s43,v239\n"
"v_sub_f32 v240,v240,v55\n"
"v_sub_f32 v241,v241,v55\n"
"v_sub_f32 v242,v242,v55\n"
"v_sub_f32 v243,v243,v55\n"
"v_sub_f32 v244,v244,v55\n"
"v_sub_f32 v245,v245,v55\n"
"v_sub_f32 v246,v246,v55\n"
"v_sub_f32 v247,v247,v55\n"
"v_fmac_f32 v51,v240,v240\n"
"v_fmac_f32 v52,v241,v241\n"
"v_fmac_f32 v48,v242,v242\n"
"v_fmac_f32 v49,v243,v243\n"
"v_fmac_f32 v51,v244,v244\n"
"v_fmac_f32 v52,v245,v245\n"
"v_fmac_f32 v48,v246,v246\n"
"v_fmac_f32 v49,v247,v247\n"
"v_accvgpr_read_b32 v236,a228\n"
"v_accvgpr_read_b32 v237,a229\n"
"v_accvgpr_read_b32 v238,a230\n"
"v_accvgpr_read_b32 v239,a231\n"
"v_lshlrev_b32 v240,16,v236\n"
"v_lshlrev_b32 v241,16,v237\n"
"v_lshlrev_b32 v242,16,v238\n"
"v_lshlrev_b32 v243,16,v239\n"
"v_and_b32 v244,s43,v236\n"
"v_and_b32 v245,s43,v237\n"
"v_and_b32 v246,s43,v238\n"
"v_and_b32 v247,s43,v239\n"
"v_sub_f32 v240,v240,v55\n"
"v_sub_f32 v241,v241,v55\n"
"v_sub_f32 v242,v242,v55\n"
"v_sub_f32 v243,v243,v55\n"
"v_sub_f32 v244,v244,v55\n"
"v_sub_f32 v245,v245,v55\n"
"v_sub_f32 v246,v246,v55\n"
"v_sub_f32 v247,v247,v55\n"
"v_fmac_f32 v51,v240,v240\n"
"v_fmac_f32 v52,v241,v241\n"
"v_fmac_f32 v48,v242,v242\n"
"v_fmac_f32 v49,v243,v243\n"
"v_fmac_f32 v51,v244,v244\n"
"v_fmac_f32 v52,v245,v245\n"
"v_fmac_f32 v48,v246,v246\n"
"v_fmac_f32 v49,v247,v247\n"
"v_accvgpr_read_b32 v236,a232\n"
"v_accvgpr_read_b32 v237,a233\n"
"v_accvgpr_read_b32 v238,a234\n"
"v_accvgpr_read_b32 v239,a235\n"
"v_lshlrev_b32 v240,16,v236\n"
"v_lshlrev_b32 v241,16,v237\n"
"v_lshlrev_b32 v242,16,v238\n"
"v_lshlrev_b32 v243,16,v239\n"
"v_and_b32 v244,s43,v236\n"
"v_and_b32 v245,s43,v237\n"
"v_and_b32 v246,s43,v238\n"
"v_and_b32 v247,s43,v239\n"
"v_sub_f32 v240,v240,v55\n"
"v_sub_f32 v241,v241,v55\n"
"v_sub_f32 v242,v242,v55\n"
"v_sub_f32 v243,v243,v55\n"
"v_sub_f32 v244,v244,v55\n"
"v_sub_f32 v245,v245,v55\n"
"v_sub_f32 v246,v246,v55\n"
"v_sub_f32 v247,v247,v55\n"
"v_fmac_f32 v51,v240,v240\n"
"v_fmac_f32 v52,v241,v241\n"
"v_fmac_f32 v48,v242,v242\n"
"v_fmac_f32 v49,v243,v243\n"
"v_fmac_f32 v51,v244,v244\n"
"v_fmac_f32 v52,v245,v245\n"
"v_fmac_f32 v48,v246,v246\n"
"v_fmac_f32 v49,v247,v247\n"
"v_accvgpr_read_b32 v236,a236\n"
"v_accvgpr_read_b32 v237,a237\n"
"v_accvgpr_read_b32 v238,a238\n"
"v_accvgpr_read_b32 v239,a239\n"
"v_lshlrev_b32 v240,16,v236\n"
"v_lshlrev_b32 v241,16,v237\n"
"v_lshlrev_b32 v242,16,v238\n"
"v_lshlrev_b32 v243,16,v239\n"
"v_and_b32 v244,s43,v236\n"
"v_and_b32 v245,s43,v237\n"
"v_and_b32 v246,s43,v238\n"
"v_and_b32 v247,s43,v239\n"
"v_sub_f32 v240,v240,v55\n"
"v_sub_f32 v241,v241,v55\n"
"v_sub_f32 v242,v242,v55\n"
"v_sub_f32 v243,v243,v55\n"
"v_sub_f32 v244,v244,v55\n"
"v_sub_f32 v245,v245,v55\n"
"v_sub_f32 v246,v246,v55\n"
"v_sub_f32 v247,v247,v55\n"
"v_fmac_f32 v51,v240,v240\n"
"v_fmac_f32 v52,v241,v241\n"
"v_fmac_f32 v48,v242,v242\n"
"v_fmac_f32 v49,v243,v243\n"
"v_fmac_f32 v51,v244,v244\n"
"v_fmac_f32 v52,v245,v245\n"
"v_fmac_f32 v48,v246,v246\n"
"v_fmac_f32 v49,v247,v247\n"
"v_accvgpr_read_b32 v236,a240\n"
"v_accvgpr_read_b32 v237,a241\n"
"v_accvgpr_read_b32 v238,a242\n"
"v_accvgpr_read_b32 v239,a243\n"
"v_lshlrev_b32 v240,16,v236\n"
"v_lshlrev_b32 v241,16,v237\n"
"v_lshlrev_b32 v242,16,v238\n"
"v_lshlrev_b32 v243,16,v239\n"
"v_and_b32 v244,s43,v236\n"
"v_and_b32 v245,s43,v237\n"
"v_and_b32 v246,s43,v238\n"
"v_and_b32 v247,s43,v239\n"
"v_sub_f32 v240,v240,v55\n"
"v_sub_f32 v241,v241,v55\n"
"v_sub_f32 v242,v242,v55\n"
"v_sub_f32 v243,v243,v55\n"
"v_sub_f32 v244,v244,v55\n"
"v_sub_f32 v245,v245,v55\n"
"v_sub_f32 v246,v246,v55\n"
"v_sub_f32 v247,v247,v55\n"
"v_fmac_f32 v51,v240,v240\n"
"v_fmac_f32 v52,v241,v241\n"
"v_fmac_f32 v48,v242,v242\n"
"v_fmac_f32 v49,v243,v243\n"
"v_fmac_f32 v51,v244,v244\n"
"v_fmac_f32 v52,v245,v245\n"
"v_fmac_f32 v48,v246,v246\n"
"v_fmac_f32 v49,v247,v247\n"
"v_add_f32 v51,v51,v48\n"
"v_add_f32 v52,v52,v49\n"
"v_add_f32 v51,v51,v52\n"
"v_mov_b32 v52,v51\n"
"s_nop 1\n"
"v_permlane32_swap_b32 v51,v52\n"
"s_nop 1\n"
"v_add_f32 v51,v51,v52\n"
"v_mov_b32 v52,v51\n"
"s_nop 1\n"
"v_permlane16_swap_b32 v51,v52\n"
"s_nop 1\n"
"v_add_f32 v51,v51,v52\n"
"ds_write_b32 v53,v51 offset:1024\n"
"s_waitcnt lgkmcnt(0)\n"
"s_barrier\n"
"ds_read_b32 v52,v54 offset:1024\n"
"s_waitcnt lgkmcnt(0)\n"
"v_add_f32 v51,v51,v52\n"
"v_mul_f32 v51,0x3ae38e39,v51\n"
"v_cvt_pk_bf16_f32 v51,v51,v51\n"
"v_lshlrev_b32 v51,16,v51\n"
"v_add_f32 v51,0x3727c5ac,v51\n"
"v_cvt_pk_bf16_f32 v51,v51,v51\n"
"v_lshlrev_b32 v51,16,v51\n"
"v_sqrt_f32 v51,v51\n"
"v_cvt_pk_bf16_f32 v55,v55,v55\n"
"v_lshlrev_b32 v55,16,v55\n"
"v_cvt_pk_bf16_f32 v51,v51,v51\n"
"v_lshlrev_b32 v51,16,v51\n"
"v_rcp_f32 v50,v51\n"
"v_accvgpr_read_b32 v236,a208\n"
"v_accvgpr_read_b32 v237,a209\n"
"v_accvgpr_read_b32 v238,a210\n"
"v_accvgpr_read_b32 v239,a211\n"
"v_lshlrev_b32 v240,16,v236\n"
"v_lshlrev_b32 v241,16,v237\n"
"v_lshlrev_b32 v242,16,v238\n"
"v_lshlrev_b32 v243,16,v239\n"
"v_and_b32 v244,s43,v236\n"
"v_and_b32 v245,s43,v237\n"
"v_and_b32 v246,s43,v238\n"
"v_and_b32 v247,s43,v239\n"
"v_mul_f32 v55,v55,v50\n"
"v_fma_f32 v240,v240,v50,-v55\n"
"v_fma_f32 v241,v241,v50,-v55\n"
"v_fma_f32 v242,v242,v50,-v55\n"
"v_fma_f32 v243,v243,v50,-v55\n"
"v_fma_f32 v244,v244,v50,-v55\n"
"v_fma_f32 v245,v245,v50,-v55\n"
"v_fma_f32 v246,v246,v50,-v55\n"
"v_fma_f32 v247,v247,v50,-v55\n"
"v_cvt_pk_bf16_f32 v228,v240,v244\n"
"v_cvt_pk_bf16_f32 v229,v241,v245\n"
"v_cvt_pk_bf16_f32 v230,v242,v246\n"
"v_cvt_pk_bf16_f32 v231,v243,v247\n"
"s_add_u32 s42,s58,7077888\n"
"buffer_store_dwordx4 v[228:231],v209,s[64:67],s42 offen sc1\n"
"v_accvgpr_read_b32 v236,a212\n"
"v_accvgpr_read_b32 v237,a213\n"
"v_accvgpr_read_b32 v238,a214\n"
"v_accvgpr_read_b32 v239,a215\n"
"v_lshlrev_b32 v240,16,v236\n"
"v_lshlrev_b32 v241,16,v237\n"
"v_lshlrev_b32 v242,16,v238\n"
"v_lshlrev_b32 v243,16,v239\n"
"v_and_b32 v244,s43,v236\n"
"v_and_b32 v245,s43,v237\n"
"v_and_b32 v246,s43,v238\n"
"v_and_b32 v247,s43,v239\n"
"v_fma_f32 v240,v240,v50,-v55\n"
"v_fma_f32 v241,v241,v50,-v55\n"
"v_fma_f32 v242,v242,v50,-v55\n"
"v_fma_f32 v243,v243,v50,-v55\n"
"v_fma_f32 v244,v244,v50,-v55\n"
"v_fma_f32 v245,v245,v50,-v55\n"
"v_fma_f32 v246,v246,v50,-v55\n"
"v_fma_f32 v247,v247,v50,-v55\n"
"v_cvt_pk_bf16_f32 v56,v240,v244\n"
"v_cvt_pk_bf16_f32 v57,v241,v245\n"
"v_cvt_pk_bf16_f32 v58,v242,v246\n"
"v_cvt_pk_bf16_f32 v59,v243,v247\n"
"s_add_u32 s42,s58,7081984\n"
"buffer_store_dwordx4 v[56:59],v209,s[64:67],s42 offen sc1\n"
"v_accvgpr_read_b32 v236,a216\n"
"v_accvgpr_read_b32 v237,a217\n"
"v_accvgpr_read_b32 v238,a218\n"
"v_accvgpr_read_b32 v239,a219\n"
"v_lshlrev_b32 v240,16,v236\n"
"v_lshlrev_b32 v241,16,v237\n"
"v_lshlrev_b32 v242,16,v238\n"
"v_lshlrev_b32 v243,16,v239\n"
"v_and_b32 v244,s43,v236\n"
"v_and_b32 v245,s43,v237\n"
"v_and_b32 v246,s43,v238\n"
"v_and_b32 v247,s43,v239\n"
"v_fma_f32 v240,v240,v50,-v55\n"
"v_fma_f32 v241,v241,v50,-v55\n"
"v_fma_f32 v242,v242,v50,-v55\n"
"v_fma_f32 v243,v243,v50,-v55\n"
"v_fma_f32 v244,v244,v50,-v55\n"
"v_fma_f32 v245,v245,v50,-v55\n"
"v_fma_f32 v246,v246,v50,-v55\n"
"v_fma_f32 v247,v247,v50,-v55\n"
"v_cvt_pk_bf16_f32 v228,v240,v244\n"
"v_cvt_pk_bf16_f32 v229,v241,v245\n"
"v_cvt_pk_bf16_f32 v230,v242,v246\n"
"v_cvt_pk_bf16_f32 v231,v243,v247\n"
"s_add_u32 s42,s58,7086080\n"
"buffer_store_dwordx4 v[228:231],v209,s[64:67],s42 offen sc1\n"
"v_accvgpr_read_b32 v236,a220\n"
"v_accvgpr_read_b32 v237,a221\n"
"v_accvgpr_read_b32 v238,a222\n"
"v_accvgpr_read_b32 v239,a223\n"
"v_lshlrev_b32 v240,16,v236\n"
"v_lshlrev_b32 v241,16,v237\n"
"v_lshlrev_b32 v242,16,v238\n"
"v_lshlrev_b32 v243,16,v239\n"
"v_and_b32 v244,s43,v236\n"
"v_and_b32 v245,s43,v237\n"
"v_and_b32 v246,s43,v238\n"
"v_and_b32 v247,s43,v239\n"
"v_fma_f32 v240,v240,v50,-v55\n"
"v_fma_f32 v241,v241,v50,-v55\n"
"v_fma_f32 v242,v242,v50,-v55\n"
"v_fma_f32 v243,v243,v50,-v55\n"
"v_fma_f32 v244,v244,v50,-v55\n"
"v_fma_f32 v245,v245,v50,-v55\n"
"v_fma_f32 v246,v246,v50,-v55\n"
"v_fma_f32 v247,v247,v50,-v55\n"
"v_cvt_pk_bf16_f32 v56,v240,v244\n"
"v_cvt_pk_bf16_f32 v57,v241,v245\n"
"v_cvt_pk_bf16_f32 v58,v242,v246\n"
"v_cvt_pk_bf16_f32 v59,v243,v247\n"
"s_add_u32 s42,s58,7090176\n"
"buffer_store_dwordx4 v[56:59],v209,s[64:67],s42 offen sc1\n"
"v_accvgpr_read_b32 v236,a224\n"
"v_accvgpr_read_b32 v237,a225\n"
"v_accvgpr_read_b32 v238,a226\n"
"v_accvgpr_read_b32 v239,a227\n"
"v_lshlrev_b32 v240,16,v236\n"
"v_lshlrev_b32 v241,16,v237\n"
"v_lshlrev_b32 v242,16,v238\n"
"v_lshlrev_b32 v243,16,v239\n"
"v_and_b32 v244,s43,v236\n"
"v_and_b32 v245,s43,v237\n"
"v_and_b32 v246,s43,v238\n"
"v_and_b32 v247,s43,v239\n"
"v_fma_f32 v240,v240,v50,-v55\n"
"v_fma_f32 v241,v241,v50,-v55\n"
"v_fma_f32 v242,v242,v50,-v55\n"
"v_fma_f32 v243,v243,v50,-v55\n"
"v_fma_f32 v244,v244,v50,-v55\n"
"v_fma_f32 v245,v245,v50,-v55\n"
"v_fma_f32 v246,v246,v50,-v55\n"
"v_fma_f32 v247,v247,v50,-v55\n"
"v_cvt_pk_bf16_f32 v228,v240,v244\n"
"v_cvt_pk_bf16_f32 v229,v241,v245\n"
"v_cvt_pk_bf16_f32 v230,v242,v246\n"
"v_cvt_pk_bf16_f32 v231,v243,v247\n"
"s_add_u32 s42,s58,7094272\n"
"buffer_store_dwordx4 v[228:231],v209,s[64:67],s42 offen sc1\n"
"v_accvgpr_read_b32 v236,a228\n"
"v_accvgpr_read_b32 v237,a229\n"
"v_accvgpr_read_b32 v238,a230\n"
"v_accvgpr_read_b32 v239,a231\n"
"v_lshlrev_b32 v240,16,v236\n"
"v_lshlrev_b32 v241,16,v237\n"
"v_lshlrev_b32 v242,16,v238\n"
"v_lshlrev_b32 v243,16,v239\n"
"v_and_b32 v244,s43,v236\n"
"v_and_b32 v245,s43,v237\n"
"v_and_b32 v246,s43,v238\n"
"v_and_b32 v247,s43,v239\n"
"v_fma_f32 v240,v240,v50,-v55\n"
"v_fma_f32 v241,v241,v50,-v55\n"
"v_fma_f32 v242,v242,v50,-v55\n"
"v_fma_f32 v243,v243,v50,-v55\n"
"v_fma_f32 v244,v244,v50,-v55\n"
"v_fma_f32 v245,v245,v50,-v55\n"
"v_fma_f32 v246,v246,v50,-v55\n"
"v_fma_f32 v247,v247,v50,-v55\n"
"v_cvt_pk_bf16_f32 v56,v240,v244\n"
"v_cvt_pk_bf16_f32 v57,v241,v245\n"
"v_cvt_pk_bf16_f32 v58,v242,v246\n"
"v_cvt_pk_bf16_f32 v59,v243,v247\n"
"s_add_u32 s42,s58,7098368\n"
"buffer_store_dwordx4 v[56:59],v209,s[64:67],s42 offen sc1\n"
"v_accvgpr_read_b32 v236,a232\n"
"v_accvgpr_read_b32 v237,a233\n"
"v_accvgpr_read_b32 v238,a234\n"
"v_accvgpr_read_b32 v239,a235\n"
"v_lshlrev_b32 v240,16,v236\n"
"v_lshlrev_b32 v241,16,v237\n"
"v_lshlrev_b32 v242,16,v238\n"
"v_lshlrev_b32 v243,16,v239\n"
"v_and_b32 v244,s43,v236\n"
"v_and_b32 v245,s43,v237\n"
"v_and_b32 v246,s43,v238\n"
"v_and_b32 v247,s43,v239\n"
"v_fma_f32 v240,v240,v50,-v55\n"
"v_fma_f32 v241,v241,v50,-v55\n"
"v_fma_f32 v242,v242,v50,-v55\n"
"v_fma_f32 v243,v243,v50,-v55\n"
"v_fma_f32 v244,v244,v50,-v55\n"
"v_fma_f32 v245,v245,v50,-v55\n"
"v_fma_f32 v246,v246,v50,-v55\n"
"v_fma_f32 v247,v247,v50,-v55\n"
"v_cvt_pk_bf16_f32 v228,v240,v244\n"
"v_cvt_pk_bf16_f32 v229,v241,v245\n"
"v_cvt_pk_bf16_f32 v230,v242,v246\n"
"v_cvt_pk_bf16_f32 v231,v243,v247\n"
"s_add_u32 s42,s58,7102464\n"
"buffer_store_dwordx4 v[228:231],v209,s[64:67],s42 offen sc1\n"
"v_accvgpr_read_b32 v236,a236\n"
"v_accvgpr_read_b32 v237,a237\n"
"v_accvgpr_read_b32 v238,a238\n"
"v_accvgpr_read_b32 v239,a239\n"
"v_lshlrev_b32 v240,16,v236\n"
"v_lshlrev_b32 v241,16,v237\n"
"v_lshlrev_b32 v242,16,v238\n"
"v_lshlrev_b32 v243,16,v239\n"
"v_and_b32 v244,s43,v236\n"
"v_and_b32 v245,s43,v237\n"
"v_and_b32 v246,s43,v238\n"
"v_and_b32 v247,s43,v239\n"
"v_fma_f32 v240,v240,v50,-v55\n"
"v_fma_f32 v241,v241,v50,-v55\n"
"v_fma_f32 v242,v242,v50,-v55\n"
"v_fma_f32 v243,v243,v50,-v55\n"
"v_fma_f32 v244,v244,v50,-v55\n"
"v_fma_f32 v245,v245,v50,-v55\n"
"v_fma_f32 v246,v246,v50,-v55\n"
"v_fma_f32 v247,v247,v50,-v55\n"
"v_cvt_pk_bf16_f32 v56,v240,v244\n"
"v_cvt_pk_bf16_f32 v57,v241,v245\n"
"v_cvt_pk_bf16_f32 v58,v242,v246\n"
"v_cvt_pk_bf16_f32 v59,v243,v247\n"
"s_add_u32 s42,s58,7106560\n"
"buffer_store_dwordx4 v[56:59],v209,s[64:67],s42 offen sc1\n"
"v_accvgpr_read_b32 v236,a240\n"
"v_accvgpr_read_b32 v237,a241\n"
"v_accvgpr_read_b32 v238,a242\n"
"v_accvgpr_read_b32 v239,a243\n"
"v_lshlrev_b32 v240,16,v236\n"
"v_lshlrev_b32 v241,16,v237\n"
"v_lshlrev_b32 v242,16,v238\n"
"v_lshlrev_b32 v243,16,v239\n"
"v_and_b32 v244,s43,v236\n"
"v_and_b32 v245,s43,v237\n"
"v_and_b32 v246,s43,v238\n"
"v_and_b32 v247,s43,v239\n"
"v_fma_f32 v240,v240,v50,-v55\n"
"v_fma_f32 v241,v241,v50,-v55\n"
"v_fma_f32 v242,v242,v50,-v55\n"
"v_fma_f32 v243,v243,v50,-v55\n"
"v_fma_f32 v244,v244,v50,-v55\n"
"v_fma_f32 v245,v245,v50,-v55\n"
"v_fma_f32 v246,v246,v50,-v55\n"
"v_fma_f32 v247,v247,v50,-v55\n"
"v_cvt_pk_bf16_f32 v228,v240,v244\n"
"v_cvt_pk_bf16_f32 v229,v241,v245\n"
"v_cvt_pk_bf16_f32 v230,v242,v246\n"
"v_cvt_pk_bf16_f32 v231,v243,v247\n"
"s_add_u32 s42,s58,7110656\n"
"buffer_store_dwordx4 v[228:231],v209,s[64:67],s42 offen sc1\n"
"s_branch LM_BODY\n"
"LM_EXACT:\n"
"s_waitcnt vmcnt(0) lgkmcnt(0)\n"
"s_barrier\n"
"s_add_u32 m0,s62,0\n"
"s_add_u32 s41,s61,0\n"
"buffer_load_dwordx4 v198,s[76:79],s41 offen lds\n"
"buffer_load_dwordx4 v199,s[76:79],s41 offen offset:1024 lds\n"
"buffer_load_dwordx4 v200,s[76:79],s41 offen offset:2048 lds\n"
"buffer_load_dwordx4 v201,s[76:79],s41 offen offset:3072 lds\n"
"s_add_u32 m0,s62,16384\n"
"s_add_u32 s41,s61,16384\n"
"buffer_load_dwordx4 v198,s[76:79],s41 offen lds\n"
"buffer_load_dwordx4 v199,s[76:79],s41 offen offset:1024 lds\n"
"buffer_load_dwordx4 v200,s[76:79],s41 offen offset:2048 lds\n"
"buffer_load_dwordx4 v201,s[76:79],s41 offen offset:3072 lds\n"
"s_add_u32 m0,s62,32768\n"
"s_add_u32 s41,s61,32768\n"
"buffer_load_dwordx4 v198,s[76:79],s41 offen lds\n"
"buffer_load_dwordx4 v199,s[76:79],s41 offen offset:1024 lds\n"
"buffer_load_dwordx4 v200,s[76:79],s41 offen offset:2048 lds\n"
"buffer_load_dwordx4 v201,s[76:79],s41 offen offset:3072 lds\n"
"s_add_u32 m0,s63,49152\n"
"s_add_u32 s41,s61,0\n"
"buffer_load_dwordx4 v202,s[80:83],s41 offen lds\n"
"buffer_load_dwordx4 v203,s[80:83],s41 offen offset:1024 lds\n"
"buffer_load_dwordx4 v202,s[80:83],s41 offen offset:2048 lds\n"
"buffer_load_dwordx4 v203,s[80:83],s41 offen offset:3072 lds\n"
"s_add_u32 m0,s63,0\n"
"s_add_u32 s41,s61,16384\n"
"buffer_load_dwordx4 v202,s[80:83],s41 offen lds\n"
"buffer_load_dwordx4 v203,s[80:83],s41 offen offset:1024 lds\n"
"buffer_load_dwordx4 v202,s[80:83],s41 offen offset:2048 lds\n"
"buffer_load_dwordx4 v203,s[80:83],s41 offen offset:3072 lds\n"
"s_add_u32 m0,s62,49152\n"
"s_add_u32 s41,s61,49152\n"
"buffer_load_dwordx4 v198,s[76:79],s41 offen lds\n"
"buffer_load_dwordx4 v199,s[76:79],s41 offen offset:1024 lds\n"
"buffer_load_dwordx4 v200,s[76:79],s41 offen offset:2048 lds\n"
"buffer_load_dwordx4 v201,s[76:79],s41 offen offset:3072 lds\n"
"v_and_b32 v223,0xf0,v209\n"
"v_lshlrev_b32 v223,4,v223\n"
"v_lshrrev_b32 v226,8,v209\n"
"v_lshl_add_u32 v223,v226,4,v223\n"
"v_mov_b32 v236,0xff800000\n"
"v_mov_b32 v238,0xff800000\n"
"v_mov_b32 v240,0xff800000\n"
"v_mov_b32 v242,0xff800000\n"
"s_mov_b32 s42,0\n"
"LM_EXL:\n"
"buffer_load_dwordx4 a[128:131],v223,s[76:79],s42 offen\n"
"buffer_load_dwordx4 a[132:135],v223,s[76:79],s42 offen offset:64\n"
"buffer_load_dwordx4 a[136:139],v223,s[76:79],s42 offen offset:128\n"
"buffer_load_dwordx4 a[140:143],v223,s[76:79],s42 offen offset:192\n"
"s_waitcnt vmcnt(0)\n"
"v_mfma_f32_16x16x32_bf16 v[0:3],a[128:131],v[128:131],0\n"
"v_mfma_f32_16x16x32_bf16 v[0:3],a[132:135],v[132:135],v[0:3]\n"
"v_mfma_f32_16x16x32_bf16 v[0:3],a[136:139],v[136:139],v[0:3]\n"
"v_mfma_f32_16x16x32_bf16 v[0:3],a[140:143],v[140:143],v[0:3]\n"
"v_mfma_f32_16x16x32_bf16 v[4:7],a[128:131],v[144:147],0\n"
"v_mfma_f32_16x16x32_bf16 v[4:7],a[132:135],v[148:151],v[4:7]\n"
"v_mfma_f32_16x16x32_bf16 v[4:7],a[136:139],v[152:155],v[4:7]\n"
"v_mfma_f32_16x16x32_bf16 v[4:7],a[140:143],v[156:159],v[4:7]\n"
"v_mfma_f32_16x16x32_bf16 v[8:11],a[128:131],v[160:163],0\n"
"v_mfma_f32_16x16x32_bf16 v[8:11],a[132:135],v[164:167],v[8:11]\n"
"v_mfma_f32_16x16x32_bf16 v[8:11],a[136:139],v[168:171],v[8:11]\n"
"v_mfma_f32_16x16x32_bf16 v[8:11],a[140:143],v[172:175],v[8:11]\n"
"v_mfma_f32_16x16x32_bf16 v[12:15],a[128:131],v[176:179],0\n"
"v_mfma_f32_16x16x32_bf16 v[12:15],a[132:135],v[180:183],v[12:15]\n"
"v_mfma_f32_16x16x32_bf16 v[12:15],a[136:139],v[184:187],v[12:15]\n"
"v_mfma_f32_16x16x32_bf16 v[12:15],a[140:143],v[188:191],v[12:15]\n"
"s_nop 15\n"
"v_max3_f32 v236,v236,v0,v1\n"
"v_max3_f32 v236,v236,v2,v3\n"
"v_max3_f32 v238,v238,v4,v5\n"
"v_max3_f32 v238,v238,v6,v7\n"
"v_max3_f32 v240,v240,v8,v9\n"
"v_max3_f32 v240,v240,v10,v11\n"
"v_max3_f32 v242,v242,v12,v13\n"
"v_max3_f32 v242,v242,v14,v15\n"
"s_add_u32 s42,s42,4096\n"
"s_cmp_lt_u32 s42,524288\n"
"s_cbranch_scc1 LM_EXL\n"
"v_mov_b32 v237,v236\n"
"v_mov_b32 v239,v238\n"
"v_mov_b32 v241,v240\n"
"v_mov_b32 v243,v242\n"
"s_nop 1\n"
"v_permlane32_swap_b32 v236,v237\n"
"v_permlane32_swap_b32 v238,v239\n"
"v_permlane32_swap_b32 v240,v241\n"
"v_permlane32_swap_b32 v242,v243\n"
"s_nop 1\n"
"v_max_f32 v236,v236,v237\n"
"v_max_f32 v238,v238,v239\n"
"v_max_f32 v240,v240,v241\n"
"v_max_f32 v242,v242,v243\n"
"v_mov_b32 v237,v236\n"
"v_mov_b32 v239,v238\n"
"v_mov_b32 v241,v240\n"
"v_mov_b32 v243,v242\n"
"s_nop 1\n"
"v_permlane16_swap_b32 v236,v237\n"
"v_permlane16_swap_b32 v238,v239\n"
"v_permlane16_swap_b32 v240,v241\n"
"v_permlane16_swap_b32 v242,v243\n"
"s_nop 1\n"
"v_max_f32 v236,v236,v237\n"
"v_max_f32 v238,v238,v239\n"
"v_max_f32 v240,v240,v241\n"
"v_max_f32 v242,v242,v243\n"
"v_sub_f32 v32,0,v236\n"
"v_sub_f32 v33,0,v236\n"
"v_sub_f32 v34,0,v236\n"
"v_sub_f32 v35,0,v236\n"
"v_sub_f32 v36,0,v238\n"
"v_sub_f32 v37,0,v238\n"
"v_sub_f32 v38,0,v238\n"
"v_sub_f32 v39,0,v238\n"
"v_sub_f32 v40,0,v240\n"
"v_sub_f32 v41,0,v240\n"
"v_sub_f32 v42,0,v240\n"
"v_sub_f32 v43,0,v240\n"
"v_sub_f32 v44,0,v242\n"
"v_sub_f32 v45,0,v242\n"
"v_sub_f32 v46,0,v242\n"
"v_sub_f32 v47,0,v242\n"
"s_mov_b32 s54,1\n"
"s_branch LM_ATT\n"
"LM_TAIL:\n"
"v_readlane_b32 s72,v210,16\n"
"v_readlane_b32 s73,v210,17\n"
"v_readlane_b32 s74,v210,18\n"
"s_mov_b32 s75,0x20000\n"
"v_readlane_b32 s84,v210,11\n"
"v_readlane_b32 s85,v210,12\n"
"s_mov_b32 s86,0x4000\n"
"s_mov_b32 s87,0x20000\n"
"v_readlane_b32 s40,v210,39\n"
"s_mul_i32 s39,s48,147456\n"
"s_mov_b32 s52,73728\n"
"s_mov_b32 s53,516096\n"
"v_mfma_f32_16x16x32_bf16 v[16:19],a[144:147],v[128:131],v[32:35]\n"
"v_exp_f32 v0,v0\n"
"v_mfma_f32_16x16x32_bf16 v[16:19],a[148:151],v[132:135],v[16:19]\n"
"ds_read_b128 a[128:131],v192 offset:24576\n"
"v_cvt_pk_bf16_f32 v95,v30,v31\n"
"v_mfma_f32_16x16x32_bf16 v[16:19],a[152:155],v[136:139],v[16:19]\n"
"s_add_u32 s37,s37,16384\n"
"v_exp_f32 v1,v1\n"
"v_mfma_f32_16x16x32_bf16 v[16:19],a[156:159],v[140:143],v[16:19]\n"
"ds_read_b128 a[132:135],v193 offset:24576\n"
"v_mfma_f32_16x16x32_bf16 v[20:23],a[144:147],v[144:147],v[36:39]\n"
"v_exp_f32 v2,v2\n"
"v_mfma_f32_16x16x32_bf16 v[20:23],a[148:151],v[148:151],v[20:23]\n"
"ds_read_b128 a[136:139],v194 offset:24576\n"
"v_cvt_pk_bf16_f32 v96,v0,v1\n"
"v_mfma_f32_16x16x32_bf16 v[20:23],a[152:155],v[152:155],v[20:23]\n"
"v_exp_f32 v3,v3\n"
"v_mfma_f32_16x16x32_bf16 v[20:23],a[156:159],v[156:159],v[20:23]\n"
"ds_read_b128 a[140:143],v195 offset:24576\n"
"v_mfma_f32_16x16x32_bf16 v[24:27],a[144:147],v[160:163],v[40:43]\n"
"v_exp_f32 v4,v4\n"
"v_mfma_f32_16x16x32_bf16 v[24:27],a[148:151],v[164:167],v[24:27]\n"
"ds_read_b64_tr_b16 a[176:177],v196 offset:49408\n"
"v_cvt_pk_bf16_f32 v97,v2,v3\n"
"v_mfma_f32_16x16x32_bf16 v[24:27],a[152:155],v[168:171],v[24:27]\n"
"v_exp_f32 v5,v5\n"
"v_mfma_f32_16x16x32_bf16 v[24:27],a[156:159],v[172:175],v[24:27]\n"
"ds_read_b64_tr_b16 a[178:179],v196 offset:53504\n"
"v_mfma_f32_16x16x32_bf16 v[28:31],a[144:147],v[176:179],v[44:47]\n"
"s_add_u32 m0,s51,139264\n"
"v_exp_f32 v6,v6\n"
"v_mfma_f32_16x16x32_bf16 v[28:31],a[148:151],v[180:183],v[28:31]\n"
"buffer_load_dwordx4 v216,s[72:75],s39 offen lds\n"
"v_mfma_f32_16x16x32_bf16 v[28:31],a[152:155],v[184:187],v[28:31]\n"
"v_mfma_f32_16x16x32_bf16 v[28:31],a[156:159],v[188:191],v[28:31]\n"
"v_exp_f32 v7,v7\n"
"v_mfma_f32_16x16x32_bf16 a[0:3],a[160:163],v[64:67],a[0:3]\n"
"ds_read_b64_tr_b16 a[180:181],v196 offset:57600\n"
"v_cvt_pk_bf16_f32 v104,v4,v5\n"
"v_mfma_f32_16x16x32_bf16 a[0:3],a[164:167],v[68:71],a[0:3]\n"
"v_exp_f32 v8,v8\n"
"v_mfma_f32_16x16x32_bf16 a[4:7],a[160:163],v[72:75],a[4:7]\n"
"ds_read_b64_tr_b16 a[182:183],v196 offset:61696\n"
"v_cvt_pk_bf16_f32 v105,v6,v7\n"
"v_mfma_f32_16x16x32_bf16 a[4:7],a[164:167],v[76:79],a[4:7]\n"
"v_exp_f32 v9,v9\n"
"v_mfma_f32_16x16x32_bf16 a[8:11],a[160:163],v[80:83],a[8:11]\n"
"ds_read_b64_tr_b16 a[184:185],v197 offset:49408\n"
"v_mfma_f32_16x16x32_bf16 a[8:11],a[164:167],v[84:87],a[8:11]\n"
"v_exp_f32 v10,v10\n"
"v_mfma_f32_16x16x32_bf16 a[12:15],a[160:163],v[88:91],a[12:15]\n"
"ds_read_b64_tr_b16 a[186:187],v197 offset:53504\n"
"v_cvt_pk_bf16_f32 v112,v8,v9\n"
"v_mfma_f32_16x16x32_bf16 a[12:15],a[164:167],v[92:95],a[12:15]\n"
"v_exp_f32 v11,v11\n"
"v_mfma_f32_16x16x32_bf16 a[16:19],a[168:171],v[64:67],a[16:19]\n"
"ds_read_b64_tr_b16 a[188:189],v197 offset:57600\n"
"v_mfma_f32_16x16x32_bf16 a[16:19],a[172:175],v[68:71],a[16:19]\n"
"v_exp_f32 v12,v12\n"
"v_mfma_f32_16x16x32_bf16 a[20:23],a[168:171],v[72:75],a[20:23]\n"
"ds_read_b64_tr_b16 a[190:191],v197 offset:61696\n"
"v_cvt_pk_bf16_f32 v113,v10,v11\n"
"v_mfma_f32_16x16x32_bf16 a[20:23],a[172:175],v[76:79],a[20:23]\n"
"v_exp_f32 v13,v13\n"
"v_mfma_f32_16x16x32_bf16 a[24:27],a[168:171],v[80:83],a[24:27]\n"
"buffer_load_dwordx4 v217,s[72:75],s39 offen offset:1024 lds\n"
"v_cvt_pk_bf16_f32 v120,v12,v13\n"
"v_mfma_f32_16x16x32_bf16 a[24:27],a[172:175],v[84:87],a[24:27]\n"
"v_exp_f32 v14,v14\n"
"v_mfma_f32_16x16x32_bf16 a[28:31],a[168:171],v[88:91],a[28:31]\n"
"v_exp_f32 v15,v15\n"
"v_mfma_f32_16x16x32_bf16 a[28:31],a[172:175],v[92:95],a[28:31]\n"
"buffer_load_dwordx4 v218,s[72:75],s39 offen offset:2048 lds\n"
"v_smfmac_f32_16x16x64_bf16 a[192:195],v[232:235],v[64:71],v61\n"
"s_add_u32 s39,s39,s52\n"
"s_waitcnt lgkmcnt(0)\n"
"v_mfma_f32_16x16x32_bf16 v[0:3],a[128:131],v[128:131],v[32:35]\n"
"v_exp_f32 v16,v16\n"
"v_mfma_f32_16x16x32_bf16 v[0:3],a[132:135],v[132:135],v[0:3]\n"
"ds_read_b128 a[144:147],v192 offset:28672\n"
"v_cvt_pk_bf16_f32 v121,v14,v15\n"
"v_mfma_f32_16x16x32_bf16 v[0:3],a[136:139],v[136:139],v[0:3]\n"
"v_exp_f32 v17,v17\n"
"v_mfma_f32_16x16x32_bf16 v[0:3],a[140:143],v[140:143],v[0:3]\n"
"ds_read_b128 a[148:151],v193 offset:28672\n"
"v_mfma_f32_16x16x32_bf16 v[4:7],a[128:131],v[144:147],v[36:39]\n"
"v_exp_f32 v18,v18\n"
"v_mfma_f32_16x16x32_bf16 v[4:7],a[132:135],v[148:151],v[4:7]\n"
"ds_read_b128 a[152:155],v194 offset:28672\n"
"v_cvt_pk_bf16_f32 v98,v16,v17\n"
"v_mfma_f32_16x16x32_bf16 v[4:7],a[136:139],v[152:155],v[4:7]\n"
"v_exp_f32 v19,v19\n"
"v_mfma_f32_16x16x32_bf16 v[4:7],a[140:143],v[156:159],v[4:7]\n"
"ds_read_b128 a[156:159],v195 offset:28672\n"
"v_mfma_f32_16x16x32_bf16 v[8:11],a[128:131],v[160:163],v[40:43]\n"
"v_exp_f32 v20,v20\n"
"v_mfma_f32_16x16x32_bf16 v[8:11],a[132:135],v[164:167],v[8:11]\n"
"ds_read_b64_tr_b16 a[160:161],v196 offset:49664\n"
"v_cvt_pk_bf16_f32 v99,v18,v19\n"
"v_mfma_f32_16x16x32_bf16 v[8:11],a[136:139],v[168:171],v[8:11]\n"
"v_exp_f32 v21,v21\n"
"v_mfma_f32_16x16x32_bf16 v[8:11],a[140:143],v[172:175],v[8:11]\n"
"ds_read_b64_tr_b16 a[162:163],v196 offset:53760\n"
"v_mfma_f32_16x16x32_bf16 v[12:15],a[128:131],v[176:179],v[44:47]\n"
"s_add_u32 m0,s51,142336\n"
"v_exp_f32 v22,v22\n"
"v_mfma_f32_16x16x32_bf16 v[12:15],a[132:135],v[180:183],v[12:15]\n"
"buffer_load_dwordx4 v216,s[72:75],s39 offen lds\n"
"v_mfma_f32_16x16x32_bf16 v[12:15],a[136:139],v[184:187],v[12:15]\n"
"v_mfma_f32_16x16x32_bf16 v[12:15],a[140:143],v[188:191],v[12:15]\n"
"v_exp_f32 v23,v23\n"
"v_mfma_f32_16x16x32_bf16 a[32:35],a[176:179],v[64:67],a[32:35]\n"
"ds_read_b64_tr_b16 a[164:165],v196 offset:57856\n"
"v_cvt_pk_bf16_f32 v106,v20,v21\n"
"v_mfma_f32_16x16x32_bf16 a[32:35],a[180:183],v[68:71],a[32:35]\n"
"v_exp_f32 v24,v24\n"
"v_mfma_f32_16x16x32_bf16 a[36:39],a[176:179],v[72:75],a[36:39]\n"
"ds_read_b64_tr_b16 a[166:167],v196 offset:61952\n"
"v_cvt_pk_bf16_f32 v107,v22,v23\n"
"v_mfma_f32_16x16x32_bf16 a[36:39],a[180:183],v[76:79],a[36:39]\n"
"v_exp_f32 v25,v25\n"
"v_mfma_f32_16x16x32_bf16 a[40:43],a[176:179],v[80:83],a[40:43]\n"
"ds_read_b64_tr_b16 a[168:169],v197 offset:49664\n"
"v_mfma_f32_16x16x32_bf16 a[40:43],a[180:183],v[84:87],a[40:43]\n"
"v_exp_f32 v26,v26\n"
"v_mfma_f32_16x16x32_bf16 a[44:47],a[176:179],v[88:91],a[44:47]\n"
"ds_read_b64_tr_b16 a[170:171],v197 offset:53760\n"
"v_cvt_pk_bf16_f32 v114,v24,v25\n"
"v_mfma_f32_16x16x32_bf16 a[44:47],a[180:183],v[92:95],a[44:47]\n"
"v_exp_f32 v27,v27\n"
"v_mfma_f32_16x16x32_bf16 a[48:51],a[184:187],v[64:67],a[48:51]\n"
"ds_read_b64_tr_b16 a[172:173],v197 offset:57856\n"
"v_mfma_f32_16x16x32_bf16 a[48:51],a[188:191],v[68:71],a[48:51]\n"
"v_exp_f32 v28,v28\n"
"v_mfma_f32_16x16x32_bf16 a[52:55],a[184:187],v[72:75],a[52:55]\n"
"ds_read_b64_tr_b16 a[174:175],v197 offset:61952\n"
"v_cvt_pk_bf16_f32 v115,v26,v27\n"
"v_mfma_f32_16x16x32_bf16 a[52:55],a[188:191],v[76:79],a[52:55]\n"
"v_exp_f32 v29,v29\n"
"v_mfma_f32_16x16x32_bf16 a[56:59],a[184:187],v[80:83],a[56:59]\n"
"buffer_load_dwordx4 v217,s[72:75],s39 offen offset:1024 lds\n"
"v_cvt_pk_bf16_f32 v122,v28,v29\n"
"v_mfma_f32_16x16x32_bf16 a[56:59],a[188:191],v[84:87],a[56:59]\n"
"v_exp_f32 v30,v30\n"
"v_mfma_f32_16x16x32_bf16 a[60:63],a[184:187],v[88:91],a[60:63]\n"
"v_exp_f32 v31,v31\n"
"v_mfma_f32_16x16x32_bf16 a[60:63],a[188:191],v[92:95],a[60:63]\n"
"buffer_load_dwordx4 v218,s[72:75],s39 offen offset:2048 lds\n"
"v_smfmac_f32_16x16x64_bf16 a[196:199],v[232:235],v[72:79],v61\n"
"s_add_u32 s39,s39,s53\n"
"s_waitcnt vmcnt(14) lgkmcnt(0)\n"
"s_barrier\n"
"v_mfma_f32_16x16x32_bf16 v[16:19],a[144:147],v[128:131],v[32:35]\n"
"v_exp_f32 v0,v0\n"
"v_mfma_f32_16x16x32_bf16 v[16:19],a[148:151],v[132:135],v[16:19]\n"
"ds_read_b128 a[128:131],v192 offset:32768\n"
"v_cvt_pk_bf16_f32 v123,v30,v31\n"
"v_mfma_f32_16x16x32_bf16 v[16:19],a[152:155],v[136:139],v[16:19]\n"
"s_add_u32 s36,s36,16384\n"
"v_exp_f32 v1,v1\n"
"v_mfma_f32_16x16x32_bf16 v[16:19],a[156:159],v[140:143],v[16:19]\n"
"ds_read_b128 a[132:135],v193 offset:32768\n"
"v_mfma_f32_16x16x32_bf16 v[20:23],a[144:147],v[144:147],v[36:39]\n"
"v_exp_f32 v2,v2\n"
"v_mfma_f32_16x16x32_bf16 v[20:23],a[148:151],v[148:151],v[20:23]\n"
"ds_read_b128 a[136:139],v194 offset:32768\n"
"v_cvt_pk_bf16_f32 v100,v0,v1\n"
"v_mfma_f32_16x16x32_bf16 v[20:23],a[152:155],v[152:155],v[20:23]\n"
"v_exp_f32 v3,v3\n"
"v_mfma_f32_16x16x32_bf16 v[20:23],a[156:159],v[156:159],v[20:23]\n"
"ds_read_b128 a[140:143],v195 offset:32768\n"
"v_mfma_f32_16x16x32_bf16 v[24:27],a[144:147],v[160:163],v[40:43]\n"
"v_exp_f32 v4,v4\n"
"v_mfma_f32_16x16x32_bf16 v[24:27],a[148:151],v[164:167],v[24:27]\n"
"ds_read_b64_tr_b16 a[176:177],v196 offset:49920\n"
"v_cvt_pk_bf16_f32 v101,v2,v3\n"
"v_mfma_f32_16x16x32_bf16 v[24:27],a[152:155],v[168:171],v[24:27]\n"
"v_exp_f32 v5,v5\n"
"v_mfma_f32_16x16x32_bf16 v[24:27],a[156:159],v[172:175],v[24:27]\n"
"ds_read_b64_tr_b16 a[178:179],v196 offset:54016\n"
"v_mfma_f32_16x16x32_bf16 v[28:31],a[144:147],v[176:179],v[44:47]\n"
"s_add_u32 m0,s63,32768\n"
"v_exp_f32 v6,v6\n"
"v_mfma_f32_16x16x32_bf16 v[28:31],a[148:151],v[180:183],v[28:31]\n"
"buffer_load_dwordx4 v202,s[80:83],s37 offen lds\n"
"v_mfma_f32_16x16x32_bf16 v[28:31],a[152:155],v[184:187],v[28:31]\n"
"v_mfma_f32_16x16x32_bf16 v[28:31],a[156:159],v[188:191],v[28:31]\n"
"v_exp_f32 v7,v7\n"
"v_mfma_f32_16x16x32_bf16 a[64:67],a[160:163],v[64:67],a[64:67]\n"
"ds_read_b64_tr_b16 a[180:181],v196 offset:58112\n"
"v_cvt_pk_bf16_f32 v108,v4,v5\n"
"v_mfma_f32_16x16x32_bf16 a[64:67],a[164:167],v[68:71],a[64:67]\n"
"v_exp_f32 v8,v8\n"
"v_mfma_f32_16x16x32_bf16 a[68:71],a[160:163],v[72:75],a[68:71]\n"
"ds_read_b64_tr_b16 a[182:183],v196 offset:62208\n"
"v_cvt_pk_bf16_f32 v109,v6,v7\n"
"v_mfma_f32_16x16x32_bf16 a[68:71],a[164:167],v[76:79],a[68:71]\n"
"v_exp_f32 v9,v9\n"
"v_mfma_f32_16x16x32_bf16 a[72:75],a[160:163],v[80:83],a[72:75]\n"
"ds_read_b64_tr_b16 a[184:185],v197 offset:49920\n"
"v_mfma_f32_16x16x32_bf16 a[72:75],a[164:167],v[84:87],a[72:75]\n"
"v_exp_f32 v10,v10\n"
"v_mfma_f32_16x16x32_bf16 a[76:79],a[160:163],v[88:91],a[76:79]\n"
"ds_read_b64_tr_b16 a[186:187],v197 offset:54016\n"
"v_cvt_pk_bf16_f32 v116,v8,v9\n"
"v_mfma_f32_16x16x32_bf16 a[76:79],a[164:167],v[92:95],a[76:79]\n"
"v_exp_f32 v11,v11\n"
"v_mfma_f32_16x16x32_bf16 a[80:83],a[168:171],v[64:67],a[80:83]\n"
"ds_read_b64_tr_b16 a[188:189],v197 offset:58112\n"
"v_mfma_f32_16x16x32_bf16 a[80:83],a[172:175],v[68:71],a[80:83]\n"
"v_exp_f32 v12,v12\n"
"v_mfma_f32_16x16x32_bf16 a[84:87],a[168:171],v[72:75],a[84:87]\n"
"ds_read_b64_tr_b16 a[190:191],v197 offset:62208\n"
"v_cvt_pk_bf16_f32 v117,v10,v11\n"
"v_mfma_f32_16x16x32_bf16 a[84:87],a[172:175],v[76:79],a[84:87]\n"
"v_exp_f32 v13,v13\n"
"v_mfma_f32_16x16x32_bf16 a[88:91],a[168:171],v[80:83],a[88:91]\n"
"v_cvt_pk_bf16_f32 v124,v12,v13\n"
"v_mfma_f32_16x16x32_bf16 a[88:91],a[172:175],v[84:87],a[88:91]\n"
"v_exp_f32 v14,v14\n"
"v_mfma_f32_16x16x32_bf16 a[92:95],a[168:171],v[88:91],a[92:95]\n"
"v_exp_f32 v15,v15\n"
"v_mfma_f32_16x16x32_bf16 a[92:95],a[172:175],v[92:95],a[92:95]\n"
"buffer_load_dwordx4 v203,s[80:83],s37 offen offset:1024 lds\n"
"v_smfmac_f32_16x16x64_bf16 a[200:203],v[232:235],v[80:87],v61\n"
"s_waitcnt lgkmcnt(0)\n"
"v_mfma_f32_16x16x32_bf16 v[0:3],a[128:131],v[128:131],v[32:35]\n"
"v_exp_f32 v16,v16\n"
"v_mfma_f32_16x16x32_bf16 v[0:3],a[132:135],v[132:135],v[0:3]\n"
"ds_read_b128 a[144:147],v192 offset:36864\n"
"v_cvt_pk_bf16_f32 v125,v14,v15\n"
"v_mfma_f32_16x16x32_bf16 v[0:3],a[136:139],v[136:139],v[0:3]\n"
"v_exp_f32 v17,v17\n"
"v_mfma_f32_16x16x32_bf16 v[0:3],a[140:143],v[140:143],v[0:3]\n"
"ds_read_b128 a[148:151],v193 offset:36864\n"
"v_mfma_f32_16x16x32_bf16 v[4:7],a[128:131],v[144:147],v[36:39]\n"
"v_exp_f32 v18,v18\n"
"v_mfma_f32_16x16x32_bf16 v[4:7],a[132:135],v[148:151],v[4:7]\n"
"ds_read_b128 a[152:155],v194 offset:36864\n"
"v_cvt_pk_bf16_f32 v102,v16,v17\n"
"v_mfma_f32_16x16x32_bf16 v[4:7],a[136:139],v[152:155],v[4:7]\n"
"v_exp_f32 v19,v19\n"
"v_mfma_f32_16x16x32_bf16 v[4:7],a[140:143],v[156:159],v[4:7]\n"
"ds_read_b128 a[156:159],v195 offset:36864\n"
"v_mfma_f32_16x16x32_bf16 v[8:11],a[128:131],v[160:163],v[40:43]\n"
"v_exp_f32 v20,v20\n"
"v_mfma_f32_16x16x32_bf16 v[8:11],a[132:135],v[164:167],v[8:11]\n"
"ds_read_b64_tr_b16 a[160:161],v196\n"
"v_cvt_pk_bf16_f32 v103,v18,v19\n"
"v_mfma_f32_16x16x32_bf16 v[8:11],a[136:139],v[168:171],v[8:11]\n"
"v_exp_f32 v21,v21\n"
"v_mfma_f32_16x16x32_bf16 v[8:11],a[140:143],v[172:175],v[8:11]\n"
"ds_read_b64_tr_b16 a[162:163],v196 offset:4096\n"
"v_mfma_f32_16x16x32_bf16 v[12:15],a[128:131],v[176:179],v[44:47]\n"
"v_exp_f32 v22,v22\n"
"v_mfma_f32_16x16x32_bf16 v[12:15],a[132:135],v[180:183],v[12:15]\n"
"buffer_load_dwordx4 v202,s[80:83],s37 offen offset:2048 lds\n"
"v_mfma_f32_16x16x32_bf16 v[12:15],a[136:139],v[184:187],v[12:15]\n"
"v_mfma_f32_16x16x32_bf16 v[12:15],a[140:143],v[188:191],v[12:15]\n"
"v_exp_f32 v23,v23\n"
"v_mfma_f32_16x16x32_bf16 a[96:99],a[176:179],v[64:67],a[96:99]\n"
"ds_read_b64_tr_b16 a[164:165],v196 offset:8192\n"
"v_cvt_pk_bf16_f32 v110,v20,v21\n"
"v_mfma_f32_16x16x32_bf16 a[96:99],a[180:183],v[68:71],a[96:99]\n"
"v_exp_f32 v24,v24\n"
"v_mfma_f32_16x16x32_bf16 a[100:103],a[176:179],v[72:75],a[100:103]\n"
"ds_read_b64_tr_b16 a[166:167],v196 offset:12288\n"
"v_cvt_pk_bf16_f32 v111,v22,v23\n"
"v_mfma_f32_16x16x32_bf16 a[100:103],a[180:183],v[76:79],a[100:103]\n"
"v_exp_f32 v25,v25\n"
"v_mfma_f32_16x16x32_bf16 a[104:107],a[176:179],v[80:83],a[104:107]\n"
"ds_read_b64_tr_b16 a[168:169],v197\n"
"v_mfma_f32_16x16x32_bf16 a[104:107],a[180:183],v[84:87],a[104:107]\n"
"v_exp_f32 v26,v26\n"
"v_mfma_f32_16x16x32_bf16 a[108:111],a[176:179],v[88:91],a[108:111]\n"
"ds_read_b64_tr_b16 a[170:171],v197 offset:4096\n"
"v_cvt_pk_bf16_f32 v118,v24,v25\n"
"v_mfma_f32_16x16x32_bf16 a[108:111],a[180:183],v[92:95],a[108:111]\n"
"v_exp_f32 v27,v27\n"
"v_mfma_f32_16x16x32_bf16 a[112:115],a[184:187],v[64:67],a[112:115]\n"
"ds_read_b64_tr_b16 a[172:173],v197 offset:8192\n"
"v_mfma_f32_16x16x32_bf16 a[112:115],a[188:191],v[68:71],a[112:115]\n"
"v_exp_f32 v28,v28\n"
"v_mfma_f32_16x16x32_bf16 a[116:119],a[184:187],v[72:75],a[116:119]\n"
"ds_read_b64_tr_b16 a[174:175],v197 offset:12288\n"
"v_cvt_pk_bf16_f32 v119,v26,v27\n"
"v_mfma_f32_16x16x32_bf16 a[116:119],a[188:191],v[76:79],a[116:119]\n"
"v_exp_f32 v29,v29\n"
"v_mfma_f32_16x16x32_bf16 a[120:123],a[184:187],v[80:83],a[120:123]\n"
"v_cvt_pk_bf16_f32 v126,v28,v29\n"
"v_mfma_f32_16x16x32_bf16 a[120:123],a[188:191],v[84:87],a[120:123]\n"
"v_exp_f32 v30,v30\n"
"v_mfma_f32_16x16x32_bf16 a[124:127],a[184:187],v[88:91],a[124:127]\n"
"v_exp_f32 v31,v31\n"
"v_mfma_f32_16x16x32_bf16 a[124:127],a[188:191],v[92:95],a[124:127]\n"
"buffer_load_dwordx4 v203,s[80:83],s37 offen offset:3072 lds\n"
"v_smfmac_f32_16x16x64_bf16 a[204:207],v[232:235],v[88:95],v61\n"
"s_waitcnt lgkmcnt(0)\n"
"v_mfma_f32_16x16x32_bf16 v[16:19],a[144:147],v[128:131],v[32:35]\n"
"v_exp_f32 v0,v0\n"
"v_mfma_f32_16x16x32_bf16 v[16:19],a[148:151],v[132:135],v[16:19]\n"
"ds_read_b128 a[128:131],v192 offset:40960\n"
"v_cvt_pk_bf16_f32 v127,v30,v31\n"
"v_mfma_f32_16x16x32_bf16 v[16:19],a[152:155],v[136:139],v[16:19]\n"
"v_exp_f32 v1,v1\n"
"v_mfma_f32_16x16x32_bf16 v[16:19],a[156:159],v[140:143],v[16:19]\n"
"ds_read_b128 a[132:135],v193 offset:40960\n"
"v_mfma_f32_16x16x32_bf16 v[20:23],a[144:147],v[144:147],v[36:39]\n"
"v_exp_f32 v2,v2\n"
"v_mfma_f32_16x16x32_bf16 v[20:23],a[148:151],v[148:151],v[20:23]\n"
"ds_read_b128 a[136:139],v194 offset:40960\n"
"v_cvt_pk_bf16_f32 v64,v0,v1\n"
"v_mfma_f32_16x16x32_bf16 v[20:23],a[152:155],v[152:155],v[20:23]\n"
"v_exp_f32 v3,v3\n"
"v_mfma_f32_16x16x32_bf16 v[20:23],a[156:159],v[156:159],v[20:23]\n"
"ds_read_b128 a[140:143],v195 offset:40960\n"
"v_mfma_f32_16x16x32_bf16 v[24:27],a[144:147],v[160:163],v[40:43]\n"
"v_exp_f32 v4,v4\n"
"v_mfma_f32_16x16x32_bf16 v[24:27],a[148:151],v[164:167],v[24:27]\n"
"ds_read_b64_tr_b16 a[176:177],v196 offset:256\n"
"v_cvt_pk_bf16_f32 v65,v2,v3\n"
"v_mfma_f32_16x16x32_bf16 v[24:27],a[152:155],v[168:171],v[24:27]\n"
"v_exp_f32 v5,v5\n"
"v_mfma_f32_16x16x32_bf16 v[24:27],a[156:159],v[172:175],v[24:27]\n"
"ds_read_b64_tr_b16 a[178:179],v196 offset:4352\n"
"v_mfma_f32_16x16x32_bf16 v[28:31],a[144:147],v[176:179],v[44:47]\n"
"s_add_u32 m0,s51,65536\n"
"v_exp_f32 v6,v6\n"
"v_mfma_f32_16x16x32_bf16 v[28:31],a[148:151],v[180:183],v[28:31]\n"
"buffer_load_dwordx4 v216,s[72:75],s39 offen lds\n"
"v_mfma_f32_16x16x32_bf16 v[28:31],a[152:155],v[184:187],v[28:31]\n"
"v_mfma_f32_16x16x32_bf16 v[28:31],a[156:159],v[188:191],v[28:31]\n"
"v_exp_f32 v7,v7\n"
"v_mfma_f32_16x16x32_bf16 a[0:3],a[160:163],v[96:99],a[0:3]\n"
"ds_read_b64_tr_b16 a[180:181],v196 offset:8448\n"
"v_cvt_pk_bf16_f32 v72,v4,v5\n"
"v_mfma_f32_16x16x32_bf16 a[0:3],a[164:167],v[100:103],a[0:3]\n"
"v_exp_f32 v8,v8\n"
"v_mfma_f32_16x16x32_bf16 a[4:7],a[160:163],v[104:107],a[4:7]\n"
"ds_read_b64_tr_b16 a[182:183],v196 offset:12544\n"
"v_cvt_pk_bf16_f32 v73,v6,v7\n"
"v_mfma_f32_16x16x32_bf16 a[4:7],a[164:167],v[108:111],a[4:7]\n"
"v_exp_f32 v9,v9\n"
"v_mfma_f32_16x16x32_bf16 a[8:11],a[160:163],v[112:115],a[8:11]\n"
"ds_read_b64_tr_b16 a[184:185],v197 offset:256\n"
"v_mfma_f32_16x16x32_bf16 a[8:11],a[164:167],v[116:119],a[8:11]\n"
"v_exp_f32 v10,v10\n"
"v_mfma_f32_16x16x32_bf16 a[12:15],a[160:163],v[120:123],a[12:15]\n"
"ds_read_b64_tr_b16 a[186:187],v197 offset:4352\n"
"v_cvt_pk_bf16_f32 v80,v8,v9\n"
"v_mfma_f32_16x16x32_bf16 a[12:15],a[164:167],v[124:127],a[12:15]\n"
"v_exp_f32 v11,v11\n"
"v_mfma_f32_16x16x32_bf16 a[16:19],a[168:171],v[96:99],a[16:19]\n"
"ds_read_b64_tr_b16 a[188:189],v197 offset:8448\n"
"v_mfma_f32_16x16x32_bf16 a[16:19],a[172:175],v[100:103],a[16:19]\n"
"v_exp_f32 v12,v12\n"
"v_mfma_f32_16x16x32_bf16 a[20:23],a[168:171],v[104:107],a[20:23]\n"
"ds_read_b64_tr_b16 a[190:191],v197 offset:12544\n"
"v_cvt_pk_bf16_f32 v81,v10,v11\n"
"v_mfma_f32_16x16x32_bf16 a[20:23],a[172:175],v[108:111],a[20:23]\n"
"v_exp_f32 v13,v13\n"
"v_mfma_f32_16x16x32_bf16 a[24:27],a[168:171],v[112:115],a[24:27]\n"
"buffer_load_dwordx4 v217,s[72:75],s39 offen offset:1024 lds\n"
"v_cvt_pk_bf16_f32 v88,v12,v13\n"
"v_mfma_f32_16x16x32_bf16 a[24:27],a[172:175],v[116:119],a[24:27]\n"
"v_exp_f32 v14,v14\n"
"v_mfma_f32_16x16x32_bf16 a[28:31],a[168:171],v[120:123],a[28:31]\n"
"v_exp_f32 v15,v15\n"
"v_mfma_f32_16x16x32_bf16 a[28:31],a[172:175],v[124:127],a[28:31]\n"
"buffer_load_dwordx4 v218,s[72:75],s39 offen offset:2048 lds\n"
"v_smfmac_f32_16x16x64_bf16 a[192:195],v[232:235],v[96:103],v61\n"
"s_add_u32 s39,s39,s52\n"
"s_waitcnt lgkmcnt(0)\n"
"v_mfma_f32_16x16x32_bf16 v[0:3],a[128:131],v[128:131],v[32:35]\n"
"v_exp_f32 v16,v16\n"
"v_mfma_f32_16x16x32_bf16 v[0:3],a[132:135],v[132:135],v[0:3]\n"
"ds_read_b128 a[144:147],v192 offset:45056\n"
"v_cvt_pk_bf16_f32 v89,v14,v15\n"
"v_mfma_f32_16x16x32_bf16 v[0:3],a[136:139],v[136:139],v[0:3]\n"
"v_exp_f32 v17,v17\n"
"v_mfma_f32_16x16x32_bf16 v[0:3],a[140:143],v[140:143],v[0:3]\n"
"ds_read_b128 a[148:151],v193 offset:45056\n"
"v_mfma_f32_16x16x32_bf16 v[4:7],a[128:131],v[144:147],v[36:39]\n"
"v_exp_f32 v18,v18\n"
"v_mfma_f32_16x16x32_bf16 v[4:7],a[132:135],v[148:151],v[4:7]\n"
"ds_read_b128 a[152:155],v194 offset:45056\n"
"v_cvt_pk_bf16_f32 v66,v16,v17\n"
"v_mfma_f32_16x16x32_bf16 v[4:7],a[136:139],v[152:155],v[4:7]\n"
"v_exp_f32 v19,v19\n"
"v_mfma_f32_16x16x32_bf16 v[4:7],a[140:143],v[156:159],v[4:7]\n"
"ds_read_b128 a[156:159],v195 offset:45056\n"
"v_mfma_f32_16x16x32_bf16 v[8:11],a[128:131],v[160:163],v[40:43]\n"
"v_exp_f32 v20,v20\n"
"v_mfma_f32_16x16x32_bf16 v[8:11],a[132:135],v[164:167],v[8:11]\n"
"ds_read_b64_tr_b16 a[160:161],v196 offset:512\n"
"v_cvt_pk_bf16_f32 v67,v18,v19\n"
"v_mfma_f32_16x16x32_bf16 v[8:11],a[136:139],v[168:171],v[8:11]\n"
"v_exp_f32 v21,v21\n"
"v_mfma_f32_16x16x32_bf16 v[8:11],a[140:143],v[172:175],v[8:11]\n"
"ds_read_b64_tr_b16 a[162:163],v196 offset:4608\n"
"v_mfma_f32_16x16x32_bf16 v[12:15],a[128:131],v[176:179],v[44:47]\n"
"s_add_u32 m0,s51,68608\n"
"v_exp_f32 v22,v22\n"
"v_mfma_f32_16x16x32_bf16 v[12:15],a[132:135],v[180:183],v[12:15]\n"
"buffer_load_dwordx4 v216,s[72:75],s39 offen lds\n"
"v_mfma_f32_16x16x32_bf16 v[12:15],a[136:139],v[184:187],v[12:15]\n"
"v_mfma_f32_16x16x32_bf16 v[12:15],a[140:143],v[188:191],v[12:15]\n"
"v_exp_f32 v23,v23\n"
"v_mfma_f32_16x16x32_bf16 a[32:35],a[176:179],v[96:99],a[32:35]\n"
"ds_read_b64_tr_b16 a[164:165],v196 offset:8704\n"
"v_cvt_pk_bf16_f32 v74,v20,v21\n"
"v_mfma_f32_16x16x32_bf16 a[32:35],a[180:183],v[100:103],a[32:35]\n"
"v_exp_f32 v24,v24\n"
"v_mfma_f32_16x16x32_bf16 a[36:39],a[176:179],v[104:107],a[36:39]\n"
"ds_read_b64_tr_b16 a[166:167],v196 offset:12800\n"
"v_cvt_pk_bf16_f32 v75,v22,v23\n"
"v_mfma_f32_16x16x32_bf16 a[36:39],a[180:183],v[108:111],a[36:39]\n"
"v_exp_f32 v25,v25\n"
"v_mfma_f32_16x16x32_bf16 a[40:43],a[176:179],v[112:115],a[40:43]\n"
"ds_read_b64_tr_b16 a[168:169],v197 offset:512\n"
"v_mfma_f32_16x16x32_bf16 a[40:43],a[180:183],v[116:119],a[40:43]\n"
"v_exp_f32 v26,v26\n"
"v_mfma_f32_16x16x32_bf16 a[44:47],a[176:179],v[120:123],a[44:47]\n"
"ds_read_b64_tr_b16 a[170:171],v197 offset:4608\n"
"v_cvt_pk_bf16_f32 v82,v24,v25\n"
"v_mfma_f32_16x16x32_bf16 a[44:47],a[180:183],v[124:127],a[44:47]\n"
"v_exp_f32 v27,v27\n"
"v_mfma_f32_16x16x32_bf16 a[48:51],a[184:187],v[96:99],a[48:51]\n"
"ds_read_b64_tr_b16 a[172:173],v197 offset:8704\n"
"v_mfma_f32_16x16x32_bf16 a[48:51],a[188:191],v[100:103],a[48:51]\n"
"v_exp_f32 v28,v28\n"
"v_mfma_f32_16x16x32_bf16 a[52:55],a[184:187],v[104:107],a[52:55]\n"
"ds_read_b64_tr_b16 a[174:175],v197 offset:12800\n"
"v_cvt_pk_bf16_f32 v83,v26,v27\n"
"v_mfma_f32_16x16x32_bf16 a[52:55],a[188:191],v[108:111],a[52:55]\n"
"v_exp_f32 v29,v29\n"
"v_mfma_f32_16x16x32_bf16 a[56:59],a[184:187],v[112:115],a[56:59]\n"
"buffer_load_dwordx4 v217,s[72:75],s39 offen offset:1024 lds\n"
"v_cvt_pk_bf16_f32 v90,v28,v29\n"
"v_mfma_f32_16x16x32_bf16 a[56:59],a[188:191],v[116:119],a[56:59]\n"
"v_exp_f32 v30,v30\n"
"v_mfma_f32_16x16x32_bf16 a[60:63],a[184:187],v[120:123],a[60:63]\n"
"v_exp_f32 v31,v31\n"
"v_mfma_f32_16x16x32_bf16 a[60:63],a[188:191],v[124:127],a[60:63]\n"
"buffer_load_dwordx4 v218,s[72:75],s39 offen offset:2048 lds\n"
"v_smfmac_f32_16x16x64_bf16 a[196:199],v[232:235],v[104:111],v61\n"
"s_add_u32 s39,s39,s53\n"
"s_waitcnt vmcnt(16) lgkmcnt(0)\n"
"s_barrier\n"
"v_mfma_f32_16x16x32_bf16 v[16:19],a[144:147],v[128:131],v[32:35]\n"
"v_exp_f32 v0,v0\n"
"v_mfma_f32_16x16x32_bf16 v[16:19],a[148:151],v[132:135],v[16:19]\n"
"ds_read_b128 a[128:131],v192 offset:49152\n"
"v_cvt_pk_bf16_f32 v91,v30,v31\n"
"v_mfma_f32_16x16x32_bf16 v[16:19],a[152:155],v[136:139],v[16:19]\n"
"v_exp_f32 v1,v1\n"
"v_mfma_f32_16x16x32_bf16 v[16:19],a[156:159],v[140:143],v[16:19]\n"
"ds_read_b128 a[132:135],v193 offset:49152\n"
"v_mfma_f32_16x16x32_bf16 v[20:23],a[144:147],v[144:147],v[36:39]\n"
"v_exp_f32 v2,v2\n"
"v_mfma_f32_16x16x32_bf16 v[20:23],a[148:151],v[148:151],v[20:23]\n"
"ds_read_b128 a[136:139],v194 offset:49152\n"
"v_cvt_pk_bf16_f32 v68,v0,v1\n"
"v_mfma_f32_16x16x32_bf16 v[20:23],a[152:155],v[152:155],v[20:23]\n"
"v_exp_f32 v3,v3\n"
"v_mfma_f32_16x16x32_bf16 v[20:23],a[156:159],v[156:159],v[20:23]\n"
"ds_read_b128 a[140:143],v195 offset:49152\n"
"v_mfma_f32_16x16x32_bf16 v[24:27],a[144:147],v[160:163],v[40:43]\n"
"v_exp_f32 v4,v4\n"
"v_mfma_f32_16x16x32_bf16 v[24:27],a[148:151],v[164:167],v[24:27]\n"
"ds_read_b64_tr_b16 a[176:177],v196 offset:768\n"
"v_cvt_pk_bf16_f32 v69,v2,v3\n"
"v_mfma_f32_16x16x32_bf16 v[24:27],a[152:155],v[168:171],v[24:27]\n"
"v_exp_f32 v5,v5\n"
"v_mfma_f32_16x16x32_bf16 v[24:27],a[156:159],v[172:175],v[24:27]\n"
"ds_read_b64_tr_b16 a[178:179],v196 offset:4864\n"
"v_mfma_f32_16x16x32_bf16 v[28:31],a[144:147],v[176:179],v[44:47]\n"
"v_exp_f32 v6,v6\n"
"v_mfma_f32_16x16x32_bf16 v[28:31],a[148:151],v[180:183],v[28:31]\n"
"buffer_load_dwordx4 a[208:211],v209,s[64:67],s40 offen sc1\n"
"v_mfma_f32_16x16x32_bf16 v[28:31],a[152:155],v[184:187],v[28:31]\n"
"v_mfma_f32_16x16x32_bf16 v[28:31],a[156:159],v[188:191],v[28:31]\n"
"v_exp_f32 v7,v7\n"
"v_mfma_f32_16x16x32_bf16 a[64:67],a[160:163],v[96:99],a[64:67]\n"
"ds_read_b64_tr_b16 a[180:181],v196 offset:8960\n"
"v_cvt_pk_bf16_f32 v76,v4,v5\n"
"v_mfma_f32_16x16x32_bf16 a[64:67],a[164:167],v[100:103],a[64:67]\n"
"v_exp_f32 v8,v8\n"
"v_mfma_f32_16x16x32_bf16 a[68:71],a[160:163],v[104:107],a[68:71]\n"
"ds_read_b64_tr_b16 a[182:183],v196 offset:13056\n"
"v_cvt_pk_bf16_f32 v77,v6,v7\n"
"v_mfma_f32_16x16x32_bf16 a[68:71],a[164:167],v[108:111],a[68:71]\n"
"v_exp_f32 v9,v9\n"
"v_mfma_f32_16x16x32_bf16 a[72:75],a[160:163],v[112:115],a[72:75]\n"
"ds_read_b64_tr_b16 a[184:185],v197 offset:768\n"
"v_mfma_f32_16x16x32_bf16 a[72:75],a[164:167],v[116:119],a[72:75]\n"
"v_exp_f32 v10,v10\n"
"v_mfma_f32_16x16x32_bf16 a[76:79],a[160:163],v[120:123],a[76:79]\n"
"ds_read_b64_tr_b16 a[186:187],v197 offset:4864\n"
"v_cvt_pk_bf16_f32 v84,v8,v9\n"
"v_mfma_f32_16x16x32_bf16 a[76:79],a[164:167],v[124:127],a[76:79]\n"
"v_exp_f32 v11,v11\n"
"v_mfma_f32_16x16x32_bf16 a[80:83],a[168:171],v[96:99],a[80:83]\n"
"ds_read_b64_tr_b16 a[188:189],v197 offset:8960\n"
"v_mfma_f32_16x16x32_bf16 a[80:83],a[172:175],v[100:103],a[80:83]\n"
"v_exp_f32 v12,v12\n"
"v_mfma_f32_16x16x32_bf16 a[84:87],a[168:171],v[104:107],a[84:87]\n"
"ds_read_b64_tr_b16 a[190:191],v197 offset:13056\n"
"v_cvt_pk_bf16_f32 v85,v10,v11\n"
"v_mfma_f32_16x16x32_bf16 a[84:87],a[172:175],v[108:111],a[84:87]\n"
"v_exp_f32 v13,v13\n"
"v_mfma_f32_16x16x32_bf16 a[88:91],a[168:171],v[112:115],a[88:91]\n"
"buffer_load_dwordx4 a[212:215],v209,s[64:67],s40 offen offset:1024 sc1\n"
"v_cvt_pk_bf16_f32 v92,v12,v13\n"
"v_mfma_f32_16x16x32_bf16 a[88:91],a[172:175],v[116:119],a[88:91]\n"
"v_exp_f32 v14,v14\n"
"v_mfma_f32_16x16x32_bf16 a[92:95],a[168:171],v[120:123],a[92:95]\n"
"v_exp_f32 v15,v15\n"
"v_mfma_f32_16x16x32_bf16 a[92:95],a[172:175],v[124:127],a[92:95]\n"
"buffer_load_dwordx4 a[216:219],v209,s[64:67],s40 offen offset:2048 sc1\n"
"v_smfmac_f32_16x16x64_bf16 a[200:203],v[232:235],v[112:119],v61\n"
"s_waitcnt lgkmcnt(0)\n"
"v_mfma_f32_16x16x32_bf16 v[0:3],a[128:131],v[128:131],v[32:35]\n"
"v_exp_f32 v16,v16\n"
"v_mfma_f32_16x16x32_bf16 v[0:3],a[132:135],v[132:135],v[0:3]\n"
"ds_read_b128 a[144:147],v192 offset:53248\n"
"v_cvt_pk_bf16_f32 v93,v14,v15\n"
"v_mfma_f32_16x16x32_bf16 v[0:3],a[136:139],v[136:139],v[0:3]\n"
"v_exp_f32 v17,v17\n"
"v_mfma_f32_16x16x32_bf16 v[0:3],a[140:143],v[140:143],v[0:3]\n"
"ds_read_b128 a[148:151],v193 offset:53248\n"
"v_mfma_f32_16x16x32_bf16 v[4:7],a[128:131],v[144:147],v[36:39]\n"
"v_exp_f32 v18,v18\n"
"v_mfma_f32_16x16x32_bf16 v[4:7],a[132:135],v[148:151],v[4:7]\n"
"ds_read_b128 a[152:155],v194 offset:53248\n"
"v_cvt_pk_bf16_f32 v70,v16,v17\n"
"v_mfma_f32_16x16x32_bf16 v[4:7],a[136:139],v[152:155],v[4:7]\n"
"v_exp_f32 v19,v19\n"
"v_mfma_f32_16x16x32_bf16 v[4:7],a[140:143],v[156:159],v[4:7]\n"
"ds_read_b128 a[156:159],v195 offset:53248\n"
"v_mfma_f32_16x16x32_bf16 v[8:11],a[128:131],v[160:163],v[40:43]\n"
"v_exp_f32 v20,v20\n"
"v_mfma_f32_16x16x32_bf16 v[8:11],a[132:135],v[164:167],v[8:11]\n"
"ds_read_b64_tr_b16 a[160:161],v196 offset:16384\n"
"v_cvt_pk_bf16_f32 v71,v18,v19\n"
"v_mfma_f32_16x16x32_bf16 v[8:11],a[136:139],v[168:171],v[8:11]\n"
"v_exp_f32 v21,v21\n"
"v_mfma_f32_16x16x32_bf16 v[8:11],a[140:143],v[172:175],v[8:11]\n"
"ds_read_b64_tr_b16 a[162:163],v196 offset:20480\n"
"v_mfma_f32_16x16x32_bf16 v[12:15],a[128:131],v[176:179],v[44:47]\n"
"v_exp_f32 v22,v22\n"
"v_mfma_f32_16x16x32_bf16 v[12:15],a[132:135],v[180:183],v[12:15]\n"
"buffer_load_dwordx4 a[220:223],v209,s[64:67],s40 offen offset:3072 sc1\n"
"v_mfma_f32_16x16x32_bf16 v[12:15],a[136:139],v[184:187],v[12:15]\n"
"v_mfma_f32_16x16x32_bf16 v[12:15],a[140:143],v[188:191],v[12:15]\n"
"v_exp_f32 v23,v23\n"
"v_mfma_f32_16x16x32_bf16 a[96:99],a[176:179],v[96:99],a[96:99]\n"
"ds_read_b64_tr_b16 a[164:165],v196 offset:24576\n"
"v_cvt_pk_bf16_f32 v78,v20,v21\n"
"v_mfma_f32_16x16x32_bf16 a[96:99],a[180:183],v[100:103],a[96:99]\n"
"v_exp_f32 v24,v24\n"
"v_mfma_f32_16x16x32_bf16 a[100:103],a[176:179],v[104:107],a[100:103]\n"
"ds_read_b64_tr_b16 a[166:167],v196 offset:28672\n"
"v_cvt_pk_bf16_f32 v79,v22,v23\n"
"v_mfma_f32_16x16x32_bf16 a[100:103],a[180:183],v[108:111],a[100:103]\n"
"v_exp_f32 v25,v25\n"
"v_mfma_f32_16x16x32_bf16 a[104:107],a[176:179],v[112:115],a[104:107]\n"
"ds_read_b64_tr_b16 a[168:169],v197 offset:16384\n"
"v_mfma_f32_16x16x32_bf16 a[104:107],a[180:183],v[116:119],a[104:107]\n"
"v_exp_f32 v26,v26\n"
"v_mfma_f32_16x16x32_bf16 a[108:111],a[176:179],v[120:123],a[108:111]\n"
"ds_read_b64_tr_b16 a[170:171],v197 offset:20480\n"
"v_cvt_pk_bf16_f32 v86,v24,v25\n"
"v_mfma_f32_16x16x32_bf16 a[108:111],a[180:183],v[124:127],a[108:111]\n"
"v_exp_f32 v27,v27\n"
"v_mfma_f32_16x16x32_bf16 a[112:115],a[184:187],v[96:99],a[112:115]\n"
"ds_read_b64_tr_b16 a[172:173],v197 offset:24576\n"
"v_mfma_f32_16x16x32_bf16 a[112:115],a[188:191],v[100:103],a[112:115]\n"
"v_exp_f32 v28,v28\n"
"v_mfma_f32_16x16x32_bf16 a[116:119],a[184:187],v[104:107],a[116:119]\n"
"ds_read_b64_tr_b16 a[174:175],v197 offset:28672\n"
"v_cvt_pk_bf16_f32 v87,v26,v27\n"
"v_mfma_f32_16x16x32_bf16 a[116:119],a[188:191],v[108:111],a[116:119]\n"
"s_add_u32 s40,s40,4096\n"
"v_exp_f32 v29,v29\n"
"v_mfma_f32_16x16x32_bf16 a[120:123],a[184:187],v[112:115],a[120:123]\n"
"buffer_load_dwordx4 a[224:227],v209,s[64:67],s40 offen sc1\n"
"v_cvt_pk_bf16_f32 v94,v28,v29\n"
"v_mfma_f32_16x16x32_bf16 a[120:123],a[188:191],v[116:119],a[120:123]\n"
"v_exp_f32 v30,v30\n"
"v_mfma_f32_16x16x32_bf16 a[124:127],a[184:187],v[120:123],a[124:127]\n"
"v_exp_f32 v31,v31\n"
"v_mfma_f32_16x16x32_bf16 a[124:127],a[188:191],v[124:127],a[124:127]\n"
"buffer_load_dwordx4 a[228:231],v209,s[64:67],s40 offen offset:1024 sc1\n"
"v_smfmac_f32_16x16x64_bf16 a[204:207],v[232:235],v[120:127],v61\n"
"s_waitcnt lgkmcnt(0)\n"
"v_mfma_f32_16x16x32_bf16 v[16:19],a[144:147],v[128:131],v[32:35]\n"
"v_exp_f32 v0,v0\n"
"v_mfma_f32_16x16x32_bf16 v[16:19],a[148:151],v[132:135],v[16:19]\n"
"ds_read_b128 a[128:131],v192 offset:57344\n"
"v_cvt_pk_bf16_f32 v95,v30,v31\n"
"v_mfma_f32_16x16x32_bf16 v[16:19],a[152:155],v[136:139],v[16:19]\n"
"v_exp_f32 v1,v1\n"
"v_mfma_f32_16x16x32_bf16 v[16:19],a[156:159],v[140:143],v[16:19]\n"
"ds_read_b128 a[132:135],v193 offset:57344\n"
"v_mfma_f32_16x16x32_bf16 v[20:23],a[144:147],v[144:147],v[36:39]\n"
"v_exp_f32 v2,v2\n"
"v_mfma_f32_16x16x32_bf16 v[20:23],a[148:151],v[148:151],v[20:23]\n"
"ds_read_b128 a[136:139],v194 offset:57344\n"
"v_cvt_pk_bf16_f32 v96,v0,v1\n"
"v_mfma_f32_16x16x32_bf16 v[20:23],a[152:155],v[152:155],v[20:23]\n"
"v_exp_f32 v3,v3\n"
"v_mfma_f32_16x16x32_bf16 v[20:23],a[156:159],v[156:159],v[20:23]\n"
"ds_read_b128 a[140:143],v195 offset:57344\n"
"v_mfma_f32_16x16x32_bf16 v[24:27],a[144:147],v[160:163],v[40:43]\n"
"v_exp_f32 v4,v4\n"
"v_mfma_f32_16x16x32_bf16 v[24:27],a[148:151],v[164:167],v[24:27]\n"
"ds_read_b64_tr_b16 a[176:177],v196 offset:16640\n"
"v_cvt_pk_bf16_f32 v97,v2,v3\n"
"v_mfma_f32_16x16x32_bf16 v[24:27],a[152:155],v[168:171],v[24:27]\n"
"v_exp_f32 v5,v5\n"
"v_mfma_f32_16x16x32_bf16 v[24:27],a[156:159],v[172:175],v[24:27]\n"
"ds_read_b64_tr_b16 a[178:179],v196 offset:20736\n"
"v_mfma_f32_16x16x32_bf16 v[28:31],a[144:147],v[176:179],v[44:47]\n"
"v_exp_f32 v6,v6\n"
"v_mfma_f32_16x16x32_bf16 v[28:31],a[148:151],v[180:183],v[28:31]\n"
"buffer_load_dwordx4 a[232:235],v209,s[64:67],s40 offen offset:2048 sc1\n"
"v_mfma_f32_16x16x32_bf16 v[28:31],a[152:155],v[184:187],v[28:31]\n"
"v_mfma_f32_16x16x32_bf16 v[28:31],a[156:159],v[188:191],v[28:31]\n"
"v_exp_f32 v7,v7\n"
"v_mfma_f32_16x16x32_bf16 a[0:3],a[160:163],v[64:67],a[0:3]\n"
"ds_read_b64_tr_b16 a[180:181],v196 offset:24832\n"
"v_cvt_pk_bf16_f32 v104,v4,v5\n"
"v_mfma_f32_16x16x32_bf16 a[0:3],a[164:167],v[68:71],a[0:3]\n"
"v_exp_f32 v8,v8\n"
"v_mfma_f32_16x16x32_bf16 a[4:7],a[160:163],v[72:75],a[4:7]\n"
"ds_read_b64_tr_b16 a[182:183],v196 offset:28928\n"
"v_cvt_pk_bf16_f32 v105,v6,v7\n"
"v_mfma_f32_16x16x32_bf16 a[4:7],a[164:167],v[76:79],a[4:7]\n"
"v_exp_f32 v9,v9\n"
"v_mfma_f32_16x16x32_bf16 a[8:11],a[160:163],v[80:83],a[8:11]\n"
"ds_read_b64_tr_b16 a[184:185],v197 offset:16640\n"
"v_mfma_f32_16x16x32_bf16 a[8:11],a[164:167],v[84:87],a[8:11]\n"
"v_exp_f32 v10,v10\n"
"v_mfma_f32_16x16x32_bf16 a[12:15],a[160:163],v[88:91],a[12:15]\n"
"ds_read_b64_tr_b16 a[186:187],v197 offset:20736\n"
"v_cvt_pk_bf16_f32 v112,v8,v9\n"
"v_mfma_f32_16x16x32_bf16 a[12:15],a[164:167],v[92:95],a[12:15]\n"
"v_exp_f32 v11,v11\n"
"v_mfma_f32_16x16x32_bf16 a[16:19],a[168:171],v[64:67],a[16:19]\n"
"ds_read_b64_tr_b16 a[188:189],v197 offset:24832\n"
"v_mfma_f32_16x16x32_bf16 a[16:19],a[172:175],v[68:71],a[16:19]\n"
"v_exp_f32 v12,v12\n"
"v_mfma_f32_16x16x32_bf16 a[20:23],a[168:171],v[72:75],a[20:23]\n"
"ds_read_b64_tr_b16 a[190:191],v197 offset:28928\n"
"v_cvt_pk_bf16_f32 v113,v10,v11\n"
"v_mfma_f32_16x16x32_bf16 a[20:23],a[172:175],v[76:79],a[20:23]\n"
"v_exp_f32 v13,v13\n"
"v_mfma_f32_16x16x32_bf16 a[24:27],a[168:171],v[80:83],a[24:27]\n"
"buffer_load_dwordx4 a[236:239],v209,s[64:67],s40 offen offset:3072 sc1\n"
"v_cvt_pk_bf16_f32 v120,v12,v13\n"
"v_mfma_f32_16x16x32_bf16 a[24:27],a[172:175],v[84:87],a[24:27]\n"
"v_exp_f32 v14,v14\n"
"v_mfma_f32_16x16x32_bf16 a[28:31],a[168:171],v[88:91],a[28:31]\n"
"v_exp_f32 v15,v15\n"
"v_mfma_f32_16x16x32_bf16 a[28:31],a[172:175],v[92:95],a[28:31]\n"
"v_smfmac_f32_16x16x64_bf16 a[192:195],v[232:235],v[64:71],v61\n"
"s_add_u32 s40,s40,4096\n"
"s_waitcnt lgkmcnt(0)\n"
"v_mfma_f32_16x16x32_bf16 v[0:3],a[128:131],v[128:131],v[32:35]\n"
"v_exp_f32 v16,v16\n"
"v_mfma_f32_16x16x32_bf16 v[0:3],a[132:135],v[132:135],v[0:3]\n"
"ds_read_b128 a[144:147],v192 offset:61440\n"
"v_cvt_pk_bf16_f32 v121,v14,v15\n"
"v_mfma_f32_16x16x32_bf16 v[0:3],a[136:139],v[136:139],v[0:3]\n"
"v_exp_f32 v17,v17\n"
"v_mfma_f32_16x16x32_bf16 v[0:3],a[140:143],v[140:143],v[0:3]\n"
"ds_read_b128 a[148:151],v193 offset:61440\n"
"v_mfma_f32_16x16x32_bf16 v[4:7],a[128:131],v[144:147],v[36:39]\n"
"v_exp_f32 v18,v18\n"
"v_mfma_f32_16x16x32_bf16 v[4:7],a[132:135],v[148:151],v[4:7]\n"
"ds_read_b128 a[152:155],v194 offset:61440\n"
"v_cvt_pk_bf16_f32 v98,v16,v17\n"
"v_mfma_f32_16x16x32_bf16 v[4:7],a[136:139],v[152:155],v[4:7]\n"
"v_exp_f32 v19,v19\n"
"v_mfma_f32_16x16x32_bf16 v[4:7],a[140:143],v[156:159],v[4:7]\n"
"ds_read_b128 a[156:159],v195 offset:61440\n"
"v_mfma_f32_16x16x32_bf16 v[8:11],a[128:131],v[160:163],v[40:43]\n"
"v_exp_f32 v20,v20\n"
"v_mfma_f32_16x16x32_bf16 v[8:11],a[132:135],v[164:167],v[8:11]\n"
"ds_read_b64_tr_b16 a[160:161],v196 offset:16896\n"
"v_cvt_pk_bf16_f32 v99,v18,v19\n"
"v_mfma_f32_16x16x32_bf16 v[8:11],a[136:139],v[168:171],v[8:11]\n"
"v_exp_f32 v21,v21\n"
"v_mfma_f32_16x16x32_bf16 v[8:11],a[140:143],v[172:175],v[8:11]\n"
"ds_read_b64_tr_b16 a[162:163],v196 offset:20992\n"
"v_mfma_f32_16x16x32_bf16 v[12:15],a[128:131],v[176:179],v[44:47]\n"
"s_add_u32 m0,s51,90112\n"
"v_exp_f32 v22,v22\n"
"v_mfma_f32_16x16x32_bf16 v[12:15],a[132:135],v[180:183],v[12:15]\n"
"buffer_load_dwordx4 v216,s[72:75],s39 offen lds\n"
"v_mfma_f32_16x16x32_bf16 v[12:15],a[136:139],v[184:187],v[12:15]\n"
"v_mfma_f32_16x16x32_bf16 v[12:15],a[140:143],v[188:191],v[12:15]\n"
"v_exp_f32 v23,v23\n"
"v_mfma_f32_16x16x32_bf16 a[32:35],a[176:179],v[64:67],a[32:35]\n"
"ds_read_b64_tr_b16 a[164:165],v196 offset:25088\n"
"v_cvt_pk_bf16_f32 v106,v20,v21\n"
"v_mfma_f32_16x16x32_bf16 a[32:35],a[180:183],v[68:71],a[32:35]\n"
"v_exp_f32 v24,v24\n"
"v_mfma_f32_16x16x32_bf16 a[36:39],a[176:179],v[72:75],a[36:39]\n"
"ds_read_b64_tr_b16 a[166:167],v196 offset:29184\n"
"v_cvt_pk_bf16_f32 v107,v22,v23\n"
"v_mfma_f32_16x16x32_bf16 a[36:39],a[180:183],v[76:79],a[36:39]\n"
"v_exp_f32 v25,v25\n"
"v_mfma_f32_16x16x32_bf16 a[40:43],a[176:179],v[80:83],a[40:43]\n"
"ds_read_b64_tr_b16 a[168:169],v197 offset:16896\n"
"v_mfma_f32_16x16x32_bf16 a[40:43],a[180:183],v[84:87],a[40:43]\n"
"v_exp_f32 v26,v26\n"
"v_mfma_f32_16x16x32_bf16 a[44:47],a[176:179],v[88:91],a[44:47]\n"
"ds_read_b64_tr_b16 a[170:171],v197 offset:20992\n"
"v_cvt_pk_bf16_f32 v114,v24,v25\n"
"v_mfma_f32_16x16x32_bf16 a[44:47],a[180:183],v[92:95],a[44:47]\n"
"v_exp_f32 v27,v27\n"
"v_mfma_f32_16x16x32_bf16 a[48:51],a[184:187],v[64:67],a[48:51]\n"
"ds_read_b64_tr_b16 a[172:173],v197 offset:25088\n"
"v_mfma_f32_16x16x32_bf16 a[48:51],a[188:191],v[68:71],a[48:51]\n"
"v_exp_f32 v28,v28\n"
"v_mfma_f32_16x16x32_bf16 a[52:55],a[184:187],v[72:75],a[52:55]\n"
"ds_read_b64_tr_b16 a[174:175],v197 offset:29184\n"
"v_cvt_pk_bf16_f32 v115,v26,v27\n"
"v_mfma_f32_16x16x32_bf16 a[52:55],a[188:191],v[76:79],a[52:55]\n"
"v_exp_f32 v29,v29\n"
"v_mfma_f32_16x16x32_bf16 a[56:59],a[184:187],v[80:83],a[56:59]\n"
"buffer_load_dwordx4 v217,s[72:75],s39 offen offset:1024 lds\n"
"v_cvt_pk_bf16_f32 v122,v28,v29\n"
"v_mfma_f32_16x16x32_bf16 a[56:59],a[188:191],v[84:87],a[56:59]\n"
"v_exp_f32 v30,v30\n"
"v_mfma_f32_16x16x32_bf16 a[60:63],a[184:187],v[88:91],a[60:63]\n"
"v_exp_f32 v31,v31\n"
"v_mfma_f32_16x16x32_bf16 a[60:63],a[188:191],v[92:95],a[60:63]\n"
"buffer_load_dwordx4 v218,s[72:75],s39 offen offset:2048 lds\n"
"v_smfmac_f32_16x16x64_bf16 a[196:199],v[232:235],v[72:79],v61\n"
"s_add_u32 s39,s39,s52\n"
"s_waitcnt vmcnt(17) lgkmcnt(0)\n"
"s_barrier\n"
"v_mfma_f32_16x16x32_bf16 v[16:19],a[144:147],v[128:131],v[32:35]\n"
"v_exp_f32 v0,v0\n"
"v_mfma_f32_16x16x32_bf16 v[16:19],a[148:151],v[132:135],v[16:19]\n"
"v_cvt_pk_bf16_f32 v123,v30,v31\n"
"v_mfma_f32_16x16x32_bf16 v[16:19],a[152:155],v[136:139],v[16:19]\n"
"v_exp_f32 v1,v1\n"
"v_mfma_f32_16x16x32_bf16 v[16:19],a[156:159],v[140:143],v[16:19]\n"
"v_mfma_f32_16x16x32_bf16 v[20:23],a[144:147],v[144:147],v[36:39]\n"
"v_exp_f32 v2,v2\n"
"v_mfma_f32_16x16x32_bf16 v[20:23],a[148:151],v[148:151],v[20:23]\n"
"v_cvt_pk_bf16_f32 v100,v0,v1\n"
"v_mfma_f32_16x16x32_bf16 v[20:23],a[152:155],v[152:155],v[20:23]\n"
"v_exp_f32 v3,v3\n"
"v_mfma_f32_16x16x32_bf16 v[20:23],a[156:159],v[156:159],v[20:23]\n"
"v_mfma_f32_16x16x32_bf16 v[24:27],a[144:147],v[160:163],v[40:43]\n"
"v_exp_f32 v4,v4\n"
"v_mfma_f32_16x16x32_bf16 v[24:27],a[148:151],v[164:167],v[24:27]\n"
"ds_read_b64_tr_b16 a[176:177],v196 offset:17152\n"
"v_cvt_pk_bf16_f32 v101,v2,v3\n"
"v_mfma_f32_16x16x32_bf16 v[24:27],a[152:155],v[168:171],v[24:27]\n"
"v_exp_f32 v5,v5\n"
"v_mfma_f32_16x16x32_bf16 v[24:27],a[156:159],v[172:175],v[24:27]\n"
"ds_read_b64_tr_b16 a[178:179],v196 offset:21248\n"
"v_mfma_f32_16x16x32_bf16 v[28:31],a[144:147],v[176:179],v[44:47]\n"
"s_add_u32 m0,s51,93184\n"
"v_exp_f32 v6,v6\n"
"v_mfma_f32_16x16x32_bf16 v[28:31],a[148:151],v[180:183],v[28:31]\n"
"buffer_load_dwordx4 v216,s[72:75],s39 offen lds\n"
"v_mfma_f32_16x16x32_bf16 v[28:31],a[152:155],v[184:187],v[28:31]\n"
"v_mfma_f32_16x16x32_bf16 v[28:31],a[156:159],v[188:191],v[28:31]\n"
"v_exp_f32 v7,v7\n"
"v_mfma_f32_16x16x32_bf16 a[64:67],a[160:163],v[64:67],a[64:67]\n"
"ds_read_b64_tr_b16 a[180:181],v196 offset:25344\n"
"v_cvt_pk_bf16_f32 v108,v4,v5\n"
"v_mfma_f32_16x16x32_bf16 a[64:67],a[164:167],v[68:71],a[64:67]\n"
"v_exp_f32 v8,v8\n"
"v_mfma_f32_16x16x32_bf16 a[68:71],a[160:163],v[72:75],a[68:71]\n"
"ds_read_b64_tr_b16 a[182:183],v196 offset:29440\n"
"v_cvt_pk_bf16_f32 v109,v6,v7\n"
"v_mfma_f32_16x16x32_bf16 a[68:71],a[164:167],v[76:79],a[68:71]\n"
"v_exp_f32 v9,v9\n"
"v_mfma_f32_16x16x32_bf16 a[72:75],a[160:163],v[80:83],a[72:75]\n"
"ds_read_b64_tr_b16 a[184:185],v197 offset:17152\n"
"v_mfma_f32_16x16x32_bf16 a[72:75],a[164:167],v[84:87],a[72:75]\n"
"v_exp_f32 v10,v10\n"
"v_mfma_f32_16x16x32_bf16 a[76:79],a[160:163],v[88:91],a[76:79]\n"
"ds_read_b64_tr_b16 a[186:187],v197 offset:21248\n"
"v_cvt_pk_bf16_f32 v116,v8,v9\n"
"v_mfma_f32_16x16x32_bf16 a[76:79],a[164:167],v[92:95],a[76:79]\n"
"v_exp_f32 v11,v11\n"
"v_mfma_f32_16x16x32_bf16 a[80:83],a[168:171],v[64:67],a[80:83]\n"
"ds_read_b64_tr_b16 a[188:189],v197 offset:25344\n"
"v_mfma_f32_16x16x32_bf16 a[80:83],a[172:175],v[68:71],a[80:83]\n"
"v_exp_f32 v12,v12\n"
"v_mfma_f32_16x16x32_bf16 a[84:87],a[168:171],v[72:75],a[84:87]\n"
"ds_read_b64_tr_b16 a[190:191],v197 offset:29440\n"
"v_cvt_pk_bf16_f32 v117,v10,v11\n"
"v_mfma_f32_16x16x32_bf16 a[84:87],a[172:175],v[76:79],a[84:87]\n"
"v_exp_f32 v13,v13\n"
"v_mfma_f32_16x16x32_bf16 a[88:91],a[168:171],v[80:83],a[88:91]\n"
"buffer_load_dwordx4 v217,s[72:75],s39 offen offset:1024 lds\n"
"v_cvt_pk_bf16_f32 v124,v12,v13\n"
"v_mfma_f32_16x16x32_bf16 a[88:91],a[172:175],v[84:87],a[88:91]\n"
"v_exp_f32 v14,v14\n"
"v_mfma_f32_16x16x32_bf16 a[92:95],a[168:171],v[88:91],a[92:95]\n"
"v_exp_f32 v15,v15\n"
"v_mfma_f32_16x16x32_bf16 a[92:95],a[172:175],v[92:95],a[92:95]\n"
"buffer_load_dwordx4 v218,s[72:75],s39 offen offset:2048 lds\n"
"v_smfmac_f32_16x16x64_bf16 a[200:203],v[232:235],v[80:87],v61\n"
"s_add_u32 s39,s39,s53\n"
"s_waitcnt lgkmcnt(0)\n"
"v_cvt_pk_bf16_f32 v125,v14,v15\n"
"s_nop 1\n"
"v_mfma_f32_16x16x32_bf16 a[96:99],a[176:179],v[64:67],a[96:99]\n"
"v_exp_f32 v16,v16\n"
"v_mfma_f32_16x16x32_bf16 a[96:99],a[180:183],v[68:71],a[96:99]\n"
"v_exp_f32 v17,v17\n"
"buffer_load_dwordx4 a[128:131],v209,s[64:67],s40 offen sc1\n"
"v_mfma_f32_16x16x32_bf16 a[100:103],a[176:179],v[72:75],a[100:103]\n"
"v_exp_f32 v18,v18\n"
"v_cvt_pk_bf16_f32 v102,v16,v17\n"
"v_mfma_f32_16x16x32_bf16 a[100:103],a[180:183],v[76:79],a[100:103]\n"
"v_exp_f32 v19,v19\n"
"buffer_load_dwordx4 a[132:135],v209,s[64:67],s40 offen offset:1024 sc1\n"
"v_mfma_f32_16x16x32_bf16 a[104:107],a[176:179],v[80:83],a[104:107]\n"
"v_exp_f32 v20,v20\n"
"ds_read_b64_tr_b16 a[160:161],v196 offset:32768\n"
"v_cvt_pk_bf16_f32 v103,v18,v19\n"
"v_mfma_f32_16x16x32_bf16 a[104:107],a[180:183],v[84:87],a[104:107]\n"
"v_exp_f32 v21,v21\n"
"buffer_load_dwordx4 a[136:139],v209,s[64:67],s40 offen offset:2048 sc1\n"
"ds_read_b64_tr_b16 a[162:163],v196 offset:36864\n"
"v_mfma_f32_16x16x32_bf16 a[108:111],a[176:179],v[88:91],a[108:111]\n"
"v_exp_f32 v22,v22\n"
"v_mfma_f32_16x16x32_bf16 a[108:111],a[180:183],v[92:95],a[108:111]\n"
"buffer_load_dwordx4 a[140:143],v209,s[64:67],s40 offen offset:3072 sc1\n"
"v_exp_f32 v23,v23\n"
"v_mfma_f32_16x16x32_bf16 a[112:115],a[184:187],v[64:67],a[112:115]\n"
"ds_read_b64_tr_b16 a[164:165],v196 offset:40960\n"
"v_cvt_pk_bf16_f32 v110,v20,v21\n"
"v_exp_f32 v24,v24\n"
"v_mfma_f32_16x16x32_bf16 a[112:115],a[188:191],v[68:71],a[112:115]\n"
"ds_read_b64_tr_b16 a[166:167],v196 offset:45056\n"
"v_cvt_pk_bf16_f32 v111,v22,v23\n"
"s_add_u32 s40,s40,4096\n"
"buffer_load_dwordx4 a[144:147],v209,s[64:67],s40 offen sc1\n"
"v_exp_f32 v25,v25\n"
"v_mfma_f32_16x16x32_bf16 a[116:119],a[184:187],v[72:75],a[116:119]\n"
"ds_read_b64_tr_b16 a[168:169],v197 offset:32768\n"
"v_exp_f32 v26,v26\n"
"v_mfma_f32_16x16x32_bf16 a[116:119],a[188:191],v[76:79],a[116:119]\n"
"ds_read_b64_tr_b16 a[170:171],v197 offset:36864\n"
"v_cvt_pk_bf16_f32 v118,v24,v25\n"
"buffer_load_dwordx4 a[148:151],v209,s[64:67],s40 offen offset:1024 sc1\n"
"v_exp_f32 v27,v27\n"
"v_mfma_f32_16x16x32_bf16 a[120:123],a[184:187],v[80:83],a[120:123]\n"
"ds_read_b64_tr_b16 a[172:173],v197 offset:40960\n"
"v_exp_f32 v28,v28\n"
"v_mfma_f32_16x16x32_bf16 a[120:123],a[188:191],v[84:87],a[120:123]\n"
"ds_read_b64_tr_b16 a[174:175],v197 offset:45056\n"
"v_cvt_pk_bf16_f32 v119,v26,v27\n"
"buffer_load_dwordx4 a[152:155],v209,s[64:67],s40 offen offset:2048 sc1\n"
"v_exp_f32 v29,v29\n"
"v_mfma_f32_16x16x32_bf16 a[124:127],a[184:187],v[88:91],a[124:127]\n"
"v_cvt_pk_bf16_f32 v126,v28,v29\n"
"v_exp_f32 v30,v30\n"
"v_mfma_f32_16x16x32_bf16 a[124:127],a[188:191],v[92:95],a[124:127]\n"
"v_exp_f32 v31,v31\n"
"buffer_load_dwordx4 a[156:159],v209,s[64:67],s40 offen offset:3072 sc1\n"
"v_smfmac_f32_16x16x64_bf16 a[204:207],v[232:235],v[88:95],v61\n"
"s_waitcnt lgkmcnt(0)\n"
"v_cvt_pk_bf16_f32 v127,v30,v31\n"
"v_mov_b32 v240,0\n"
"v_mov_b32 v241,0\n"
"v_mov_b32 v242,0\n"
"v_mov_b32 v243,0\n"
"v_smfmac_f32_16x16x64_bf16 a[192:195],v[232:235],v[96:103],v61\n"
"ds_read_b64_tr_b16 a[176:177],v196 offset:33024\n"
"ds_read_b64_tr_b16 a[178:179],v196 offset:37120\n"
"v_smfmac_f32_16x16x64_bf16 a[196:199],v[232:235],v[104:111],v61\n"
"ds_read_b64_tr_b16 a[180:181],v196 offset:41216\n"
"ds_read_b64_tr_b16 a[182:183],v196 offset:45312\n"
"v_smfmac_f32_16x16x64_bf16 a[200:203],v[232:235],v[112:119],v61\n"
"ds_read_b64_tr_b16 a[184:185],v197 offset:33024\n"
"ds_read_b64_tr_b16 a[186:187],v197 offset:37120\n"
"v_smfmac_f32_16x16x64_bf16 a[204:207],v[232:235],v[120:127],v61\n"
"ds_read_b64_tr_b16 a[188:189],v197 offset:41216\n"
"ds_read_b64_tr_b16 a[190:191],v197 offset:45312\n"
"v_mfma_f32_16x16x32_bf16 a[0:3],a[160:163],v[96:99],a[0:3]\n"
"s_nop 0\n"
"s_nop 0\n"
"v_mfma_f32_16x16x32_bf16 a[0:3],a[164:167],v[100:103],a[0:3]\n"
"s_add_u32 s40,s40,4096\n"
"s_nop 0\n"
"s_nop 0\n"
"v_mfma_f32_16x16x32_bf16 a[4:7],a[160:163],v[104:107],a[4:7]\n"
"s_nop 0\n"
"s_nop 0\n"
"v_mfma_f32_16x16x32_bf16 a[4:7],a[164:167],v[108:111],a[4:7]\n"
"s_nop 3\n"
"v_accvgpr_read_b32 v228,a192\n"
"v_mfma_f32_16x16x32_bf16 a[8:11],a[160:163],v[112:115],a[8:11]\n"
"v_accvgpr_read_b32 v229,a196\n"
"v_accvgpr_read_b32 v230,a200\n"
"v_mfma_f32_16x16x32_bf16 a[8:11],a[164:167],v[116:119],a[8:11]\n"
"v_accvgpr_read_b32 v231,a204\n"
"v_accvgpr_read_b32 v236,a193\n"
"v_mfma_f32_16x16x32_bf16 a[12:15],a[160:163],v[120:123],a[12:15]\n"
"v_accvgpr_read_b32 v237,a197\n"
"v_accvgpr_read_b32 v238,a201\n"
"v_mfma_f32_16x16x32_bf16 a[12:15],a[164:167],v[124:127],a[12:15]\n"
"v_accvgpr_read_b32 v239,a205\n"
"v_add_f32 v228,v228,v236\n"
"v_mfma_f32_16x16x32_bf16 a[16:19],a[168:171],v[96:99],a[16:19]\n"
"v_add_f32 v229,v229,v237\n"
"v_add_f32 v230,v230,v238\n"
"v_mfma_f32_16x16x32_bf16 a[16:19],a[172:175],v[100:103],a[16:19]\n"
"v_add_f32 v231,v231,v239\n"
"v_rcp_f32 v236,v228\n"
"v_mfma_f32_16x16x32_bf16 a[20:23],a[168:171],v[104:107],a[20:23]\n"
"v_rcp_f32 v237,v229\n"
"v_rcp_f32 v238,v230\n"
"v_mfma_f32_16x16x32_bf16 a[20:23],a[172:175],v[108:111],a[20:23]\n"
"v_rcp_f32 v239,v231\n"
"v_add_f32 v245,v228,v229\n"
"v_mfma_f32_16x16x32_bf16 a[24:27],a[168:171],v[112:115],a[24:27]\n"
"v_add_f32 v244,v230,v231\n"
"v_add_f32 v244,v244,v245\n"
"v_mfma_f32_16x16x32_bf16 a[24:27],a[172:175],v[116:119],a[24:27]\n"
"s_mov_b32 s41,0x71800000\n"
"v_cmp_nlt_f32 s[44:45],v244,s41\n"
"v_mfma_f32_16x16x32_bf16 a[28:31],a[168:171],v[120:123],a[28:31]\n"
"v_mfma_f32_16x16x32_bf16 a[28:31],a[172:175],v[124:127],a[28:31]\n"
"s_waitcnt lgkmcnt(0)\n"
"v_mfma_f32_16x16x32_bf16 a[32:35],a[176:179],v[96:99],a[32:35]\n"
"ds_read_b64_tr_b16 a[160:161],v196 offset:33280\n"
"s_nop 0\n"
"s_nop 0\n"
"s_nop 0\n"
"s_nop 0\n"
"s_nop 0\n"
"s_nop 0\n"
"s_mov_b32 s42,0\n"
"v_mfma_f32_16x16x32_bf16 a[32:35],a[180:183],v[100:103],a[32:35]\n"
"ds_read_b64_tr_b16 a[162:163],v196 offset:37376\n"
"v_accvgpr_read_b32 v8,a0\n"
"v_accvgpr_read_b32 v9,a1\n"
"v_accvgpr_read_b32 v10,a2\n"
"v_accvgpr_read_b32 v11,a3\n"
"v_accvgpr_read_b32 v12,a16\n"
"v_accvgpr_read_b32 v13,a17\n"
"v_accvgpr_read_b32 v14,a18\n"
"v_mfma_f32_16x16x32_bf16 a[36:39],a[176:179],v[104:107],a[36:39]\n"
"ds_read_b64_tr_b16 a[164:165],v196 offset:41472\n"
"v_accvgpr_read_b32 v15,a19\n"
"v_mul_f32 v8,v8,v236\n"
"v_mul_f32 v9,v9,v236\n"
"v_mul_f32 v10,v10,v236\n"
"v_mul_f32 v11,v11,v236\n"
"v_mul_f32 v12,v12,v236\n"
"v_mul_f32 v13,v13,v236\n"
"v_mfma_f32_16x16x32_bf16 a[36:39],a[180:183],v[108:111],a[36:39]\n"
"ds_read_b64_tr_b16 a[166:167],v196 offset:45568\n"
"v_mul_f32 v14,v14,v236\n"
"v_mul_f32 v15,v15,v236\n"
"v_cvt_pk_bf16_f32 v64,v8,v9\n"
"v_cvt_pk_bf16_f32 v65,v10,v11\n"
"v_cvt_pk_bf16_f32 v66,v12,v13\n"
"v_cvt_pk_bf16_f32 v67,v14,v15\n"
"buffer_store_dwordx4 v[64:67],v209,s[84:87],s42 offen sc1\n"
"v_mfma_f32_16x16x32_bf16 a[40:43],a[176:179],v[112:115],a[40:43]\n"
"ds_read_b64_tr_b16 a[168:169],v197 offset:33280\n"
"s_nop 0\n"
"v_mfma_f32_16x16x32_bf16 v[0:3],v[240:243],v[64:67],0\n"
"v_accvgpr_read_b32 v16,a4\n"
"v_accvgpr_read_b32 v17,a5\n"
"v_accvgpr_read_b32 v18,a6\n"
"v_accvgpr_read_b32 v19,a7\n"
"v_accvgpr_read_b32 v20,a20\n"
"v_mfma_f32_16x16x32_bf16 a[40:43],a[180:183],v[116:119],a[40:43]\n"
"ds_read_b64_tr_b16 a[170:171],v197 offset:37376\n"
"v_accvgpr_read_b32 v21,a21\n"
"v_accvgpr_read_b32 v22,a22\n"
"v_accvgpr_read_b32 v23,a23\n"
"v_mul_f32 v16,v16,v237\n"
"v_mul_f32 v17,v17,v237\n"
"v_mul_f32 v18,v18,v237\n"
"v_mul_f32 v19,v19,v237\n"
"v_mfma_f32_16x16x32_bf16 a[44:47],a[176:179],v[120:123],a[44:47]\n"
"ds_read_b64_tr_b16 a[172:173],v197 offset:41472\n"
"v_mul_f32 v20,v20,v237\n"
"v_mul_f32 v21,v21,v237\n"
"v_mul_f32 v22,v22,v237\n"
"v_mul_f32 v23,v23,v237\n"
"v_cvt_pk_bf16_f32 v68,v16,v17\n"
"v_cvt_pk_bf16_f32 v69,v18,v19\n"
"v_cvt_pk_bf16_f32 v70,v20,v21\n"
"v_mfma_f32_16x16x32_bf16 a[44:47],a[180:183],v[124:127],a[44:47]\n"
"ds_read_b64_tr_b16 a[174:175],v197 offset:45568\n"
"v_cvt_pk_bf16_f32 v71,v22,v23\n"
"buffer_store_dwordx4 v[68:71],v209,s[84:87],s42 offen offset:1024 sc1\n"
"s_nop 0\n"
"v_mfma_f32_16x16x32_bf16 v[0:3],v[240:243],v[68:71],v[0:3]\n"
"v_accvgpr_read_b32 v24,a8\n"
"v_accvgpr_read_b32 v25,a9\n"
"v_accvgpr_read_b32 v26,a10\n"
"v_mfma_f32_16x16x32_bf16 a[48:51],a[184:187],v[96:99],a[48:51]\n"
"v_accvgpr_read_b32 v27,a11\n"
"v_accvgpr_read_b32 v28,a24\n"
"v_accvgpr_read_b32 v29,a25\n"
"v_accvgpr_read_b32 v30,a26\n"
"v_accvgpr_read_b32 v31,a27\n"
"v_mul_f32 v24,v24,v238\n"
"v_mul_f32 v25,v25,v238\n"
"v_mfma_f32_16x16x32_bf16 a[48:51],a[188:191],v[100:103],a[48:51]\n"
"v_mul_f32 v26,v26,v238\n"
"v_mul_f32 v27,v27,v238\n"
"v_mul_f32 v28,v28,v238\n"
"v_mul_f32 v29,v29,v238\n"
"v_mul_f32 v30,v30,v238\n"
"v_mul_f32 v31,v31,v238\n"
"v_cvt_pk_bf16_f32 v72,v24,v25\n"
"v_mfma_f32_16x16x32_bf16 a[52:55],a[184:187],v[104:107],a[52:55]\n"
"v_cvt_pk_bf16_f32 v73,v26,v27\n"
"v_cvt_pk_bf16_f32 v74,v28,v29\n"
"v_cvt_pk_bf16_f32 v75,v30,v31\n"
"buffer_store_dwordx4 v[72:75],v209,s[84:87],s42 offen offset:2048 sc1\n"
"s_nop 0\n"
"v_mfma_f32_16x16x32_bf16 v[0:3],v[240:243],v[72:75],v[0:3]\n"
"v_accvgpr_read_b32 v32,a12\n"
"v_mfma_f32_16x16x32_bf16 a[52:55],a[188:191],v[108:111],a[52:55]\n"
"v_accvgpr_read_b32 v33,a13\n"
"v_accvgpr_read_b32 v34,a14\n"
"v_accvgpr_read_b32 v35,a15\n"
"v_accvgpr_read_b32 v36,a28\n"
"v_accvgpr_read_b32 v37,a29\n"
"v_accvgpr_read_b32 v38,a30\n"
"v_accvgpr_read_b32 v39,a31\n"
"v_mfma_f32_16x16x32_bf16 a[56:59],a[184:187],v[112:115],a[56:59]\n"
"v_mul_f32 v32,v32,v239\n"
"v_mul_f32 v33,v33,v239\n"
"v_mul_f32 v34,v34,v239\n"
"v_mul_f32 v35,v35,v239\n"
"v_mul_f32 v36,v36,v239\n"
"v_mul_f32 v37,v37,v239\n"
"v_mul_f32 v38,v38,v239\n"
"v_mfma_f32_16x16x32_bf16 a[56:59],a[188:191],v[116:119],a[56:59]\n"
"v_mul_f32 v39,v39,v239\n"
"v_cvt_pk_bf16_f32 v76,v32,v33\n"
"v_cvt_pk_bf16_f32 v77,v34,v35\n"
"v_cvt_pk_bf16_f32 v78,v36,v37\n"
"v_cvt_pk_bf16_f32 v79,v38,v39\n"
"buffer_store_dwordx4 v[76:79],v209,s[84:87],s42 offen offset:3072 sc1\n"
"s_nop 0\n"
"v_mfma_f32_16x16x32_bf16 a[60:63],a[184:187],v[120:123],a[60:63]\n"
"v_mfma_f32_16x16x32_bf16 v[0:3],v[240:243],v[76:79],v[0:3]\n"
"v_mfma_f32_16x16x32_bf16 a[60:63],a[188:191],v[124:127],a[60:63]\n"
"s_waitcnt lgkmcnt(0)\n"
"v_mfma_f32_16x16x32_bf16 a[64:67],a[160:163],v[96:99],a[64:67]\n"
"ds_read_b64_tr_b16 a[176:177],v196 offset:33536\n"
"s_nop 0\n"
"s_nop 0\n"
"s_nop 0\n"
"s_nop 0\n"
"s_nop 0\n"
"s_nop 0\n"
"s_mov_b32 s42,4096\n"
"v_mfma_f32_16x16x32_bf16 a[64:67],a[164:167],v[100:103],a[64:67]\n"
"ds_read_b64_tr_b16 a[178:179],v196 offset:37632\n"
"v_accvgpr_read_b32 v8,a32\n"
"v_accvgpr_read_b32 v9,a33\n"
"v_accvgpr_read_b32 v10,a34\n"
"v_accvgpr_read_b32 v11,a35\n"
"v_accvgpr_read_b32 v12,a48\n"
"v_accvgpr_read_b32 v13,a49\n"
"v_accvgpr_read_b32 v14,a50\n"
"v_mfma_f32_16x16x32_bf16 a[68:71],a[160:163],v[104:107],a[68:71]\n"
"ds_read_b64_tr_b16 a[180:181],v196 offset:41728\n"
"v_accvgpr_read_b32 v15,a51\n"
"v_mul_f32 v8,v8,v236\n"
"v_mul_f32 v9,v9,v236\n"
"v_mul_f32 v10,v10,v236\n"
"v_mul_f32 v11,v11,v236\n"
"v_mul_f32 v12,v12,v236\n"
"v_mul_f32 v13,v13,v236\n"
"v_mfma_f32_16x16x32_bf16 a[68:71],a[164:167],v[108:111],a[68:71]\n"
"ds_read_b64_tr_b16 a[182:183],v196 offset:45824\n"
"v_mul_f32 v14,v14,v236\n"
"v_mul_f32 v15,v15,v236\n"
"v_cvt_pk_bf16_f32 v80,v8,v9\n"
"v_cvt_pk_bf16_f32 v81,v10,v11\n"
"v_cvt_pk_bf16_f32 v82,v12,v13\n"
"v_cvt_pk_bf16_f32 v83,v14,v15\n"
"buffer_store_dwordx4 v[80:83],v209,s[84:87],s42 offen sc1\n"
"v_mfma_f32_16x16x32_bf16 a[72:75],a[160:163],v[112:115],a[72:75]\n"
"ds_read_b64_tr_b16 a[184:185],v197 offset:33536\n"
"s_nop 0\n"
"v_mfma_f32_16x16x32_bf16 v[0:3],v[240:243],v[80:83],v[0:3]\n"
"v_accvgpr_read_b32 v16,a36\n"
"v_accvgpr_read_b32 v17,a37\n"
"v_accvgpr_read_b32 v18,a38\n"
"v_accvgpr_read_b32 v19,a39\n"
"v_accvgpr_read_b32 v20,a52\n"
"v_mfma_f32_16x16x32_bf16 a[72:75],a[164:167],v[116:119],a[72:75]\n"
"ds_read_b64_tr_b16 a[186:187],v197 offset:37632\n"
"v_accvgpr_read_b32 v21,a53\n"
"v_accvgpr_read_b32 v22,a54\n"
"v_accvgpr_read_b32 v23,a55\n"
"v_mul_f32 v16,v16,v237\n"
"v_mul_f32 v17,v17,v237\n"
"v_mul_f32 v18,v18,v237\n"
"v_mul_f32 v19,v19,v237\n"
"v_mfma_f32_16x16x32_bf16 a[76:79],a[160:163],v[120:123],a[76:79]\n"
"ds_read_b64_tr_b16 a[188:189],v197 offset:41728\n"
"v_mul_f32 v20,v20,v237\n"
"v_mul_f32 v21,v21,v237\n"
"v_mul_f32 v22,v22,v237\n"
"v_mul_f32 v23,v23,v237\n"
"v_cvt_pk_bf16_f32 v84,v16,v17\n"
"v_cvt_pk_bf16_f32 v85,v18,v19\n"
"v_cvt_pk_bf16_f32 v86,v20,v21\n"
"v_mfma_f32_16x16x32_bf16 a[76:79],a[164:167],v[124:127],a[76:79]\n"
"ds_read_b64_tr_b16 a[190:191],v197 offset:45824\n"
"v_cvt_pk_bf16_f32 v87,v22,v23\n"
"buffer_store_dwordx4 v[84:87],v209,s[84:87],s42 offen offset:1024 sc1\n"
"s_nop 0\n"
"v_mfma_f32_16x16x32_bf16 v[0:3],v[240:243],v[84:87],v[0:3]\n"
"v_accvgpr_read_b32 v24,a40\n"
"v_accvgpr_read_b32 v25,a41\n"
"v_accvgpr_read_b32 v26,a42\n"
"v_mfma_f32_16x16x32_bf16 a[80:83],a[168:171],v[96:99],a[80:83]\n"
"v_accvgpr_read_b32 v27,a43\n"
"v_accvgpr_read_b32 v28,a56\n"
"v_accvgpr_read_b32 v29,a57\n"
"v_accvgpr_read_b32 v30,a58\n"
"v_accvgpr_read_b32 v31,a59\n"
"v_mul_f32 v24,v24,v238\n"
"v_mul_f32 v25,v25,v238\n"
"v_mfma_f32_16x16x32_bf16 a[80:83],a[172:175],v[100:103],a[80:83]\n"
"v_mul_f32 v26,v26,v238\n"
"v_mul_f32 v27,v27,v238\n"
"v_mul_f32 v28,v28,v238\n"
"v_mul_f32 v29,v29,v238\n"
"v_mul_f32 v30,v30,v238\n"
"v_mul_f32 v31,v31,v238\n"
"v_cvt_pk_bf16_f32 v88,v24,v25\n"
"v_mfma_f32_16x16x32_bf16 a[84:87],a[168:171],v[104:107],a[84:87]\n"
"v_cvt_pk_bf16_f32 v89,v26,v27\n"
"v_cvt_pk_bf16_f32 v90,v28,v29\n"
"v_cvt_pk_bf16_f32 v91,v30,v31\n"
"buffer_store_dwordx4 v[88:91],v209,s[84:87],s42 offen offset:2048 sc1\n"
"s_nop 0\n"
"v_mfma_f32_16x16x32_bf16 v[0:3],v[240:243],v[88:91],v[0:3]\n"
"v_accvgpr_read_b32 v32,a44\n"
"v_mfma_f32_16x16x32_bf16 a[84:87],a[172:175],v[108:111],a[84:87]\n"
"v_accvgpr_read_b32 v33,a45\n"
"v_accvgpr_read_b32 v34,a46\n"
"v_accvgpr_read_b32 v35,a47\n"
"v_accvgpr_read_b32 v36,a60\n"
"v_accvgpr_read_b32 v37,a61\n"
"v_accvgpr_read_b32 v38,a62\n"
"v_accvgpr_read_b32 v39,a63\n"
"v_mfma_f32_16x16x32_bf16 a[88:91],a[168:171],v[112:115],a[88:91]\n"
"v_mul_f32 v32,v32,v239\n"
"v_mul_f32 v33,v33,v239\n"
"v_mul_f32 v34,v34,v239\n"
"v_mul_f32 v35,v35,v239\n"
"v_mul_f32 v36,v36,v239\n"
"v_mul_f32 v37,v37,v239\n"
"v_mul_f32 v38,v38,v239\n"
"v_mfma_f32_16x16x32_bf16 a[88:91],a[172:175],v[116:119],a[88:91]\n"
"v_mul_f32 v39,v39,v239\n"
"v_cvt_pk_bf16_f32 v92,v32,v33\n"
"v_cvt_pk_bf16_f32 v93,v34,v35\n"
"v_cvt_pk_bf16_f32 v94,v36,v37\n"
"v_cvt_pk_bf16_f32 v95,v38,v39\n"
"buffer_store_dwordx4 v[92:95],v209,s[84:87],s42 offen offset:3072 sc1\n"
"s_nop 0\n"
"v_mfma_f32_16x16x32_bf16 a[92:95],a[168:171],v[120:123],a[92:95]\n"
"v_mfma_f32_16x16x32_bf16 v[0:3],v[240:243],v[92:95],v[0:3]\n"
"v_mfma_f32_16x16x32_bf16 a[92:95],a[172:175],v[124:127],a[92:95]\n"
"s_waitcnt lgkmcnt(0)\n"
"v_mfma_f32_16x16x32_bf16 a[96:99],a[176:179],v[96:99],a[96:99]\n"
"s_nop 0\n"
"s_nop 0\n"
"s_nop 0\n"
"s_nop 0\n"
"s_nop 0\n"
"s_nop 0\n"
"s_mov_b32 s42,8192\n"
"v_mfma_f32_16x16x32_bf16 a[96:99],a[180:183],v[100:103],a[96:99]\n"
"v_accvgpr_read_b32 v8,a64\n"
"v_accvgpr_read_b32 v9,a65\n"
"v_accvgpr_read_b32 v10,a66\n"
"v_accvgpr_read_b32 v11,a67\n"
"v_accvgpr_read_b32 v12,a80\n"
"v_accvgpr_read_b32 v13,a81\n"
"v_accvgpr_read_b32 v14,a82\n"
"v_mfma_f32_16x16x32_bf16 a[100:103],a[176:179],v[104:107],a[100:103]\n"
"v_accvgpr_read_b32 v15,a83\n"
"v_mul_f32 v8,v8,v236\n"
"v_mul_f32 v9,v9,v236\n"
"v_mul_f32 v10,v10,v236\n"
"v_mul_f32 v11,v11,v236\n"
"v_mul_f32 v12,v12,v236\n"
"v_mul_f32 v13,v13,v236\n"
"v_mfma_f32_16x16x32_bf16 a[100:103],a[180:183],v[108:111],a[100:103]\n"
"v_mul_f32 v14,v14,v236\n"
"v_mul_f32 v15,v15,v236\n"
"v_cvt_pk_bf16_f32 v64,v8,v9\n"
"v_cvt_pk_bf16_f32 v65,v10,v11\n"
"v_cvt_pk_bf16_f32 v66,v12,v13\n"
"v_cvt_pk_bf16_f32 v67,v14,v15\n"
"buffer_store_dwordx4 v[64:67],v209,s[84:87],s42 offen sc1\n"
"v_mfma_f32_16x16x32_bf16 a[104:107],a[176:179],v[112:115],a[104:107]\n"
"s_nop 0\n"
"v_mfma_f32_16x16x32_bf16 v[0:3],v[240:243],v[64:67],v[0:3]\n"
"v_accvgpr_read_b32 v16,a68\n"
"v_accvgpr_read_b32 v17,a69\n"
"v_accvgpr_read_b32 v18,a70\n"
"v_accvgpr_read_b32 v19,a71\n"
"v_accvgpr_read_b32 v20,a84\n"
"v_mfma_f32_16x16x32_bf16 a[104:107],a[180:183],v[116:119],a[104:107]\n"
"v_accvgpr_read_b32 v21,a85\n"
"v_accvgpr_read_b32 v22,a86\n"
"v_accvgpr_read_b32 v23,a87\n"
"v_mul_f32 v16,v16,v237\n"
"v_mul_f32 v17,v17,v237\n"
"v_mul_f32 v18,v18,v237\n"
"v_mul_f32 v19,v19,v237\n"
"v_mfma_f32_16x16x32_bf16 a[108:111],a[176:179],v[120:123],a[108:111]\n"
"v_mul_f32 v20,v20,v237\n"
"v_mul_f32 v21,v21,v237\n"
"v_mul_f32 v22,v22,v237\n"
"v_mul_f32 v23,v23,v237\n"
"v_cvt_pk_bf16_f32 v68,v16,v17\n"
"v_cvt_pk_bf16_f32 v69,v18,v19\n"
"v_cvt_pk_bf16_f32 v70,v20,v21\n"
"v_mfma_f32_16x16x32_bf16 a[108:111],a[180:183],v[124:127],a[108:111]\n"
"v_cvt_pk_bf16_f32 v71,v22,v23\n"
"buffer_store_dwordx4 v[68:71],v209,s[84:87],s42 offen offset:1024 sc1\n"
"s_nop 0\n"
"v_mfma_f32_16x16x32_bf16 v[0:3],v[240:243],v[68:71],v[0:3]\n"
"v_accvgpr_read_b32 v24,a72\n"
"v_accvgpr_read_b32 v25,a73\n"
"v_accvgpr_read_b32 v26,a74\n"
"v_mfma_f32_16x16x32_bf16 a[112:115],a[184:187],v[96:99],a[112:115]\n"
"v_accvgpr_read_b32 v27,a75\n"
"v_accvgpr_read_b32 v28,a88\n"
"v_accvgpr_read_b32 v29,a89\n"
"v_accvgpr_read_b32 v30,a90\n"
"v_accvgpr_read_b32 v31,a91\n"
"v_mul_f32 v24,v24,v238\n"
"v_mul_f32 v25,v25,v238\n"
"v_mfma_f32_16x16x32_bf16 a[112:115],a[188:191],v[100:103],a[112:115]\n"
"v_mul_f32 v26,v26,v238\n"
"v_mul_f32 v27,v27,v238\n"
"v_mul_f32 v28,v28,v238\n"
"v_mul_f32 v29,v29,v238\n"
"v_mul_f32 v30,v30,v238\n"
"v_mul_f32 v31,v31,v238\n"
"v_cvt_pk_bf16_f32 v72,v24,v25\n"
"v_mfma_f32_16x16x32_bf16 a[116:119],a[184:187],v[104:107],a[116:119]\n"
"v_cvt_pk_bf16_f32 v73,v26,v27\n"
"v_cvt_pk_bf16_f32 v74,v28,v29\n"
"v_cvt_pk_bf16_f32 v75,v30,v31\n"
"buffer_store_dwordx4 v[72:75],v209,s[84:87],s42 offen offset:2048 sc1\n"
"s_nop 0\n"
"v_mfma_f32_16x16x32_bf16 v[0:3],v[240:243],v[72:75],v[0:3]\n"
"v_accvgpr_read_b32 v32,a76\n"
"v_mfma_f32_16x16x32_bf16 a[116:119],a[188:191],v[108:111],a[116:119]\n"
"v_accvgpr_read_b32 v33,a77\n"
"v_accvgpr_read_b32 v34,a78\n"
"v_accvgpr_read_b32 v35,a79\n"
"v_accvgpr_read_b32 v36,a92\n"
"v_accvgpr_read_b32 v37,a93\n"
"v_accvgpr_read_b32 v38,a94\n"
"v_accvgpr_read_b32 v39,a95\n"
"v_mfma_f32_16x16x32_bf16 a[120:123],a[184:187],v[112:115],a[120:123]\n"
"v_mul_f32 v32,v32,v239\n"
"v_mul_f32 v33,v33,v239\n"
"v_mul_f32 v34,v34,v239\n"
"v_mul_f32 v35,v35,v239\n"
"v_mul_f32 v36,v36,v239\n"
"v_mul_f32 v37,v37,v239\n"
"v_mul_f32 v38,v38,v239\n"
"v_mfma_f32_16x16x32_bf16 a[120:123],a[188:191],v[116:119],a[120:123]\n"
"v_mul_f32 v39,v39,v239\n"
"v_cvt_pk_bf16_f32 v76,v32,v33\n"
"v_cvt_pk_bf16_f32 v77,v34,v35\n"
"v_cvt_pk_bf16_f32 v78,v36,v37\n"
"v_cvt_pk_bf16_f32 v79,v38,v39\n"
"buffer_store_dwordx4 v[76:79],v209,s[84:87],s42 offen offset:3072 sc1\n"
"s_nop 0\n"
"v_mfma_f32_16x16x32_bf16 a[124:127],a[184:187],v[120:123],a[124:127]\n"
"v_mfma_f32_16x16x32_bf16 v[0:3],v[240:243],v[76:79],v[0:3]\n"
"v_mfma_f32_16x16x32_bf16 a[124:127],a[188:191],v[124:127],a[124:127]\n"
"s_nop 7\n"
"s_mov_b32 s42,12288\n"
"v_accvgpr_read_b32 v8,a96\n"
"v_accvgpr_read_b32 v9,a97\n"
"v_accvgpr_read_b32 v10,a98\n"
"v_accvgpr_read_b32 v11,a99\n"
"buffer_load_dwordx4 a[160:163],v209,s[64:67],s40 offen sc1\n"
"v_accvgpr_read_b32 v12,a112\n"
"v_accvgpr_read_b32 v13,a113\n"
"v_accvgpr_read_b32 v14,a114\n"
"v_accvgpr_read_b32 v15,a115\n"
"v_mul_f32 v8,v8,v236\n"
"v_mul_f32 v9,v9,v236\n"
"v_mul_f32 v10,v10,v236\n"
"v_mul_f32 v11,v11,v236\n"
"v_mul_f32 v12,v12,v236\n"
"v_mul_f32 v13,v13,v236\n"
"buffer_load_dwordx4 a[164:167],v209,s[64:67],s40 offen offset:1024 sc1\n"
"v_mul_f32 v14,v14,v236\n"
"v_mul_f32 v15,v15,v236\n"
"v_cvt_pk_bf16_f32 v80,v8,v9\n"
"v_cvt_pk_bf16_f32 v81,v10,v11\n"
"v_cvt_pk_bf16_f32 v82,v12,v13\n"
"v_cvt_pk_bf16_f32 v83,v14,v15\n"
"buffer_store_dwordx4 v[80:83],v209,s[84:87],s42 offen sc1\n"
"s_nop 0\n"
"v_mfma_f32_16x16x32_bf16 v[0:3],v[240:243],v[80:83],v[0:3]\n"
"v_accvgpr_read_b32 v16,a100\n"
"buffer_load_dwordx4 a[168:171],v209,s[64:67],s40 offen offset:2048 sc1\n"
"v_accvgpr_read_b32 v17,a101\n"
"v_accvgpr_read_b32 v18,a102\n"
"v_accvgpr_read_b32 v19,a103\n"
"v_accvgpr_read_b32 v20,a116\n"
"v_accvgpr_read_b32 v21,a117\n"
"v_accvgpr_read_b32 v22,a118\n"
"v_accvgpr_read_b32 v23,a119\n"
"v_mul_f32 v16,v16,v237\n"
"v_mul_f32 v17,v17,v237\n"
"v_mul_f32 v18,v18,v237\n"
"buffer_load_dwordx4 a[172:175],v209,s[64:67],s40 offen offset:3072 sc1\n"
"v_mul_f32 v19,v19,v237\n"
"v_mul_f32 v20,v20,v237\n"
"v_mul_f32 v21,v21,v237\n"
"v_mul_f32 v22,v22,v237\n"
"v_mul_f32 v23,v23,v237\n"
"v_cvt_pk_bf16_f32 v84,v16,v17\n"
"v_cvt_pk_bf16_f32 v85,v18,v19\n"
"v_cvt_pk_bf16_f32 v86,v20,v21\n"
"v_cvt_pk_bf16_f32 v87,v22,v23\n"
"buffer_store_dwordx4 v[84:87],v209,s[84:87],s42 offen offset:1024 sc1\n"
"s_add_u32 s40,s40,4096\n"
"buffer_load_dwordx4 a[176:179],v209,s[64:67],s40 offen sc1\n"
"s_nop 0\n"
"v_mfma_f32_16x16x32_bf16 v[0:3],v[240:243],v[84:87],v[0:3]\n"
"v_accvgpr_read_b32 v24,a104\n"
"v_accvgpr_read_b32 v25,a105\n"
"v_accvgpr_read_b32 v26,a106\n"
"v_accvgpr_read_b32 v27,a107\n"
"v_accvgpr_read_b32 v28,a120\n"
"v_accvgpr_read_b32 v29,a121\n"
"v_accvgpr_read_b32 v30,a122\n"
"v_accvgpr_read_b32 v31,a123\n"
"buffer_load_dwordx4 a[180:183],v209,s[64:67],s40 offen offset:1024 sc1\n"
"v_mul_f32 v24,v24,v238\n"
"v_mul_f32 v25,v25,v238\n"
"v_mul_f32 v26,v26,v238\n"
"v_mul_f32 v27,v27,v238\n"
"v_mul_f32 v28,v28,v238\n"
"v_mul_f32 v29,v29,v238\n"
"v_mul_f32 v30,v30,v238\n"
"v_mul_f32 v31,v31,v238\n"
"v_cvt_pk_bf16_f32 v88,v24,v25\n"
"v_cvt_pk_bf16_f32 v89,v26,v27\n"
"buffer_load_dwordx4 a[184:187],v209,s[64:67],s40 offen offset:2048 sc1\n"
"v_cvt_pk_bf16_f32 v90,v28,v29\n"
"v_cvt_pk_bf16_f32 v91,v30,v31\n"
"buffer_store_dwordx4 v[88:91],v209,s[84:87],s42 offen offset:2048 sc1\n"
"s_nop 0\n"
"v_mfma_f32_16x16x32_bf16 v[0:3],v[240:243],v[88:91],v[0:3]\n"
"v_accvgpr_read_b32 v32,a108\n"
"v_accvgpr_read_b32 v33,a109\n"
"v_accvgpr_read_b32 v34,a110\n"
"v_accvgpr_read_b32 v35,a111\n"
"v_accvgpr_read_b32 v36,a124\n"
"buffer_load_dwordx4 a[188:191],v209,s[64:67],s40 offen offset:3072 sc1\n"
"v_accvgpr_read_b32 v37,a125\n"
"v_accvgpr_read_b32 v38,a126\n"
"v_accvgpr_read_b32 v39,a127\n"
"v_mul_f32 v32,v32,v239\n"
"v_mul_f32 v33,v33,v239\n"
"v_mul_f32 v34,v34,v239\n"
"v_mul_f32 v35,v35,v239\n"
"v_mul_f32 v36,v36,v239\n"
"v_mul_f32 v37,v37,v239\n"
"v_mul_f32 v38,v38,v239\n"
"s_add_u32 s40,s40,4096\n"
"v_mul_f32 v39,v39,v239\n"
"v_cvt_pk_bf16_f32 v92,v32,v33\n"
"v_cvt_pk_bf16_f32 v93,v34,v35\n"
"v_cvt_pk_bf16_f32 v94,v36,v37\n"
"v_cvt_pk_bf16_f32 v95,v38,v39\n"
"buffer_store_dwordx4 v[92:95],v209,s[84:87],s42 offen offset:3072 sc1\n"
"s_nop 0\n"
"v_mfma_f32_16x16x32_bf16 v[0:3],v[240:243],v[92:95],v[0:3]\n"
"v_readlane_b32 s43,v210,42\n"
"s_add_u32 s41,s43,0\n"
"s_and_b32 s42,s41,1\n"
"s_lshl_b32 s42,s42,5\n"
"s_lshr_b32 s41,s41,1\n"
"s_lshl_b32 s41,s41,8\n"
"v_xor_b32 v252,s42,v212\n"
"v_add3_u32 v252,v252,s41,v211\n"
"v_add_u32 v254,114688,v252\n"
"v_add_u32 v252,65536,v252\n"
"s_add_u32 s41,s43,1\n"
"s_and_b32 s42,s41,1\n"
"s_lshl_b32 s42,s42,5\n"
"s_lshr_b32 s41,s41,1\n"
"s_lshl_b32 s41,s41,8\n"
"v_xor_b32 v253,s42,v212\n"
"v_add3_u32 v253,v253,s41,v211\n"
"v_add_u32 v255,114688,v253\n"
"v_add_u32 v253,65536,v253\n"
"v_cmp_u_f32 vcc,v0,v0\n"
"v_readlane_b32 s41,v210,37\n"
"s_or_b64 s[44:45],s[44:45],vcc\n"
"s_cselect_b32 s42,1,0\n"
"s_lshl_b32 s43,s48,2\n"
"s_add_u32 s41,s41,49152\n"
"s_add_u32 s43,s43,s41\n"
"v_mov_b32 v236,s43\n"
"v_mov_b32 v237,s42\n"
"v_mov_b32 v238,s41\n"
"ds_write_b32 v236,v237\n"
"s_waitcnt vmcnt(38) lgkmcnt(0)\n"
"s_barrier\n"
"ds_read_b128 v[240:243],v238\n"
"ds_read_b64_tr_b16 a[32:33],v254 offset:24576\n"
"ds_read_b64_tr_b16 a[34:35],v254 offset:30720\n"
"ds_read_b64_tr_b16 a[36:37],v254 offset:36864\n"
"ds_read_b64_tr_b16 a[38:39],v254 offset:43008\n"
"ds_read_b64_tr_b16 a[40:41],v255 offset:24576\n"
"ds_read_b64_tr_b16 a[42:43],v255 offset:30720\n"
"ds_read_b64_tr_b16 a[44:45],v255 offset:36864\n"
"ds_read_b64_tr_b16 a[46:47],v255 offset:43008\n"
"ds_read_b64_tr_b16 a[48:49],v254 offset:24832\n"
"ds_read_b64_tr_b16 a[50:51],v254 offset:30976\n"
"ds_read_b64_tr_b16 a[52:53],v254 offset:37120\n"
"ds_read_b64_tr_b16 a[54:55],v254 offset:43264\n"
"s_waitcnt lgkmcnt(12)\n"
"v_or3_b32 v240,v240,v241,v242\n"
"v_or_b32 v240,v240,v243\n"
"s_nop 0\n"
"v_readfirstlane_b32 s41,v240\n"
"s_nop 1\n"
"s_andn2_b32 s41,s41,s54\n"
"s_cbranch_scc1 LM_EXACT\n"
"s_waitcnt lgkmcnt(8)\n"
"v_mfma_f32_16x16x32_bf16 v[0:3],a[32:35],a[208:211],0\n"
"ds_read_b64_tr_b16 a[56:57],v255 offset:24832\n"
"v_mfma_f32_16x16x32_bf16 v[0:3],a[36:39],a[224:227],v[0:3]\n"
"v_mfma_f32_16x16x32_bf16 v[4:7],a[32:35],a[212:215],0\n"
"s_add_u32 m0,s51,114688\n"
"ds_read_b64_tr_b16 a[58:59],v255 offset:30976\n"
"v_mfma_f32_16x16x32_bf16 v[4:7],a[36:39],a[228:231],v[4:7]\n"
"buffer_load_dwordx4 v216,s[72:75],s39 offen lds\n"
"v_mfma_f32_16x16x32_bf16 v[8:11],a[32:35],a[216:219],0\n"
"ds_read_b64_tr_b16 a[60:61],v255 offset:37120\n"
"v_mfma_f32_16x16x32_bf16 v[8:11],a[36:39],a[232:235],v[8:11]\n"
"v_mfma_f32_16x16x32_bf16 v[12:15],a[32:35],a[220:223],0\n"
"ds_read_b64_tr_b16 a[62:63],v255 offset:43264\n"
"v_mfma_f32_16x16x32_bf16 v[12:15],a[36:39],a[236:239],v[12:15]\n"
"buffer_load_dwordx4 v217,s[72:75],s39 offen offset:1024 lds\n"
"s_waitcnt lgkmcnt(8)\n"
"v_mfma_f32_16x16x32_bf16 v[16:19],a[40:43],a[208:211],0\n"
"ds_read_b64_tr_b16 a[64:65],v254 offset:25088\n"
"v_mfma_f32_16x16x32_bf16 v[16:19],a[44:47],a[224:227],v[16:19]\n"
"v_mfma_f32_16x16x32_bf16 v[20:23],a[40:43],a[212:215],0\n"
"ds_read_b64_tr_b16 a[66:67],v254 offset:31232\n"
"v_mfma_f32_16x16x32_bf16 v[20:23],a[44:47],a[228:231],v[20:23]\n"
"v_mfma_f32_16x16x32_bf16 v[24:27],a[40:43],a[216:219],0\n"
"ds_read_b64_tr_b16 a[68:69],v254 offset:37376\n"
"v_mfma_f32_16x16x32_bf16 v[24:27],a[44:47],a[232:235],v[24:27]\n"
"buffer_load_dwordx4 v218,s[72:75],s39 offen offset:2048 lds\n"
"v_mfma_f32_16x16x32_bf16 v[28:31],a[40:43],a[220:223],0\n"
"ds_read_b64_tr_b16 a[70:71],v254 offset:43520\n"
"v_mfma_f32_16x16x32_bf16 v[28:31],a[44:47],a[236:239],v[28:31]\n"
"s_waitcnt lgkmcnt(8)\n"
"v_mfma_f32_16x16x32_bf16 v[32:35],a[48:51],a[208:211],0\n"
"ds_read_b64_tr_b16 a[72:73],v255 offset:25088\n"
"v_mfma_f32_16x16x32_bf16 v[32:35],a[52:55],a[224:227],v[32:35]\n"
"v_mfma_f32_16x16x32_bf16 v[36:39],a[48:51],a[212:215],0\n"
"s_add_u32 s39,s39,s52\n"
"s_add_u32 m0,s51,117760\n"
"ds_read_b64_tr_b16 a[74:75],v255 offset:31232\n"
"v_mfma_f32_16x16x32_bf16 v[36:39],a[52:55],a[228:231],v[36:39]\n"
"buffer_load_dwordx4 v216,s[72:75],s39 offen lds\n"
"v_mfma_f32_16x16x32_bf16 v[40:43],a[48:51],a[216:219],0\n"
"ds_read_b64_tr_b16 a[76:77],v255 offset:37376\n"
"v_mfma_f32_16x16x32_bf16 v[40:43],a[52:55],a[232:235],v[40:43]\n"
"v_mfma_f32_16x16x32_bf16 v[44:47],a[48:51],a[220:223],0\n"
"ds_read_b64_tr_b16 a[78:79],v255 offset:43520\n"
"v_mfma_f32_16x16x32_bf16 v[44:47],a[52:55],a[236:239],v[44:47]\n"
"buffer_load_dwordx4 v217,s[72:75],s39 offen offset:1024 lds\n"
"s_waitcnt lgkmcnt(8)\n"
"v_mfma_f32_16x16x32_bf16 v[48:51],a[56:59],a[208:211],0\n"
"ds_read_b64_tr_b16 a[32:33],v254 offset:25344\n"
"v_mfma_f32_16x16x32_bf16 v[48:51],a[60:63],a[224:227],v[48:51]\n"
"v_mfma_f32_16x16x32_bf16 v[52:55],a[56:59],a[212:215],0\n"
"ds_read_b64_tr_b16 a[34:35],v254 offset:31488\n"
"v_mfma_f32_16x16x32_bf16 v[52:55],a[60:63],a[228:231],v[52:55]\n"
"v_mfma_f32_16x16x32_bf16 v[56:59],a[56:59],a[216:219],0\n"
"ds_read_b64_tr_b16 a[36:37],v254 offset:37632\n"
"v_mfma_f32_16x16x32_bf16 v[56:59],a[60:63],a[232:235],v[56:59]\n"
"buffer_load_dwordx4 v218,s[72:75],s39 offen offset:2048 lds\n"
"v_mfma_f32_16x16x32_bf16 v[60:63],a[56:59],a[220:223],0\n"
"ds_read_b64_tr_b16 a[38:39],v254 offset:43776\n"
"v_mfma_f32_16x16x32_bf16 v[60:63],a[60:63],a[236:239],v[60:63]\n"
"s_waitcnt lgkmcnt(8)\n"
"v_mfma_f32_16x16x32_bf16 v[64:67],a[64:67],a[208:211],0\n"
"s_add_u32 s39,s39,s53\n"
"ds_read_b64_tr_b16 a[40:41],v255 offset:25344\n"
"v_mfma_f32_16x16x32_bf16 v[64:67],a[68:71],a[224:227],v[64:67]\n"
"buffer_load_dwordx4 a[0:3],v209,s[64:67],s40 offen sc1\n"
"v_mfma_f32_16x16x32_bf16 v[68:71],a[64:67],a[212:215],0\n"
"ds_read_b64_tr_b16 a[42:43],v255 offset:31488\n"
"v_mfma_f32_16x16x32_bf16 v[68:71],a[68:71],a[228:231],v[68:71]\n"
"v_mfma_f32_16x16x32_bf16 v[72:75],a[64:67],a[216:219],0\n"
"ds_read_b64_tr_b16 a[44:45],v255 offset:37632\n"
"v_mfma_f32_16x16x32_bf16 v[72:75],a[68:71],a[232:235],v[72:75]\n"
"v_mfma_f32_16x16x32_bf16 v[76:79],a[64:67],a[220:223],0\n"
"ds_read_b64_tr_b16 a[46:47],v255 offset:43776\n"
"v_mfma_f32_16x16x32_bf16 v[76:79],a[68:71],a[236:239],v[76:79]\n"
"buffer_load_dwordx4 a[4:7],v209,s[64:67],s40 offen offset:1024 sc1\n"
"s_waitcnt lgkmcnt(8)\n"
"v_mfma_f32_16x16x32_bf16 v[80:83],a[72:75],a[208:211],0\n"
"ds_read_b64_tr_b16 a[48:49],v254 offset:25600\n"
"v_mfma_f32_16x16x32_bf16 v[80:83],a[76:79],a[224:227],v[80:83]\n"
"ds_read_b64_tr_b16 a[50:51],v254 offset:31744\n"
"v_mfma_f32_16x16x32_bf16 v[84:87],a[72:75],a[212:215],0\n"
"ds_read_b64_tr_b16 a[52:53],v254 offset:37888\n"
"v_mfma_f32_16x16x32_bf16 v[84:87],a[76:79],a[228:231],v[84:87]\n"
"ds_read_b64_tr_b16 a[54:55],v254 offset:44032\n"
"buffer_load_dwordx4 a[8:11],v209,s[64:67],s40 offen offset:2048 sc1\n"
"v_mfma_f32_16x16x32_bf16 v[88:91],a[72:75],a[216:219],0\n"
"v_mfma_f32_16x16x32_bf16 v[88:91],a[76:79],a[232:235],v[88:91]\n"
"v_mfma_f32_16x16x32_bf16 v[92:95],a[72:75],a[220:223],0\n"
"v_mfma_f32_16x16x32_bf16 v[92:95],a[76:79],a[236:239],v[92:95]\n"
"s_waitcnt vmcnt(33) lgkmcnt(0)\n"
"s_barrier\n"
"v_mfma_f32_16x16x32_bf16 v[96:99],a[32:35],a[208:211],0\n"
"ds_read_b64_tr_b16 a[56:57],v252\n"
"v_mfma_f32_16x16x32_bf16 v[96:99],a[36:39],a[224:227],v[96:99]\n"
"buffer_load_dwordx4 a[12:15],v209,s[64:67],s40 offen offset:3072 sc1\n"
"v_mfma_f32_16x16x32_bf16 v[100:103],a[32:35],a[212:215],0\n"
"ds_read_b64_tr_b16 a[58:59],v252 offset:6144\n"
"v_mfma_f32_16x16x32_bf16 v[100:103],a[36:39],a[228:231],v[100:103]\n"
"v_mfma_f32_16x16x32_bf16 v[104:107],a[32:35],a[216:219],0\n"
"ds_read_b64_tr_b16 a[60:61],v252 offset:12288\n"
"v_mfma_f32_16x16x32_bf16 v[104:107],a[36:39],a[232:235],v[104:107]\n"
"v_mfma_f32_16x16x32_bf16 v[108:111],a[32:35],a[220:223],0\n"
"s_add_u32 s40,s40,4096\n"
"ds_read_b64_tr_b16 a[62:63],v252 offset:18432\n"
"v_mfma_f32_16x16x32_bf16 v[108:111],a[36:39],a[236:239],v[108:111]\n"
"buffer_load_dwordx4 a[16:19],v209,s[64:67],s40 offen sc1\n"
"v_mfma_f32_16x16x32_bf16 v[112:115],a[40:43],a[208:211],0\n"
"ds_read_b64_tr_b16 a[64:65],v253\n"
"v_mfma_f32_16x16x32_bf16 v[112:115],a[44:47],a[224:227],v[112:115]\n"
"v_mfma_f32_16x16x32_bf16 v[116:119],a[40:43],a[212:215],0\n"
"ds_read_b64_tr_b16 a[66:67],v253 offset:6144\n"
"v_mfma_f32_16x16x32_bf16 v[116:119],a[44:47],a[228:231],v[116:119]\n"
"buffer_load_dwordx4 a[20:23],v209,s[64:67],s40 offen offset:1024 sc1\n"
"v_mfma_f32_16x16x32_bf16 v[120:123],a[40:43],a[216:219],0\n"
"ds_read_b64_tr_b16 a[68:69],v253 offset:12288\n"
"v_mfma_f32_16x16x32_bf16 v[120:123],a[44:47],a[232:235],v[120:123]\n"
"v_mfma_f32_16x16x32_bf16 v[124:127],a[40:43],a[220:223],0\n"
"ds_read_b64_tr_b16 a[70:71],v253 offset:18432\n"
"v_mfma_f32_16x16x32_bf16 v[124:127],a[44:47],a[236:239],v[124:127]\n"
"v_mfma_f32_16x16x32_bf16 v[128:131],a[48:51],a[208:211],0\n"
"ds_read_b64_tr_b16 a[72:73],v252 offset:256\n"
"v_mfma_f32_16x16x32_bf16 v[128:131],a[52:55],a[224:227],v[128:131]\n"
"buffer_load_dwordx4 a[24:27],v209,s[64:67],s40 offen offset:2048 sc1\n"
"v_mfma_f32_16x16x32_bf16 v[132:135],a[48:51],a[212:215],0\n"
"ds_read_b64_tr_b16 a[74:75],v252 offset:6400\n"
"v_mfma_f32_16x16x32_bf16 v[132:135],a[52:55],a[228:231],v[132:135]\n"
"v_mfma_f32_16x16x32_bf16 v[136:139],a[48:51],a[216:219],0\n"
"ds_read_b64_tr_b16 a[76:77],v252 offset:12544\n"
"v_mfma_f32_16x16x32_bf16 v[136:139],a[52:55],a[232:235],v[136:139]\n"
"buffer_load_dwordx4 a[28:31],v209,s[64:67],s40 offen offset:3072 sc1\n"
"v_mfma_f32_16x16x32_bf16 v[140:143],a[48:51],a[220:223],0\n"
"ds_read_b64_tr_b16 a[78:79],v252 offset:18688\n"
"v_mfma_f32_16x16x32_bf16 v[140:143],a[52:55],a[236:239],v[140:143]\n"
"s_add_u32 s40,s40,4096\n"
"s_waitcnt lgkmcnt(8)\n"
"v_mfma_f32_16x16x32_bf16 v[0:3],a[56:59],a[128:131],v[0:3]\n"
"ds_read_b64_tr_b16 a[32:33],v253 offset:256\n"
"v_mfma_f32_16x16x32_bf16 v[0:3],a[60:63],a[144:147],v[0:3]\n"
"v_mfma_f32_16x16x32_bf16 v[4:7],a[56:59],a[132:135],v[4:7]\n"
"s_add_u32 m0,s51,139264\n"
"ds_read_b64_tr_b16 a[34:35],v253 offset:6400\n"
"v_mfma_f32_16x16x32_bf16 v[4:7],a[60:63],a[148:151],v[4:7]\n"
"buffer_load_dwordx4 v216,s[72:75],s39 offen lds\n"
"v_mfma_f32_16x16x32_bf16 v[8:11],a[56:59],a[136:139],v[8:11]\n"
"ds_read_b64_tr_b16 a[36:37],v253 offset:12544\n"
"v_mfma_f32_16x16x32_bf16 v[8:11],a[60:63],a[152:155],v[8:11]\n"
"v_mfma_f32_16x16x32_bf16 v[12:15],a[56:59],a[140:143],v[12:15]\n"
"ds_read_b64_tr_b16 a[38:39],v253 offset:18688\n"
"v_mfma_f32_16x16x32_bf16 v[12:15],a[60:63],a[156:159],v[12:15]\n"
"buffer_load_dwordx4 v217,s[72:75],s39 offen offset:1024 lds\n"
"s_waitcnt lgkmcnt(8)\n"
"v_mfma_f32_16x16x32_bf16 v[16:19],a[64:67],a[128:131],v[16:19]\n"
"ds_read_b64_tr_b16 a[40:41],v252 offset:512\n"
"v_mfma_f32_16x16x32_bf16 v[16:19],a[68:71],a[144:147],v[16:19]\n"
"v_mfma_f32_16x16x32_bf16 v[20:23],a[64:67],a[132:135],v[20:23]\n"
"ds_read_b64_tr_b16 a[42:43],v252 offset:6656\n"
"v_mfma_f32_16x16x32_bf16 v[20:23],a[68:71],a[148:151],v[20:23]\n"
"v_mfma_f32_16x16x32_bf16 v[24:27],a[64:67],a[136:139],v[24:27]\n"
"ds_read_b64_tr_b16 a[44:45],v252 offset:12800\n"
"v_mfma_f32_16x16x32_bf16 v[24:27],a[68:71],a[152:155],v[24:27]\n"
"buffer_load_dwordx4 v218,s[72:75],s39 offen offset:2048 lds\n"
"v_mfma_f32_16x16x32_bf16 v[28:31],a[64:67],a[140:143],v[28:31]\n"
"ds_read_b64_tr_b16 a[46:47],v252 offset:18944\n"
"v_mfma_f32_16x16x32_bf16 v[28:31],a[68:71],a[156:159],v[28:31]\n"
"s_waitcnt lgkmcnt(8)\n"
"v_mfma_f32_16x16x32_bf16 v[32:35],a[72:75],a[128:131],v[32:35]\n"
"ds_read_b64_tr_b16 a[48:49],v253 offset:512\n"
"v_mfma_f32_16x16x32_bf16 v[32:35],a[76:79],a[144:147],v[32:35]\n"
"v_mfma_f32_16x16x32_bf16 v[36:39],a[72:75],a[132:135],v[36:39]\n"
"s_add_u32 s39,s39,s52\n"
"s_add_u32 m0,s51,142336\n"
"ds_read_b64_tr_b16 a[50:51],v253 offset:6656\n"
"v_mfma_f32_16x16x32_bf16 v[36:39],a[76:79],a[148:151],v[36:39]\n"
"buffer_load_dwordx4 v216,s[72:75],s39 offen lds\n"
"v_mfma_f32_16x16x32_bf16 v[40:43],a[72:75],a[136:139],v[40:43]\n"
"ds_read_b64_tr_b16 a[52:53],v253 offset:12800\n"
"v_mfma_f32_16x16x32_bf16 v[40:43],a[76:79],a[152:155],v[40:43]\n"
"v_mfma_f32_16x16x32_bf16 v[44:47],a[72:75],a[140:143],v[44:47]\n"
"ds_read_b64_tr_b16 a[54:55],v253 offset:18944\n"
"v_mfma_f32_16x16x32_bf16 v[44:47],a[76:79],a[156:159],v[44:47]\n"
"buffer_load_dwordx4 v217,s[72:75],s39 offen offset:1024 lds\n"
"s_waitcnt lgkmcnt(8)\n"
"v_mfma_f32_16x16x32_bf16 v[48:51],a[32:35],a[128:131],v[48:51]\n"
"ds_read_b64_tr_b16 a[56:57],v252 offset:768\n"
"v_mfma_f32_16x16x32_bf16 v[48:51],a[36:39],a[144:147],v[48:51]\n"
"v_mfma_f32_16x16x32_bf16 v[52:55],a[32:35],a[132:135],v[52:55]\n"
"ds_read_b64_tr_b16 a[58:59],v252 offset:6912\n"
"v_mfma_f32_16x16x32_bf16 v[52:55],a[36:39],a[148:151],v[52:55]\n"
"v_mfma_f32_16x16x32_bf16 v[56:59],a[32:35],a[136:139],v[56:59]\n"
"ds_read_b64_tr_b16 a[60:61],v252 offset:13056\n"
"v_mfma_f32_16x16x32_bf16 v[56:59],a[36:39],a[152:155],v[56:59]\n"
"buffer_load_dwordx4 v218,s[72:75],s39 offen offset:2048 lds\n"
"v_mfma_f32_16x16x32_bf16 v[60:63],a[32:35],a[140:143],v[60:63]\n"
"ds_read_b64_tr_b16 a[62:63],v252 offset:19200\n"
"v_mfma_f32_16x16x32_bf16 v[60:63],a[36:39],a[156:159],v[60:63]\n"
"s_waitcnt lgkmcnt(8)\n"
"v_mfma_f32_16x16x32_bf16 v[64:67],a[40:43],a[128:131],v[64:67]\n"
"s_add_u32 s39,s39,s53\n"
"ds_read_b64_tr_b16 a[64:65],v253 offset:768\n"
"v_mfma_f32_16x16x32_bf16 v[64:67],a[44:47],a[144:147],v[64:67]\n"
"buffer_load_dwordx4 a[208:211],v209,s[64:67],s40 offen sc1\n"
"v_mfma_f32_16x16x32_bf16 v[68:71],a[40:43],a[132:135],v[68:71]\n"
"ds_read_b64_tr_b16 a[66:67],v253 offset:6912\n"
"v_mfma_f32_16x16x32_bf16 v[68:71],a[44:47],a[148:151],v[68:71]\n"
"v_mfma_f32_16x16x32_bf16 v[72:75],a[40:43],a[136:139],v[72:75]\n"
"ds_read_b64_tr_b16 a[68:69],v253 offset:13056\n"
"v_mfma_f32_16x16x32_bf16 v[72:75],a[44:47],a[152:155],v[72:75]\n"
"v_mfma_f32_16x16x32_bf16 v[76:79],a[40:43],a[140:143],v[76:79]\n"
"ds_read_b64_tr_b16 a[70:71],v253 offset:19200\n"
"v_mfma_f32_16x16x32_bf16 v[76:79],a[44:47],a[156:159],v[76:79]\n"
"buffer_load_dwordx4 a[212:215],v209,s[64:67],s40 offen offset:1024 sc1\n"
"s_waitcnt lgkmcnt(8)\n"
"v_mfma_f32_16x16x32_bf16 v[80:83],a[48:51],a[128:131],v[80:83]\n"
"ds_read_b64_tr_b16 a[72:73],v252 offset:1024\n"
"v_mfma_f32_16x16x32_bf16 v[80:83],a[52:55],a[144:147],v[80:83]\n"
"ds_read_b64_tr_b16 a[74:75],v252 offset:7168\n"
"v_mfma_f32_16x16x32_bf16 v[84:87],a[48:51],a[132:135],v[84:87]\n"
"ds_read_b64_tr_b16 a[76:77],v252 offset:13312\n"
"v_mfma_f32_16x16x32_bf16 v[84:87],a[52:55],a[148:151],v[84:87]\n"
"ds_read_b64_tr_b16 a[78:79],v252 offset:19456\n"
"buffer_load_dwordx4 a[216:219],v209,s[64:67],s40 offen offset:2048 sc1\n"
"v_mfma_f32_16x16x32_bf16 v[88:91],a[48:51],a[136:139],v[88:91]\n"
"v_mfma_f32_16x16x32_bf16 v[88:91],a[52:55],a[152:155],v[88:91]\n"
"v_mfma_f32_16x16x32_bf16 v[92:95],a[48:51],a[140:143],v[92:95]\n"
"v_mfma_f32_16x16x32_bf16 v[92:95],a[52:55],a[156:159],v[92:95]\n"
"s_waitcnt vmcnt(23) lgkmcnt(0)\n"
"s_barrier\n"
"v_readlane_b32 s43,v210,53\n"
"s_nop 0\n"
"s_lshl_b32 s43,s43,6\n"
"v_lshl_add_u32 v223,v209,2,s43\n"
"s_cmp_lg_u32 s48,0\n"
"s_cbranch_scc1 LM_FS14\n"
"s_mov_b64 exec,1\n"
"buffer_store_dwordx2 v[224:225],v223,s[68:71],0 offen sc1\n"
"s_mov_b64 exec,-1\n"
"LM_FS14:\n"
"v_mfma_f32_16x16x32_bf16 v[96:99],a[56:59],a[128:131],v[96:99]\n"
"ds_read_b64_tr_b16 a[32:33],v252 offset:24576\n"
"v_mfma_f32_16x16x32_bf16 v[96:99],a[60:63],a[144:147],v[96:99]\n"
"buffer_load_dwordx4 a[220:223],v209,s[64:67],s40 offen offset:3072 sc1\n"
"v_mfma_f32_16x16x32_bf16 v[100:103],a[56:59],a[132:135],v[100:103]\n"
"ds_read_b64_tr_b16 a[34:35],v252 offset:30720\n"
"v_mfma_f32_16x16x32_bf16 v[100:103],a[60:63],a[148:151],v[100:103]\n"
"v_mfma_f32_16x16x32_bf16 v[104:107],a[56:59],a[136:139],v[104:107]\n"
"ds_read_b64_tr_b16 a[36:37],v252 offset:36864\n"
"v_mfma_f32_16x16x32_bf16 v[104:107],a[60:63],a[152:155],v[104:107]\n"
"v_mfma_f32_16x16x32_bf16 v[108:111],a[56:59],a[140:143],v[108:111]\n"
"s_add_u32 s40,s40,4096\n"
"ds_read_b64_tr_b16 a[38:39],v252 offset:43008\n"
"v_mfma_f32_16x16x32_bf16 v[108:111],a[60:63],a[156:159],v[108:111]\n"
"buffer_load_dwordx4 a[224:227],v209,s[64:67],s40 offen sc1\n"
"v_mfma_f32_16x16x32_bf16 v[112:115],a[64:67],a[128:131],v[112:115]\n"
"ds_read_b64_tr_b16 a[40:41],v253 offset:24576\n"
"v_mfma_f32_16x16x32_bf16 v[112:115],a[68:71],a[144:147],v[112:115]\n"
"v_mfma_f32_16x16x32_bf16 v[116:119],a[64:67],a[132:135],v[116:119]\n"
"ds_read_b64_tr_b16 a[42:43],v253 offset:30720\n"
"v_mfma_f32_16x16x32_bf16 v[116:119],a[68:71],a[148:151],v[116:119]\n"
"buffer_load_dwordx4 a[228:231],v209,s[64:67],s40 offen offset:1024 sc1\n"
"v_mfma_f32_16x16x32_bf16 v[120:123],a[64:67],a[136:139],v[120:123]\n"
"ds_read_b64_tr_b16 a[44:45],v253 offset:36864\n"
"v_mfma_f32_16x16x32_bf16 v[120:123],a[68:71],a[152:155],v[120:123]\n"
"v_mfma_f32_16x16x32_bf16 v[124:127],a[64:67],a[140:143],v[124:127]\n"
"ds_read_b64_tr_b16 a[46:47],v253 offset:43008\n"
"v_mfma_f32_16x16x32_bf16 v[124:127],a[68:71],a[156:159],v[124:127]\n"
"v_mfma_f32_16x16x32_bf16 v[128:131],a[72:75],a[128:131],v[128:131]\n"
"ds_read_b64_tr_b16 a[48:49],v252 offset:24832\n"
"v_mfma_f32_16x16x32_bf16 v[128:131],a[76:79],a[144:147],v[128:131]\n"
"buffer_load_dwordx4 a[232:235],v209,s[64:67],s40 offen offset:2048 sc1\n"
"v_mfma_f32_16x16x32_bf16 v[132:135],a[72:75],a[132:135],v[132:135]\n"
"ds_read_b64_tr_b16 a[50:51],v252 offset:30976\n"
"v_mfma_f32_16x16x32_bf16 v[132:135],a[76:79],a[148:151],v[132:135]\n"
"v_mfma_f32_16x16x32_bf16 v[136:139],a[72:75],a[136:139],v[136:139]\n"
"ds_read_b64_tr_b16 a[52:53],v252 offset:37120\n"
"v_mfma_f32_16x16x32_bf16 v[136:139],a[76:79],a[152:155],v[136:139]\n"
"buffer_load_dwordx4 a[236:239],v209,s[64:67],s40 offen offset:3072 sc1\n"
"v_mfma_f32_16x16x32_bf16 v[140:143],a[72:75],a[140:143],v[140:143]\n"
"ds_read_b64_tr_b16 a[54:55],v252 offset:43264\n"
"v_mfma_f32_16x16x32_bf16 v[140:143],a[76:79],a[156:159],v[140:143]\n"
"s_add_u32 s40,s40,4096\n"
"s_waitcnt lgkmcnt(8)\n"
"v_mfma_f32_16x16x32_bf16 v[0:3],a[32:35],a[160:163],v[0:3]\n"
"ds_read_b64_tr_b16 a[56:57],v253 offset:24832\n"
"v_mfma_f32_16x16x32_bf16 v[0:3],a[36:39],a[176:179],v[0:3]\n"
"v_mfma_f32_16x16x32_bf16 v[4:7],a[32:35],a[164:167],v[4:7]\n"
"s_add_u32 m0,s51,65536\n"
"ds_read_b64_tr_b16 a[58:59],v253 offset:30976\n"
"v_mfma_f32_16x16x32_bf16 v[4:7],a[36:39],a[180:183],v[4:7]\n"
"buffer_load_dwordx4 v216,s[72:75],s39 offen lds\n"
"v_mfma_f32_16x16x32_bf16 v[8:11],a[32:35],a[168:171],v[8:11]\n"
"ds_read_b64_tr_b16 a[60:61],v253 offset:37120\n"
"v_mfma_f32_16x16x32_bf16 v[8:11],a[36:39],a[184:187],v[8:11]\n"
"v_mfma_f32_16x16x32_bf16 v[12:15],a[32:35],a[172:175],v[12:15]\n"
"ds_read_b64_tr_b16 a[62:63],v253 offset:43264\n"
"v_mfma_f32_16x16x32_bf16 v[12:15],a[36:39],a[188:191],v[12:15]\n"
"buffer_load_dwordx4 v217,s[72:75],s39 offen offset:1024 lds\n"
"s_waitcnt lgkmcnt(8)\n"
"v_mfma_f32_16x16x32_bf16 v[16:19],a[40:43],a[160:163],v[16:19]\n"
"ds_read_b64_tr_b16 a[64:65],v252 offset:25088\n"
"v_mfma_f32_16x16x32_bf16 v[16:19],a[44:47],a[176:179],v[16:19]\n"
"v_mfma_f32_16x16x32_bf16 v[20:23],a[40:43],a[164:167],v[20:23]\n"
"ds_read_b64_tr_b16 a[66:67],v252 offset:31232\n"
"v_mfma_f32_16x16x32_bf16 v[20:23],a[44:47],a[180:183],v[20:23]\n"
"v_mfma_f32_16x16x32_bf16 v[24:27],a[40:43],a[168:171],v[24:27]\n"
"ds_read_b64_tr_b16 a[68:69],v252 offset:37376\n"
"v_mfma_f32_16x16x32_bf16 v[24:27],a[44:47],a[184:187],v[24:27]\n"
"buffer_load_dwordx4 v218,s[72:75],s39 offen offset:2048 lds\n"
"v_mfma_f32_16x16x32_bf16 v[28:31],a[40:43],a[172:175],v[28:31]\n"
"ds_read_b64_tr_b16 a[70:71],v252 offset:43520\n"
"v_mfma_f32_16x16x32_bf16 v[28:31],a[44:47],a[188:191],v[28:31]\n"
"s_waitcnt lgkmcnt(8)\n"
"v_mfma_f32_16x16x32_bf16 v[32:35],a[48:51],a[160:163],v[32:35]\n"
"ds_read_b64_tr_b16 a[72:73],v253 offset:25088\n"
"v_mfma_f32_16x16x32_bf16 v[32:35],a[52:55],a[176:179],v[32:35]\n"
"v_mfma_f32_16x16x32_bf16 v[36:39],a[48:51],a[164:167],v[36:39]\n"
"s_add_u32 s39,s39,s52\n"
"s_add_u32 m0,s51,68608\n"
"ds_read_b64_tr_b16 a[74:75],v253 offset:31232\n"
"v_mfma_f32_16x16x32_bf16 v[36:39],a[52:55],a[180:183],v[36:39]\n"
"buffer_load_dwordx4 v216,s[72:75],s39 offen lds\n"
"v_mfma_f32_16x16x32_bf16 v[40:43],a[48:51],a[168:171],v[40:43]\n"
"ds_read_b64_tr_b16 a[76:77],v253 offset:37376\n"
"v_mfma_f32_16x16x32_bf16 v[40:43],a[52:55],a[184:187],v[40:43]\n"
"v_mfma_f32_16x16x32_bf16 v[44:47],a[48:51],a[172:175],v[44:47]\n"
"ds_read_b64_tr_b16 a[78:79],v253 offset:43520\n"
"v_mfma_f32_16x16x32_bf16 v[44:47],a[52:55],a[188:191],v[44:47]\n"
"buffer_load_dwordx4 v217,s[72:75],s39 offen offset:1024 lds\n"
"s_waitcnt lgkmcnt(8)\n"
"v_mfma_f32_16x16x32_bf16 v[48:51],a[56:59],a[160:163],v[48:51]\n"
"ds_read_b64_tr_b16 a[32:33],v252 offset:25344\n"
"v_mfma_f32_16x16x32_bf16 v[48:51],a[60:63],a[176:179],v[48:51]\n"
"v_mfma_f32_16x16x32_bf16 v[52:55],a[56:59],a[164:167],v[52:55]\n"
"ds_read_b64_tr_b16 a[34:35],v252 offset:31488\n"
"v_mfma_f32_16x16x32_bf16 v[52:55],a[60:63],a[180:183],v[52:55]\n"
"v_mfma_f32_16x16x32_bf16 v[56:59],a[56:59],a[168:171],v[56:59]\n"
"ds_read_b64_tr_b16 a[36:37],v252 offset:37632\n"
"v_mfma_f32_16x16x32_bf16 v[56:59],a[60:63],a[184:187],v[56:59]\n"
"buffer_load_dwordx4 v218,s[72:75],s39 offen offset:2048 lds\n"
"v_mfma_f32_16x16x32_bf16 v[60:63],a[56:59],a[172:175],v[60:63]\n"
"ds_read_b64_tr_b16 a[38:39],v252 offset:43776\n"
"v_mfma_f32_16x16x32_bf16 v[60:63],a[60:63],a[188:191],v[60:63]\n"
"s_waitcnt lgkmcnt(8)\n"
"v_mfma_f32_16x16x32_bf16 v[64:67],a[64:67],a[160:163],v[64:67]\n"
"s_add_u32 s39,s39,s53\n"
"ds_read_b64_tr_b16 a[40:41],v253 offset:25344\n"
"v_mfma_f32_16x16x32_bf16 v[64:67],a[68:71],a[176:179],v[64:67]\n"
"buffer_load_dwordx4 a[128:131],v209,s[64:67],s40 offen sc1\n"
"v_mfma_f32_16x16x32_bf16 v[68:71],a[64:67],a[164:167],v[68:71]\n"
"ds_read_b64_tr_b16 a[42:43],v253 offset:31488\n"
"v_mfma_f32_16x16x32_bf16 v[68:71],a[68:71],a[180:183],v[68:71]\n"
"v_mfma_f32_16x16x32_bf16 v[72:75],a[64:67],a[168:171],v[72:75]\n"
"ds_read_b64_tr_b16 a[44:45],v253 offset:37632\n"
"v_mfma_f32_16x16x32_bf16 v[72:75],a[68:71],a[184:187],v[72:75]\n"
"v_mfma_f32_16x16x32_bf16 v[76:79],a[64:67],a[172:175],v[76:79]\n"
"ds_read_b64_tr_b16 a[46:47],v253 offset:43776\n"
"v_mfma_f32_16x16x32_bf16 v[76:79],a[68:71],a[188:191],v[76:79]\n"
"buffer_load_dwordx4 a[132:135],v209,s[64:67],s40 offen offset:1024 sc1\n"
"s_waitcnt lgkmcnt(8)\n"
"v_mfma_f32_16x16x32_bf16 v[80:83],a[72:75],a[160:163],v[80:83]\n"
"ds_read_b64_tr_b16 a[48:49],v252 offset:25600\n"
"v_mfma_f32_16x16x32_bf16 v[80:83],a[76:79],a[176:179],v[80:83]\n"
"ds_read_b64_tr_b16 a[50:51],v252 offset:31744\n"
"v_mfma_f32_16x16x32_bf16 v[84:87],a[72:75],a[164:167],v[84:87]\n"
"ds_read_b64_tr_b16 a[52:53],v252 offset:37888\n"
"v_mfma_f32_16x16x32_bf16 v[84:87],a[76:79],a[180:183],v[84:87]\n"
"ds_read_b64_tr_b16 a[54:55],v252 offset:44032\n"
"buffer_load_dwordx4 a[136:139],v209,s[64:67],s40 offen offset:2048 sc1\n"
"v_mfma_f32_16x16x32_bf16 v[88:91],a[72:75],a[168:171],v[88:91]\n"
"v_mfma_f32_16x16x32_bf16 v[88:91],a[76:79],a[184:187],v[88:91]\n"
"v_mfma_f32_16x16x32_bf16 v[92:95],a[72:75],a[172:175],v[92:95]\n"
"v_mfma_f32_16x16x32_bf16 v[92:95],a[76:79],a[188:191],v[92:95]\n"
"s_waitcnt vmcnt(23) lgkmcnt(0)\n"
"s_barrier\n"
"v_mfma_f32_16x16x32_bf16 v[96:99],a[32:35],a[160:163],v[96:99]\n"
"ds_read_b64_tr_b16 a[56:57],v254\n"
"v_mfma_f32_16x16x32_bf16 v[96:99],a[36:39],a[176:179],v[96:99]\n"
"buffer_load_dwordx4 a[140:143],v209,s[64:67],s40 offen offset:3072 sc1\n"
"v_mfma_f32_16x16x32_bf16 v[100:103],a[32:35],a[164:167],v[100:103]\n"
"ds_read_b64_tr_b16 a[58:59],v254 offset:6144\n"
"v_mfma_f32_16x16x32_bf16 v[100:103],a[36:39],a[180:183],v[100:103]\n"
"v_mfma_f32_16x16x32_bf16 v[104:107],a[32:35],a[168:171],v[104:107]\n"
"ds_read_b64_tr_b16 a[60:61],v254 offset:12288\n"
"v_mfma_f32_16x16x32_bf16 v[104:107],a[36:39],a[184:187],v[104:107]\n"
"v_mfma_f32_16x16x32_bf16 v[108:111],a[32:35],a[172:175],v[108:111]\n"
"s_add_u32 s40,s40,4096\n"
"ds_read_b64_tr_b16 a[62:63],v254 offset:18432\n"
"v_mfma_f32_16x16x32_bf16 v[108:111],a[36:39],a[188:191],v[108:111]\n"
"buffer_load_dwordx4 a[144:147],v209,s[64:67],s40 offen sc1\n"
"v_mfma_f32_16x16x32_bf16 v[112:115],a[40:43],a[160:163],v[112:115]\n"
"ds_read_b64_tr_b16 a[64:65],v255\n"
"v_mfma_f32_16x16x32_bf16 v[112:115],a[44:47],a[176:179],v[112:115]\n"
"v_mfma_f32_16x16x32_bf16 v[116:119],a[40:43],a[164:167],v[116:119]\n"
"ds_read_b64_tr_b16 a[66:67],v255 offset:6144\n"
"v_mfma_f32_16x16x32_bf16 v[116:119],a[44:47],a[180:183],v[116:119]\n"
"buffer_load_dwordx4 a[148:151],v209,s[64:67],s40 offen offset:1024 sc1\n"
"v_mfma_f32_16x16x32_bf16 v[120:123],a[40:43],a[168:171],v[120:123]\n"
"ds_read_b64_tr_b16 a[68:69],v255 offset:12288\n"
"v_mfma_f32_16x16x32_bf16 v[120:123],a[44:47],a[184:187],v[120:123]\n"
"v_mfma_f32_16x16x32_bf16 v[124:127],a[40:43],a[172:175],v[124:127]\n"
"ds_read_b64_tr_b16 a[70:71],v255 offset:18432\n"
"v_mfma_f32_16x16x32_bf16 v[124:127],a[44:47],a[188:191],v[124:127]\n"
"v_mfma_f32_16x16x32_bf16 v[128:131],a[48:51],a[160:163],v[128:131]\n"
"ds_read_b64_tr_b16 a[72:73],v254 offset:256\n"
"v_mfma_f32_16x16x32_bf16 v[128:131],a[52:55],a[176:179],v[128:131]\n"
"buffer_load_dwordx4 a[152:155],v209,s[64:67],s40 offen offset:2048 sc1\n"
"v_mfma_f32_16x16x32_bf16 v[132:135],a[48:51],a[164:167],v[132:135]\n"
"ds_read_b64_tr_b16 a[74:75],v254 offset:6400\n"
"v_mfma_f32_16x16x32_bf16 v[132:135],a[52:55],a[180:183],v[132:135]\n"
"v_mfma_f32_16x16x32_bf16 v[136:139],a[48:51],a[168:171],v[136:139]\n"
"ds_read_b64_tr_b16 a[76:77],v254 offset:12544\n"
"v_mfma_f32_16x16x32_bf16 v[136:139],a[52:55],a[184:187],v[136:139]\n"
"buffer_load_dwordx4 a[156:159],v209,s[64:67],s40 offen offset:3072 sc1\n"
"v_mfma_f32_16x16x32_bf16 v[140:143],a[48:51],a[172:175],v[140:143]\n"
"ds_read_b64_tr_b16 a[78:79],v254 offset:18688\n"
"v_mfma_f32_16x16x32_bf16 v[140:143],a[52:55],a[188:191],v[140:143]\n"
"s_add_u32 s40,s40,4096\n"
"s_waitcnt lgkmcnt(8)\n"
"v_mfma_f32_16x16x32_bf16 v[0:3],a[56:59],a[0:3],v[0:3]\n"
"ds_read_b64_tr_b16 a[32:33],v255 offset:256\n"
"v_mfma_f32_16x16x32_bf16 v[0:3],a[60:63],a[16:19],v[0:3]\n"
"v_mfma_f32_16x16x32_bf16 v[4:7],a[56:59],a[4:7],v[4:7]\n"
"s_add_u32 m0,s51,90112\n"
"ds_read_b64_tr_b16 a[34:35],v255 offset:6400\n"
"v_mfma_f32_16x16x32_bf16 v[4:7],a[60:63],a[20:23],v[4:7]\n"
"buffer_load_dwordx4 v216,s[72:75],s39 offen lds\n"
"v_mfma_f32_16x16x32_bf16 v[8:11],a[56:59],a[8:11],v[8:11]\n"
"ds_read_b64_tr_b16 a[36:37],v255 offset:12544\n"
"v_mfma_f32_16x16x32_bf16 v[8:11],a[60:63],a[24:27],v[8:11]\n"
"v_mfma_f32_16x16x32_bf16 v[12:15],a[56:59],a[12:15],v[12:15]\n"
"ds_read_b64_tr_b16 a[38:39],v255 offset:18688\n"
"v_mfma_f32_16x16x32_bf16 v[12:15],a[60:63],a[28:31],v[12:15]\n"
"buffer_load_dwordx4 v217,s[72:75],s39 offen offset:1024 lds\n"
"s_waitcnt lgkmcnt(8)\n"
"v_mfma_f32_16x16x32_bf16 v[16:19],a[64:67],a[0:3],v[16:19]\n"
"ds_read_b64_tr_b16 a[40:41],v254 offset:512\n"
"v_mfma_f32_16x16x32_bf16 v[16:19],a[68:71],a[16:19],v[16:19]\n"
"v_mfma_f32_16x16x32_bf16 v[20:23],a[64:67],a[4:7],v[20:23]\n"
"ds_read_b64_tr_b16 a[42:43],v254 offset:6656\n"
"v_mfma_f32_16x16x32_bf16 v[20:23],a[68:71],a[20:23],v[20:23]\n"
"v_mfma_f32_16x16x32_bf16 v[24:27],a[64:67],a[8:11],v[24:27]\n"
"ds_read_b64_tr_b16 a[44:45],v254 offset:12800\n"
"v_mfma_f32_16x16x32_bf16 v[24:27],a[68:71],a[24:27],v[24:27]\n"
"buffer_load_dwordx4 v218,s[72:75],s39 offen offset:2048 lds\n"
"v_mfma_f32_16x16x32_bf16 v[28:31],a[64:67],a[12:15],v[28:31]\n"
"ds_read_b64_tr_b16 a[46:47],v254 offset:18944\n"
"v_mfma_f32_16x16x32_bf16 v[28:31],a[68:71],a[28:31],v[28:31]\n"
"s_waitcnt lgkmcnt(8)\n"
"v_mfma_f32_16x16x32_bf16 v[32:35],a[72:75],a[0:3],v[32:35]\n"
"ds_read_b64_tr_b16 a[48:49],v255 offset:512\n"
"v_mfma_f32_16x16x32_bf16 v[32:35],a[76:79],a[16:19],v[32:35]\n"
"v_mfma_f32_16x16x32_bf16 v[36:39],a[72:75],a[4:7],v[36:39]\n"
"s_add_u32 s39,s39,s52\n"
"s_add_u32 m0,s51,93184\n"
"ds_read_b64_tr_b16 a[50:51],v255 offset:6656\n"
"v_mfma_f32_16x16x32_bf16 v[36:39],a[76:79],a[20:23],v[36:39]\n"
"buffer_load_dwordx4 v216,s[72:75],s39 offen lds\n"
"v_mfma_f32_16x16x32_bf16 v[40:43],a[72:75],a[8:11],v[40:43]\n"
"ds_read_b64_tr_b16 a[52:53],v255 offset:12800\n"
"v_mfma_f32_16x16x32_bf16 v[40:43],a[76:79],a[24:27],v[40:43]\n"
"v_mfma_f32_16x16x32_bf16 v[44:47],a[72:75],a[12:15],v[44:47]\n"
"ds_read_b64_tr_b16 a[54:55],v255 offset:18944\n"
"v_mfma_f32_16x16x32_bf16 v[44:47],a[76:79],a[28:31],v[44:47]\n"
"buffer_load_dwordx4 v217,s[72:75],s39 offen offset:1024 lds\n"
"s_waitcnt lgkmcnt(8)\n"
"v_mfma_f32_16x16x32_bf16 v[48:51],a[32:35],a[0:3],v[48:51]\n"
"ds_read_b64_tr_b16 a[56:57],v254 offset:768\n"
"v_mfma_f32_16x16x32_bf16 v[48:51],a[36:39],a[16:19],v[48:51]\n"
"v_mfma_f32_16x16x32_bf16 v[52:55],a[32:35],a[4:7],v[52:55]\n"
"ds_read_b64_tr_b16 a[58:59],v254 offset:6912\n"
"v_mfma_f32_16x16x32_bf16 v[52:55],a[36:39],a[20:23],v[52:55]\n"
"v_mfma_f32_16x16x32_bf16 v[56:59],a[32:35],a[8:11],v[56:59]\n"
"ds_read_b64_tr_b16 a[60:61],v254 offset:13056\n"
"v_mfma_f32_16x16x32_bf16 v[56:59],a[36:39],a[24:27],v[56:59]\n"
"buffer_load_dwordx4 v218,s[72:75],s39 offen offset:2048 lds\n"
"v_mfma_f32_16x16x32_bf16 v[60:63],a[32:35],a[12:15],v[60:63]\n"
"ds_read_b64_tr_b16 a[62:63],v254 offset:19200\n"
"v_mfma_f32_16x16x32_bf16 v[60:63],a[36:39],a[28:31],v[60:63]\n"
"s_waitcnt lgkmcnt(8)\n"
"v_mfma_f32_16x16x32_bf16 v[64:67],a[40:43],a[0:3],v[64:67]\n"
"s_add_u32 s39,s39,s53\n"
"ds_read_b64_tr_b16 a[64:65],v255 offset:768\n"
"v_mfma_f32_16x16x32_bf16 v[64:67],a[44:47],a[16:19],v[64:67]\n"
"buffer_load_dwordx4 a[160:163],v209,s[64:67],s40 offen sc1\n"
"v_mfma_f32_16x16x32_bf16 v[68:71],a[40:43],a[4:7],v[68:71]\n"
"ds_read_b64_tr_b16 a[66:67],v255 offset:6912\n"
"v_mfma_f32_16x16x32_bf16 v[68:71],a[44:47],a[20:23],v[68:71]\n"
"v_mfma_f32_16x16x32_bf16 v[72:75],a[40:43],a[8:11],v[72:75]\n"
"ds_read_b64_tr_b16 a[68:69],v255 offset:13056\n"
"v_mfma_f32_16x16x32_bf16 v[72:75],a[44:47],a[24:27],v[72:75]\n"
"v_mfma_f32_16x16x32_bf16 v[76:79],a[40:43],a[12:15],v[76:79]\n"
"ds_read_b64_tr_b16 a[70:71],v255 offset:19200\n"
"v_mfma_f32_16x16x32_bf16 v[76:79],a[44:47],a[28:31],v[76:79]\n"
"buffer_load_dwordx4 a[164:167],v209,s[64:67],s40 offen offset:1024 sc1\n"
"s_waitcnt lgkmcnt(8)\n"
"v_mfma_f32_16x16x32_bf16 v[80:83],a[48:51],a[0:3],v[80:83]\n"
"ds_read_b64_tr_b16 a[72:73],v254 offset:1024\n"
"v_mfma_f32_16x16x32_bf16 v[80:83],a[52:55],a[16:19],v[80:83]\n"
"ds_read_b64_tr_b16 a[74:75],v254 offset:7168\n"
"v_mfma_f32_16x16x32_bf16 v[84:87],a[48:51],a[4:7],v[84:87]\n"
"ds_read_b64_tr_b16 a[76:77],v254 offset:13312\n"
"v_mfma_f32_16x16x32_bf16 v[84:87],a[52:55],a[20:23],v[84:87]\n"
"ds_read_b64_tr_b16 a[78:79],v254 offset:19456\n"
"buffer_load_dwordx4 a[168:171],v209,s[64:67],s40 offen offset:2048 sc1\n"
"v_mfma_f32_16x16x32_bf16 v[88:91],a[48:51],a[8:11],v[88:91]\n"
"v_mfma_f32_16x16x32_bf16 v[88:91],a[52:55],a[24:27],v[88:91]\n"
"v_mfma_f32_16x16x32_bf16 v[92:95],a[48:51],a[12:15],v[92:95]\n"
"v_mfma_f32_16x16x32_bf16 v[92:95],a[52:55],a[28:31],v[92:95]\n"
"s_waitcnt vmcnt(23) lgkmcnt(0)\n"
"s_barrier\n"
"v_mfma_f32_16x16x32_bf16 v[96:99],a[56:59],a[0:3],v[96:99]\n"
"ds_read_b64_tr_b16 a[32:33],v254 offset:24576\n"
"v_mfma_f32_16x16x32_bf16 v[96:99],a[60:63],a[16:19],v[96:99]\n"
"buffer_load_dwordx4 a[172:175],v209,s[64:67],s40 offen offset:3072 sc1\n"
"v_mfma_f32_16x16x32_bf16 v[100:103],a[56:59],a[4:7],v[100:103]\n"
"ds_read_b64_tr_b16 a[34:35],v254 offset:30720\n"
"v_mfma_f32_16x16x32_bf16 v[100:103],a[60:63],a[20:23],v[100:103]\n"
"v_mfma_f32_16x16x32_bf16 v[104:107],a[56:59],a[8:11],v[104:107]\n"
"ds_read_b64_tr_b16 a[36:37],v254 offset:36864\n"
"v_mfma_f32_16x16x32_bf16 v[104:107],a[60:63],a[24:27],v[104:107]\n"
"v_mfma_f32_16x16x32_bf16 v[108:111],a[56:59],a[12:15],v[108:111]\n"
"s_add_u32 s40,s40,4096\n"
"ds_read_b64_tr_b16 a[38:39],v254 offset:43008\n"
"v_mfma_f32_16x16x32_bf16 v[108:111],a[60:63],a[28:31],v[108:111]\n"
"buffer_load_dwordx4 a[176:179],v209,s[64:67],s40 offen sc1\n"
"v_mfma_f32_16x16x32_bf16 v[112:115],a[64:67],a[0:3],v[112:115]\n"
"ds_read_b64_tr_b16 a[40:41],v255 offset:24576\n"
"v_mfma_f32_16x16x32_bf16 v[112:115],a[68:71],a[16:19],v[112:115]\n"
"v_mfma_f32_16x16x32_bf16 v[116:119],a[64:67],a[4:7],v[116:119]\n"
"ds_read_b64_tr_b16 a[42:43],v255 offset:30720\n"
"v_mfma_f32_16x16x32_bf16 v[116:119],a[68:71],a[20:23],v[116:119]\n"
"buffer_load_dwordx4 a[180:183],v209,s[64:67],s40 offen offset:1024 sc1\n"
"v_mfma_f32_16x16x32_bf16 v[120:123],a[64:67],a[8:11],v[120:123]\n"
"ds_read_b64_tr_b16 a[44:45],v255 offset:36864\n"
"v_mfma_f32_16x16x32_bf16 v[120:123],a[68:71],a[24:27],v[120:123]\n"
"v_mfma_f32_16x16x32_bf16 v[124:127],a[64:67],a[12:15],v[124:127]\n"
"ds_read_b64_tr_b16 a[46:47],v255 offset:43008\n"
"v_mfma_f32_16x16x32_bf16 v[124:127],a[68:71],a[28:31],v[124:127]\n"
"v_mfma_f32_16x16x32_bf16 v[128:131],a[72:75],a[0:3],v[128:131]\n"
"ds_read_b64_tr_b16 a[48:49],v254 offset:24832\n"
"v_mfma_f32_16x16x32_bf16 v[128:131],a[76:79],a[16:19],v[128:131]\n"
"buffer_load_dwordx4 a[184:187],v209,s[64:67],s40 offen offset:2048 sc1\n"
"v_mfma_f32_16x16x32_bf16 v[132:135],a[72:75],a[4:7],v[132:135]\n"
"ds_read_b64_tr_b16 a[50:51],v254 offset:30976\n"
"v_mfma_f32_16x16x32_bf16 v[132:135],a[76:79],a[20:23],v[132:135]\n"
"v_mfma_f32_16x16x32_bf16 v[136:139],a[72:75],a[8:11],v[136:139]\n"
"ds_read_b64_tr_b16 a[52:53],v254 offset:37120\n"
"v_mfma_f32_16x16x32_bf16 v[136:139],a[76:79],a[24:27],v[136:139]\n"
"buffer_load_dwordx4 a[188:191],v209,s[64:67],s40 offen offset:3072 sc1\n"
"v_mfma_f32_16x16x32_bf16 v[140:143],a[72:75],a[12:15],v[140:143]\n"
"ds_read_b64_tr_b16 a[54:55],v254 offset:43264\n"
"v_mfma_f32_16x16x32_bf16 v[140:143],a[76:79],a[28:31],v[140:143]\n"
"s_add_u32 s40,s40,4096\n"
"s_waitcnt lgkmcnt(8)\n"
"v_mfma_f32_16x16x32_bf16 v[0:3],a[32:35],a[208:211],v[0:3]\n"
"ds_read_b64_tr_b16 a[56:57],v255 offset:24832\n"
"v_mfma_f32_16x16x32_bf16 v[0:3],a[36:39],a[224:227],v[0:3]\n"
"v_mfma_f32_16x16x32_bf16 v[4:7],a[32:35],a[212:215],v[4:7]\n"
"s_add_u32 m0,s51,114688\n"
"ds_read_b64_tr_b16 a[58:59],v255 offset:30976\n"
"v_mfma_f32_16x16x32_bf16 v[4:7],a[36:39],a[228:231],v[4:7]\n"
"buffer_load_dwordx4 v216,s[72:75],s39 offen lds\n"
"v_mfma_f32_16x16x32_bf16 v[8:11],a[32:35],a[216:219],v[8:11]\n"
"ds_read_b64_tr_b16 a[60:61],v255 offset:37120\n"
"v_mfma_f32_16x16x32_bf16 v[8:11],a[36:39],a[232:235],v[8:11]\n"
"v_mfma_f32_16x16x32_bf16 v[12:15],a[32:35],a[220:223],v[12:15]\n"
"ds_read_b64_tr_b16 a[62:63],v255 offset:43264\n"
"v_mfma_f32_16x16x32_bf16 v[12:15],a[36:39],a[236:239],v[12:15]\n"
"buffer_load_dwordx4 v217,s[72:75],s39 offen offset:1024 lds\n"
"s_waitcnt lgkmcnt(8)\n"
"v_mfma_f32_16x16x32_bf16 v[16:19],a[40:43],a[208:211],v[16:19]\n"
"ds_read_b64_tr_b16 a[64:65],v254 offset:25088\n"
"v_mfma_f32_16x16x32_bf16 v[16:19],a[44:47],a[224:227],v[16:19]\n"
"v_mfma_f32_16x16x32_bf16 v[20:23],a[40:43],a[212:215],v[20:23]\n"
"ds_read_b64_tr_b16 a[66:67],v254 offset:31232\n"
"v_mfma_f32_16x16x32_bf16 v[20:23],a[44:47],a[228:231],v[20:23]\n"
"v_mfma_f32_16x16x32_bf16 v[24:27],a[40:43],a[216:219],v[24:27]\n"
"ds_read_b64_tr_b16 a[68:69],v254 offset:37376\n"
"v_mfma_f32_16x16x32_bf16 v[24:27],a[44:47],a[232:235],v[24:27]\n"
"buffer_load_dwordx4 v218,s[72:75],s39 offen offset:2048 lds\n"
"v_mfma_f32_16x16x32_bf16 v[28:31],a[40:43],a[220:223],v[28:31]\n"
"ds_read_b64_tr_b16 a[70:71],v254 offset:43520\n"
"v_mfma_f32_16x16x32_bf16 v[28:31],a[44:47],a[236:239],v[28:31]\n"
"s_waitcnt lgkmcnt(8)\n"
"v_mfma_f32_16x16x32_bf16 v[32:35],a[48:51],a[208:211],v[32:35]\n"
"ds_read_b64_tr_b16 a[72:73],v255 offset:25088\n"
"v_mfma_f32_16x16x32_bf16 v[32:35],a[52:55],a[224:227],v[32:35]\n"
"v_mfma_f32_16x16x32_bf16 v[36:39],a[48:51],a[212:215],v[36:39]\n"
"s_add_u32 s39,s39,s52\n"
"s_add_u32 m0,s51,117760\n"
"ds_read_b64_tr_b16 a[74:75],v255 offset:31232\n"
"v_mfma_f32_16x16x32_bf16 v[36:39],a[52:55],a[228:231],v[36:39]\n"
"buffer_load_dwordx4 v216,s[72:75],s39 offen lds\n"
"v_mfma_f32_16x16x32_bf16 v[40:43],a[48:51],a[216:219],v[40:43]\n"
"ds_read_b64_tr_b16 a[76:77],v255 offset:37376\n"
"v_mfma_f32_16x16x32_bf16 v[40:43],a[52:55],a[232:235],v[40:43]\n"
"v_mfma_f32_16x16x32_bf16 v[44:47],a[48:51],a[220:223],v[44:47]\n"
"ds_read_b64_tr_b16 a[78:79],v255 offset:43520\n"
"v_mfma_f32_16x16x32_bf16 v[44:47],a[52:55],a[236:239],v[44:47]\n"
"buffer_load_dwordx4 v217,s[72:75],s39 offen offset:1024 lds\n"
"s_waitcnt lgkmcnt(8)\n"
"v_mfma_f32_16x16x32_bf16 v[48:51],a[56:59],a[208:211],v[48:51]\n"
"ds_read_b64_tr_b16 a[32:33],v254 offset:25344\n"
"v_mfma_f32_16x16x32_bf16 v[48:51],a[60:63],a[224:227],v[48:51]\n"
"v_mfma_f32_16x16x32_bf16 v[52:55],a[56:59],a[212:215],v[52:55]\n"
"ds_read_b64_tr_b16 a[34:35],v254 offset:31488\n"
"v_mfma_f32_16x16x32_bf16 v[52:55],a[60:63],a[228:231],v[52:55]\n"
"v_mfma_f32_16x16x32_bf16 v[56:59],a[56:59],a[216:219],v[56:59]\n"
"ds_read_b64_tr_b16 a[36:37],v254 offset:37632\n"
"v_mfma_f32_16x16x32_bf16 v[56:59],a[60:63],a[232:235],v[56:59]\n"
"buffer_load_dwordx4 v218,s[72:75],s39 offen offset:2048 lds\n"
"v_mfma_f32_16x16x32_bf16 v[60:63],a[56:59],a[220:223],v[60:63]\n"
"ds_read_b64_tr_b16 a[38:39],v254 offset:43776\n"
"v_mfma_f32_16x16x32_bf16 v[60:63],a[60:63],a[236:239],v[60:63]\n"
"s_waitcnt lgkmcnt(8)\n"
"v_mfma_f32_16x16x32_bf16 v[64:67],a[64:67],a[208:211],v[64:67]\n"
"s_add_u32 s39,s39,s53\n"
"ds_read_b64_tr_b16 a[40:41],v255 offset:25344\n"
"v_mfma_f32_16x16x32_bf16 v[64:67],a[68:71],a[224:227],v[64:67]\n"
"buffer_load_dwordx4 a[0:3],v209,s[64:67],s40 offen sc1\n"
"v_mfma_f32_16x16x32_bf16 v[68:71],a[64:67],a[212:215],v[68:71]\n"
"ds_read_b64_tr_b16 a[42:43],v255 offset:31488\n"
"v_mfma_f32_16x16x32_bf16 v[68:71],a[68:71],a[228:231],v[68:71]\n"
"v_mfma_f32_16x16x32_bf16 v[72:75],a[64:67],a[216:219],v[72:75]\n"
"ds_read_b64_tr_b16 a[44:45],v255 offset:37632\n"
"v_mfma_f32_16x16x32_bf16 v[72:75],a[68:71],a[232:235],v[72:75]\n"
"v_mfma_f32_16x16x32_bf16 v[76:79],a[64:67],a[220:223],v[76:79]\n"
"ds_read_b64_tr_b16 a[46:47],v255 offset:43776\n"
"v_mfma_f32_16x16x32_bf16 v[76:79],a[68:71],a[236:239],v[76:79]\n"
"buffer_load_dwordx4 a[4:7],v209,s[64:67],s40 offen offset:1024 sc1\n"
"s_waitcnt lgkmcnt(8)\n"
"v_mfma_f32_16x16x32_bf16 v[80:83],a[72:75],a[208:211],v[80:83]\n"
"ds_read_b64_tr_b16 a[48:49],v254 offset:25600\n"
"v_mfma_f32_16x16x32_bf16 v[80:83],a[76:79],a[224:227],v[80:83]\n"
"ds_read_b64_tr_b16 a[50:51],v254 offset:31744\n"
"v_mfma_f32_16x16x32_bf16 v[84:87],a[72:75],a[212:215],v[84:87]\n"
"ds_read_b64_tr_b16 a[52:53],v254 offset:37888\n"
"v_mfma_f32_16x16x32_bf16 v[84:87],a[76:79],a[228:231],v[84:87]\n"
"ds_read_b64_tr_b16 a[54:55],v254 offset:44032\n"
"buffer_load_dwordx4 a[8:11],v209,s[64:67],s40 offen offset:2048 sc1\n"
"v_mfma_f32_16x16x32_bf16 v[88:91],a[72:75],a[216:219],v[88:91]\n"
"v_mfma_f32_16x16x32_bf16 v[88:91],a[76:79],a[232:235],v[88:91]\n"
"v_mfma_f32_16x16x32_bf16 v[92:95],a[72:75],a[220:223],v[92:95]\n"
"v_mfma_f32_16x16x32_bf16 v[92:95],a[76:79],a[236:239],v[92:95]\n"
"s_waitcnt vmcnt(23) lgkmcnt(0)\n"
"s_barrier\n"
"v_mfma_f32_16x16x32_bf16 v[96:99],a[32:35],a[208:211],v[96:99]\n"
"ds_read_b64_tr_b16 a[56:57],v252\n"
"v_mfma_f32_16x16x32_bf16 v[96:99],a[36:39],a[224:227],v[96:99]\n"
"buffer_load_dwordx4 a[12:15],v209,s[64:67],s40 offen offset:3072 sc1\n"
"v_mfma_f32_16x16x32_bf16 v[100:103],a[32:35],a[212:215],v[100:103]\n"
"ds_read_b64_tr_b16 a[58:59],v252 offset:6144\n"
"v_mfma_f32_16x16x32_bf16 v[100:103],a[36:39],a[228:231],v[100:103]\n"
"v_mfma_f32_16x16x32_bf16 v[104:107],a[32:35],a[216:219],v[104:107]\n"
"ds_read_b64_tr_b16 a[60:61],v252 offset:12288\n"
"v_mfma_f32_16x16x32_bf16 v[104:107],a[36:39],a[232:235],v[104:107]\n"
"v_mfma_f32_16x16x32_bf16 v[108:111],a[32:35],a[220:223],v[108:111]\n"
"s_add_u32 s40,s40,4096\n"
"ds_read_b64_tr_b16 a[62:63],v252 offset:18432\n"
"v_mfma_f32_16x16x32_bf16 v[108:111],a[36:39],a[236:239],v[108:111]\n"
"buffer_load_dwordx4 a[16:19],v209,s[64:67],s40 offen sc1\n"
"v_mfma_f32_16x16x32_bf16 v[112:115],a[40:43],a[208:211],v[112:115]\n"
"ds_read_b64_tr_b16 a[64:65],v253\n"
"v_mfma_f32_16x16x32_bf16 v[112:115],a[44:47],a[224:227],v[112:115]\n"
"v_mfma_f32_16x16x32_bf16 v[116:119],a[40:43],a[212:215],v[116:119]\n"
"ds_read_b64_tr_b16 a[66:67],v253 offset:6144\n"
"v_mfma_f32_16x16x32_bf16 v[116:119],a[44:47],a[228:231],v[116:119]\n"
"buffer_load_dwordx4 a[20:23],v209,s[64:67],s40 offen offset:1024 sc1\n"
"v_mfma_f32_16x16x32_bf16 v[120:123],a[40:43],a[216:219],v[120:123]\n"
"ds_read_b64_tr_b16 a[68:69],v253 offset:12288\n"
"v_mfma_f32_16x16x32_bf16 v[120:123],a[44:47],a[232:235],v[120:123]\n"
"v_mfma_f32_16x16x32_bf16 v[124:127],a[40:43],a[220:223],v[124:127]\n"
"ds_read_b64_tr_b16 a[70:71],v253 offset:18432\n"
"v_mfma_f32_16x16x32_bf16 v[124:127],a[44:47],a[236:239],v[124:127]\n"
"v_mfma_f32_16x16x32_bf16 v[128:131],a[48:51],a[208:211],v[128:131]\n"
"ds_read_b64_tr_b16 a[72:73],v252 offset:256\n"
"v_mfma_f32_16x16x32_bf16 v[128:131],a[52:55],a[224:227],v[128:131]\n"
"buffer_load_dwordx4 a[24:27],v209,s[64:67],s40 offen offset:2048 sc1\n"
"v_mfma_f32_16x16x32_bf16 v[132:135],a[48:51],a[212:215],v[132:135]\n"
"ds_read_b64_tr_b16 a[74:75],v252 offset:6400\n"
"v_mfma_f32_16x16x32_bf16 v[132:135],a[52:55],a[228:231],v[132:135]\n"
"v_mfma_f32_16x16x32_bf16 v[136:139],a[48:51],a[216:219],v[136:139]\n"
"ds_read_b64_tr_b16 a[76:77],v252 offset:12544\n"
"v_mfma_f32_16x16x32_bf16 v[136:139],a[52:55],a[232:235],v[136:139]\n"
"buffer_load_dwordx4 a[28:31],v209,s[64:67],s40 offen offset:3072 sc1\n"
"v_mfma_f32_16x16x32_bf16 v[140:143],a[48:51],a[220:223],v[140:143]\n"
"ds_read_b64_tr_b16 a[78:79],v252 offset:18688\n"
"v_mfma_f32_16x16x32_bf16 v[140:143],a[52:55],a[236:239],v[140:143]\n"
"s_add_u32 s40,s40,4096\n"
"s_waitcnt lgkmcnt(8)\n"
"v_mfma_f32_16x16x32_bf16 v[0:3],a[56:59],a[128:131],v[0:3]\n"
"s_add_u32 m0,s51,139264\n"
"ds_read_b64_tr_b16 a[32:33],v253 offset:256\n"
"v_mfma_f32_16x16x32_bf16 v[0:3],a[60:63],a[144:147],v[0:3]\n"
"buffer_load_dwordx4 v216,s[72:75],s39 offen lds\n"
"v_mfma_f32_16x16x32_bf16 v[4:7],a[56:59],a[132:135],v[4:7]\n"
"ds_read_b64_tr_b16 a[34:35],v253 offset:6400\n"
"v_mfma_f32_16x16x32_bf16 v[4:7],a[60:63],a[148:151],v[4:7]\n"
"v_mfma_f32_16x16x32_bf16 v[8:11],a[56:59],a[136:139],v[8:11]\n"
"ds_read_b64_tr_b16 a[36:37],v253 offset:12544\n"
"v_mfma_f32_16x16x32_bf16 v[8:11],a[60:63],a[152:155],v[8:11]\n"
"buffer_load_dwordx4 v217,s[72:75],s39 offen offset:1024 lds\n"
"v_mfma_f32_16x16x32_bf16 v[12:15],a[56:59],a[140:143],v[12:15]\n"
"ds_read_b64_tr_b16 a[38:39],v253 offset:18688\n"
"v_mfma_f32_16x16x32_bf16 v[12:15],a[60:63],a[156:159],v[12:15]\n"
"buffer_load_dwordx4 v218,s[72:75],s39 offen offset:2048 lds\n"
"s_waitcnt lgkmcnt(8)\n"
"v_mfma_f32_16x16x32_bf16 v[16:19],a[64:67],a[128:131],v[16:19]\n"
"ds_read_b64_tr_b16 a[40:41],v252 offset:512\n"
"v_mfma_f32_16x16x32_bf16 v[16:19],a[68:71],a[144:147],v[16:19]\n"
"v_mfma_f32_16x16x32_bf16 v[20:23],a[64:67],a[132:135],v[20:23]\n"
"s_add_u32 s39,s39,s52\n"
"s_add_u32 m0,s51,142336\n"
"ds_read_b64_tr_b16 a[42:43],v252 offset:6656\n"
"v_mfma_f32_16x16x32_bf16 v[20:23],a[68:71],a[148:151],v[20:23]\n"
"buffer_load_dwordx4 v216,s[72:75],s39 offen lds\n"
"v_mfma_f32_16x16x32_bf16 v[24:27],a[64:67],a[136:139],v[24:27]\n"
"ds_read_b64_tr_b16 a[44:45],v252 offset:12800\n"
"v_mfma_f32_16x16x32_bf16 v[24:27],a[68:71],a[152:155],v[24:27]\n"
"buffer_load_dwordx4 v217,s[72:75],s39 offen offset:1024 lds\n"
"v_mfma_f32_16x16x32_bf16 v[28:31],a[64:67],a[140:143],v[28:31]\n"
"ds_read_b64_tr_b16 a[46:47],v252 offset:18944\n"
"v_mfma_f32_16x16x32_bf16 v[28:31],a[68:71],a[156:159],v[28:31]\n"
"buffer_load_dwordx4 v218,s[72:75],s39 offen offset:2048 lds\n"
"s_waitcnt lgkmcnt(8)\n"
"v_mfma_f32_16x16x32_bf16 v[32:35],a[72:75],a[128:131],v[32:35]\n"
"ds_read_b64_tr_b16 a[48:49],v253 offset:512\n"
"v_mfma_f32_16x16x32_bf16 v[32:35],a[76:79],a[144:147],v[32:35]\n"
"v_mfma_f32_16x16x32_bf16 v[36:39],a[72:75],a[132:135],v[36:39]\n"
"s_add_u32 s39,s39,s53\n"
"ds_read_b64_tr_b16 a[50:51],v253 offset:6656\n"
"v_mfma_f32_16x16x32_bf16 v[36:39],a[76:79],a[148:151],v[36:39]\n"
"buffer_load_dwordx4 a[208:211],v209,s[64:67],s40 offen sc1\n"
"v_mfma_f32_16x16x32_bf16 v[40:43],a[72:75],a[136:139],v[40:43]\n"
"ds_read_b64_tr_b16 a[52:53],v253 offset:12800\n"
"v_mfma_f32_16x16x32_bf16 v[40:43],a[76:79],a[152:155],v[40:43]\n"
"buffer_load_dwordx4 a[212:215],v209,s[64:67],s40 offen offset:1024 sc1\n"
"v_mfma_f32_16x16x32_bf16 v[44:47],a[72:75],a[140:143],v[44:47]\n"
"ds_read_b64_tr_b16 a[54:55],v253 offset:18944\n"
"v_mfma_f32_16x16x32_bf16 v[44:47],a[76:79],a[156:159],v[44:47]\n"
"s_waitcnt lgkmcnt(8)\n"
"v_mfma_f32_16x16x32_bf16 v[48:51],a[32:35],a[128:131],v[48:51]\n"
"ds_read_b64_tr_b16 a[56:57],v252 offset:768\n"
"v_mfma_f32_16x16x32_bf16 v[48:51],a[36:39],a[144:147],v[48:51]\n"
"buffer_load_dwordx4 a[216:219],v209,s[64:67],s40 offen offset:2048 sc1\n"
"v_mfma_f32_16x16x32_bf16 v[52:55],a[32:35],a[132:135],v[52:55]\n"
"ds_read_b64_tr_b16 a[58:59],v252 offset:6912\n"
"v_mfma_f32_16x16x32_bf16 v[52:55],a[36:39],a[148:151],v[52:55]\n"
"buffer_load_dwordx4 a[220:223],v209,s[64:67],s40 offen offset:3072 sc1\n"
"v_mfma_f32_16x16x32_bf16 v[56:59],a[32:35],a[136:139],v[56:59]\n"
"ds_read_b64_tr_b16 a[60:61],v252 offset:13056\n"
"v_mfma_f32_16x16x32_bf16 v[56:59],a[36:39],a[152:155],v[56:59]\n"
"v_mfma_f32_16x16x32_bf16 v[60:63],a[32:35],a[140:143],v[60:63]\n"
"s_add_u32 s40,s40,4096\n"
"ds_read_b64_tr_b16 a[62:63],v252 offset:19200\n"
"v_mfma_f32_16x16x32_bf16 v[60:63],a[36:39],a[156:159],v[60:63]\n"
"buffer_load_dwordx4 a[224:227],v209,s[64:67],s40 offen sc1\n"
"s_waitcnt lgkmcnt(8)\n"
"v_mfma_f32_16x16x32_bf16 v[64:67],a[40:43],a[128:131],v[64:67]\n"
"ds_read_b64_tr_b16 a[64:65],v253 offset:768\n"
"v_mfma_f32_16x16x32_bf16 v[64:67],a[44:47],a[144:147],v[64:67]\n"
"buffer_load_dwordx4 a[228:231],v209,s[64:67],s40 offen offset:1024 sc1\n"
"v_mfma_f32_16x16x32_bf16 v[68:71],a[40:43],a[132:135],v[68:71]\n"
"ds_read_b64_tr_b16 a[66:67],v253 offset:6912\n"
"v_mfma_f32_16x16x32_bf16 v[68:71],a[44:47],a[148:151],v[68:71]\n"
"buffer_load_dwordx4 a[232:235],v209,s[64:67],s40 offen offset:2048 sc1\n"
"v_mfma_f32_16x16x32_bf16 v[72:75],a[40:43],a[136:139],v[72:75]\n"
"ds_read_b64_tr_b16 a[68:69],v253 offset:13056\n"
"v_mfma_f32_16x16x32_bf16 v[72:75],a[44:47],a[152:155],v[72:75]\n"
"v_mfma_f32_16x16x32_bf16 v[76:79],a[40:43],a[140:143],v[76:79]\n"
"ds_read_b64_tr_b16 a[70:71],v253 offset:19200\n"
"v_mfma_f32_16x16x32_bf16 v[76:79],a[44:47],a[156:159],v[76:79]\n"
"buffer_load_dwordx4 a[236:239],v209,s[64:67],s40 offen offset:3072 sc1\n"
"v_readlane_b32 s43,v210,55\n"
"s_nop 0\n"
"s_lshl_b32 s43,s43,6\n"
"v_lshl_add_u32 v223,v209,2,s43\n"
"v_readlane_b32 s46,v210,28\n"
"s_mov_b32 s47,0\n"
"buffer_load_dwordx2 v[226:227],v223,s[68:71],0 offen sc1\n"
"s_waitcnt lgkmcnt(8)\n"
"v_mfma_f32_16x16x32_bf16 v[80:83],a[48:51],a[128:131],v[80:83]\n"
"ds_read_b64_tr_b16 a[72:73],v252 offset:1024\n"
"v_mfma_f32_16x16x32_bf16 v[80:83],a[52:55],a[144:147],v[80:83]\n"
"ds_read_b64_tr_b16 a[74:75],v252 offset:7168\n"
"v_mfma_f32_16x16x32_bf16 v[84:87],a[48:51],a[132:135],v[84:87]\n"
"ds_read_b64_tr_b16 a[76:77],v252 offset:13312\n"
"v_mfma_f32_16x16x32_bf16 v[84:87],a[52:55],a[148:151],v[84:87]\n"
"ds_read_b64_tr_b16 a[78:79],v252 offset:19456\n"
"v_mfma_f32_16x16x32_bf16 v[88:91],a[48:51],a[136:139],v[88:91]\n"
"v_mfma_f32_16x16x32_bf16 v[88:91],a[52:55],a[152:155],v[88:91]\n"
"v_mfma_f32_16x16x32_bf16 v[92:95],a[48:51],a[140:143],v[92:95]\n"
"v_mfma_f32_16x16x32_bf16 v[92:95],a[52:55],a[156:159],v[92:95]\n"
"s_waitcnt vmcnt(29) lgkmcnt(0)\n"
"s_barrier\n"
"v_mfma_f32_16x16x32_bf16 v[96:99],a[56:59],a[128:131],v[96:99]\n"
"v_mfma_f32_16x16x32_bf16 v[96:99],a[60:63],a[144:147],v[96:99]\n"
"v_mfma_f32_16x16x32_bf16 v[100:103],a[56:59],a[132:135],v[100:103]\n"
"v_mfma_f32_16x16x32_bf16 v[100:103],a[60:63],a[148:151],v[100:103]\n"
"v_mfma_f32_16x16x32_bf16 v[104:107],a[56:59],a[136:139],v[104:107]\n"
"v_mfma_f32_16x16x32_bf16 v[104:107],a[60:63],a[152:155],v[104:107]\n"
"v_mfma_f32_16x16x32_bf16 v[108:111],a[56:59],a[140:143],v[108:111]\n"
"v_mfma_f32_16x16x32_bf16 v[108:111],a[60:63],a[156:159],v[108:111]\n"
"v_mfma_f32_16x16x32_bf16 v[112:115],a[64:67],a[128:131],v[112:115]\n"
"v_mfma_f32_16x16x32_bf16 v[112:115],a[68:71],a[144:147],v[112:115]\n"
"v_mfma_f32_16x16x32_bf16 v[116:119],a[64:67],a[132:135],v[116:119]\n"
"v_mfma_f32_16x16x32_bf16 v[116:119],a[68:71],a[148:151],v[116:119]\n"
"v_mfma_f32_16x16x32_bf16 v[120:123],a[64:67],a[136:139],v[120:123]\n"
"v_mfma_f32_16x16x32_bf16 v[120:123],a[68:71],a[152:155],v[120:123]\n"
"v_mfma_f32_16x16x32_bf16 v[124:127],a[64:67],a[140:143],v[124:127]\n"
"v_mfma_f32_16x16x32_bf16 v[124:127],a[68:71],a[156:159],v[124:127]\n"
"v_mfma_f32_16x16x32_bf16 v[128:131],a[72:75],a[128:131],v[128:131]\n"
"v_mfma_f32_16x16x32_bf16 v[128:131],a[76:79],a[144:147],v[128:131]\n"
"v_mfma_f32_16x16x32_bf16 v[132:135],a[72:75],a[132:135],v[132:135]\n"
"v_mfma_f32_16x16x32_bf16 v[132:135],a[76:79],a[148:151],v[132:135]\n"
"v_mfma_f32_16x16x32_bf16 v[136:139],a[72:75],a[136:139],v[136:139]\n"
"v_mfma_f32_16x16x32_bf16 v[136:139],a[76:79],a[152:155],v[136:139]\n"
"v_mfma_f32_16x16x32_bf16 v[140:143],a[72:75],a[140:143],v[140:143]\n"
"v_mfma_f32_16x16x32_bf16 v[140:143],a[76:79],a[156:159],v[140:143]\n"
"s_add_u32 s40,s40,4096\n"
"v_readlane_b32 s72,v210,24\n"
"v_readlane_b32 s73,v210,25\n"
"v_readlane_b32 s74,v210,26\n"
"s_mov_b32 s75,0x20000\n"
"s_mov_b32 s39,s60\n"
"s_mov_b32 s52,9216\n"
"s_mov_b32 s53,64512\n"
"s_nop 2\n"
"s_mov_b32 s55,0x40000\n"
"s_waitcnt vmcnt(0)\n"
"LM_POLL15:\n"
"v_cmp_ne_u64 vcc,v[226:227],v[224:225]\n"
"s_nop 1\n"
"s_and_b64 s[44:45],vcc,s[46:47]\n"
"s_cbranch_scc0 LM_POK16\n"
"s_sleep 1\n"
"buffer_load_dwordx2 v[226:227],v223,s[68:71],0 offen sc1\n"
"s_waitcnt vmcnt(0)\n"
"s_sub_u32 s55,s55,1\n"
"s_cbranch_scc0 LM_POLL15\n"
"LM_POK16:\n"
"v_readlane_b32 s88,v210,21\n"
"v_readlane_b32 s89,v210,22\n"
"v_readlane_b32 s90,v210,23\n"
"s_mov_b32 s91,0x20000\n"
"s_mov_b32 s40,0\n"
"v_mov_b32 v188,v209\n"
"v_add_u32 v189,8,v209\n"
"s_nop 1\n"
"s_waitcnt vmcnt(1)\n"
"s_barrier\n"
"v_readlane_b32 s84,v210,19\n"
"v_readlane_b32 s85,v210,20\n"
"s_mov_b32 s86,0x5000\n"
"s_mov_b32 s87,0x20000\n"
"s_mov_b32 s56,0xbdd2d3e8\n"
"s_mov_b32 s57,0xbdd2d3e8\n"
"v_mov_b32 v184,0xc0135761\n"
"v_mov_b32 v185,0xc0135761\n"
"v_mov_b32 v186,1.0\n"
"v_mov_b32 v187,1.0\n"
"v_readlane_b32 s49,v210,44\n"
"v_lshrrev_b32 v191,1,v209\n"
"s_cmp_eq_u32 s49,0\n"
"s_cbranch_scc0 LM_GODD\n"
"ds_read_b64_tr_b16 a[32:33],v252 offset:24576\n"
"ds_read_b64_tr_b16 a[34:35],v252 offset:30720\n"
"ds_read_b64_tr_b16 a[36:37],v252 offset:36864\n"
"ds_read_b64_tr_b16 a[38:39],v252 offset:43008\n"
"ds_read_b64_tr_b16 a[40:41],v254\n"
"ds_read_b64_tr_b16 a[42:43],v254 offset:6144\n"
"ds_read_b64_tr_b16 a[44:45],v254 offset:12288\n"
"ds_read_b64_tr_b16 a[46:47],v254 offset:18432\n"
"ds_read_b64_tr_b16 a[48:49],v254 offset:24576\n"
"ds_read_b64_tr_b16 a[50:51],v254 offset:30720\n"
"ds_read_b64_tr_b16 a[52:53],v254 offset:36864\n"
"ds_read_b64_tr_b16 a[54:55],v254 offset:43008\n"
"s_waitcnt lgkmcnt(0)\n"
"v_mfma_f32_16x16x32_bf16 v[0:3],a[32:35],a[160:163],v[0:3]\n"
"ds_read_b64_tr_b16 a[56:57],v253 offset:24576\n"
"v_mfma_f32_16x16x32_bf16 v[0:3],a[36:39],a[176:179],v[0:3]\n"
"ds_read_b64_tr_b16 a[58:59],v253 offset:30720\n"
"v_mfma_f32_16x16x32_bf16 v[0:3],a[40:43],a[0:3],v[0:3]\n"
"ds_read_b64_tr_b16 a[60:61],v253 offset:36864\n"
"v_mfma_f32_16x16x32_bf16 v[0:3],a[44:47],a[16:19],v[0:3]\n"
"ds_read_b64_tr_b16 a[62:63],v253 offset:43008\n"
"v_mfma_f32_16x16x32_bf16 v[0:3],a[48:51],a[208:211],v[0:3]\n"
"ds_read_b64_tr_b16 a[64:65],v255\n"
"v_mfma_f32_16x16x32_bf16 v[0:3],a[52:55],a[224:227],v[0:3]\n"
"ds_read_b64_tr_b16 a[66:67],v255 offset:6144\n"
"v_mfma_f32_16x16x32_bf16 v[4:7],a[32:35],a[164:167],v[4:7]\n"
"ds_read_b64_tr_b16 a[68:69],v255 offset:12288\n"
"v_mfma_f32_16x16x32_bf16 v[4:7],a[36:39],a[180:183],v[4:7]\n"
"ds_read_b64_tr_b16 a[70:71],v255 offset:18432\n"
"v_mfma_f32_16x16x32_bf16 v[4:7],a[40:43],a[4:7],v[4:7]\n"
"ds_read_b64_tr_b16 a[72:73],v255 offset:24576\n"
"v_mfma_f32_16x16x32_bf16 v[4:7],a[44:47],a[20:23],v[4:7]\n"
"ds_read_b64_tr_b16 a[74:75],v255 offset:30720\n"
"v_mfma_f32_16x16x32_bf16 v[4:7],a[48:51],a[212:215],v[4:7]\n"
"ds_read_b64_tr_b16 a[76:77],v255 offset:36864\n"
"v_mfma_f32_16x16x32_bf16 v[4:7],a[52:55],a[228:231],v[4:7]\n"
"ds_read_b64_tr_b16 a[78:79],v255 offset:43008\n"
"v_mfma_f32_16x16x32_bf16 v[8:11],a[32:35],a[168:171],v[8:11]\n"
"v_readlane_b32 s39,v248,16\n"
"s_nop 4\n"
"s_add_u32 m0,s51,0\n"
"s_nop 0\n"
"buffer_load_dwordx4 v213,s[72:75],s39 offen lds\n"
"v_mfma_f32_16x16x32_bf16 v[8:11],a[36:39],a[184:187],v[8:11]\n"
"v_mfma_f32_16x16x32_bf16 v[8:11],a[40:43],a[8:11],v[8:11]\n"
"buffer_load_dwordx4 v214,s[72:75],s39 offen offset:1024 lds\n"
"v_mfma_f32_16x16x32_bf16 v[8:11],a[44:47],a[24:27],v[8:11]\n"
"v_mfma_f32_16x16x32_bf16 v[8:11],a[48:51],a[216:219],v[8:11]\n"
"buffer_load_dwordx4 v215,s[72:75],s39 offen offset:2048 lds\n"
"v_mfma_f32_16x16x32_bf16 v[8:11],a[52:55],a[232:235],v[8:11]\n"
"v_mfma_f32_16x16x32_bf16 v[12:15],a[32:35],a[172:175],v[12:15]\n"
"s_add_u32 s39,s39,s52\n"
"s_add_u32 m0,s51,3072\n"
"s_nop 0\n"
"buffer_load_dwordx4 v213,s[72:75],s39 offen lds\n"
"v_mfma_f32_16x16x32_bf16 v[12:15],a[36:39],a[188:191],v[12:15]\n"
"v_mfma_f32_16x16x32_bf16 v[12:15],a[40:43],a[12:15],v[12:15]\n"
"buffer_load_dwordx4 v214,s[72:75],s39 offen offset:1024 lds\n"
"v_mfma_f32_16x16x32_bf16 v[12:15],a[44:47],a[28:31],v[12:15]\n"
"v_mfma_f32_16x16x32_bf16 v[12:15],a[48:51],a[220:223],v[12:15]\n"
"buffer_load_dwordx4 v215,s[72:75],s39 offen offset:2048 lds\n"
"v_mfma_f32_16x16x32_bf16 v[12:15],a[52:55],a[236:239],v[12:15]\n"
"s_waitcnt lgkmcnt(0)\n"
"v_mfma_f32_16x16x32_bf16 v[16:19],a[56:59],a[160:163],v[16:19]\n"
"ds_read_b64_tr_b16 a[32:33],v252 offset:24832\n"
"v_mul_f32 v144,v0,v0\n"
"v_mul_f32 v145,v1,v1\n"
"v_mul_f32 v146,v2,v2\n"
"v_mul_f32 v147,v3,v3\n"
"v_mul_f32 v148,v4,v4\n"
"v_mfma_f32_16x16x32_bf16 v[16:19],a[60:63],a[176:179],v[16:19]\n"
"ds_read_b64_tr_b16 a[34:35],v252 offset:30976\n"
"v_mul_f32 v149,v5,v5\n"
"v_mul_f32 v150,v6,v6\n"
"v_mul_f32 v151,v7,v7\n"
"v_fma_f32 v144,v144,s56,v184\n"
"v_fma_f32 v145,v145,s56,v184\n"
"v_mfma_f32_16x16x32_bf16 v[16:19],a[64:67],a[0:3],v[16:19]\n"
"ds_read_b64_tr_b16 a[36:37],v252 offset:37120\n"
"v_fma_f32 v146,v146,s56,v184\n"
"v_fma_f32 v147,v147,s56,v184\n"
"v_fma_f32 v148,v148,s56,v184\n"
"v_fma_f32 v149,v149,s56,v184\n"
"v_fma_f32 v150,v150,s56,v184\n"
"v_mfma_f32_16x16x32_bf16 v[16:19],a[68:71],a[16:19],v[16:19]\n"
"ds_read_b64_tr_b16 a[38:39],v252 offset:43264\n"
"v_fma_f32 v151,v151,s56,v184\n"
"v_mul_f32 v144,v144,v0\n"
"v_mul_f32 v145,v145,v1\n"
"v_mul_f32 v146,v146,v2\n"
"v_mul_f32 v147,v147,v3\n"
"v_mfma_f32_16x16x32_bf16 v[16:19],a[72:75],a[208:211],v[16:19]\n"
"ds_read_b64_tr_b16 a[40:41],v254 offset:256\n"
"v_mul_f32 v148,v148,v4\n"
"v_mul_f32 v149,v149,v5\n"
"v_mul_f32 v150,v150,v6\n"
"v_mul_f32 v151,v151,v7\n"
"v_exp_f32 v144,v144\n"
"v_mfma_f32_16x16x32_bf16 v[16:19],a[76:79],a[224:227],v[16:19]\n"
"ds_read_b64_tr_b16 a[42:43],v254 offset:6400\n"
"v_exp_f32 v145,v145\n"
"v_exp_f32 v146,v146\n"
"v_exp_f32 v147,v147\n"
"v_exp_f32 v148,v148\n"
"v_exp_f32 v149,v149\n"
"v_mfma_f32_16x16x32_bf16 v[20:23],a[56:59],a[164:167],v[20:23]\n"
"ds_read_b64_tr_b16 a[44:45],v254 offset:12544\n"
"v_exp_f32 v150,v150\n"
"v_exp_f32 v151,v151\n"
"v_add_f32 v144,1.0,v144\n"
"v_add_f32 v145,1.0,v145\n"
"v_add_f32 v146,1.0,v146\n"
"v_mfma_f32_16x16x32_bf16 v[20:23],a[60:63],a[180:183],v[20:23]\n"
"ds_read_b64_tr_b16 a[46:47],v254 offset:18688\n"
"v_add_f32 v147,1.0,v147\n"
"v_add_f32 v148,1.0,v148\n"
"v_add_f32 v149,1.0,v149\n"
"v_add_f32 v150,1.0,v150\n"
"v_add_f32 v151,1.0,v151\n"
"v_mfma_f32_16x16x32_bf16 v[20:23],a[64:67],a[4:7],v[20:23]\n"
"ds_read_b64_tr_b16 a[48:49],v254 offset:24832\n"
"v_rcp_f32 v144,v144\n"
"v_rcp_f32 v145,v145\n"
"v_rcp_f32 v146,v146\n"
"v_rcp_f32 v147,v147\n"
"v_rcp_f32 v148,v148\n"
"v_mfma_f32_16x16x32_bf16 v[20:23],a[68:71],a[20:23],v[20:23]\n"
"ds_read_b64_tr_b16 a[50:51],v254 offset:30976\n"
"v_rcp_f32 v149,v149\n"
"v_rcp_f32 v150,v150\n"
"v_rcp_f32 v151,v151\n"
"v_mul_f32 v144,v144,v0\n"
"v_mul_f32 v145,v145,v1\n"
"v_mfma_f32_16x16x32_bf16 v[20:23],a[72:75],a[212:215],v[20:23]\n"
"ds_read_b64_tr_b16 a[52:53],v254 offset:37120\n"
"v_mul_f32 v146,v146,v2\n"
"v_mul_f32 v147,v147,v3\n"
"v_mul_f32 v148,v148,v4\n"
"v_mul_f32 v149,v149,v5\n"
"v_mul_f32 v150,v150,v6\n"
"v_mfma_f32_16x16x32_bf16 v[20:23],a[76:79],a[228:231],v[20:23]\n"
"ds_read_b64_tr_b16 a[54:55],v254 offset:43264\n"
"v_mul_f32 v151,v151,v7\n"
"v_cvt_pk_bf16_f32 v152,v144,v145\n"
"v_cvt_pk_bf16_f32 v153,v146,v147\n"
"v_cvt_pk_bf16_f32 v156,v148,v149\n"
"v_cvt_pk_bf16_f32 v157,v150,v151\n"
"v_mfma_f32_16x16x32_bf16 v[24:27],a[56:59],a[168:171],v[24:27]\n"
"s_add_u32 s39,s39,s53\n"
"v_readlane_b32 s40,v248,22\n"
"s_nop 4\n"
"buffer_load_dwordx4 a[80:83],v209,s[88:91],s40 offen sc1\n"
"v_mul_f32 v144,v8,v8\n"
"v_mul_f32 v145,v9,v9\n"
"v_mul_f32 v146,v10,v10\n"
"v_mul_f32 v147,v11,v11\n"
"v_mul_f32 v148,v12,v12\n"
"v_mfma_f32_16x16x32_bf16 v[24:27],a[60:63],a[184:187],v[24:27]\n"
"v_mul_f32 v149,v13,v13\n"
"v_mul_f32 v150,v14,v14\n"
"v_mul_f32 v151,v15,v15\n"
"v_fma_f32 v144,v144,s56,v184\n"
"v_fma_f32 v145,v145,s56,v184\n"
"v_mfma_f32_16x16x32_bf16 v[24:27],a[64:67],a[8:11],v[24:27]\n"
"buffer_load_dwordx4 a[84:87],v209,s[88:91],s40 offen offset:1024 sc1\n"
"v_fma_f32 v146,v146,s56,v184\n"
"v_fma_f32 v147,v147,s56,v184\n"
"v_fma_f32 v148,v148,s56,v184\n"
"v_fma_f32 v149,v149,s56,v184\n"
"v_fma_f32 v150,v150,s56,v184\n"
"v_mfma_f32_16x16x32_bf16 v[24:27],a[68:71],a[24:27],v[24:27]\n"
"v_fma_f32 v151,v151,s56,v184\n"
"v_mul_f32 v144,v144,v8\n"
"v_mul_f32 v145,v145,v9\n"
"v_mul_f32 v146,v146,v10\n"
"v_mul_f32 v147,v147,v11\n"
"v_mfma_f32_16x16x32_bf16 v[24:27],a[72:75],a[216:219],v[24:27]\n"
"buffer_load_dwordx4 a[88:91],v209,s[88:91],s40 offen offset:2048 sc1\n"
"v_mul_f32 v148,v148,v12\n"
"v_mul_f32 v149,v149,v13\n"
"v_mul_f32 v150,v150,v14\n"
"v_mul_f32 v151,v151,v15\n"
"v_exp_f32 v144,v144\n"
"v_mfma_f32_16x16x32_bf16 v[24:27],a[76:79],a[232:235],v[24:27]\n"
"v_exp_f32 v145,v145\n"
"v_exp_f32 v146,v146\n"
"v_exp_f32 v147,v147\n"
"v_exp_f32 v148,v148\n"
"v_exp_f32 v149,v149\n"
"v_mfma_f32_16x16x32_bf16 v[28:31],a[56:59],a[172:175],v[28:31]\n"
"buffer_load_dwordx4 a[92:95],v209,s[88:91],s40 offen offset:3072 sc1\n"
"v_exp_f32 v150,v150\n"
"v_exp_f32 v151,v151\n"
"v_add_f32 v144,1.0,v144\n"
"v_add_f32 v145,1.0,v145\n"
"v_add_f32 v146,1.0,v146\n"
"v_mfma_f32_16x16x32_bf16 v[28:31],a[60:63],a[188:191],v[28:31]\n"
"v_add_f32 v147,1.0,v147\n"
"v_add_f32 v148,1.0,v148\n"
"v_add_f32 v149,1.0,v149\n"
"v_add_f32 v150,1.0,v150\n"
"v_add_f32 v151,1.0,v151\n"
"v_mfma_f32_16x16x32_bf16 v[28:31],a[64:67],a[12:15],v[28:31]\n"
"s_add_u32 s40,s40,4096\n"
"buffer_load_dwordx4 a[96:99],v209,s[88:91],s40 offen sc1\n"
"v_rcp_f32 v144,v144\n"
"v_rcp_f32 v145,v145\n"
"v_rcp_f32 v146,v146\n"
"v_rcp_f32 v147,v147\n"
"v_rcp_f32 v148,v148\n"
"v_mfma_f32_16x16x32_bf16 v[28:31],a[68:71],a[28:31],v[28:31]\n"
"v_rcp_f32 v149,v149\n"
"v_rcp_f32 v150,v150\n"
"v_rcp_f32 v151,v151\n"
"v_mul_f32 v144,v144,v8\n"
"v_mul_f32 v145,v145,v9\n"
"v_mfma_f32_16x16x32_bf16 v[28:31],a[72:75],a[220:223],v[28:31]\n"
"buffer_load_dwordx4 a[100:103],v209,s[88:91],s40 offen offset:1024 sc1\n"
"v_mul_f32 v146,v146,v10\n"
"v_mul_f32 v147,v147,v11\n"
"v_mul_f32 v148,v148,v12\n"
"v_mul_f32 v149,v149,v13\n"
"v_mul_f32 v150,v150,v14\n"
"v_mfma_f32_16x16x32_bf16 v[28:31],a[76:79],a[236:239],v[28:31]\n"
"v_mul_f32 v151,v151,v15\n"
"v_cvt_pk_bf16_f32 v160,v144,v145\n"
"v_cvt_pk_bf16_f32 v161,v146,v147\n"
"v_cvt_pk_bf16_f32 v164,v148,v149\n"
"v_cvt_pk_bf16_f32 v165,v150,v151\n"
"s_waitcnt lgkmcnt(0)\n"
"v_mfma_f32_16x16x32_bf16 v[32:35],a[32:35],a[160:163],v[32:35]\n"
"ds_read_b64_tr_b16 a[56:57],v253 offset:24832\n"
"v_mul_f32 v144,v16,v16\n"
"v_mul_f32 v145,v17,v17\n"
"v_mul_f32 v146,v18,v18\n"
"v_mul_f32 v147,v19,v19\n"
"v_mul_f32 v148,v20,v20\n"
"v_mul_f32 v149,v21,v21\n"
"v_mfma_f32_16x16x32_bf16 v[32:35],a[36:39],a[176:179],v[32:35]\n"
"ds_read_b64_tr_b16 a[58:59],v253 offset:30976\n"
"v_mul_f32 v150,v22,v22\n"
"v_mul_f32 v151,v23,v23\n"
"v_fma_f32 v144,v144,s56,v184\n"
"v_fma_f32 v145,v145,s56,v184\n"
"v_fma_f32 v146,v146,s56,v184\n"
"v_fma_f32 v147,v147,s56,v184\n"
"v_mfma_f32_16x16x32_bf16 v[32:35],a[40:43],a[0:3],v[32:35]\n"
"ds_read_b64_tr_b16 a[60:61],v253 offset:37120\n"
"v_fma_f32 v148,v148,s56,v184\n"
"v_fma_f32 v149,v149,s56,v184\n"
"v_fma_f32 v150,v150,s56,v184\n"
"v_fma_f32 v151,v151,s56,v184\n"
"v_mul_f32 v144,v144,v16\n"
"v_mul_f32 v145,v145,v17\n"
"v_mfma_f32_16x16x32_bf16 v[32:35],a[44:47],a[16:19],v[32:35]\n"
"ds_read_b64_tr_b16 a[62:63],v253 offset:43264\n"
"v_mul_f32 v146,v146,v18\n"
"v_mul_f32 v147,v147,v19\n"
"v_mul_f32 v148,v148,v20\n"
"v_mul_f32 v149,v149,v21\n"
"v_mul_f32 v150,v150,v22\n"
"v_mul_f32 v151,v151,v23\n"
"v_mfma_f32_16x16x32_bf16 v[32:35],a[48:51],a[208:211],v[32:35]\n"
"ds_read_b64_tr_b16 a[64:65],v255 offset:256\n"
"v_exp_f32 v144,v144\n"
"v_exp_f32 v145,v145\n"
"v_exp_f32 v146,v146\n"
"v_exp_f32 v147,v147\n"
"v_exp_f32 v148,v148\n"
"v_exp_f32 v149,v149\n"
"v_mfma_f32_16x16x32_bf16 v[32:35],a[52:55],a[224:227],v[32:35]\n"
"ds_read_b64_tr_b16 a[66:67],v255 offset:6400\n"
"v_exp_f32 v150,v150\n"
"v_exp_f32 v151,v151\n"
"v_add_f32 v144,1.0,v144\n"
"v_add_f32 v145,1.0,v145\n"
"v_add_f32 v146,1.0,v146\n"
"v_add_f32 v147,1.0,v147\n"
"v_mfma_f32_16x16x32_bf16 v[36:39],a[32:35],a[164:167],v[36:39]\n"
"ds_read_b64_tr_b16 a[68:69],v255 offset:12544\n"
"v_add_f32 v148,1.0,v148\n"
"v_add_f32 v149,1.0,v149\n"
"v_add_f32 v150,1.0,v150\n"
"v_add_f32 v151,1.0,v151\n"
"v_rcp_f32 v144,v144\n"
"v_rcp_f32 v145,v145\n"
"v_mfma_f32_16x16x32_bf16 v[36:39],a[36:39],a[180:183],v[36:39]\n"
"ds_read_b64_tr_b16 a[70:71],v255 offset:18688\n"
"v_rcp_f32 v146,v146\n"
"v_rcp_f32 v147,v147\n"
"v_rcp_f32 v148,v148\n"
"v_rcp_f32 v149,v149\n"
"v_rcp_f32 v150,v150\n"
"v_rcp_f32 v151,v151\n"
"v_mfma_f32_16x16x32_bf16 v[36:39],a[40:43],a[4:7],v[36:39]\n"
"ds_read_b64_tr_b16 a[72:73],v255 offset:24832\n"
"v_mul_f32 v144,v144,v16\n"
"v_mul_f32 v145,v145,v17\n"
"v_mul_f32 v146,v146,v18\n"
"v_mul_f32 v147,v147,v19\n"
"v_mul_f32 v148,v148,v20\n"
"v_mul_f32 v149,v149,v21\n"
"v_mfma_f32_16x16x32_bf16 v[36:39],a[44:47],a[20:23],v[36:39]\n"
"ds_read_b64_tr_b16 a[74:75],v255 offset:30976\n"
"v_mul_f32 v150,v150,v22\n"
"v_mul_f32 v151,v151,v23\n"
"v_cvt_pk_bf16_f32 v154,v144,v145\n"
"v_cvt_pk_bf16_f32 v155,v146,v147\n"
"v_cvt_pk_bf16_f32 v158,v148,v149\n"
"v_cvt_pk_bf16_f32 v159,v150,v151\n"
"v_mfma_f32_16x16x32_bf16 v[36:39],a[48:51],a[212:215],v[36:39]\n"
"ds_read_b64_tr_b16 a[76:77],v255 offset:37120\n"
"s_mov_b32 s41,0\n"
"buffer_store_dwordx4 v[152:155],v209,s[84:87],s41 offen sc1\n"
"buffer_store_dwordx4 v[156:159],v209,s[84:87],s41 offen offset:1024 sc1\n"
"v_mul_f32 v144,v24,v24\n"
"v_mul_f32 v145,v25,v25\n"
"v_mul_f32 v146,v26,v26\n"
"v_mfma_f32_16x16x32_bf16 v[36:39],a[52:55],a[228:231],v[36:39]\n"
"ds_read_b64_tr_b16 a[78:79],v255 offset:43264\n"
"v_mul_f32 v147,v27,v27\n"
"v_mul_f32 v148,v28,v28\n"
"v_mul_f32 v149,v29,v29\n"
"v_mul_f32 v150,v30,v30\n"
"v_mul_f32 v151,v31,v31\n"
"v_fma_f32 v144,v144,s56,v184\n"
"v_mfma_f32_16x16x32_bf16 v[40:43],a[32:35],a[168:171],v[40:43]\n"
"buffer_load_dwordx4 a[104:107],v209,s[88:91],s40 offen offset:2048 sc1\n"
"v_fma_f32 v145,v145,s56,v184\n"
"v_fma_f32 v146,v146,s56,v184\n"
"v_fma_f32 v147,v147,s56,v184\n"
"v_fma_f32 v148,v148,s56,v184\n"
"v_fma_f32 v149,v149,s56,v184\n"
"v_fma_f32 v150,v150,s56,v184\n"
"v_mfma_f32_16x16x32_bf16 v[40:43],a[36:39],a[184:187],v[40:43]\n"
"v_fma_f32 v151,v151,s56,v184\n"
"v_mul_f32 v144,v144,v24\n"
"v_mul_f32 v145,v145,v25\n"
"v_mul_f32 v146,v146,v26\n"
"v_mul_f32 v147,v147,v27\n"
"v_mul_f32 v148,v148,v28\n"
"v_mfma_f32_16x16x32_bf16 v[40:43],a[40:43],a[8:11],v[40:43]\n"
"buffer_load_dwordx4 a[108:111],v209,s[88:91],s40 offen offset:3072 sc1\n"
"v_mul_f32 v149,v149,v29\n"
"v_mul_f32 v150,v150,v30\n"
"v_mul_f32 v151,v151,v31\n"
"v_exp_f32 v144,v144\n"
"v_exp_f32 v145,v145\n"
"v_exp_f32 v146,v146\n"
"v_mfma_f32_16x16x32_bf16 v[40:43],a[44:47],a[24:27],v[40:43]\n"
"v_exp_f32 v147,v147\n"
"v_exp_f32 v148,v148\n"
"v_exp_f32 v149,v149\n"
"v_exp_f32 v150,v150\n"
"v_exp_f32 v151,v151\n"
"v_add_f32 v144,1.0,v144\n"
"v_mfma_f32_16x16x32_bf16 v[40:43],a[48:51],a[216:219],v[40:43]\n"
"s_add_u32 s40,s40,4096\n"
"s_add_u32 m0,s51,24576\n"
"s_nop 0\n"
"buffer_load_dwordx4 v213,s[72:75],s39 offen lds\n"
"v_add_f32 v145,1.0,v145\n"
"v_add_f32 v146,1.0,v146\n"
"v_add_f32 v147,1.0,v147\n"
"v_add_f32 v148,1.0,v148\n"
"v_add_f32 v149,1.0,v149\n"
"v_add_f32 v150,1.0,v150\n"
"v_mfma_f32_16x16x32_bf16 v[40:43],a[52:55],a[232:235],v[40:43]\n"
"v_add_f32 v151,1.0,v151\n"
"v_rcp_f32 v144,v144\n"
"v_rcp_f32 v145,v145\n"
"v_rcp_f32 v146,v146\n"
"v_rcp_f32 v147,v147\n"
"v_rcp_f32 v148,v148\n"
"v_mfma_f32_16x16x32_bf16 v[44:47],a[32:35],a[172:175],v[44:47]\n"
"buffer_load_dwordx4 v214,s[72:75],s39 offen offset:1024 lds\n"
"v_rcp_f32 v149,v149\n"
"v_rcp_f32 v150,v150\n"
"v_rcp_f32 v151,v151\n"
"v_mul_f32 v144,v144,v24\n"
"v_mul_f32 v145,v145,v25\n"
"v_mul_f32 v146,v146,v26\n"
"v_mfma_f32_16x16x32_bf16 v[44:47],a[36:39],a[188:191],v[44:47]\n"
"v_mul_f32 v147,v147,v27\n"
"v_mul_f32 v148,v148,v28\n"
"v_mul_f32 v149,v149,v29\n"
"v_mul_f32 v150,v150,v30\n"
"v_mul_f32 v151,v151,v31\n"
"v_cvt_pk_bf16_f32 v162,v144,v145\n"
"v_mfma_f32_16x16x32_bf16 v[44:47],a[40:43],a[12:15],v[44:47]\n"
"buffer_load_dwordx4 v215,s[72:75],s39 offen offset:2048 lds\n"
"v_cvt_pk_bf16_f32 v163,v146,v147\n"
"v_cvt_pk_bf16_f32 v166,v148,v149\n"
"v_cvt_pk_bf16_f32 v167,v150,v151\n"
"s_mov_b32 s41,0\n"
"buffer_store_dwordx4 v[160:163],v209,s[84:87],s41 offen offset:2048 sc1\n"
"buffer_store_dwordx4 v[164:167],v209,s[84:87],s41 offen offset:3072 sc1\n"
"v_mfma_f32_16x16x32_bf16 v[44:47],a[44:47],a[28:31],v[44:47]\n"
"v_mfma_f32_16x16x32_bf16 v[44:47],a[48:51],a[220:223],v[44:47]\n"
"s_add_u32 s39,s39,s52\n"
"s_add_u32 m0,s51,27648\n"
"s_nop 0\n"
"buffer_load_dwordx4 v213,s[72:75],s39 offen lds\n"
"v_mfma_f32_16x16x32_bf16 v[44:47],a[52:55],a[236:239],v[44:47]\n"
"s_waitcnt lgkmcnt(0)\n"
"v_mfma_f32_16x16x32_bf16 v[48:51],a[56:59],a[160:163],v[48:51]\n"
"ds_read_b64_tr_b16 a[32:33],v252 offset:25088\n"
"v_mul_f32 v144,v32,v32\n"
"v_mul_f32 v145,v33,v33\n"
"v_mul_f32 v146,v34,v34\n"
"v_mul_f32 v147,v35,v35\n"
"v_mul_f32 v148,v36,v36\n"
"v_mfma_f32_16x16x32_bf16 v[48:51],a[60:63],a[176:179],v[48:51]\n"
"ds_read_b64_tr_b16 a[34:35],v252 offset:31232\n"
"v_mul_f32 v149,v37,v37\n"
"v_mul_f32 v150,v38,v38\n"
"v_mul_f32 v151,v39,v39\n"
"v_fma_f32 v144,v144,s56,v184\n"
"v_fma_f32 v145,v145,s56,v184\n"
"v_mfma_f32_16x16x32_bf16 v[48:51],a[64:67],a[0:3],v[48:51]\n"
"ds_read_b64_tr_b16 a[36:37],v252 offset:37376\n"
"v_fma_f32 v146,v146,s56,v184\n"
"v_fma_f32 v147,v147,s56,v184\n"
"v_fma_f32 v148,v148,s56,v184\n"
"v_fma_f32 v149,v149,s56,v184\n"
"v_fma_f32 v150,v150,s56,v184\n"
"v_mfma_f32_16x16x32_bf16 v[48:51],a[68:71],a[16:19],v[48:51]\n"
"ds_read_b64_tr_b16 a[38:39],v252 offset:43520\n"
"v_fma_f32 v151,v151,s56,v184\n"
"v_mul_f32 v144,v144,v32\n"
"v_mul_f32 v145,v145,v33\n"
"v_mul_f32 v146,v146,v34\n"
"v_mul_f32 v147,v147,v35\n"
"v_mfma_f32_16x16x32_bf16 v[48:51],a[72:75],a[208:211],v[48:51]\n"
"ds_read_b64_tr_b16 a[40:41],v254 offset:512\n"
"v_mul_f32 v148,v148,v36\n"
"v_mul_f32 v149,v149,v37\n"
"v_mul_f32 v150,v150,v38\n"
"v_mul_f32 v151,v151,v39\n"
"v_exp_f32 v144,v144\n"
"v_mfma_f32_16x16x32_bf16 v[48:51],a[76:79],a[224:227],v[48:51]\n"
"ds_read_b64_tr_b16 a[42:43],v254 offset:6656\n"
"v_exp_f32 v145,v145\n"
"v_exp_f32 v146,v146\n"
"v_exp_f32 v147,v147\n"
"v_exp_f32 v148,v148\n"
"v_exp_f32 v149,v149\n"
"v_mfma_f32_16x16x32_bf16 v[52:55],a[56:59],a[164:167],v[52:55]\n"
"ds_read_b64_tr_b16 a[44:45],v254 offset:12800\n"
"v_exp_f32 v150,v150\n"
"v_exp_f32 v151,v151\n"
"v_add_f32 v144,1.0,v144\n"
"v_add_f32 v145,1.0,v145\n"
"v_add_f32 v146,1.0,v146\n"
"v_mfma_f32_16x16x32_bf16 v[52:55],a[60:63],a[180:183],v[52:55]\n"
"ds_read_b64_tr_b16 a[46:47],v254 offset:18944\n"
"v_add_f32 v147,1.0,v147\n"
"v_add_f32 v148,1.0,v148\n"
"v_add_f32 v149,1.0,v149\n"
"v_add_f32 v150,1.0,v150\n"
"v_add_f32 v151,1.0,v151\n"
"v_mfma_f32_16x16x32_bf16 v[52:55],a[64:67],a[4:7],v[52:55]\n"
"ds_read_b64_tr_b16 a[48:49],v254 offset:25088\n"
"v_rcp_f32 v144,v144\n"
"v_rcp_f32 v145,v145\n"
"v_rcp_f32 v146,v146\n"
"v_rcp_f32 v147,v147\n"
"v_rcp_f32 v148,v148\n"
"v_mfma_f32_16x16x32_bf16 v[52:55],a[68:71],a[20:23],v[52:55]\n"
"ds_read_b64_tr_b16 a[50:51],v254 offset:31232\n"
"v_rcp_f32 v149,v149\n"
"v_rcp_f32 v150,v150\n"
"v_rcp_f32 v151,v151\n"
"v_mul_f32 v144,v144,v32\n"
"v_mul_f32 v145,v145,v33\n"
"v_mfma_f32_16x16x32_bf16 v[52:55],a[72:75],a[212:215],v[52:55]\n"
"ds_read_b64_tr_b16 a[52:53],v254 offset:37376\n"
"v_mul_f32 v146,v146,v34\n"
"v_mul_f32 v147,v147,v35\n"
"v_mul_f32 v148,v148,v36\n"
"v_mul_f32 v149,v149,v37\n"
"v_mul_f32 v150,v150,v38\n"
"v_mfma_f32_16x16x32_bf16 v[52:55],a[76:79],a[228:231],v[52:55]\n"
"ds_read_b64_tr_b16 a[54:55],v254 offset:43520\n"
"v_mul_f32 v151,v151,v39\n"
"v_cvt_pk_bf16_f32 v168,v144,v145\n"
"v_cvt_pk_bf16_f32 v169,v146,v147\n"
"v_cvt_pk_bf16_f32 v172,v148,v149\n"
"v_cvt_pk_bf16_f32 v173,v150,v151\n"
"v_mfma_f32_16x16x32_bf16 v[56:59],a[56:59],a[168:171],v[56:59]\n"
"buffer_load_dwordx4 v214,s[72:75],s39 offen offset:1024 lds\n"
"v_mul_f32 v144,v40,v40\n"
"v_mul_f32 v145,v41,v41\n"
"v_mul_f32 v146,v42,v42\n"
"v_mul_f32 v147,v43,v43\n"
"v_mul_f32 v148,v44,v44\n"
"v_mfma_f32_16x16x32_bf16 v[56:59],a[60:63],a[184:187],v[56:59]\n"
"v_mul_f32 v149,v45,v45\n"
"v_mul_f32 v150,v46,v46\n"
"v_mul_f32 v151,v47,v47\n"
"v_fma_f32 v144,v144,s56,v184\n"
"v_fma_f32 v145,v145,s56,v184\n"
"v_mfma_f32_16x16x32_bf16 v[56:59],a[64:67],a[8:11],v[56:59]\n"
"buffer_load_dwordx4 v215,s[72:75],s39 offen offset:2048 lds\n"
"v_fma_f32 v146,v146,s56,v184\n"
"v_fma_f32 v147,v147,s56,v184\n"
"v_fma_f32 v148,v148,s56,v184\n"
"v_fma_f32 v149,v149,s56,v184\n"
"v_fma_f32 v150,v150,s56,v184\n"
"v_mfma_f32_16x16x32_bf16 v[56:59],a[68:71],a[24:27],v[56:59]\n"
"v_fma_f32 v151,v151,s56,v184\n"
"v_mul_f32 v144,v144,v40\n"
"v_mul_f32 v145,v145,v41\n"
"v_mul_f32 v146,v146,v42\n"
"v_mul_f32 v147,v147,v43\n"
"v_mfma_f32_16x16x32_bf16 v[56:59],a[72:75],a[216:219],v[56:59]\n"
"s_add_u32 s39,s39,s53\n"
"buffer_load_dwordx4 a[128:131],v209,s[88:91],s40 offen sc1\n"
"v_mul_f32 v148,v148,v44\n"
"v_mul_f32 v149,v149,v45\n"
"v_mul_f32 v150,v150,v46\n"
"v_mul_f32 v151,v151,v47\n"
"v_exp_f32 v144,v144\n"
"v_mfma_f32_16x16x32_bf16 v[56:59],a[76:79],a[232:235],v[56:59]\n"
"v_exp_f32 v145,v145\n"
"v_exp_f32 v146,v146\n"
"v_exp_f32 v147,v147\n"
"v_exp_f32 v148,v148\n"
"v_exp_f32 v149,v149\n"
"v_mfma_f32_16x16x32_bf16 v[60:63],a[56:59],a[172:175],v[60:63]\n"
"buffer_load_dwordx4 a[132:135],v209,s[88:91],s40 offen offset:1024 sc1\n"
"v_exp_f32 v150,v150\n"
"v_exp_f32 v151,v151\n"
"v_add_f32 v144,1.0,v144\n"
"v_add_f32 v145,1.0,v145\n"
"v_add_f32 v146,1.0,v146\n"
"v_mfma_f32_16x16x32_bf16 v[60:63],a[60:63],a[188:191],v[60:63]\n"
"v_add_f32 v147,1.0,v147\n"
"v_add_f32 v148,1.0,v148\n"
"v_add_f32 v149,1.0,v149\n"
"v_add_f32 v150,1.0,v150\n"
"v_add_f32 v151,1.0,v151\n"
"v_mfma_f32_16x16x32_bf16 v[60:63],a[64:67],a[12:15],v[60:63]\n"
"buffer_load_dwordx4 a[136:139],v209,s[88:91],s40 offen offset:2048 sc1\n"
"v_rcp_f32 v144,v144\n"
"v_rcp_f32 v145,v145\n"
"v_rcp_f32 v146,v146\n"
"v_rcp_f32 v147,v147\n"
"v_rcp_f32 v148,v148\n"
"v_mfma_f32_16x16x32_bf16 v[60:63],a[68:71],a[28:31],v[60:63]\n"
"v_rcp_f32 v149,v149\n"
"v_rcp_f32 v150,v150\n"
"v_rcp_f32 v151,v151\n"
"v_mul_f32 v144,v144,v40\n"
"v_mul_f32 v145,v145,v41\n"
"v_mfma_f32_16x16x32_bf16 v[60:63],a[72:75],a[220:223],v[60:63]\n"
"buffer_load_dwordx4 a[140:143],v209,s[88:91],s40 offen offset:3072 sc1\n"
"v_mul_f32 v146,v146,v42\n"
"v_mul_f32 v147,v147,v43\n"
"v_mul_f32 v148,v148,v44\n"
"v_mul_f32 v149,v149,v45\n"
"v_mul_f32 v150,v150,v46\n"
"v_mfma_f32_16x16x32_bf16 v[60:63],a[76:79],a[236:239],v[60:63]\n"
"v_mul_f32 v151,v151,v47\n"
"v_cvt_pk_bf16_f32 v176,v144,v145\n"
"v_cvt_pk_bf16_f32 v177,v146,v147\n"
"v_cvt_pk_bf16_f32 v180,v148,v149\n"
"v_cvt_pk_bf16_f32 v181,v150,v151\n"
"s_waitcnt lgkmcnt(0)\n"
"v_mfma_f32_16x16x32_bf16 v[64:67],a[32:35],a[160:163],v[64:67]\n"
"ds_read_b64_tr_b16 a[56:57],v253 offset:25088\n"
"v_mul_f32 v144,v48,v48\n"
"v_mul_f32 v145,v49,v49\n"
"v_mul_f32 v146,v50,v50\n"
"v_mul_f32 v147,v51,v51\n"
"v_mul_f32 v148,v52,v52\n"
"v_mul_f32 v149,v53,v53\n"
"v_mfma_f32_16x16x32_bf16 v[64:67],a[36:39],a[176:179],v[64:67]\n"
"ds_read_b64_tr_b16 a[58:59],v253 offset:31232\n"
"v_mul_f32 v150,v54,v54\n"
"v_mul_f32 v151,v55,v55\n"
"v_fma_f32 v144,v144,s56,v184\n"
"v_fma_f32 v145,v145,s56,v184\n"
"v_fma_f32 v146,v146,s56,v184\n"
"v_fma_f32 v147,v147,s56,v184\n"
"v_mfma_f32_16x16x32_bf16 v[64:67],a[40:43],a[0:3],v[64:67]\n"
"ds_read_b64_tr_b16 a[60:61],v253 offset:37376\n"
"v_fma_f32 v148,v148,s56,v184\n"
"v_fma_f32 v149,v149,s56,v184\n"
"v_fma_f32 v150,v150,s56,v184\n"
"v_fma_f32 v151,v151,s56,v184\n"
"v_mul_f32 v144,v144,v48\n"
"v_mul_f32 v145,v145,v49\n"
"v_mfma_f32_16x16x32_bf16 v[64:67],a[44:47],a[16:19],v[64:67]\n"
"ds_read_b64_tr_b16 a[62:63],v253 offset:43520\n"
"v_mul_f32 v146,v146,v50\n"
"v_mul_f32 v147,v147,v51\n"
"v_mul_f32 v148,v148,v52\n"
"v_mul_f32 v149,v149,v53\n"
"v_mul_f32 v150,v150,v54\n"
"v_mul_f32 v151,v151,v55\n"
"v_mfma_f32_16x16x32_bf16 v[64:67],a[48:51],a[208:211],v[64:67]\n"
"ds_read_b64_tr_b16 a[64:65],v255 offset:512\n"
"v_exp_f32 v144,v144\n"
"v_exp_f32 v145,v145\n"
"v_exp_f32 v146,v146\n"
"v_exp_f32 v147,v147\n"
"v_exp_f32 v148,v148\n"
"v_exp_f32 v149,v149\n"
"v_mfma_f32_16x16x32_bf16 v[64:67],a[52:55],a[224:227],v[64:67]\n"
"ds_read_b64_tr_b16 a[66:67],v255 offset:6656\n"
"v_exp_f32 v150,v150\n"
"v_exp_f32 v151,v151\n"
"v_add_f32 v144,1.0,v144\n"
"v_add_f32 v145,1.0,v145\n"
"v_add_f32 v146,1.0,v146\n"
"v_add_f32 v147,1.0,v147\n"
"v_mfma_f32_16x16x32_bf16 v[68:71],a[32:35],a[164:167],v[68:71]\n"
"ds_read_b64_tr_b16 a[68:69],v255 offset:12800\n"
"v_add_f32 v148,1.0,v148\n"
"v_add_f32 v149,1.0,v149\n"
"v_add_f32 v150,1.0,v150\n"
"v_add_f32 v151,1.0,v151\n"
"v_rcp_f32 v144,v144\n"
"v_rcp_f32 v145,v145\n"
"v_mfma_f32_16x16x32_bf16 v[68:71],a[36:39],a[180:183],v[68:71]\n"
"ds_read_b64_tr_b16 a[70:71],v255 offset:18944\n"
"v_rcp_f32 v146,v146\n"
"v_rcp_f32 v147,v147\n"
"v_rcp_f32 v148,v148\n"
"v_rcp_f32 v149,v149\n"
"v_rcp_f32 v150,v150\n"
"v_rcp_f32 v151,v151\n"
"v_mfma_f32_16x16x32_bf16 v[68:71],a[40:43],a[4:7],v[68:71]\n"
"ds_read_b64_tr_b16 a[72:73],v255 offset:25088\n"
"v_mul_f32 v144,v144,v48\n"
"v_mul_f32 v145,v145,v49\n"
"v_mul_f32 v146,v146,v50\n"
"v_mul_f32 v147,v147,v51\n"
"v_mul_f32 v148,v148,v52\n"
"v_mul_f32 v149,v149,v53\n"
"v_mfma_f32_16x16x32_bf16 v[68:71],a[44:47],a[20:23],v[68:71]\n"
"ds_read_b64_tr_b16 a[74:75],v255 offset:31232\n"
"v_mul_f32 v150,v150,v54\n"
"v_mul_f32 v151,v151,v55\n"
"v_cvt_pk_bf16_f32 v170,v144,v145\n"
"v_cvt_pk_bf16_f32 v171,v146,v147\n"
"v_cvt_pk_bf16_f32 v174,v148,v149\n"
"v_cvt_pk_bf16_f32 v175,v150,v151\n"
"v_mfma_f32_16x16x32_bf16 v[68:71],a[48:51],a[212:215],v[68:71]\n"
"ds_read_b64_tr_b16 a[76:77],v255 offset:37376\n"
"s_mov_b32 s41,4096\n"
"buffer_store_dwordx4 v[168:171],v209,s[84:87],s41 offen sc1\n"
"buffer_store_dwordx4 v[172:175],v209,s[84:87],s41 offen offset:1024 sc1\n"
"v_mul_f32 v144,v56,v56\n"
"v_mul_f32 v145,v57,v57\n"
"v_mul_f32 v146,v58,v58\n"
"v_mfma_f32_16x16x32_bf16 v[68:71],a[52:55],a[228:231],v[68:71]\n"
"ds_read_b64_tr_b16 a[78:79],v255 offset:43520\n"
"v_mul_f32 v147,v59,v59\n"
"v_mul_f32 v148,v60,v60\n"
"v_mul_f32 v149,v61,v61\n"
"v_mul_f32 v150,v62,v62\n"
"v_mul_f32 v151,v63,v63\n"
"v_fma_f32 v144,v144,s56,v184\n"
"v_mfma_f32_16x16x32_bf16 v[72:75],a[32:35],a[168:171],v[72:75]\n"
"s_add_u32 s40,s40,4096\n"
"buffer_load_dwordx4 a[144:147],v209,s[88:91],s40 offen sc1\n"
"v_fma_f32 v145,v145,s56,v184\n"
"v_fma_f32 v146,v146,s56,v184\n"
"v_fma_f32 v147,v147,s56,v184\n"
"v_fma_f32 v148,v148,s56,v184\n"
"v_fma_f32 v149,v149,s56,v184\n"
"v_fma_f32 v150,v150,s56,v184\n"
"v_mfma_f32_16x16x32_bf16 v[72:75],a[36:39],a[184:187],v[72:75]\n"
"v_fma_f32 v151,v151,s56,v184\n"
"v_mul_f32 v144,v144,v56\n"
"v_mul_f32 v145,v145,v57\n"
"v_mul_f32 v146,v146,v58\n"
"v_mul_f32 v147,v147,v59\n"
"v_mul_f32 v148,v148,v60\n"
"v_mfma_f32_16x16x32_bf16 v[72:75],a[40:43],a[8:11],v[72:75]\n"
"buffer_load_dwordx4 a[148:151],v209,s[88:91],s40 offen offset:1024 sc1\n"
"v_mul_f32 v149,v149,v61\n"
"v_mul_f32 v150,v150,v62\n"
"v_mul_f32 v151,v151,v63\n"
"v_exp_f32 v144,v144\n"
"v_exp_f32 v145,v145\n"
"v_exp_f32 v146,v146\n"
"v_mfma_f32_16x16x32_bf16 v[72:75],a[44:47],a[24:27],v[72:75]\n"
"v_exp_f32 v147,v147\n"
"v_exp_f32 v148,v148\n"
"v_exp_f32 v149,v149\n"
"v_exp_f32 v150,v150\n"
"v_exp_f32 v151,v151\n"
"v_add_f32 v144,1.0,v144\n"
"v_mfma_f32_16x16x32_bf16 v[72:75],a[48:51],a[216:219],v[72:75]\n"
"buffer_load_dwordx4 a[152:155],v209,s[88:91],s40 offen offset:2048 sc1\n"
"v_add_f32 v145,1.0,v145\n"
"v_add_f32 v146,1.0,v146\n"
"v_add_f32 v147,1.0,v147\n"
"v_add_f32 v148,1.0,v148\n"
"v_add_f32 v149,1.0,v149\n"
"v_add_f32 v150,1.0,v150\n"
"v_mfma_f32_16x16x32_bf16 v[72:75],a[52:55],a[232:235],v[72:75]\n"
"v_add_f32 v151,1.0,v151\n"
"v_rcp_f32 v144,v144\n"
"v_rcp_f32 v145,v145\n"
"v_rcp_f32 v146,v146\n"
"v_rcp_f32 v147,v147\n"
"v_rcp_f32 v148,v148\n"
"v_mfma_f32_16x16x32_bf16 v[76:79],a[32:35],a[172:175],v[76:79]\n"
"buffer_load_dwordx4 a[156:159],v209,s[88:91],s40 offen offset:3072 sc1\n"
"v_rcp_f32 v149,v149\n"
"v_rcp_f32 v150,v150\n"
"v_rcp_f32 v151,v151\n"
"v_mul_f32 v144,v144,v56\n"
"v_mul_f32 v145,v145,v57\n"
"v_mul_f32 v146,v146,v58\n"
"v_mfma_f32_16x16x32_bf16 v[76:79],a[36:39],a[188:191],v[76:79]\n"
"v_mul_f32 v147,v147,v59\n"
"v_mul_f32 v148,v148,v60\n"
"v_mul_f32 v149,v149,v61\n"
"v_mul_f32 v150,v150,v62\n"
"v_mul_f32 v151,v151,v63\n"
"v_cvt_pk_bf16_f32 v178,v144,v145\n"
"v_mfma_f32_16x16x32_bf16 v[76:79],a[40:43],a[12:15],v[76:79]\n"
"s_add_u32 s40,s40,4096\n"
"v_readlane_b32 s39,v248,17\n"
"s_nop 4\n"
"s_add_u32 m0,s51,65536\n"
"s_nop 0\n"
"buffer_load_dwordx4 v213,s[72:75],s39 offen lds\n"
"v_cvt_pk_bf16_f32 v179,v146,v147\n"
"v_cvt_pk_bf16_f32 v182,v148,v149\n"
"v_cvt_pk_bf16_f32 v183,v150,v151\n"
"s_mov_b32 s41,4096\n"
"buffer_store_dwordx4 v[176:179],v209,s[84:87],s41 offen offset:2048 sc1\n"
"buffer_store_dwordx4 v[180:183],v209,s[84:87],s41 offen offset:3072 sc1\n"
"v_mfma_f32_16x16x32_bf16 v[76:79],a[44:47],a[28:31],v[76:79]\n"
"v_mfma_f32_16x16x32_bf16 v[76:79],a[48:51],a[220:223],v[76:79]\n"
"buffer_load_dwordx4 v214,s[72:75],s39 offen offset:1024 lds\n"
"v_mfma_f32_16x16x32_bf16 v[76:79],a[52:55],a[236:239],v[76:79]\n"
"s_waitcnt lgkmcnt(0)\n"
"v_mfma_f32_16x16x32_bf16 v[80:83],a[56:59],a[160:163],v[80:83]\n"
"ds_read_b64_tr_b16 a[32:33],v252 offset:25344\n"
"v_mul_f32 v144,v64,v64\n"
"v_mul_f32 v145,v65,v65\n"
"v_mul_f32 v146,v66,v66\n"
"v_mul_f32 v147,v67,v67\n"
"v_mul_f32 v148,v68,v68\n"
"v_mfma_f32_16x16x32_bf16 v[80:83],a[60:63],a[176:179],v[80:83]\n"
"ds_read_b64_tr_b16 a[34:35],v252 offset:31488\n"
"v_mul_f32 v149,v69,v69\n"
"v_mul_f32 v150,v70,v70\n"
"v_mul_f32 v151,v71,v71\n"
"v_fma_f32 v144,v144,s56,v184\n"
"v_fma_f32 v145,v145,s56,v184\n"
"v_mfma_f32_16x16x32_bf16 v[80:83],a[64:67],a[0:3],v[80:83]\n"
"ds_read_b64_tr_b16 a[36:37],v252 offset:37632\n"
"v_fma_f32 v146,v146,s56,v184\n"
"v_fma_f32 v147,v147,s56,v184\n"
"v_fma_f32 v148,v148,s56,v184\n"
"v_fma_f32 v149,v149,s56,v184\n"
"v_fma_f32 v150,v150,s56,v184\n"
"v_mfma_f32_16x16x32_bf16 v[80:83],a[68:71],a[16:19],v[80:83]\n"
"ds_read_b64_tr_b16 a[38:39],v252 offset:43776\n"
"v_fma_f32 v151,v151,s56,v184\n"
"v_mul_f32 v144,v144,v64\n"
"v_mul_f32 v145,v145,v65\n"
"v_mul_f32 v146,v146,v66\n"
"v_mul_f32 v147,v147,v67\n"
"v_mfma_f32_16x16x32_bf16 v[80:83],a[72:75],a[208:211],v[80:83]\n"
"ds_read_b64_tr_b16 a[40:41],v254 offset:768\n"
"v_mul_f32 v148,v148,v68\n"
"v_mul_f32 v149,v149,v69\n"
"v_mul_f32 v150,v150,v70\n"
"v_mul_f32 v151,v151,v71\n"
"v_exp_f32 v144,v144\n"
"v_mfma_f32_16x16x32_bf16 v[80:83],a[76:79],a[224:227],v[80:83]\n"
"ds_read_b64_tr_b16 a[42:43],v254 offset:6912\n"
"v_exp_f32 v145,v145\n"
"v_exp_f32 v146,v146\n"
"v_exp_f32 v147,v147\n"
"v_exp_f32 v148,v148\n"
"v_exp_f32 v149,v149\n"
"v_mfma_f32_16x16x32_bf16 v[84:87],a[56:59],a[164:167],v[84:87]\n"
"ds_read_b64_tr_b16 a[44:45],v254 offset:13056\n"
"v_exp_f32 v150,v150\n"
"v_exp_f32 v151,v151\n"
"v_add_f32 v144,1.0,v144\n"
"v_add_f32 v145,1.0,v145\n"
"v_add_f32 v146,1.0,v146\n"
"v_mfma_f32_16x16x32_bf16 v[84:87],a[60:63],a[180:183],v[84:87]\n"
"ds_read_b64_tr_b16 a[46:47],v254 offset:19200\n"
"v_add_f32 v147,1.0,v147\n"
"v_add_f32 v148,1.0,v148\n"
"v_add_f32 v149,1.0,v149\n"
"v_add_f32 v150,1.0,v150\n"
"v_add_f32 v151,1.0,v151\n"
"v_mfma_f32_16x16x32_bf16 v[84:87],a[64:67],a[4:7],v[84:87]\n"
"ds_read_b64_tr_b16 a[48:49],v254 offset:25344\n"
"v_rcp_f32 v144,v144\n"
"v_rcp_f32 v145,v145\n"
"v_rcp_f32 v146,v146\n"
"v_rcp_f32 v147,v147\n"
"v_rcp_f32 v148,v148\n"
"v_mfma_f32_16x16x32_bf16 v[84:87],a[68:71],a[20:23],v[84:87]\n"
"ds_read_b64_tr_b16 a[50:51],v254 offset:31488\n"
"v_rcp_f32 v149,v149\n"
"v_rcp_f32 v150,v150\n"
"v_rcp_f32 v151,v151\n"
"v_mul_f32 v144,v144,v64\n"
"v_mul_f32 v145,v145,v65\n"
"v_mfma_f32_16x16x32_bf16 v[84:87],a[72:75],a[212:215],v[84:87]\n"
"ds_read_b64_tr_b16 a[52:53],v254 offset:37632\n"
"v_mul_f32 v146,v146,v66\n"
"v_mul_f32 v147,v147,v67\n"
"v_mul_f32 v148,v148,v68\n"
"v_mul_f32 v149,v149,v69\n"
"v_mul_f32 v150,v150,v70\n"
"v_mfma_f32_16x16x32_bf16 v[84:87],a[76:79],a[228:231],v[84:87]\n"
"ds_read_b64_tr_b16 a[54:55],v254 offset:43776\n"
"v_mul_f32 v151,v151,v71\n"
"v_cvt_pk_bf16_f32 v192,v144,v145\n"
"v_cvt_pk_bf16_f32 v193,v146,v147\n"
"v_cvt_pk_bf16_f32 v196,v148,v149\n"
"v_cvt_pk_bf16_f32 v197,v150,v151\n"
"v_mfma_f32_16x16x32_bf16 v[88:91],a[56:59],a[168:171],v[88:91]\n"
"buffer_load_dwordx4 v215,s[72:75],s39 offen offset:2048 lds\n"
"v_mul_f32 v144,v72,v72\n"
"v_mul_f32 v145,v73,v73\n"
"v_mul_f32 v146,v74,v74\n"
"v_mul_f32 v147,v75,v75\n"
"v_mul_f32 v148,v76,v76\n"
"v_mfma_f32_16x16x32_bf16 v[88:91],a[60:63],a[184:187],v[88:91]\n"
"v_mul_f32 v149,v77,v77\n"
"v_mul_f32 v150,v78,v78\n"
"v_mul_f32 v151,v79,v79\n"
"v_fma_f32 v144,v144,s56,v184\n"
"v_fma_f32 v145,v145,s56,v184\n"
"v_mfma_f32_16x16x32_bf16 v[88:91],a[64:67],a[8:11],v[88:91]\n"
"s_add_u32 s39,s39,s52\n"
"s_add_u32 m0,s51,68608\n"
"s_nop 0\n"
"buffer_load_dwordx4 v213,s[72:75],s39 offen lds\n"
"v_fma_f32 v146,v146,s56,v184\n"
"v_fma_f32 v147,v147,s56,v184\n"
"v_fma_f32 v148,v148,s56,v184\n"
"v_fma_f32 v149,v149,s56,v184\n"
"v_fma_f32 v150,v150,s56,v184\n"
"v_mfma_f32_16x16x32_bf16 v[88:91],a[68:71],a[24:27],v[88:91]\n"
"v_fma_f32 v151,v151,s56,v184\n"
"v_mul_f32 v144,v144,v72\n"
"v_mul_f32 v145,v145,v73\n"
"v_mul_f32 v146,v146,v74\n"
"v_mul_f32 v147,v147,v75\n"
"v_mfma_f32_16x16x32_bf16 v[88:91],a[72:75],a[216:219],v[88:91]\n"
"buffer_load_dwordx4 v214,s[72:75],s39 offen offset:1024 lds\n"
"v_mul_f32 v148,v148,v76\n"
"v_mul_f32 v149,v149,v77\n"
"v_mul_f32 v150,v150,v78\n"
"v_mul_f32 v151,v151,v79\n"
"v_exp_f32 v144,v144\n"
"v_mfma_f32_16x16x32_bf16 v[88:91],a[76:79],a[232:235],v[88:91]\n"
"v_exp_f32 v145,v145\n"
"v_exp_f32 v146,v146\n"
"v_exp_f32 v147,v147\n"
"v_exp_f32 v148,v148\n"
"v_exp_f32 v149,v149\n"
"v_mfma_f32_16x16x32_bf16 v[92:95],a[56:59],a[172:175],v[92:95]\n"
"buffer_load_dwordx4 v215,s[72:75],s39 offen offset:2048 lds\n"
"v_exp_f32 v150,v150\n"
"v_exp_f32 v151,v151\n"
"v_add_f32 v144,1.0,v144\n"
"v_add_f32 v145,1.0,v145\n"
"v_add_f32 v146,1.0,v146\n"
"v_mfma_f32_16x16x32_bf16 v[92:95],a[60:63],a[188:191],v[92:95]\n"
"v_add_f32 v147,1.0,v147\n"
"v_add_f32 v148,1.0,v148\n"
"v_add_f32 v149,1.0,v149\n"
"v_add_f32 v150,1.0,v150\n"
"v_add_f32 v151,1.0,v151\n"
"v_mfma_f32_16x16x32_bf16 v[92:95],a[64:67],a[12:15],v[92:95]\n"
"s_add_u32 s39,s39,s53\n"
"v_readlane_b32 s40,v248,23\n"
"s_nop 4\n"
"buffer_load_dwordx4 a[192:195],v209,s[88:91],s40 offen sc1\n"
"v_rcp_f32 v144,v144\n"
"v_rcp_f32 v145,v145\n"
"v_rcp_f32 v146,v146\n"
"v_rcp_f32 v147,v147\n"
"v_rcp_f32 v148,v148\n"
"v_mfma_f32_16x16x32_bf16 v[92:95],a[68:71],a[28:31],v[92:95]\n"
"v_rcp_f32 v149,v149\n"
"v_rcp_f32 v150,v150\n"
"v_rcp_f32 v151,v151\n"
"v_mul_f32 v144,v144,v72\n"
"v_mul_f32 v145,v145,v73\n"
"v_mfma_f32_16x16x32_bf16 v[92:95],a[72:75],a[220:223],v[92:95]\n"
"buffer_load_dwordx4 a[196:199],v209,s[88:91],s40 offen offset:1024 sc1\n"
"v_mul_f32 v146,v146,v74\n"
"v_mul_f32 v147,v147,v75\n"
"v_mul_f32 v148,v148,v76\n"
"v_mul_f32 v149,v149,v77\n"
"v_mul_f32 v150,v150,v78\n"
"v_mfma_f32_16x16x32_bf16 v[92:95],a[76:79],a[236:239],v[92:95]\n"
"v_mul_f32 v151,v151,v79\n"
"v_cvt_pk_bf16_f32 v200,v144,v145\n"
"v_cvt_pk_bf16_f32 v201,v146,v147\n"
"v_cvt_pk_bf16_f32 v204,v148,v149\n"
"v_cvt_pk_bf16_f32 v205,v150,v151\n"
"s_waitcnt lgkmcnt(0)\n"
"v_mfma_f32_16x16x32_bf16 v[96:99],a[32:35],a[160:163],v[96:99]\n"
"ds_read_b64_tr_b16 a[56:57],v253 offset:25344\n"
"v_mul_f32 v144,v80,v80\n"
"v_mul_f32 v145,v81,v81\n"
"v_mul_f32 v146,v82,v82\n"
"v_mul_f32 v147,v83,v83\n"
"v_mul_f32 v148,v84,v84\n"
"v_mul_f32 v149,v85,v85\n"
"v_mfma_f32_16x16x32_bf16 v[96:99],a[36:39],a[176:179],v[96:99]\n"
"ds_read_b64_tr_b16 a[58:59],v253 offset:31488\n"
"v_mul_f32 v150,v86,v86\n"
"v_mul_f32 v151,v87,v87\n"
"v_fma_f32 v144,v144,s56,v184\n"
"v_fma_f32 v145,v145,s56,v184\n"
"v_fma_f32 v146,v146,s56,v184\n"
"v_fma_f32 v147,v147,s56,v184\n"
"v_mfma_f32_16x16x32_bf16 v[96:99],a[40:43],a[0:3],v[96:99]\n"
"ds_read_b64_tr_b16 a[60:61],v253 offset:37632\n"
"v_fma_f32 v148,v148,s56,v184\n"
"v_fma_f32 v149,v149,s56,v184\n"
"v_fma_f32 v150,v150,s56,v184\n"
"v_fma_f32 v151,v151,s56,v184\n"
"v_mul_f32 v144,v144,v80\n"
"v_mul_f32 v145,v145,v81\n"
"v_mfma_f32_16x16x32_bf16 v[96:99],a[44:47],a[16:19],v[96:99]\n"
"ds_read_b64_tr_b16 a[62:63],v253 offset:43776\n"
"v_mul_f32 v146,v146,v82\n"
"v_mul_f32 v147,v147,v83\n"
"v_mul_f32 v148,v148,v84\n"
"v_mul_f32 v149,v149,v85\n"
"v_mul_f32 v150,v150,v86\n"
"v_mul_f32 v151,v151,v87\n"
"v_mfma_f32_16x16x32_bf16 v[96:99],a[48:51],a[208:211],v[96:99]\n"
"ds_read_b64_tr_b16 a[64:65],v255 offset:768\n"
"v_exp_f32 v144,v144\n"
"v_exp_f32 v145,v145\n"
"v_exp_f32 v146,v146\n"
"v_exp_f32 v147,v147\n"
"v_exp_f32 v148,v148\n"
"v_exp_f32 v149,v149\n"
"v_mfma_f32_16x16x32_bf16 v[96:99],a[52:55],a[224:227],v[96:99]\n"
"ds_read_b64_tr_b16 a[66:67],v255 offset:6912\n"
"v_exp_f32 v150,v150\n"
"v_exp_f32 v151,v151\n"
"v_add_f32 v144,1.0,v144\n"
"v_add_f32 v145,1.0,v145\n"
"v_add_f32 v146,1.0,v146\n"
"v_add_f32 v147,1.0,v147\n"
"v_mfma_f32_16x16x32_bf16 v[100:103],a[32:35],a[164:167],v[100:103]\n"
"ds_read_b64_tr_b16 a[68:69],v255 offset:13056\n"
"v_add_f32 v148,1.0,v148\n"
"v_add_f32 v149,1.0,v149\n"
"v_add_f32 v150,1.0,v150\n"
"v_add_f32 v151,1.0,v151\n"
"v_rcp_f32 v144,v144\n"
"v_rcp_f32 v145,v145\n"
"v_mfma_f32_16x16x32_bf16 v[100:103],a[36:39],a[180:183],v[100:103]\n"
"ds_read_b64_tr_b16 a[70:71],v255 offset:19200\n"
"v_rcp_f32 v146,v146\n"
"v_rcp_f32 v147,v147\n"
"v_rcp_f32 v148,v148\n"
"v_rcp_f32 v149,v149\n"
"v_rcp_f32 v150,v150\n"
"v_rcp_f32 v151,v151\n"
"v_mfma_f32_16x16x32_bf16 v[100:103],a[40:43],a[4:7],v[100:103]\n"
"ds_read_b64_tr_b16 a[72:73],v255 offset:25344\n"
"v_mul_f32 v144,v144,v80\n"
"v_mul_f32 v145,v145,v81\n"
"v_mul_f32 v146,v146,v82\n"
"v_mul_f32 v147,v147,v83\n"
"v_mul_f32 v148,v148,v84\n"
"v_mul_f32 v149,v149,v85\n"
"v_mfma_f32_16x16x32_bf16 v[100:103],a[44:47],a[20:23],v[100:103]\n"
"ds_read_b64_tr_b16 a[74:75],v255 offset:31488\n"
"v_mul_f32 v150,v150,v86\n"
"v_mul_f32 v151,v151,v87\n"
"v_cvt_pk_bf16_f32 v194,v144,v145\n"
"v_cvt_pk_bf16_f32 v195,v146,v147\n"
"v_cvt_pk_bf16_f32 v198,v148,v149\n"
"v_cvt_pk_bf16_f32 v199,v150,v151\n"
"v_mfma_f32_16x16x32_bf16 v[100:103],a[48:51],a[212:215],v[100:103]\n"
"ds_read_b64_tr_b16 a[76:77],v255 offset:37632\n"
"s_mov_b32 s41,8192\n"
"buffer_store_dwordx4 v[192:195],v209,s[84:87],s41 offen sc1\n"
"buffer_store_dwordx4 v[196:199],v209,s[84:87],s41 offen offset:1024 sc1\n"
"v_mul_f32 v144,v88,v88\n"
"v_mul_f32 v145,v89,v89\n"
"v_mul_f32 v146,v90,v90\n"
"v_mfma_f32_16x16x32_bf16 v[100:103],a[52:55],a[228:231],v[100:103]\n"
"ds_read_b64_tr_b16 a[78:79],v255 offset:43776\n"
"v_mul_f32 v147,v91,v91\n"
"v_mul_f32 v148,v92,v92\n"
"v_mul_f32 v149,v93,v93\n"
"v_mul_f32 v150,v94,v94\n"
"v_mul_f32 v151,v95,v95\n"
"v_fma_f32 v144,v144,s56,v184\n"
"v_mfma_f32_16x16x32_bf16 v[104:107],a[32:35],a[168:171],v[104:107]\n"
"buffer_load_dwordx4 a[200:203],v209,s[88:91],s40 offen offset:2048 sc1\n"
"v_fma_f32 v145,v145,s56,v184\n"
"v_fma_f32 v146,v146,s56,v184\n"
"v_fma_f32 v147,v147,s56,v184\n"
"v_fma_f32 v148,v148,s56,v184\n"
"v_fma_f32 v149,v149,s56,v184\n"
"v_fma_f32 v150,v150,s56,v184\n"
"v_mfma_f32_16x16x32_bf16 v[104:107],a[36:39],a[184:187],v[104:107]\n"
"v_fma_f32 v151,v151,s56,v184\n"
"v_mul_f32 v144,v144,v88\n"
"v_mul_f32 v145,v145,v89\n"
"v_mul_f32 v146,v146,v90\n"
"v_mul_f32 v147,v147,v91\n"
"v_mul_f32 v148,v148,v92\n"
"v_mfma_f32_16x16x32_bf16 v[104:107],a[40:43],a[8:11],v[104:107]\n"
"buffer_load_dwordx4 a[204:207],v209,s[88:91],s40 offen offset:3072 sc1\n"
"v_mul_f32 v149,v149,v93\n"
"v_mul_f32 v150,v150,v94\n"
"v_mul_f32 v151,v151,v95\n"
"v_exp_f32 v144,v144\n"
"v_exp_f32 v145,v145\n"
"v_exp_f32 v146,v146\n"
"v_mfma_f32_16x16x32_bf16 v[104:107],a[44:47],a[24:27],v[104:107]\n"
"v_exp_f32 v147,v147\n"
"v_exp_f32 v148,v148\n"
"v_exp_f32 v149,v149\n"
"v_exp_f32 v150,v150\n"
"v_exp_f32 v151,v151\n"
"v_add_f32 v144,1.0,v144\n"
"v_mfma_f32_16x16x32_bf16 v[104:107],a[48:51],a[216:219],v[104:107]\n"
"s_add_u32 s40,s40,4096\n"
"buffer_load_dwordx4 a[240:243],v209,s[88:91],s40 offen sc1\n"
"v_add_f32 v145,1.0,v145\n"
"v_add_f32 v146,1.0,v146\n"
"v_add_f32 v147,1.0,v147\n"
"v_add_f32 v148,1.0,v148\n"
"v_add_f32 v149,1.0,v149\n"
"v_add_f32 v150,1.0,v150\n"
"v_mfma_f32_16x16x32_bf16 v[104:107],a[52:55],a[232:235],v[104:107]\n"
"v_add_f32 v151,1.0,v151\n"
"v_rcp_f32 v144,v144\n"
"v_rcp_f32 v145,v145\n"
"v_rcp_f32 v146,v146\n"
"v_rcp_f32 v147,v147\n"
"v_rcp_f32 v148,v148\n"
"v_mfma_f32_16x16x32_bf16 v[108:111],a[32:35],a[172:175],v[108:111]\n"
"buffer_load_dwordx4 a[244:247],v209,s[88:91],s40 offen offset:1024 sc1\n"
"v_rcp_f32 v149,v149\n"
"v_rcp_f32 v150,v150\n"
"v_rcp_f32 v151,v151\n"
"v_mul_f32 v144,v144,v88\n"
"v_mul_f32 v145,v145,v89\n"
"v_mul_f32 v146,v146,v90\n"
"v_mfma_f32_16x16x32_bf16 v[108:111],a[36:39],a[188:191],v[108:111]\n"
"v_mul_f32 v147,v147,v91\n"
"v_mul_f32 v148,v148,v92\n"
"v_mul_f32 v149,v149,v93\n"
"v_mul_f32 v150,v150,v94\n"
"v_mul_f32 v151,v151,v95\n"
"v_cvt_pk_bf16_f32 v202,v144,v145\n"
"v_mfma_f32_16x16x32_bf16 v[108:111],a[40:43],a[12:15],v[108:111]\n"
"buffer_load_dwordx4 a[248:251],v209,s[88:91],s40 offen offset:2048 sc1\n"
"v_cvt_pk_bf16_f32 v203,v146,v147\n"
"v_cvt_pk_bf16_f32 v206,v148,v149\n"
"v_cvt_pk_bf16_f32 v207,v150,v151\n"
"s_mov_b32 s41,8192\n"
"buffer_store_dwordx4 v[200:203],v209,s[84:87],s41 offen offset:2048 sc1\n"
"buffer_store_dwordx4 v[204:207],v209,s[84:87],s41 offen offset:3072 sc1\n"
"v_mfma_f32_16x16x32_bf16 v[108:111],a[44:47],a[28:31],v[108:111]\n"
"v_mfma_f32_16x16x32_bf16 v[108:111],a[48:51],a[220:223],v[108:111]\n"
"buffer_load_dwordx4 a[252:255],v209,s[88:91],s40 offen offset:3072 sc1\n"
"v_mfma_f32_16x16x32_bf16 v[108:111],a[52:55],a[236:239],v[108:111]\n"
"s_waitcnt lgkmcnt(0)\n"
"v_mfma_f32_16x16x32_bf16 v[112:115],a[56:59],a[160:163],v[112:115]\n"
"ds_read_b64_tr_b16 a[32:33],v252 offset:25600\n"
"v_mul_f32 v144,v96,v96\n"
"v_mul_f32 v145,v97,v97\n"
"v_mul_f32 v146,v98,v98\n"
"v_mul_f32 v147,v99,v99\n"
"v_mul_f32 v148,v100,v100\n"
"v_mfma_f32_16x16x32_bf16 v[112:115],a[60:63],a[176:179],v[112:115]\n"
"ds_read_b64_tr_b16 a[34:35],v252 offset:31744\n"
"v_mul_f32 v149,v101,v101\n"
"v_mul_f32 v150,v102,v102\n"
"v_mul_f32 v151,v103,v103\n"
"v_fma_f32 v144,v144,s56,v184\n"
"v_fma_f32 v145,v145,s56,v184\n"
"v_mfma_f32_16x16x32_bf16 v[112:115],a[64:67],a[0:3],v[112:115]\n"
"ds_read_b64_tr_b16 a[36:37],v252 offset:37888\n"
"v_fma_f32 v146,v146,s56,v184\n"
"v_fma_f32 v147,v147,s56,v184\n"
"v_fma_f32 v148,v148,s56,v184\n"
"v_fma_f32 v149,v149,s56,v184\n"
"v_fma_f32 v150,v150,s56,v184\n"
"v_mfma_f32_16x16x32_bf16 v[112:115],a[68:71],a[16:19],v[112:115]\n"
"ds_read_b64_tr_b16 a[38:39],v252 offset:44032\n"
"v_fma_f32 v151,v151,s56,v184\n"
"v_mul_f32 v144,v144,v96\n"
"v_mul_f32 v145,v145,v97\n"
"v_mul_f32 v146,v146,v98\n"
"v_mul_f32 v147,v147,v99\n"
"v_mfma_f32_16x16x32_bf16 v[112:115],a[72:75],a[208:211],v[112:115]\n"
"ds_read_b64_tr_b16 a[40:41],v254 offset:1024\n"
"v_mul_f32 v148,v148,v100\n"
"v_mul_f32 v149,v149,v101\n"
"v_mul_f32 v150,v150,v102\n"
"v_mul_f32 v151,v151,v103\n"
"v_exp_f32 v144,v144\n"
"v_mfma_f32_16x16x32_bf16 v[112:115],a[76:79],a[224:227],v[112:115]\n"
"ds_read_b64_tr_b16 a[42:43],v254 offset:7168\n"
"v_exp_f32 v145,v145\n"
"v_exp_f32 v146,v146\n"
"v_exp_f32 v147,v147\n"
"v_exp_f32 v148,v148\n"
"v_exp_f32 v149,v149\n"
"v_mfma_f32_16x16x32_bf16 v[116:119],a[56:59],a[164:167],v[116:119]\n"
"ds_read_b64_tr_b16 a[44:45],v254 offset:13312\n"
"v_exp_f32 v150,v150\n"
"v_exp_f32 v151,v151\n"
"v_add_f32 v144,1.0,v144\n"
"v_add_f32 v145,1.0,v145\n"
"v_add_f32 v146,1.0,v146\n"
"v_mfma_f32_16x16x32_bf16 v[116:119],a[60:63],a[180:183],v[116:119]\n"
"ds_read_b64_tr_b16 a[46:47],v254 offset:19456\n"
"v_add_f32 v147,1.0,v147\n"
"v_add_f32 v148,1.0,v148\n"
"v_add_f32 v149,1.0,v149\n"
"v_add_f32 v150,1.0,v150\n"
"v_add_f32 v151,1.0,v151\n"
"v_mfma_f32_16x16x32_bf16 v[116:119],a[64:67],a[4:7],v[116:119]\n"
"ds_read_b64_tr_b16 a[48:49],v254 offset:25600\n"
"v_rcp_f32 v144,v144\n"
"v_rcp_f32 v145,v145\n"
"v_rcp_f32 v146,v146\n"
"v_rcp_f32 v147,v147\n"
"v_rcp_f32 v148,v148\n"
"v_mfma_f32_16x16x32_bf16 v[116:119],a[68:71],a[20:23],v[116:119]\n"
"ds_read_b64_tr_b16 a[50:51],v254 offset:31744\n"
"v_rcp_f32 v149,v149\n"
"v_rcp_f32 v150,v150\n"
"v_rcp_f32 v151,v151\n"
"v_mul_f32 v144,v144,v96\n"
"v_mul_f32 v145,v145,v97\n"
"v_mfma_f32_16x16x32_bf16 v[116:119],a[72:75],a[212:215],v[116:119]\n"
"ds_read_b64_tr_b16 a[52:53],v254 offset:37888\n"
"v_mul_f32 v146,v146,v98\n"
"v_mul_f32 v147,v147,v99\n"
"v_mul_f32 v148,v148,v100\n"
"v_mul_f32 v149,v149,v101\n"
"v_mul_f32 v150,v150,v102\n"
"v_mfma_f32_16x16x32_bf16 v[116:119],a[76:79],a[228:231],v[116:119]\n"
"ds_read_b64_tr_b16 a[54:55],v254 offset:44032\n"
"v_mul_f32 v151,v151,v103\n"
"v_cvt_pk_bf16_f32 v228,v144,v145\n"
"v_cvt_pk_bf16_f32 v229,v146,v147\n"
"v_cvt_pk_bf16_f32 v232,v148,v149\n"
"v_cvt_pk_bf16_f32 v233,v150,v151\n"
"v_mfma_f32_16x16x32_bf16 v[120:123],a[56:59],a[168:171],v[120:123]\n"
"s_add_u32 s40,s40,4096\n"
"v_mul_f32 v144,v104,v104\n"
"v_mul_f32 v145,v105,v105\n"
"v_mul_f32 v146,v106,v106\n"
"v_mul_f32 v147,v107,v107\n"
"v_mul_f32 v148,v108,v108\n"
"v_mfma_f32_16x16x32_bf16 v[120:123],a[60:63],a[184:187],v[120:123]\n"
"v_mul_f32 v149,v109,v109\n"
"v_mul_f32 v150,v110,v110\n"
"v_mul_f32 v151,v111,v111\n"
"v_fma_f32 v144,v144,s56,v184\n"
"v_fma_f32 v145,v145,s56,v184\n"
"v_mfma_f32_16x16x32_bf16 v[120:123],a[64:67],a[8:11],v[120:123]\n"
"v_fma_f32 v146,v146,s56,v184\n"
"v_fma_f32 v147,v147,s56,v184\n"
"v_fma_f32 v148,v148,s56,v184\n"
"v_fma_f32 v149,v149,s56,v184\n"
"v_fma_f32 v150,v150,s56,v184\n"
"v_mfma_f32_16x16x32_bf16 v[120:123],a[68:71],a[24:27],v[120:123]\n"
"v_fma_f32 v151,v151,s56,v184\n"
"v_mul_f32 v144,v144,v104\n"
"v_mul_f32 v145,v145,v105\n"
"v_mul_f32 v146,v146,v106\n"
"v_mul_f32 v147,v147,v107\n"
"v_mfma_f32_16x16x32_bf16 v[120:123],a[72:75],a[216:219],v[120:123]\n"
"v_mul_f32 v148,v148,v108\n"
"v_mul_f32 v149,v149,v109\n"
"v_mul_f32 v150,v150,v110\n"
"v_mul_f32 v151,v151,v111\n"
"v_exp_f32 v144,v144\n"
"v_mfma_f32_16x16x32_bf16 v[120:123],a[76:79],a[232:235],v[120:123]\n"
"v_exp_f32 v145,v145\n"
"v_exp_f32 v146,v146\n"
"v_exp_f32 v147,v147\n"
"v_exp_f32 v148,v148\n"
"v_exp_f32 v149,v149\n"
"v_mfma_f32_16x16x32_bf16 v[124:127],a[56:59],a[172:175],v[124:127]\n"
"v_exp_f32 v150,v150\n"
"v_exp_f32 v151,v151\n"
"v_add_f32 v144,1.0,v144\n"
"v_add_f32 v145,1.0,v145\n"
"v_add_f32 v146,1.0,v146\n"
"v_mfma_f32_16x16x32_bf16 v[124:127],a[60:63],a[188:191],v[124:127]\n"
"v_add_f32 v147,1.0,v147\n"
"v_add_f32 v148,1.0,v148\n"
"v_add_f32 v149,1.0,v149\n"
"v_add_f32 v150,1.0,v150\n"
"v_add_f32 v151,1.0,v151\n"
"v_mfma_f32_16x16x32_bf16 v[124:127],a[64:67],a[12:15],v[124:127]\n"
"v_rcp_f32 v144,v144\n"
"v_rcp_f32 v145,v145\n"
"v_rcp_f32 v146,v146\n"
"v_rcp_f32 v147,v147\n"
"v_rcp_f32 v148,v148\n"
"v_mfma_f32_16x16x32_bf16 v[124:127],a[68:71],a[28:31],v[124:127]\n"
"v_rcp_f32 v149,v149\n"
"v_rcp_f32 v150,v150\n"
"v_rcp_f32 v151,v151\n"
"v_mul_f32 v144,v144,v104\n"
"v_mul_f32 v145,v145,v105\n"
"v_mfma_f32_16x16x32_bf16 v[124:127],a[72:75],a[220:223],v[124:127]\n"
"v_mul_f32 v146,v146,v106\n"
"v_mul_f32 v147,v147,v107\n"
"v_mul_f32 v148,v148,v108\n"
"v_mul_f32 v149,v149,v109\n"
"v_mul_f32 v150,v150,v110\n"
"v_mfma_f32_16x16x32_bf16 v[124:127],a[76:79],a[236:239],v[124:127]\n"
"v_mul_f32 v151,v151,v111\n"
"v_cvt_pk_bf16_f32 v236,v144,v145\n"
"v_cvt_pk_bf16_f32 v237,v146,v147\n"
"v_cvt_pk_bf16_f32 v242,v148,v149\n"
"v_cvt_pk_bf16_f32 v243,v150,v151\n"
"s_waitcnt lgkmcnt(0)\n"
"v_mfma_f32_16x16x32_bf16 v[128:131],a[32:35],a[160:163],v[128:131]\n"
"v_mul_f32 v144,v112,v112\n"
"v_mul_f32 v145,v113,v113\n"
"v_mul_f32 v146,v114,v114\n"
"v_mul_f32 v147,v115,v115\n"
"v_mul_f32 v148,v116,v116\n"
"v_mul_f32 v149,v117,v117\n"
"v_mfma_f32_16x16x32_bf16 v[128:131],a[36:39],a[176:179],v[128:131]\n"
"v_mul_f32 v150,v118,v118\n"
"v_mul_f32 v151,v119,v119\n"
"v_fma_f32 v144,v144,s56,v184\n"
"v_fma_f32 v145,v145,s56,v184\n"
"v_fma_f32 v146,v146,s56,v184\n"
"v_fma_f32 v147,v147,s56,v184\n"
"v_mfma_f32_16x16x32_bf16 v[128:131],a[40:43],a[0:3],v[128:131]\n"
"v_fma_f32 v148,v148,s56,v184\n"
"v_fma_f32 v149,v149,s56,v184\n"
"v_fma_f32 v150,v150,s56,v184\n"
"v_fma_f32 v151,v151,s56,v184\n"
"v_mul_f32 v144,v144,v112\n"
"v_mul_f32 v145,v145,v113\n"
"v_mfma_f32_16x16x32_bf16 v[128:131],a[44:47],a[16:19],v[128:131]\n"
"v_mul_f32 v146,v146,v114\n"
"v_mul_f32 v147,v147,v115\n"
"v_mul_f32 v148,v148,v116\n"
"v_mul_f32 v149,v149,v117\n"
"v_mul_f32 v150,v150,v118\n"
"v_mul_f32 v151,v151,v119\n"
"v_mfma_f32_16x16x32_bf16 v[128:131],a[48:51],a[208:211],v[128:131]\n"
"v_exp_f32 v144,v144\n"
"v_exp_f32 v145,v145\n"
"v_exp_f32 v146,v146\n"
"v_exp_f32 v147,v147\n"
"v_exp_f32 v148,v148\n"
"v_exp_f32 v149,v149\n"
"v_mfma_f32_16x16x32_bf16 v[128:131],a[52:55],a[224:227],v[128:131]\n"
"v_exp_f32 v150,v150\n"
"v_exp_f32 v151,v151\n"
"v_add_f32 v144,1.0,v144\n"
"v_add_f32 v145,1.0,v145\n"
"v_add_f32 v146,1.0,v146\n"
"v_add_f32 v147,1.0,v147\n"
"v_mfma_f32_16x16x32_bf16 v[132:135],a[32:35],a[164:167],v[132:135]\n"
"v_add_f32 v148,1.0,v148\n"
"v_add_f32 v149,1.0,v149\n"
"v_add_f32 v150,1.0,v150\n"
"v_add_f32 v151,1.0,v151\n"
"v_rcp_f32 v144,v144\n"
"v_rcp_f32 v145,v145\n"
"v_mfma_f32_16x16x32_bf16 v[132:135],a[36:39],a[180:183],v[132:135]\n"
"v_rcp_f32 v146,v146\n"
"v_rcp_f32 v147,v147\n"
"v_rcp_f32 v148,v148\n"
"v_rcp_f32 v149,v149\n"
"v_rcp_f32 v150,v150\n"
"v_rcp_f32 v151,v151\n"
"v_mfma_f32_16x16x32_bf16 v[132:135],a[40:43],a[4:7],v[132:135]\n"
"v_mul_f32 v144,v144,v112\n"
"v_mul_f32 v145,v145,v113\n"
"v_mul_f32 v146,v146,v114\n"
"v_mul_f32 v147,v147,v115\n"
"v_mul_f32 v148,v148,v116\n"
"v_mul_f32 v149,v149,v117\n"
"v_mfma_f32_16x16x32_bf16 v[132:135],a[44:47],a[20:23],v[132:135]\n"
"v_mul_f32 v150,v150,v118\n"
"v_mul_f32 v151,v151,v119\n"
"v_cvt_pk_bf16_f32 v230,v144,v145\n"
"v_cvt_pk_bf16_f32 v231,v146,v147\n"
"v_cvt_pk_bf16_f32 v234,v148,v149\n"
"v_cvt_pk_bf16_f32 v235,v150,v151\n"
"v_mfma_f32_16x16x32_bf16 v[132:135],a[48:51],a[212:215],v[132:135]\n"
"s_mov_b32 s41,12288\n"
"buffer_store_dwordx4 v[228:231],v209,s[84:87],s41 offen sc1\n"
"buffer_store_dwordx4 v[232:235],v209,s[84:87],s41 offen offset:1024 sc1\n"
"v_mul_f32 v144,v120,v120\n"
"v_mul_f32 v145,v121,v121\n"
"v_mul_f32 v146,v122,v122\n"
"v_mfma_f32_16x16x32_bf16 v[132:135],a[52:55],a[228:231],v[132:135]\n"
"v_mul_f32 v147,v123,v123\n"
"v_mul_f32 v148,v124,v124\n"
"v_mul_f32 v149,v125,v125\n"
"v_mul_f32 v150,v126,v126\n"
"v_mul_f32 v151,v127,v127\n"
"v_fma_f32 v144,v144,s56,v184\n"
"v_mfma_f32_16x16x32_bf16 v[136:139],a[32:35],a[168:171],v[136:139]\n"
"v_fma_f32 v145,v145,s56,v184\n"
"v_fma_f32 v146,v146,s56,v184\n"
"v_fma_f32 v147,v147,s56,v184\n"
"v_fma_f32 v148,v148,s56,v184\n"
"v_fma_f32 v149,v149,s56,v184\n"
"v_fma_f32 v150,v150,s56,v184\n"
"v_mfma_f32_16x16x32_bf16 v[136:139],a[36:39],a[184:187],v[136:139]\n"
"v_fma_f32 v151,v151,s56,v184\n"
"v_mul_f32 v144,v144,v120\n"
"v_mul_f32 v145,v145,v121\n"
"v_mul_f32 v146,v146,v122\n"
"v_mul_f32 v147,v147,v123\n"
"v_mul_f32 v148,v148,v124\n"
"v_mfma_f32_16x16x32_bf16 v[136:139],a[40:43],a[8:11],v[136:139]\n"
"v_mul_f32 v149,v149,v125\n"
"v_mul_f32 v150,v150,v126\n"
"v_mul_f32 v151,v151,v127\n"
"v_exp_f32 v144,v144\n"
"v_exp_f32 v145,v145\n"
"v_exp_f32 v146,v146\n"
"v_mfma_f32_16x16x32_bf16 v[136:139],a[44:47],a[24:27],v[136:139]\n"
"v_exp_f32 v147,v147\n"
"v_exp_f32 v148,v148\n"
"v_exp_f32 v149,v149\n"
"v_exp_f32 v150,v150\n"
"v_exp_f32 v151,v151\n"
"v_add_f32 v144,1.0,v144\n"
"v_mfma_f32_16x16x32_bf16 v[136:139],a[48:51],a[216:219],v[136:139]\n"
"v_add_f32 v145,1.0,v145\n"
"v_add_f32 v146,1.0,v146\n"
"v_add_f32 v147,1.0,v147\n"
"v_add_f32 v148,1.0,v148\n"
"v_add_f32 v149,1.0,v149\n"
"v_add_f32 v150,1.0,v150\n"
"v_mfma_f32_16x16x32_bf16 v[136:139],a[52:55],a[232:235],v[136:139]\n"
"v_add_f32 v151,1.0,v151\n"
"v_rcp_f32 v144,v144\n"
"v_rcp_f32 v145,v145\n"
"v_rcp_f32 v146,v146\n"
"v_rcp_f32 v147,v147\n"
"v_rcp_f32 v148,v148\n"
"v_mfma_f32_16x16x32_bf16 v[140:143],a[32:35],a[172:175],v[140:143]\n"
"v_rcp_f32 v149,v149\n"
"v_rcp_f32 v150,v150\n"
"v_rcp_f32 v151,v151\n"
"v_mul_f32 v144,v144,v120\n"
"v_mul_f32 v145,v145,v121\n"
"v_mul_f32 v146,v146,v122\n"
"v_mfma_f32_16x16x32_bf16 v[140:143],a[36:39],a[188:191],v[140:143]\n"
"v_mul_f32 v147,v147,v123\n"
"v_mul_f32 v148,v148,v124\n"
"v_mul_f32 v149,v149,v125\n"
"v_mul_f32 v150,v150,v126\n"
"v_mul_f32 v151,v151,v127\n"
"v_cvt_pk_bf16_f32 v238,v144,v145\n"
"v_mfma_f32_16x16x32_bf16 v[140:143],a[40:43],a[12:15],v[140:143]\n"
"v_cvt_pk_bf16_f32 v239,v146,v147\n"
"v_cvt_pk_bf16_f32 v244,v148,v149\n"
"v_cvt_pk_bf16_f32 v245,v150,v151\n"
"s_mov_b32 s41,12288\n"
"buffer_store_dwordx4 v[236:239],v209,s[84:87],s41 offen offset:2048 sc1\n"
"buffer_store_dwordx4 v[242:245],v209,s[84:87],s41 offen offset:3072 sc1\n"
"v_mfma_f32_16x16x32_bf16 v[140:143],a[44:47],a[28:31],v[140:143]\n"
"v_mfma_f32_16x16x32_bf16 v[140:143],a[48:51],a[220:223],v[140:143]\n"
"v_mfma_f32_16x16x32_bf16 v[140:143],a[52:55],a[236:239],v[140:143]\n"
"s_barrier\n"
"s_nop 3\n"
"s_add_u32 m0,s51,90112\n"
"s_nop 0\n"
"buffer_load_dwordx4 v213,s[72:75],s39 offen lds\n"
"v_mul_f32 v144,v128,v128\n"
"v_mul_f32 v145,v129,v129\n"
"v_mul_f32 v146,v130,v130\n"
"v_mul_f32 v147,v131,v131\n"
"v_mul_f32 v148,v132,v132\n"
"v_mul_f32 v149,v133,v133\n"
"v_mul_f32 v150,v134,v134\n"
"v_mul_f32 v151,v135,v135\n"
"buffer_load_dwordx4 v214,s[72:75],s39 offen offset:1024 lds\n"
"v_fma_f32 v144,v144,s56,v184\n"
"v_fma_f32 v145,v145,s56,v184\n"
"v_fma_f32 v146,v146,s56,v184\n"
"v_fma_f32 v147,v147,s56,v184\n"
"v_fma_f32 v148,v148,s56,v184\n"
"v_fma_f32 v149,v149,s56,v184\n"
"v_fma_f32 v150,v150,s56,v184\n"
"v_fma_f32 v151,v151,s56,v184\n"
"buffer_load_dwordx4 v215,s[72:75],s39 offen offset:2048 lds\n"
"v_mul_f32 v144,v144,v128\n"
"v_mul_f32 v145,v145,v129\n"
"v_mul_f32 v146,v146,v130\n"
"v_mul_f32 v147,v147,v131\n"
"v_mul_f32 v148,v148,v132\n"
"v_mul_f32 v149,v149,v133\n"
"v_mul_f32 v150,v150,v134\n"
"v_mul_f32 v151,v151,v135\n"
"s_add_u32 s39,s39,s52\n"
"s_add_u32 m0,s51,93184\n"
"s_nop 0\n"
"buffer_load_dwordx4 v213,s[72:75],s39 offen lds\n"
"v_exp_f32 v144,v144\n"
"v_exp_f32 v145,v145\n"
"v_exp_f32 v146,v146\n"
"v_exp_f32 v147,v147\n"
"v_exp_f32 v148,v148\n"
"v_exp_f32 v149,v149\n"
"v_exp_f32 v150,v150\n"
"v_exp_f32 v151,v151\n"
"buffer_load_dwordx4 v214,s[72:75],s39 offen offset:1024 lds\n"
"v_add_f32 v144,1.0,v144\n"
"v_add_f32 v145,1.0,v145\n"
"v_add_f32 v146,1.0,v146\n"
"v_add_f32 v147,1.0,v147\n"
"v_add_f32 v148,1.0,v148\n"
"v_add_f32 v149,1.0,v149\n"
"v_add_f32 v150,1.0,v150\n"
"v_add_f32 v151,1.0,v151\n"
"buffer_load_dwordx4 v215,s[72:75],s39 offen offset:2048 lds\n"
"v_rcp_f32 v144,v144\n"
"v_rcp_f32 v145,v145\n"
"v_rcp_f32 v146,v146\n"
"v_rcp_f32 v147,v147\n"
"v_rcp_f32 v148,v148\n"
"v_rcp_f32 v149,v149\n"
"v_rcp_f32 v150,v150\n"
"v_rcp_f32 v151,v151\n"
"s_add_u32 s39,s39,s53\n"
"buffer_load_dwordx4 a[208:211],v209,s[88:91],s40 offen sc1\n"
"v_mul_f32 v144,v144,v128\n"
"v_mul_f32 v145,v145,v129\n"
"v_mul_f32 v146,v146,v130\n"
"v_mul_f32 v147,v147,v131\n"
"v_mul_f32 v148,v148,v132\n"
"v_mul_f32 v149,v149,v133\n"
"v_mul_f32 v150,v150,v134\n"
"v_mul_f32 v151,v151,v135\n"
"buffer_load_dwordx4 a[212:215],v209,s[88:91],s40 offen offset:1024 sc1\n"
"v_cvt_pk_bf16_f32 v216,v144,v145\n"
"v_cvt_pk_bf16_f32 v217,v146,v147\n"
"v_cvt_pk_bf16_f32 v218,v148,v149\n"
"v_cvt_pk_bf16_f32 v219,v150,v151\n"
"s_mov_b32 s41,16384\n"
"buffer_store_dwordx2 v[216:217],v191,s[84:87],s41 offen sc1\n"
"buffer_store_dwordx2 v[218:219],v191,s[84:87],s41 offen offset:1024 sc1\n"
"v_mul_f32 v144,v136,v136\n"
"buffer_load_dwordx4 a[216:219],v209,s[88:91],s40 offen offset:2048 sc1\n"
"v_mul_f32 v145,v137,v137\n"
"v_mul_f32 v146,v138,v138\n"
"v_mul_f32 v147,v139,v139\n"
"v_mul_f32 v148,v140,v140\n"
"v_mul_f32 v149,v141,v141\n"
"v_mul_f32 v150,v142,v142\n"
"v_mul_f32 v151,v143,v143\n"
"v_fma_f32 v144,v144,s56,v184\n"
"buffer_load_dwordx4 a[220:223],v209,s[88:91],s40 offen offset:3072 sc1\n"
"v_fma_f32 v145,v145,s56,v184\n"
"v_fma_f32 v146,v146,s56,v184\n"
"v_fma_f32 v147,v147,s56,v184\n"
"v_fma_f32 v148,v148,s56,v184\n"
"v_fma_f32 v149,v149,s56,v184\n"
"v_fma_f32 v150,v150,s56,v184\n"
"v_fma_f32 v151,v151,s56,v184\n"
"v_mul_f32 v144,v144,v136\n"
"s_add_u32 s40,s40,4096\n"
"buffer_load_dwordx4 a[224:227],v209,s[88:91],s40 offen sc1\n"
"v_mul_f32 v145,v145,v137\n"
"v_mul_f32 v146,v146,v138\n"
"v_mul_f32 v147,v147,v139\n"
"v_mul_f32 v148,v148,v140\n"
"v_mul_f32 v149,v149,v141\n"
"v_mul_f32 v150,v150,v142\n"
"v_mul_f32 v151,v151,v143\n"
"v_exp_f32 v144,v144\n"
"buffer_load_dwordx4 a[228:231],v209,s[88:91],s40 offen offset:1024 sc1\n"
"v_exp_f32 v145,v145\n"
"v_exp_f32 v146,v146\n"
"v_exp_f32 v147,v147\n"
"v_exp_f32 v148,v148\n"
"v_exp_f32 v149,v149\n"
"v_exp_f32 v150,v150\n"
"v_exp_f32 v151,v151\n"
"v_add_f32 v144,1.0,v144\n"
"buffer_load_dwordx4 a[232:235],v209,s[88:91],s40 offen offset:2048 sc1\n"
"v_add_f32 v145,1.0,v145\n"
"v_add_f32 v146,1.0,v146\n"
"v_add_f32 v147,1.0,v147\n"
"v_add_f32 v148,1.0,v148\n"
"v_add_f32 v149,1.0,v149\n"
"v_add_f32 v150,1.0,v150\n"
"v_add_f32 v151,1.0,v151\n"
"v_rcp_f32 v144,v144\n"
"buffer_load_dwordx4 a[236:239],v209,s[88:91],s40 offen offset:3072 sc1\n"
"v_rcp_f32 v145,v145\n"
"v_rcp_f32 v146,v146\n"
"v_rcp_f32 v147,v147\n"
"v_rcp_f32 v148,v148\n"
"v_rcp_f32 v149,v149\n"
"v_rcp_f32 v150,v150\n"
"v_rcp_f32 v151,v151\n"
"v_mul_f32 v144,v144,v136\n"
"s_add_u32 s40,s40,4096\n"
"v_mul_f32 v145,v145,v137\n"
"v_mul_f32 v146,v146,v138\n"
"v_mul_f32 v147,v147,v139\n"
"v_mul_f32 v148,v148,v140\n"
"v_mul_f32 v149,v149,v141\n"
"v_mul_f32 v150,v150,v142\n"
"v_mul_f32 v151,v151,v143\n"
"v_cvt_pk_bf16_f32 v220,v144,v145\n"
"v_cvt_pk_bf16_f32 v221,v146,v147\n"
"v_cvt_pk_bf16_f32 v186,v148,v149\n"
"v_cvt_pk_bf16_f32 v187,v150,v151\n"
"s_mov_b32 s41,16384\n"
"buffer_store_dwordx2 v[220:221],v191,s[84:87],s41 offen offset:2048 sc1\n"
"buffer_store_dwordx2 v[186:187],v191,s[84:87],s41 offen offset:3072 sc1\n"
"s_branch LM_GDONE\n"
"LM_GODD:\n"
"ds_read_b64_tr_b16 a[32:33],v252 offset:24576\n"
"ds_read_b64_tr_b16 a[34:35],v252 offset:30720\n"
"ds_read_b64_tr_b16 a[36:37],v252 offset:36864\n"
"ds_read_b64_tr_b16 a[38:39],v252 offset:43008\n"
"ds_read_b64_tr_b16 a[40:41],v254\n"
"ds_read_b64_tr_b16 a[42:43],v254 offset:6144\n"
"ds_read_b64_tr_b16 a[44:45],v254 offset:12288\n"
"ds_read_b64_tr_b16 a[46:47],v254 offset:18432\n"
"ds_read_b64_tr_b16 a[48:49],v254 offset:24576\n"
"ds_read_b64_tr_b16 a[50:51],v254 offset:30720\n"
"ds_read_b64_tr_b16 a[52:53],v254 offset:36864\n"
"ds_read_b64_tr_b16 a[54:55],v254 offset:43008\n"
"s_waitcnt lgkmcnt(0)\n"
"v_mfma_f32_16x16x32_bf16 v[0:3],a[32:35],a[160:163],v[0:3]\n"
"ds_read_b64_tr_b16 a[56:57],v253 offset:24576\n"
"v_mfma_f32_16x16x32_bf16 v[0:3],a[36:39],a[176:179],v[0:3]\n"
"ds_read_b64_tr_b16 a[58:59],v253 offset:30720\n"
"v_mfma_f32_16x16x32_bf16 v[0:3],a[40:43],a[0:3],v[0:3]\n"
"ds_read_b64_tr_b16 a[60:61],v253 offset:36864\n"
"v_mfma_f32_16x16x32_bf16 v[0:3],a[44:47],a[16:19],v[0:3]\n"
"ds_read_b64_tr_b16 a[62:63],v253 offset:43008\n"
"v_mfma_f32_16x16x32_bf16 v[0:3],a[48:51],a[208:211],v[0:3]\n"
"ds_read_b64_tr_b16 a[64:65],v255\n"
"v_mfma_f32_16x16x32_bf16 v[0:3],a[52:55],a[224:227],v[0:3]\n"
"ds_read_b64_tr_b16 a[66:67],v255 offset:6144\n"
"v_mfma_f32_16x16x32_bf16 v[4:7],a[32:35],a[164:167],v[4:7]\n"
"ds_read_b64_tr_b16 a[68:69],v255 offset:12288\n"
"v_mfma_f32_16x16x32_bf16 v[4:7],a[36:39],a[180:183],v[4:7]\n"
"ds_read_b64_tr_b16 a[70:71],v255 offset:18432\n"
"v_mfma_f32_16x16x32_bf16 v[4:7],a[40:43],a[4:7],v[4:7]\n"
"ds_read_b64_tr_b16 a[72:73],v255 offset:24576\n"
"v_mfma_f32_16x16x32_bf16 v[4:7],a[44:47],a[20:23],v[4:7]\n"
"ds_read_b64_tr_b16 a[74:75],v255 offset:30720\n"
"v_mfma_f32_16x16x32_bf16 v[4:7],a[48:51],a[212:215],v[4:7]\n"
"ds_read_b64_tr_b16 a[76:77],v255 offset:36864\n"
"v_mfma_f32_16x16x32_bf16 v[4:7],a[52:55],a[228:231],v[4:7]\n"
"ds_read_b64_tr_b16 a[78:79],v255 offset:43008\n"
"v_mfma_f32_16x16x32_bf16 v[8:11],a[32:35],a[168:171],v[8:11]\n"
"v_readlane_b32 s39,v248,16\n"
"s_nop 4\n"
"s_add_u32 m0,s51,0\n"
"s_nop 0\n"
"buffer_load_dwordx4 v213,s[72:75],s39 offen lds\n"
"v_mfma_f32_16x16x32_bf16 v[8:11],a[36:39],a[184:187],v[8:11]\n"
"v_mfma_f32_16x16x32_bf16 v[8:11],a[40:43],a[8:11],v[8:11]\n"
"buffer_load_dwordx4 v214,s[72:75],s39 offen offset:1024 lds\n"
"v_mfma_f32_16x16x32_bf16 v[8:11],a[44:47],a[24:27],v[8:11]\n"
"v_mfma_f32_16x16x32_bf16 v[8:11],a[48:51],a[216:219],v[8:11]\n"
"buffer_load_dwordx4 v215,s[72:75],s39 offen offset:2048 lds\n"
"v_mfma_f32_16x16x32_bf16 v[8:11],a[52:55],a[232:235],v[8:11]\n"
"v_mfma_f32_16x16x32_bf16 v[12:15],a[32:35],a[172:175],v[12:15]\n"
"s_add_u32 s39,s39,s52\n"
"s_add_u32 m0,s51,3072\n"
"s_nop 0\n"
"buffer_load_dwordx4 v213,s[72:75],s39 offen lds\n"
"v_mfma_f32_16x16x32_bf16 v[12:15],a[36:39],a[188:191],v[12:15]\n"
"v_mfma_f32_16x16x32_bf16 v[12:15],a[40:43],a[12:15],v[12:15]\n"
"buffer_load_dwordx4 v214,s[72:75],s39 offen offset:1024 lds\n"
"v_mfma_f32_16x16x32_bf16 v[12:15],a[44:47],a[28:31],v[12:15]\n"
"v_mfma_f32_16x16x32_bf16 v[12:15],a[48:51],a[220:223],v[12:15]\n"
"buffer_load_dwordx4 v215,s[72:75],s39 offen offset:2048 lds\n"
"v_mfma_f32_16x16x32_bf16 v[12:15],a[52:55],a[236:239],v[12:15]\n"
"s_waitcnt lgkmcnt(0)\n"
"v_mfma_f32_16x16x32_bf16 v[16:19],a[56:59],a[160:163],v[16:19]\n"
"ds_read_b64_tr_b16 a[32:33],v252 offset:24832\n"
"v_mul_f32 v144,v0,v0\n"
"v_mul_f32 v145,v1,v1\n"
"v_mul_f32 v146,v2,v2\n"
"v_mul_f32 v147,v3,v3\n"
"v_mul_f32 v148,v4,v4\n"
"v_mul_f32 v149,v5,v5\n"
"v_mfma_f32_16x16x32_bf16 v[16:19],a[60:63],a[176:179],v[16:19]\n"
"ds_read_b64_tr_b16 a[34:35],v252 offset:30976\n"
"v_mul_f32 v150,v6,v6\n"
"v_mul_f32 v151,v7,v7\n"
"v_fma_f32 v144,v144,s56,v184\n"
"v_fma_f32 v145,v145,s56,v184\n"
"v_fma_f32 v146,v146,s56,v184\n"
"v_fma_f32 v147,v147,s56,v184\n"
"v_mfma_f32_16x16x32_bf16 v[16:19],a[64:67],a[0:3],v[16:19]\n"
"ds_read_b64_tr_b16 a[36:37],v252 offset:37120\n"
"v_fma_f32 v148,v148,s56,v184\n"
"v_fma_f32 v149,v149,s56,v184\n"
"v_fma_f32 v150,v150,s56,v184\n"
"v_fma_f32 v151,v151,s56,v184\n"
"v_mul_f32 v144,v144,v0\n"
"v_mul_f32 v145,v145,v1\n"
"v_mfma_f32_16x16x32_bf16 v[16:19],a[68:71],a[16:19],v[16:19]\n"
"ds_read_b64_tr_b16 a[38:39],v252 offset:43264\n"
"v_mul_f32 v146,v146,v2\n"
"v_mul_f32 v147,v147,v3\n"
"v_mul_f32 v148,v148,v4\n"
"v_mul_f32 v149,v149,v5\n"
"v_mul_f32 v150,v150,v6\n"
"v_mul_f32 v151,v151,v7\n"
"v_mfma_f32_16x16x32_bf16 v[16:19],a[72:75],a[208:211],v[16:19]\n"
"ds_read_b64_tr_b16 a[40:41],v254 offset:256\n"
"v_exp_f32 v144,v144\n"
"v_exp_f32 v145,v145\n"
"v_exp_f32 v146,v146\n"
"v_exp_f32 v147,v147\n"
"v_exp_f32 v148,v148\n"
"v_exp_f32 v149,v149\n"
"v_mfma_f32_16x16x32_bf16 v[16:19],a[76:79],a[224:227],v[16:19]\n"
"ds_read_b64_tr_b16 a[42:43],v254 offset:6400\n"
"v_exp_f32 v150,v150\n"
"v_exp_f32 v151,v151\n"
"v_add_f32 v144,1.0,v144\n"
"v_add_f32 v145,1.0,v145\n"
"v_add_f32 v146,1.0,v146\n"
"v_add_f32 v147,1.0,v147\n"
"v_mfma_f32_16x16x32_bf16 v[20:23],a[56:59],a[164:167],v[20:23]\n"
"ds_read_b64_tr_b16 a[44:45],v254 offset:12544\n"
"v_add_f32 v148,1.0,v148\n"
"v_add_f32 v149,1.0,v149\n"
"v_add_f32 v150,1.0,v150\n"
"v_add_f32 v151,1.0,v151\n"
"v_rcp_f32 v144,v144\n"
"v_rcp_f32 v145,v145\n"
"v_mfma_f32_16x16x32_bf16 v[20:23],a[60:63],a[180:183],v[20:23]\n"
"ds_read_b64_tr_b16 a[46:47],v254 offset:18688\n"
"v_rcp_f32 v146,v146\n"
"v_rcp_f32 v147,v147\n"
"v_rcp_f32 v148,v148\n"
"v_rcp_f32 v149,v149\n"
"v_rcp_f32 v150,v150\n"
"v_rcp_f32 v151,v151\n"
"v_mfma_f32_16x16x32_bf16 v[20:23],a[64:67],a[4:7],v[20:23]\n"
"ds_read_b64_tr_b16 a[48:49],v254 offset:24832\n"
"v_mul_f32 v144,v144,v0\n"
"v_mul_f32 v145,v145,v1\n"
"v_mul_f32 v146,v146,v2\n"
"v_mul_f32 v147,v147,v3\n"
"v_mul_f32 v148,v148,v4\n"
"v_mul_f32 v149,v149,v5\n"
"v_mfma_f32_16x16x32_bf16 v[20:23],a[68:71],a[20:23],v[20:23]\n"
"ds_read_b64_tr_b16 a[50:51],v254 offset:30976\n"
"v_mul_f32 v150,v150,v6\n"
"v_mul_f32 v151,v151,v7\n"
"v_cvt_pk_bf16_f32 v216,v144,v145\n"
"v_cvt_pk_bf16_f32 v217,v146,v147\n"
"v_cvt_pk_bf16_f32 v218,v148,v149\n"
"v_cvt_pk_bf16_f32 v219,v150,v151\n"
"v_mfma_f32_16x16x32_bf16 v[20:23],a[72:75],a[212:215],v[20:23]\n"
"ds_read_b64_tr_b16 a[52:53],v254 offset:37120\n"
"s_mov_b32 s41,0\n"
"buffer_store_dwordx2 v[216:217],v191,s[84:87],s41 offen offset:512 sc1\n"
"buffer_store_dwordx2 v[218:219],v191,s[84:87],s41 offen offset:1536 sc1\n"
"v_mul_f32 v144,v8,v8\n"
"v_mul_f32 v145,v9,v9\n"
"v_mul_f32 v146,v10,v10\n"
"v_mfma_f32_16x16x32_bf16 v[20:23],a[76:79],a[228:231],v[20:23]\n"
"ds_read_b64_tr_b16 a[54:55],v254 offset:43264\n"
"v_mul_f32 v147,v11,v11\n"
"v_mul_f32 v148,v12,v12\n"
"v_mul_f32 v149,v13,v13\n"
"v_mul_f32 v150,v14,v14\n"
"v_mul_f32 v151,v15,v15\n"
"v_fma_f32 v144,v144,s56,v184\n"
"v_mfma_f32_16x16x32_bf16 v[24:27],a[56:59],a[168:171],v[24:27]\n"
"s_add_u32 s39,s39,s53\n"
"v_readlane_b32 s40,v248,22\n"
"s_nop 4\n"
"buffer_load_dwordx4 a[80:83],v209,s[88:91],s40 offen sc1\n"
"v_fma_f32 v145,v145,s56,v184\n"
"v_fma_f32 v146,v146,s56,v184\n"
"v_fma_f32 v147,v147,s56,v184\n"
"v_fma_f32 v148,v148,s56,v184\n"
"v_fma_f32 v149,v149,s56,v184\n"
"v_fma_f32 v150,v150,s56,v184\n"
"v_mfma_f32_16x16x32_bf16 v[24:27],a[60:63],a[184:187],v[24:27]\n"
"v_fma_f32 v151,v151,s56,v184\n"
"v_mul_f32 v144,v144,v8\n"
"v_mul_f32 v145,v145,v9\n"
"v_mul_f32 v146,v146,v10\n"
"v_mul_f32 v147,v147,v11\n"
"v_mul_f32 v148,v148,v12\n"
"v_mfma_f32_16x16x32_bf16 v[24:27],a[64:67],a[8:11],v[24:27]\n"
"buffer_load_dwordx4 a[84:87],v209,s[88:91],s40 offen offset:1024 sc1\n"
"v_mul_f32 v149,v149,v13\n"
"v_mul_f32 v150,v150,v14\n"
"v_mul_f32 v151,v151,v15\n"
"v_exp_f32 v144,v144\n"
"v_exp_f32 v145,v145\n"
"v_exp_f32 v146,v146\n"
"v_mfma_f32_16x16x32_bf16 v[24:27],a[68:71],a[24:27],v[24:27]\n"
"v_exp_f32 v147,v147\n"
"v_exp_f32 v148,v148\n"
"v_exp_f32 v149,v149\n"
"v_exp_f32 v150,v150\n"
"v_exp_f32 v151,v151\n"
"v_add_f32 v144,1.0,v144\n"
"v_mfma_f32_16x16x32_bf16 v[24:27],a[72:75],a[216:219],v[24:27]\n"
"buffer_load_dwordx4 a[88:91],v209,s[88:91],s40 offen offset:2048 sc1\n"
"v_add_f32 v145,1.0,v145\n"
"v_add_f32 v146,1.0,v146\n"
"v_add_f32 v147,1.0,v147\n"
"v_add_f32 v148,1.0,v148\n"
"v_add_f32 v149,1.0,v149\n"
"v_add_f32 v150,1.0,v150\n"
"v_mfma_f32_16x16x32_bf16 v[24:27],a[76:79],a[232:235],v[24:27]\n"
"v_add_f32 v151,1.0,v151\n"
"v_rcp_f32 v144,v144\n"
"v_rcp_f32 v145,v145\n"
"v_rcp_f32 v146,v146\n"
"v_rcp_f32 v147,v147\n"
"v_rcp_f32 v148,v148\n"
"v_mfma_f32_16x16x32_bf16 v[28:31],a[56:59],a[172:175],v[28:31]\n"
"buffer_load_dwordx4 a[92:95],v209,s[88:91],s40 offen offset:3072 sc1\n"
"v_rcp_f32 v149,v149\n"
"v_rcp_f32 v150,v150\n"
"v_rcp_f32 v151,v151\n"
"v_mul_f32 v144,v144,v8\n"
"v_mul_f32 v145,v145,v9\n"
"v_mul_f32 v146,v146,v10\n"
"v_mfma_f32_16x16x32_bf16 v[28:31],a[60:63],a[188:191],v[28:31]\n"
"v_mul_f32 v147,v147,v11\n"
"v_mul_f32 v148,v148,v12\n"
"v_mul_f32 v149,v149,v13\n"
"v_mul_f32 v150,v150,v14\n"
"v_mul_f32 v151,v151,v15\n"
"v_cvt_pk_bf16_f32 v220,v144,v145\n"
"v_mfma_f32_16x16x32_bf16 v[28:31],a[64:67],a[12:15],v[28:31]\n"
"s_add_u32 s40,s40,4096\n"
"buffer_load_dwordx4 a[96:99],v209,s[88:91],s40 offen sc1\n"
"v_cvt_pk_bf16_f32 v221,v146,v147\n"
"v_cvt_pk_bf16_f32 v186,v148,v149\n"
"v_cvt_pk_bf16_f32 v187,v150,v151\n"
"s_mov_b32 s41,0\n"
"buffer_store_dwordx2 v[220:221],v191,s[84:87],s41 offen offset:2560 sc1\n"
"buffer_store_dwordx2 v[186:187],v191,s[84:87],s41 offen offset:3584 sc1\n"
"v_mfma_f32_16x16x32_bf16 v[28:31],a[68:71],a[28:31],v[28:31]\n"
"v_mfma_f32_16x16x32_bf16 v[28:31],a[72:75],a[220:223],v[28:31]\n"
"buffer_load_dwordx4 a[100:103],v209,s[88:91],s40 offen offset:1024 sc1\n"
"v_mfma_f32_16x16x32_bf16 v[28:31],a[76:79],a[236:239],v[28:31]\n"
"s_waitcnt lgkmcnt(0)\n"
"v_mfma_f32_16x16x32_bf16 v[32:35],a[32:35],a[160:163],v[32:35]\n"
"ds_read_b64_tr_b16 a[56:57],v253 offset:24832\n"
"v_mul_f32 v144,v16,v16\n"
"v_mul_f32 v145,v17,v17\n"
"v_mul_f32 v146,v18,v18\n"
"v_mul_f32 v147,v19,v19\n"
"v_mul_f32 v148,v20,v20\n"
"v_mfma_f32_16x16x32_bf16 v[32:35],a[36:39],a[176:179],v[32:35]\n"
"ds_read_b64_tr_b16 a[58:59],v253 offset:30976\n"
"v_mul_f32 v149,v21,v21\n"
"v_mul_f32 v150,v22,v22\n"
"v_mul_f32 v151,v23,v23\n"
"v_fma_f32 v144,v144,s56,v184\n"
"v_fma_f32 v145,v145,s56,v184\n"
"v_mfma_f32_16x16x32_bf16 v[32:35],a[40:43],a[0:3],v[32:35]\n"
"ds_read_b64_tr_b16 a[60:61],v253 offset:37120\n"
"v_fma_f32 v146,v146,s56,v184\n"
"v_fma_f32 v147,v147,s56,v184\n"
"v_fma_f32 v148,v148,s56,v184\n"
"v_fma_f32 v149,v149,s56,v184\n"
"v_fma_f32 v150,v150,s56,v184\n"
"v_mfma_f32_16x16x32_bf16 v[32:35],a[44:47],a[16:19],v[32:35]\n"
"ds_read_b64_tr_b16 a[62:63],v253 offset:43264\n"
"v_fma_f32 v151,v151,s56,v184\n"
"v_mul_f32 v144,v144,v16\n"
"v_mul_f32 v145,v145,v17\n"
"v_mul_f32 v146,v146,v18\n"
"v_mul_f32 v147,v147,v19\n"
"v_mfma_f32_16x16x32_bf16 v[32:35],a[48:51],a[208:211],v[32:35]\n"
"ds_read_b64_tr_b16 a[64:65],v255 offset:256\n"
"v_mul_f32 v148,v148,v20\n"
"v_mul_f32 v149,v149,v21\n"
"v_mul_f32 v150,v150,v22\n"
"v_mul_f32 v151,v151,v23\n"
"v_exp_f32 v144,v144\n"
"v_mfma_f32_16x16x32_bf16 v[32:35],a[52:55],a[224:227],v[32:35]\n"
"ds_read_b64_tr_b16 a[66:67],v255 offset:6400\n"
"v_exp_f32 v145,v145\n"
"v_exp_f32 v146,v146\n"
"v_exp_f32 v147,v147\n"
"v_exp_f32 v148,v148\n"
"v_exp_f32 v149,v149\n"
"v_mfma_f32_16x16x32_bf16 v[36:39],a[32:35],a[164:167],v[36:39]\n"
"ds_read_b64_tr_b16 a[68:69],v255 offset:12544\n"
"v_exp_f32 v150,v150\n"
"v_exp_f32 v151,v151\n"
"v_add_f32 v144,1.0,v144\n"
"v_add_f32 v145,1.0,v145\n"
"v_add_f32 v146,1.0,v146\n"
"v_mfma_f32_16x16x32_bf16 v[36:39],a[36:39],a[180:183],v[36:39]\n"
"ds_read_b64_tr_b16 a[70:71],v255 offset:18688\n"
"v_add_f32 v147,1.0,v147\n"
"v_add_f32 v148,1.0,v148\n"
"v_add_f32 v149,1.0,v149\n"
"v_add_f32 v150,1.0,v150\n"
"v_add_f32 v151,1.0,v151\n"
"v_mfma_f32_16x16x32_bf16 v[36:39],a[40:43],a[4:7],v[36:39]\n"
"ds_read_b64_tr_b16 a[72:73],v255 offset:24832\n"
"v_rcp_f32 v144,v144\n"
"v_rcp_f32 v145,v145\n"
"v_rcp_f32 v146,v146\n"
"v_rcp_f32 v147,v147\n"
"v_rcp_f32 v148,v148\n"
"v_mfma_f32_16x16x32_bf16 v[36:39],a[44:47],a[20:23],v[36:39]\n"
"ds_read_b64_tr_b16 a[74:75],v255 offset:30976\n"
"v_rcp_f32 v149,v149\n"
"v_rcp_f32 v150,v150\n"
"v_rcp_f32 v151,v151\n"
"v_mul_f32 v144,v144,v16\n"
"v_mul_f32 v145,v145,v17\n"
"v_mfma_f32_16x16x32_bf16 v[36:39],a[48:51],a[212:215],v[36:39]\n"
"ds_read_b64_tr_b16 a[76:77],v255 offset:37120\n"
"v_mul_f32 v146,v146,v18\n"
"v_mul_f32 v147,v147,v19\n"
"v_mul_f32 v148,v148,v20\n"
"v_mul_f32 v149,v149,v21\n"
"v_mul_f32 v150,v150,v22\n"
"v_mfma_f32_16x16x32_bf16 v[36:39],a[52:55],a[228:231],v[36:39]\n"
"ds_read_b64_tr_b16 a[78:79],v255 offset:43264\n"
"v_mul_f32 v151,v151,v23\n"
"v_cvt_pk_bf16_f32 v152,v144,v145\n"
"v_cvt_pk_bf16_f32 v153,v146,v147\n"
"v_cvt_pk_bf16_f32 v156,v148,v149\n"
"v_cvt_pk_bf16_f32 v157,v150,v151\n"
"v_mfma_f32_16x16x32_bf16 v[40:43],a[32:35],a[168:171],v[40:43]\n"
"buffer_load_dwordx4 a[104:107],v209,s[88:91],s40 offen offset:2048 sc1\n"
"v_mul_f32 v144,v24,v24\n"
"v_mul_f32 v145,v25,v25\n"
"v_mul_f32 v146,v26,v26\n"
"v_mul_f32 v147,v27,v27\n"
"v_mul_f32 v148,v28,v28\n"
"v_mfma_f32_16x16x32_bf16 v[40:43],a[36:39],a[184:187],v[40:43]\n"
"v_mul_f32 v149,v29,v29\n"
"v_mul_f32 v150,v30,v30\n"
"v_mul_f32 v151,v31,v31\n"
"v_fma_f32 v144,v144,s56,v184\n"
"v_fma_f32 v145,v145,s56,v184\n"
"v_mfma_f32_16x16x32_bf16 v[40:43],a[40:43],a[8:11],v[40:43]\n"
"buffer_load_dwordx4 a[108:111],v209,s[88:91],s40 offen offset:3072 sc1\n"
"v_fma_f32 v146,v146,s56,v184\n"
"v_fma_f32 v147,v147,s56,v184\n"
"v_fma_f32 v148,v148,s56,v184\n"
"v_fma_f32 v149,v149,s56,v184\n"
"v_fma_f32 v150,v150,s56,v184\n"
"v_mfma_f32_16x16x32_bf16 v[40:43],a[44:47],a[24:27],v[40:43]\n"
"v_fma_f32 v151,v151,s56,v184\n"
"v_mul_f32 v144,v144,v24\n"
"v_mul_f32 v145,v145,v25\n"
"v_mul_f32 v146,v146,v26\n"
"v_mul_f32 v147,v147,v27\n"
"v_mfma_f32_16x16x32_bf16 v[40:43],a[48:51],a[216:219],v[40:43]\n"
"s_add_u32 s40,s40,4096\n"
"s_add_u32 m0,s51,24576\n"
"s_nop 0\n"
"buffer_load_dwordx4 v213,s[72:75],s39 offen lds\n"
"v_mul_f32 v148,v148,v28\n"
"v_mul_f32 v149,v149,v29\n"
"v_mul_f32 v150,v150,v30\n"
"v_mul_f32 v151,v151,v31\n"
"v_exp_f32 v144,v144\n"
"v_mfma_f32_16x16x32_bf16 v[40:43],a[52:55],a[232:235],v[40:43]\n"
"v_exp_f32 v145,v145\n"
"v_exp_f32 v146,v146\n"
"v_exp_f32 v147,v147\n"
"v_exp_f32 v148,v148\n"
"v_exp_f32 v149,v149\n"
"v_mfma_f32_16x16x32_bf16 v[44:47],a[32:35],a[172:175],v[44:47]\n"
"buffer_load_dwordx4 v214,s[72:75],s39 offen offset:1024 lds\n"
"v_exp_f32 v150,v150\n"
"v_exp_f32 v151,v151\n"
"v_add_f32 v144,1.0,v144\n"
"v_add_f32 v145,1.0,v145\n"
"v_add_f32 v146,1.0,v146\n"
"v_mfma_f32_16x16x32_bf16 v[44:47],a[36:39],a[188:191],v[44:47]\n"
"v_add_f32 v147,1.0,v147\n"
"v_add_f32 v148,1.0,v148\n"
"v_add_f32 v149,1.0,v149\n"
"v_add_f32 v150,1.0,v150\n"
"v_add_f32 v151,1.0,v151\n"
"v_mfma_f32_16x16x32_bf16 v[44:47],a[40:43],a[12:15],v[44:47]\n"
"buffer_load_dwordx4 v215,s[72:75],s39 offen offset:2048 lds\n"
"v_rcp_f32 v144,v144\n"
"v_rcp_f32 v145,v145\n"
"v_rcp_f32 v146,v146\n"
"v_rcp_f32 v147,v147\n"
"v_rcp_f32 v148,v148\n"
"v_mfma_f32_16x16x32_bf16 v[44:47],a[44:47],a[28:31],v[44:47]\n"
"v_rcp_f32 v149,v149\n"
"v_rcp_f32 v150,v150\n"
"v_rcp_f32 v151,v151\n"
"v_mul_f32 v144,v144,v24\n"
"v_mul_f32 v145,v145,v25\n"
"v_mfma_f32_16x16x32_bf16 v[44:47],a[48:51],a[220:223],v[44:47]\n"
"s_add_u32 s39,s39,s52\n"
"s_add_u32 m0,s51,27648\n"
"s_nop 0\n"
"buffer_load_dwordx4 v213,s[72:75],s39 offen lds\n"
"v_mul_f32 v146,v146,v26\n"
"v_mul_f32 v147,v147,v27\n"
"v_mul_f32 v148,v148,v28\n"
"v_mul_f32 v149,v149,v29\n"
"v_mul_f32 v150,v150,v30\n"
"v_mfma_f32_16x16x32_bf16 v[44:47],a[52:55],a[236:239],v[44:47]\n"
"v_mul_f32 v151,v151,v31\n"
"v_cvt_pk_bf16_f32 v160,v144,v145\n"
"v_cvt_pk_bf16_f32 v161,v146,v147\n"
"v_cvt_pk_bf16_f32 v164,v148,v149\n"
"v_cvt_pk_bf16_f32 v165,v150,v151\n"
"s_waitcnt lgkmcnt(0)\n"
"v_mfma_f32_16x16x32_bf16 v[48:51],a[56:59],a[160:163],v[48:51]\n"
"ds_read_b64_tr_b16 a[32:33],v252 offset:25088\n"
"v_mul_f32 v144,v32,v32\n"
"v_mul_f32 v145,v33,v33\n"
"v_mul_f32 v146,v34,v34\n"
"v_mul_f32 v147,v35,v35\n"
"v_mul_f32 v148,v36,v36\n"
"v_mul_f32 v149,v37,v37\n"
"v_mfma_f32_16x16x32_bf16 v[48:51],a[60:63],a[176:179],v[48:51]\n"
"ds_read_b64_tr_b16 a[34:35],v252 offset:31232\n"
"v_mul_f32 v150,v38,v38\n"
"v_mul_f32 v151,v39,v39\n"
"v_fma_f32 v144,v144,s56,v184\n"
"v_fma_f32 v145,v145,s56,v184\n"
"v_fma_f32 v146,v146,s56,v184\n"
"v_fma_f32 v147,v147,s56,v184\n"
"v_mfma_f32_16x16x32_bf16 v[48:51],a[64:67],a[0:3],v[48:51]\n"
"ds_read_b64_tr_b16 a[36:37],v252 offset:37376\n"
"v_fma_f32 v148,v148,s56,v184\n"
"v_fma_f32 v149,v149,s56,v184\n"
"v_fma_f32 v150,v150,s56,v184\n"
"v_fma_f32 v151,v151,s56,v184\n"
"v_mul_f32 v144,v144,v32\n"
"v_mul_f32 v145,v145,v33\n"
"v_mfma_f32_16x16x32_bf16 v[48:51],a[68:71],a[16:19],v[48:51]\n"
"ds_read_b64_tr_b16 a[38:39],v252 offset:43520\n"
"v_mul_f32 v146,v146,v34\n"
"v_mul_f32 v147,v147,v35\n"
"v_mul_f32 v148,v148,v36\n"
"v_mul_f32 v149,v149,v37\n"
"v_mul_f32 v150,v150,v38\n"
"v_mul_f32 v151,v151,v39\n"
"v_mfma_f32_16x16x32_bf16 v[48:51],a[72:75],a[208:211],v[48:51]\n"
"ds_read_b64_tr_b16 a[40:41],v254 offset:512\n"
"v_exp_f32 v144,v144\n"
"v_exp_f32 v145,v145\n"
"v_exp_f32 v146,v146\n"
"v_exp_f32 v147,v147\n"
"v_exp_f32 v148,v148\n"
"v_exp_f32 v149,v149\n"
"v_mfma_f32_16x16x32_bf16 v[48:51],a[76:79],a[224:227],v[48:51]\n"
"ds_read_b64_tr_b16 a[42:43],v254 offset:6656\n"
"v_exp_f32 v150,v150\n"
"v_exp_f32 v151,v151\n"
"v_add_f32 v144,1.0,v144\n"
"v_add_f32 v145,1.0,v145\n"
"v_add_f32 v146,1.0,v146\n"
"v_add_f32 v147,1.0,v147\n"
"v_mfma_f32_16x16x32_bf16 v[52:55],a[56:59],a[164:167],v[52:55]\n"
"ds_read_b64_tr_b16 a[44:45],v254 offset:12800\n"
"v_add_f32 v148,1.0,v148\n"
"v_add_f32 v149,1.0,v149\n"
"v_add_f32 v150,1.0,v150\n"
"v_add_f32 v151,1.0,v151\n"
"v_rcp_f32 v144,v144\n"
"v_rcp_f32 v145,v145\n"
"v_mfma_f32_16x16x32_bf16 v[52:55],a[60:63],a[180:183],v[52:55]\n"
"ds_read_b64_tr_b16 a[46:47],v254 offset:18944\n"
"v_rcp_f32 v146,v146\n"
"v_rcp_f32 v147,v147\n"
"v_rcp_f32 v148,v148\n"
"v_rcp_f32 v149,v149\n"
"v_rcp_f32 v150,v150\n"
"v_rcp_f32 v151,v151\n"
"v_mfma_f32_16x16x32_bf16 v[52:55],a[64:67],a[4:7],v[52:55]\n"
"ds_read_b64_tr_b16 a[48:49],v254 offset:25088\n"
"v_mul_f32 v144,v144,v32\n"
"v_mul_f32 v145,v145,v33\n"
"v_mul_f32 v146,v146,v34\n"
"v_mul_f32 v147,v147,v35\n"
"v_mul_f32 v148,v148,v36\n"
"v_mul_f32 v149,v149,v37\n"
"v_mfma_f32_16x16x32_bf16 v[52:55],a[68:71],a[20:23],v[52:55]\n"
"ds_read_b64_tr_b16 a[50:51],v254 offset:31232\n"
"v_mul_f32 v150,v150,v38\n"
"v_mul_f32 v151,v151,v39\n"
"v_cvt_pk_bf16_f32 v154,v144,v145\n"
"v_cvt_pk_bf16_f32 v155,v146,v147\n"
"v_cvt_pk_bf16_f32 v158,v148,v149\n"
"v_cvt_pk_bf16_f32 v159,v150,v151\n"
"v_mfma_f32_16x16x32_bf16 v[52:55],a[72:75],a[212:215],v[52:55]\n"
"ds_read_b64_tr_b16 a[52:53],v254 offset:37376\n"
"s_mov_b32 s41,4096\n"
"buffer_store_dwordx4 v[152:155],v209,s[84:87],s41 offen sc1\n"
"buffer_store_dwordx4 v[156:159],v209,s[84:87],s41 offen offset:1024 sc1\n"
"v_mul_f32 v144,v40,v40\n"
"v_mul_f32 v145,v41,v41\n"
"v_mul_f32 v146,v42,v42\n"
"v_mfma_f32_16x16x32_bf16 v[52:55],a[76:79],a[228:231],v[52:55]\n"
"ds_read_b64_tr_b16 a[54:55],v254 offset:43520\n"
"v_mul_f32 v147,v43,v43\n"
"v_mul_f32 v148,v44,v44\n"
"v_mul_f32 v149,v45,v45\n"
"v_mul_f32 v150,v46,v46\n"
"v_mul_f32 v151,v47,v47\n"
"v_fma_f32 v144,v144,s56,v184\n"
"v_mfma_f32_16x16x32_bf16 v[56:59],a[56:59],a[168:171],v[56:59]\n"
"buffer_load_dwordx4 v214,s[72:75],s39 offen offset:1024 lds\n"
"v_fma_f32 v145,v145,s56,v184\n"
"v_fma_f32 v146,v146,s56,v184\n"
"v_fma_f32 v147,v147,s56,v184\n"
"v_fma_f32 v148,v148,s56,v184\n"
"v_fma_f32 v149,v149,s56,v184\n"
"v_fma_f32 v150,v150,s56,v184\n"
"v_mfma_f32_16x16x32_bf16 v[56:59],a[60:63],a[184:187],v[56:59]\n"
"v_fma_f32 v151,v151,s56,v184\n"
"v_mul_f32 v144,v144,v40\n"
"v_mul_f32 v145,v145,v41\n"
"v_mul_f32 v146,v146,v42\n"
"v_mul_f32 v147,v147,v43\n"
"v_mul_f32 v148,v148,v44\n"
"v_mfma_f32_16x16x32_bf16 v[56:59],a[64:67],a[8:11],v[56:59]\n"
"buffer_load_dwordx4 v215,s[72:75],s39 offen offset:2048 lds\n"
"v_mul_f32 v149,v149,v45\n"
"v_mul_f32 v150,v150,v46\n"
"v_mul_f32 v151,v151,v47\n"
"v_exp_f32 v144,v144\n"
"v_exp_f32 v145,v145\n"
"v_exp_f32 v146,v146\n"
"v_mfma_f32_16x16x32_bf16 v[56:59],a[68:71],a[24:27],v[56:59]\n"
"v_exp_f32 v147,v147\n"
"v_exp_f32 v148,v148\n"
"v_exp_f32 v149,v149\n"
"v_exp_f32 v150,v150\n"
"v_exp_f32 v151,v151\n"
"v_add_f32 v144,1.0,v144\n"
"v_mfma_f32_16x16x32_bf16 v[56:59],a[72:75],a[216:219],v[56:59]\n"
"s_add_u32 s39,s39,s53\n"
"buffer_load_dwordx4 a[128:131],v209,s[88:91],s40 offen sc1\n"
"v_add_f32 v145,1.0,v145\n"
"v_add_f32 v146,1.0,v146\n"
"v_add_f32 v147,1.0,v147\n"
"v_add_f32 v148,1.0,v148\n"
"v_add_f32 v149,1.0,v149\n"
"v_add_f32 v150,1.0,v150\n"
"v_mfma_f32_16x16x32_bf16 v[56:59],a[76:79],a[232:235],v[56:59]\n"
"v_add_f32 v151,1.0,v151\n"
"v_rcp_f32 v144,v144\n"
"v_rcp_f32 v145,v145\n"
"v_rcp_f32 v146,v146\n"
"v_rcp_f32 v147,v147\n"
"v_rcp_f32 v148,v148\n"
"v_mfma_f32_16x16x32_bf16 v[60:63],a[56:59],a[172:175],v[60:63]\n"
"buffer_load_dwordx4 a[132:135],v209,s[88:91],s40 offen offset:1024 sc1\n"
"v_rcp_f32 v149,v149\n"
"v_rcp_f32 v150,v150\n"
"v_rcp_f32 v151,v151\n"
"v_mul_f32 v144,v144,v40\n"
"v_mul_f32 v145,v145,v41\n"
"v_mul_f32 v146,v146,v42\n"
"v_mfma_f32_16x16x32_bf16 v[60:63],a[60:63],a[188:191],v[60:63]\n"
"v_mul_f32 v147,v147,v43\n"
"v_mul_f32 v148,v148,v44\n"
"v_mul_f32 v149,v149,v45\n"
"v_mul_f32 v150,v150,v46\n"
"v_mul_f32 v151,v151,v47\n"
"v_cvt_pk_bf16_f32 v162,v144,v145\n"
"v_mfma_f32_16x16x32_bf16 v[60:63],a[64:67],a[12:15],v[60:63]\n"
"buffer_load_dwordx4 a[136:139],v209,s[88:91],s40 offen offset:2048 sc1\n"
"v_cvt_pk_bf16_f32 v163,v146,v147\n"
"v_cvt_pk_bf16_f32 v166,v148,v149\n"
"v_cvt_pk_bf16_f32 v167,v150,v151\n"
"s_mov_b32 s41,4096\n"
"buffer_store_dwordx4 v[160:163],v209,s[84:87],s41 offen offset:2048 sc1\n"
"buffer_store_dwordx4 v[164:167],v209,s[84:87],s41 offen offset:3072 sc1\n"
"v_mfma_f32_16x16x32_bf16 v[60:63],a[68:71],a[28:31],v[60:63]\n"
"v_mfma_f32_16x16x32_bf16 v[60:63],a[72:75],a[220:223],v[60:63]\n"
"buffer_load_dwordx4 a[140:143],v209,s[88:91],s40 offen offset:3072 sc1\n"
"v_mfma_f32_16x16x32_bf16 v[60:63],a[76:79],a[236:239],v[60:63]\n"
"s_waitcnt lgkmcnt(0)\n"
"v_mfma_f32_16x16x32_bf16 v[64:67],a[32:35],a[160:163],v[64:67]\n"
"ds_read_b64_tr_b16 a[56:57],v253 offset:25088\n"
"v_mul_f32 v144,v48,v48\n"
"v_mul_f32 v145,v49,v49\n"
"v_mul_f32 v146,v50,v50\n"
"v_mul_f32 v147,v51,v51\n"
"v_mul_f32 v148,v52,v52\n"
"v_mfma_f32_16x16x32_bf16 v[64:67],a[36:39],a[176:179],v[64:67]\n"
"ds_read_b64_tr_b16 a[58:59],v253 offset:31232\n"
"v_mul_f32 v149,v53,v53\n"
"v_mul_f32 v150,v54,v54\n"
"v_mul_f32 v151,v55,v55\n"
"v_fma_f32 v144,v144,s56,v184\n"
"v_fma_f32 v145,v145,s56,v184\n"
"v_mfma_f32_16x16x32_bf16 v[64:67],a[40:43],a[0:3],v[64:67]\n"
"ds_read_b64_tr_b16 a[60:61],v253 offset:37376\n"
"v_fma_f32 v146,v146,s56,v184\n"
"v_fma_f32 v147,v147,s56,v184\n"
"v_fma_f32 v148,v148,s56,v184\n"
"v_fma_f32 v149,v149,s56,v184\n"
"v_fma_f32 v150,v150,s56,v184\n"
"v_mfma_f32_16x16x32_bf16 v[64:67],a[44:47],a[16:19],v[64:67]\n"
"ds_read_b64_tr_b16 a[62:63],v253 offset:43520\n"
"v_fma_f32 v151,v151,s56,v184\n"
"v_mul_f32 v144,v144,v48\n"
"v_mul_f32 v145,v145,v49\n"
"v_mul_f32 v146,v146,v50\n"
"v_mul_f32 v147,v147,v51\n"
"v_mfma_f32_16x16x32_bf16 v[64:67],a[48:51],a[208:211],v[64:67]\n"
"ds_read_b64_tr_b16 a[64:65],v255 offset:512\n"
"v_mul_f32 v148,v148,v52\n"
"v_mul_f32 v149,v149,v53\n"
"v_mul_f32 v150,v150,v54\n"
"v_mul_f32 v151,v151,v55\n"
"v_exp_f32 v144,v144\n"
"v_mfma_f32_16x16x32_bf16 v[64:67],a[52:55],a[224:227],v[64:67]\n"
"ds_read_b64_tr_b16 a[66:67],v255 offset:6656\n"
"v_exp_f32 v145,v145\n"
"v_exp_f32 v146,v146\n"
"v_exp_f32 v147,v147\n"
"v_exp_f32 v148,v148\n"
"v_exp_f32 v149,v149\n"
"v_mfma_f32_16x16x32_bf16 v[68:71],a[32:35],a[164:167],v[68:71]\n"
"ds_read_b64_tr_b16 a[68:69],v255 offset:12800\n"
"v_exp_f32 v150,v150\n"
"v_exp_f32 v151,v151\n"
"v_add_f32 v144,1.0,v144\n"
"v_add_f32 v145,1.0,v145\n"
"v_add_f32 v146,1.0,v146\n"
"v_mfma_f32_16x16x32_bf16 v[68:71],a[36:39],a[180:183],v[68:71]\n"
"ds_read_b64_tr_b16 a[70:71],v255 offset:18944\n"
"v_add_f32 v147,1.0,v147\n"
"v_add_f32 v148,1.0,v148\n"
"v_add_f32 v149,1.0,v149\n"
"v_add_f32 v150,1.0,v150\n"
"v_add_f32 v151,1.0,v151\n"
"v_mfma_f32_16x16x32_bf16 v[68:71],a[40:43],a[4:7],v[68:71]\n"
"ds_read_b64_tr_b16 a[72:73],v255 offset:25088\n"
"v_rcp_f32 v144,v144\n"
"v_rcp_f32 v145,v145\n"
"v_rcp_f32 v146,v146\n"
"v_rcp_f32 v147,v147\n"
"v_rcp_f32 v148,v148\n"
"v_mfma_f32_16x16x32_bf16 v[68:71],a[44:47],a[20:23],v[68:71]\n"
"ds_read_b64_tr_b16 a[74:75],v255 offset:31232\n"
"v_rcp_f32 v149,v149\n"
"v_rcp_f32 v150,v150\n"
"v_rcp_f32 v151,v151\n"
"v_mul_f32 v144,v144,v48\n"
"v_mul_f32 v145,v145,v49\n"
"v_mfma_f32_16x16x32_bf16 v[68:71],a[48:51],a[212:215],v[68:71]\n"
"ds_read_b64_tr_b16 a[76:77],v255 offset:37376\n"
"v_mul_f32 v146,v146,v50\n"
"v_mul_f32 v147,v147,v51\n"
"v_mul_f32 v148,v148,v52\n"
"v_mul_f32 v149,v149,v53\n"
"v_mul_f32 v150,v150,v54\n"
"v_mfma_f32_16x16x32_bf16 v[68:71],a[52:55],a[228:231],v[68:71]\n"
"ds_read_b64_tr_b16 a[78:79],v255 offset:43520\n"
"v_mul_f32 v151,v151,v55\n"
"v_cvt_pk_bf16_f32 v168,v144,v145\n"
"v_cvt_pk_bf16_f32 v169,v146,v147\n"
"v_cvt_pk_bf16_f32 v172,v148,v149\n"
"v_cvt_pk_bf16_f32 v173,v150,v151\n"
"v_mfma_f32_16x16x32_bf16 v[72:75],a[32:35],a[168:171],v[72:75]\n"
"s_add_u32 s40,s40,4096\n"
"buffer_load_dwordx4 a[144:147],v209,s[88:91],s40 offen sc1\n"
"v_mul_f32 v144,v56,v56\n"
"v_mul_f32 v145,v57,v57\n"
"v_mul_f32 v146,v58,v58\n"
"v_mul_f32 v147,v59,v59\n"
"v_mul_f32 v148,v60,v60\n"
"v_mfma_f32_16x16x32_bf16 v[72:75],a[36:39],a[184:187],v[72:75]\n"
"v_mul_f32 v149,v61,v61\n"
"v_mul_f32 v150,v62,v62\n"
"v_mul_f32 v151,v63,v63\n"
"v_fma_f32 v144,v144,s56,v184\n"
"v_fma_f32 v145,v145,s56,v184\n"
"v_mfma_f32_16x16x32_bf16 v[72:75],a[40:43],a[8:11],v[72:75]\n"
"buffer_load_dwordx4 a[148:151],v209,s[88:91],s40 offen offset:1024 sc1\n"
"v_fma_f32 v146,v146,s56,v184\n"
"v_fma_f32 v147,v147,s56,v184\n"
"v_fma_f32 v148,v148,s56,v184\n"
"v_fma_f32 v149,v149,s56,v184\n"
"v_fma_f32 v150,v150,s56,v184\n"
"v_mfma_f32_16x16x32_bf16 v[72:75],a[44:47],a[24:27],v[72:75]\n"
"v_fma_f32 v151,v151,s56,v184\n"
"v_mul_f32 v144,v144,v56\n"
"v_mul_f32 v145,v145,v57\n"
"v_mul_f32 v146,v146,v58\n"
"v_mul_f32 v147,v147,v59\n"
"v_mfma_f32_16x16x32_bf16 v[72:75],a[48:51],a[216:219],v[72:75]\n"
"buffer_load_dwordx4 a[152:155],v209,s[88:91],s40 offen offset:2048 sc1\n"
"v_mul_f32 v148,v148,v60\n"
"v_mul_f32 v149,v149,v61\n"
"v_mul_f32 v150,v150,v62\n"
"v_mul_f32 v151,v151,v63\n"
"v_exp_f32 v144,v144\n"
"v_mfma_f32_16x16x32_bf16 v[72:75],a[52:55],a[232:235],v[72:75]\n"
"v_exp_f32 v145,v145\n"
"v_exp_f32 v146,v146\n"
"v_exp_f32 v147,v147\n"
"v_exp_f32 v148,v148\n"
"v_exp_f32 v149,v149\n"
"v_mfma_f32_16x16x32_bf16 v[76:79],a[32:35],a[172:175],v[76:79]\n"
"buffer_load_dwordx4 a[156:159],v209,s[88:91],s40 offen offset:3072 sc1\n"
"v_exp_f32 v150,v150\n"
"v_exp_f32 v151,v151\n"
"v_add_f32 v144,1.0,v144\n"
"v_add_f32 v145,1.0,v145\n"
"v_add_f32 v146,1.0,v146\n"
"v_mfma_f32_16x16x32_bf16 v[76:79],a[36:39],a[188:191],v[76:79]\n"
"v_add_f32 v147,1.0,v147\n"
"v_add_f32 v148,1.0,v148\n"
"v_add_f32 v149,1.0,v149\n"
"v_add_f32 v150,1.0,v150\n"
"v_add_f32 v151,1.0,v151\n"
"v_mfma_f32_16x16x32_bf16 v[76:79],a[40:43],a[12:15],v[76:79]\n"
"s_add_u32 s40,s40,4096\n"
"v_readlane_b32 s39,v248,17\n"
"s_nop 4\n"
"s_add_u32 m0,s51,65536\n"
"s_nop 0\n"
"buffer_load_dwordx4 v213,s[72:75],s39 offen lds\n"
"v_rcp_f32 v144,v144\n"
"v_rcp_f32 v145,v145\n"
"v_rcp_f32 v146,v146\n"
"v_rcp_f32 v147,v147\n"
"v_rcp_f32 v148,v148\n"
"v_mfma_f32_16x16x32_bf16 v[76:79],a[44:47],a[28:31],v[76:79]\n"
"v_rcp_f32 v149,v149\n"
"v_rcp_f32 v150,v150\n"
"v_rcp_f32 v151,v151\n"
"v_mul_f32 v144,v144,v56\n"
"v_mul_f32 v145,v145,v57\n"
"v_mfma_f32_16x16x32_bf16 v[76:79],a[48:51],a[220:223],v[76:79]\n"
"buffer_load_dwordx4 v214,s[72:75],s39 offen offset:1024 lds\n"
"v_mul_f32 v146,v146,v58\n"
"v_mul_f32 v147,v147,v59\n"
"v_mul_f32 v148,v148,v60\n"
"v_mul_f32 v149,v149,v61\n"
"v_mul_f32 v150,v150,v62\n"
"v_mfma_f32_16x16x32_bf16 v[76:79],a[52:55],a[236:239],v[76:79]\n"
"v_mul_f32 v151,v151,v63\n"
"v_cvt_pk_bf16_f32 v176,v144,v145\n"
"v_cvt_pk_bf16_f32 v177,v146,v147\n"
"v_cvt_pk_bf16_f32 v180,v148,v149\n"
"v_cvt_pk_bf16_f32 v181,v150,v151\n"
"s_waitcnt lgkmcnt(0)\n"
"v_mfma_f32_16x16x32_bf16 v[80:83],a[56:59],a[160:163],v[80:83]\n"
"ds_read_b64_tr_b16 a[32:33],v252 offset:25344\n"
"v_mul_f32 v144,v64,v64\n"
"v_mul_f32 v145,v65,v65\n"
"v_mul_f32 v146,v66,v66\n"
"v_mul_f32 v147,v67,v67\n"
"v_mul_f32 v148,v68,v68\n"
"v_mul_f32 v149,v69,v69\n"
"v_mfma_f32_16x16x32_bf16 v[80:83],a[60:63],a[176:179],v[80:83]\n"
"ds_read_b64_tr_b16 a[34:35],v252 offset:31488\n"
"v_mul_f32 v150,v70,v70\n"
"v_mul_f32 v151,v71,v71\n"
"v_fma_f32 v144,v144,s56,v184\n"
"v_fma_f32 v145,v145,s56,v184\n"
"v_fma_f32 v146,v146,s56,v184\n"
"v_fma_f32 v147,v147,s56,v184\n"
"v_mfma_f32_16x16x32_bf16 v[80:83],a[64:67],a[0:3],v[80:83]\n"
"ds_read_b64_tr_b16 a[36:37],v252 offset:37632\n"
"v_fma_f32 v148,v148,s56,v184\n"
"v_fma_f32 v149,v149,s56,v184\n"
"v_fma_f32 v150,v150,s56,v184\n"
"v_fma_f32 v151,v151,s56,v184\n"
"v_mul_f32 v144,v144,v64\n"
"v_mul_f32 v145,v145,v65\n"
"v_mfma_f32_16x16x32_bf16 v[80:83],a[68:71],a[16:19],v[80:83]\n"
"ds_read_b64_tr_b16 a[38:39],v252 offset:43776\n"
"v_mul_f32 v146,v146,v66\n"
"v_mul_f32 v147,v147,v67\n"
"v_mul_f32 v148,v148,v68\n"
"v_mul_f32 v149,v149,v69\n"
"v_mul_f32 v150,v150,v70\n"
"v_mul_f32 v151,v151,v71\n"
"v_mfma_f32_16x16x32_bf16 v[80:83],a[72:75],a[208:211],v[80:83]\n"
"ds_read_b64_tr_b16 a[40:41],v254 offset:768\n"
"v_exp_f32 v144,v144\n"
"v_exp_f32 v145,v145\n"
"v_exp_f32 v146,v146\n"
"v_exp_f32 v147,v147\n"
"v_exp_f32 v148,v148\n"
"v_exp_f32 v149,v149\n"
"v_mfma_f32_16x16x32_bf16 v[80:83],a[76:79],a[224:227],v[80:83]\n"
"ds_read_b64_tr_b16 a[42:43],v254 offset:6912\n"
"v_exp_f32 v150,v150\n"
"v_exp_f32 v151,v151\n"
"v_add_f32 v144,1.0,v144\n"
"v_add_f32 v145,1.0,v145\n"
"v_add_f32 v146,1.0,v146\n"
"v_add_f32 v147,1.0,v147\n"
"v_mfma_f32_16x16x32_bf16 v[84:87],a[56:59],a[164:167],v[84:87]\n"
"ds_read_b64_tr_b16 a[44:45],v254 offset:13056\n"
"v_add_f32 v148,1.0,v148\n"
"v_add_f32 v149,1.0,v149\n"
"v_add_f32 v150,1.0,v150\n"
"v_add_f32 v151,1.0,v151\n"
"v_rcp_f32 v144,v144\n"
"v_rcp_f32 v145,v145\n"
"v_mfma_f32_16x16x32_bf16 v[84:87],a[60:63],a[180:183],v[84:87]\n"
"ds_read_b64_tr_b16 a[46:47],v254 offset:19200\n"
"v_rcp_f32 v146,v146\n"
"v_rcp_f32 v147,v147\n"
"v_rcp_f32 v148,v148\n"
"v_rcp_f32 v149,v149\n"
"v_rcp_f32 v150,v150\n"
"v_rcp_f32 v151,v151\n"
"v_mfma_f32_16x16x32_bf16 v[84:87],a[64:67],a[4:7],v[84:87]\n"
"ds_read_b64_tr_b16 a[48:49],v254 offset:25344\n"
"v_mul_f32 v144,v144,v64\n"
"v_mul_f32 v145,v145,v65\n"
"v_mul_f32 v146,v146,v66\n"
"v_mul_f32 v147,v147,v67\n"
"v_mul_f32 v148,v148,v68\n"
"v_mul_f32 v149,v149,v69\n"
"v_mfma_f32_16x16x32_bf16 v[84:87],a[68:71],a[20:23],v[84:87]\n"
"ds_read_b64_tr_b16 a[50:51],v254 offset:31488\n"
"v_mul_f32 v150,v150,v70\n"
"v_mul_f32 v151,v151,v71\n"
"v_cvt_pk_bf16_f32 v170,v144,v145\n"
"v_cvt_pk_bf16_f32 v171,v146,v147\n"
"v_cvt_pk_bf16_f32 v174,v148,v149\n"
"v_cvt_pk_bf16_f32 v175,v150,v151\n"
"v_mfma_f32_16x16x32_bf16 v[84:87],a[72:75],a[212:215],v[84:87]\n"
"ds_read_b64_tr_b16 a[52:53],v254 offset:37632\n"
"s_mov_b32 s41,8192\n"
"buffer_store_dwordx4 v[168:171],v209,s[84:87],s41 offen sc1\n"
"buffer_store_dwordx4 v[172:175],v209,s[84:87],s41 offen offset:1024 sc1\n"
"v_mul_f32 v144,v72,v72\n"
"v_mul_f32 v145,v73,v73\n"
"v_mul_f32 v146,v74,v74\n"
"v_mfma_f32_16x16x32_bf16 v[84:87],a[76:79],a[228:231],v[84:87]\n"
"ds_read_b64_tr_b16 a[54:55],v254 offset:43776\n"
"v_mul_f32 v147,v75,v75\n"
"v_mul_f32 v148,v76,v76\n"
"v_mul_f32 v149,v77,v77\n"
"v_mul_f32 v150,v78,v78\n"
"v_mul_f32 v151,v79,v79\n"
"v_fma_f32 v144,v144,s56,v184\n"
"v_mfma_f32_16x16x32_bf16 v[88:91],a[56:59],a[168:171],v[88:91]\n"
"buffer_load_dwordx4 v215,s[72:75],s39 offen offset:2048 lds\n"
"v_fma_f32 v145,v145,s56,v184\n"
"v_fma_f32 v146,v146,s56,v184\n"
"v_fma_f32 v147,v147,s56,v184\n"
"v_fma_f32 v148,v148,s56,v184\n"
"v_fma_f32 v149,v149,s56,v184\n"
"v_fma_f32 v150,v150,s56,v184\n"
"v_mfma_f32_16x16x32_bf16 v[88:91],a[60:63],a[184:187],v[88:91]\n"
"v_fma_f32 v151,v151,s56,v184\n"
"v_mul_f32 v144,v144,v72\n"
"v_mul_f32 v145,v145,v73\n"
"v_mul_f32 v146,v146,v74\n"
"v_mul_f32 v147,v147,v75\n"
"v_mul_f32 v148,v148,v76\n"
"v_mfma_f32_16x16x32_bf16 v[88:91],a[64:67],a[8:11],v[88:91]\n"
"s_add_u32 s39,s39,s52\n"
"s_add_u32 m0,s51,68608\n"
"s_nop 0\n"
"buffer_load_dwordx4 v213,s[72:75],s39 offen lds\n"
"v_mul_f32 v149,v149,v77\n"
"v_mul_f32 v150,v150,v78\n"
"v_mul_f32 v151,v151,v79\n"
"v_exp_f32 v144,v144\n"
"v_exp_f32 v145,v145\n"
"v_exp_f32 v146,v146\n"
"v_mfma_f32_16x16x32_bf16 v[88:91],a[68:71],a[24:27],v[88:91]\n"
"v_exp_f32 v147,v147\n"
"v_exp_f32 v148,v148\n"
"v_exp_f32 v149,v149\n"
"v_exp_f32 v150,v150\n"
"v_exp_f32 v151,v151\n"
"v_add_f32 v144,1.0,v144\n"
"v_mfma_f32_16x16x32_bf16 v[88:91],a[72:75],a[216:219],v[88:91]\n"
"buffer_load_dwordx4 v214,s[72:75],s39 offen offset:1024 lds\n"
"v_add_f32 v145,1.0,v145\n"
"v_add_f32 v146,1.0,v146\n"
"v_add_f32 v147,1.0,v147\n"
"v_add_f32 v148,1.0,v148\n"
"v_add_f32 v149,1.0,v149\n"
"v_add_f32 v150,1.0,v150\n"
"v_mfma_f32_16x16x32_bf16 v[88:91],a[76:79],a[232:235],v[88:91]\n"
"v_add_f32 v151,1.0,v151\n"
"v_rcp_f32 v144,v144\n"
"v_rcp_f32 v145,v145\n"
"v_rcp_f32 v146,v146\n"
"v_rcp_f32 v147,v147\n"
"v_rcp_f32 v148,v148\n"
"v_mfma_f32_16x16x32_bf16 v[92:95],a[56:59],a[172:175],v[92:95]\n"
"buffer_load_dwordx4 v215,s[72:75],s39 offen offset:2048 lds\n"
"v_rcp_f32 v149,v149\n"
"v_rcp_f32 v150,v150\n"
"v_rcp_f32 v151,v151\n"
"v_mul_f32 v144,v144,v72\n"
"v_mul_f32 v145,v145,v73\n"
"v_mul_f32 v146,v146,v74\n"
"v_mfma_f32_16x16x32_bf16 v[92:95],a[60:63],a[188:191],v[92:95]\n"
"v_mul_f32 v147,v147,v75\n"
"v_mul_f32 v148,v148,v76\n"
"v_mul_f32 v149,v149,v77\n"
"v_mul_f32 v150,v150,v78\n"
"v_mul_f32 v151,v151,v79\n"
"v_cvt_pk_bf16_f32 v178,v144,v145\n"
"v_mfma_f32_16x16x32_bf16 v[92:95],a[64:67],a[12:15],v[92:95]\n"
"s_add_u32 s39,s39,s53\n"
"v_readlane_b32 s40,v248,23\n"
"s_nop 4\n"
"buffer_load_dwordx4 a[192:195],v209,s[88:91],s40 offen sc1\n"
"v_cvt_pk_bf16_f32 v179,v146,v147\n"
"v_cvt_pk_bf16_f32 v182,v148,v149\n"
"v_cvt_pk_bf16_f32 v183,v150,v151\n"
"s_mov_b32 s41,8192\n"
"buffer_store_dwordx4 v[176:179],v209,s[84:87],s41 offen offset:2048 sc1\n"
"buffer_store_dwordx4 v[180:183],v209,s[84:87],s41 offen offset:3072 sc1\n"
"v_mfma_f32_16x16x32_bf16 v[92:95],a[68:71],a[28:31],v[92:95]\n"
"v_mfma_f32_16x16x32_bf16 v[92:95],a[72:75],a[220:223],v[92:95]\n"
"buffer_load_dwordx4 a[196:199],v209,s[88:91],s40 offen offset:1024 sc1\n"
"v_mfma_f32_16x16x32_bf16 v[92:95],a[76:79],a[236:239],v[92:95]\n"
"s_waitcnt lgkmcnt(0)\n"
"v_mfma_f32_16x16x32_bf16 v[96:99],a[32:35],a[160:163],v[96:99]\n"
"ds_read_b64_tr_b16 a[56:57],v253 offset:25344\n"
"v_mul_f32 v144,v80,v80\n"
"v_mul_f32 v145,v81,v81\n"
"v_mul_f32 v146,v82,v82\n"
"v_mul_f32 v147,v83,v83\n"
"v_mul_f32 v148,v84,v84\n"
"v_mfma_f32_16x16x32_bf16 v[96:99],a[36:39],a[176:179],v[96:99]\n"
"ds_read_b64_tr_b16 a[58:59],v253 offset:31488\n"
"v_mul_f32 v149,v85,v85\n"
"v_mul_f32 v150,v86,v86\n"
"v_mul_f32 v151,v87,v87\n"
"v_fma_f32 v144,v144,s56,v184\n"
"v_fma_f32 v145,v145,s56,v184\n"
"v_mfma_f32_16x16x32_bf16 v[96:99],a[40:43],a[0:3],v[96:99]\n"
"ds_read_b64_tr_b16 a[60:61],v253 offset:37632\n"
"v_fma_f32 v146,v146,s56,v184\n"
"v_fma_f32 v147,v147,s56,v184\n"
"v_fma_f32 v148,v148,s56,v184\n"
"v_fma_f32 v149,v149,s56,v184\n"
"v_fma_f32 v150,v150,s56,v184\n"
"v_mfma_f32_16x16x32_bf16 v[96:99],a[44:47],a[16:19],v[96:99]\n"
"ds_read_b64_tr_b16 a[62:63],v253 offset:43776\n"
"v_fma_f32 v151,v151,s56,v184\n"
"v_mul_f32 v144,v144,v80\n"
"v_mul_f32 v145,v145,v81\n"
"v_mul_f32 v146,v146,v82\n"
"v_mul_f32 v147,v147,v83\n"
"v_mfma_f32_16x16x32_bf16 v[96:99],a[48:51],a[208:211],v[96:99]\n"
"ds_read_b64_tr_b16 a[64:65],v255 offset:768\n"
"v_mul_f32 v148,v148,v84\n"
"v_mul_f32 v149,v149,v85\n"
"v_mul_f32 v150,v150,v86\n"
"v_mul_f32 v151,v151,v87\n"
"v_exp_f32 v144,v144\n"
"v_mfma_f32_16x16x32_bf16 v[96:99],a[52:55],a[224:227],v[96:99]\n"
"ds_read_b64_tr_b16 a[66:67],v255 offset:6912\n"
"v_exp_f32 v145,v145\n"
"v_exp_f32 v146,v146\n"
"v_exp_f32 v147,v147\n"
"v_exp_f32 v148,v148\n"
"v_exp_f32 v149,v149\n"
"v_mfma_f32_16x16x32_bf16 v[100:103],a[32:35],a[164:167],v[100:103]\n"
"ds_read_b64_tr_b16 a[68:69],v255 offset:13056\n"
"v_exp_f32 v150,v150\n"
"v_exp_f32 v151,v151\n"
"v_add_f32 v144,1.0,v144\n"
"v_add_f32 v145,1.0,v145\n"
"v_add_f32 v146,1.0,v146\n"
"v_mfma_f32_16x16x32_bf16 v[100:103],a[36:39],a[180:183],v[100:103]\n"
"ds_read_b64_tr_b16 a[70:71],v255 offset:19200\n"
"v_add_f32 v147,1.0,v147\n"
"v_add_f32 v148,1.0,v148\n"
"v_add_f32 v149,1.0,v149\n"
"v_add_f32 v150,1.0,v150\n"
"v_add_f32 v151,1.0,v151\n"
"v_mfma_f32_16x16x32_bf16 v[100:103],a[40:43],a[4:7],v[100:103]\n"
"ds_read_b64_tr_b16 a[72:73],v255 offset:25344\n"
"v_rcp_f32 v144,v144\n"
"v_rcp_f32 v145,v145\n"
"v_rcp_f32 v146,v146\n"
"v_rcp_f32 v147,v147\n"
"v_rcp_f32 v148,v148\n"
"v_mfma_f32_16x16x32_bf16 v[100:103],a[44:47],a[20:23],v[100:103]\n"
"ds_read_b64_tr_b16 a[74:75],v255 offset:31488\n"
"v_rcp_f32 v149,v149\n"
"v_rcp_f32 v150,v150\n"
"v_rcp_f32 v151,v151\n"
"v_mul_f32 v144,v144,v80\n"
"v_mul_f32 v145,v145,v81\n"
"v_mfma_f32_16x16x32_bf16 v[100:103],a[48:51],a[212:215],v[100:103]\n"
"ds_read_b64_tr_b16 a[76:77],v255 offset:37632\n"
"v_mul_f32 v146,v146,v82\n"
"v_mul_f32 v147,v147,v83\n"
"v_mul_f32 v148,v148,v84\n"
"v_mul_f32 v149,v149,v85\n"
"v_mul_f32 v150,v150,v86\n"
"v_mfma_f32_16x16x32_bf16 v[100:103],a[52:55],a[228:231],v[100:103]\n"
"ds_read_b64_tr_b16 a[78:79],v255 offset:43776\n"
"v_mul_f32 v151,v151,v87\n"
"v_cvt_pk_bf16_f32 v192,v144,v145\n"
"v_cvt_pk_bf16_f32 v193,v146,v147\n"
"v_cvt_pk_bf16_f32 v196,v148,v149\n"
"v_cvt_pk_bf16_f32 v197,v150,v151\n"
"v_mfma_f32_16x16x32_bf16 v[104:107],a[32:35],a[168:171],v[104:107]\n"
"buffer_load_dwordx4 a[200:203],v209,s[88:91],s40 offen offset:2048 sc1\n"
"v_mul_f32 v144,v88,v88\n"
"v_mul_f32 v145,v89,v89\n"
"v_mul_f32 v146,v90,v90\n"
"v_mul_f32 v147,v91,v91\n"
"v_mul_f32 v148,v92,v92\n"
"v_mfma_f32_16x16x32_bf16 v[104:107],a[36:39],a[184:187],v[104:107]\n"
"v_mul_f32 v149,v93,v93\n"
"v_mul_f32 v150,v94,v94\n"
"v_mul_f32 v151,v95,v95\n"
"v_fma_f32 v144,v144,s56,v184\n"
"v_fma_f32 v145,v145,s56,v184\n"
"v_mfma_f32_16x16x32_bf16 v[104:107],a[40:43],a[8:11],v[104:107]\n"
"buffer_load_dwordx4 a[204:207],v209,s[88:91],s40 offen offset:3072 sc1\n"
"v_fma_f32 v146,v146,s56,v184\n"
"v_fma_f32 v147,v147,s56,v184\n"
"v_fma_f32 v148,v148,s56,v184\n"
"v_fma_f32 v149,v149,s56,v184\n"
"v_fma_f32 v150,v150,s56,v184\n"
"v_mfma_f32_16x16x32_bf16 v[104:107],a[44:47],a[24:27],v[104:107]\n"
"v_fma_f32 v151,v151,s56,v184\n"
"v_mul_f32 v144,v144,v88\n"
"v_mul_f32 v145,v145,v89\n"
"v_mul_f32 v146,v146,v90\n"
"v_mul_f32 v147,v147,v91\n"
"v_mfma_f32_16x16x32_bf16 v[104:107],a[48:51],a[216:219],v[104:107]\n"
"s_add_u32 s40,s40,4096\n"
"buffer_load_dwordx4 a[240:243],v209,s[88:91],s40 offen sc1\n"
"v_mul_f32 v148,v148,v92\n"
"v_mul_f32 v149,v149,v93\n"
"v_mul_f32 v150,v150,v94\n"
"v_mul_f32 v151,v151,v95\n"
"v_exp_f32 v144,v144\n"
"v_mfma_f32_16x16x32_bf16 v[104:107],a[52:55],a[232:235],v[104:107]\n"
"v_exp_f32 v145,v145\n"
"v_exp_f32 v146,v146\n"
"v_exp_f32 v147,v147\n"
"v_exp_f32 v148,v148\n"
"v_exp_f32 v149,v149\n"
"v_mfma_f32_16x16x32_bf16 v[108:111],a[32:35],a[172:175],v[108:111]\n"
"buffer_load_dwordx4 a[244:247],v209,s[88:91],s40 offen offset:1024 sc1\n"
"v_exp_f32 v150,v150\n"
"v_exp_f32 v151,v151\n"
"v_add_f32 v144,1.0,v144\n"
"v_add_f32 v145,1.0,v145\n"
"v_add_f32 v146,1.0,v146\n"
"v_mfma_f32_16x16x32_bf16 v[108:111],a[36:39],a[188:191],v[108:111]\n"
"v_add_f32 v147,1.0,v147\n"
"v_add_f32 v148,1.0,v148\n"
"v_add_f32 v149,1.0,v149\n"
"v_add_f32 v150,1.0,v150\n"
"v_add_f32 v151,1.0,v151\n"
"v_mfma_f32_16x16x32_bf16 v[108:111],a[40:43],a[12:15],v[108:111]\n"
"buffer_load_dwordx4 a[248:251],v209,s[88:91],s40 offen offset:2048 sc1\n"
"v_rcp_f32 v144,v144\n"
"v_rcp_f32 v145,v145\n"
"v_rcp_f32 v146,v146\n"
"v_rcp_f32 v147,v147\n"
"v_rcp_f32 v148,v148\n"
"v_mfma_f32_16x16x32_bf16 v[108:111],a[44:47],a[28:31],v[108:111]\n"
"v_rcp_f32 v149,v149\n"
"v_rcp_f32 v150,v150\n"
"v_rcp_f32 v151,v151\n"
"v_mul_f32 v144,v144,v88\n"
"v_mul_f32 v145,v145,v89\n"
"v_mfma_f32_16x16x32_bf16 v[108:111],a[48:51],a[220:223],v[108:111]\n"
"buffer_load_dwordx4 a[252:255],v209,s[88:91],s40 offen offset:3072 sc1\n"
"v_mul_f32 v146,v146,v90\n"
"v_mul_f32 v147,v147,v91\n"
"v_mul_f32 v148,v148,v92\n"
"v_mul_f32 v149,v149,v93\n"
"v_mul_f32 v150,v150,v94\n"
"v_mfma_f32_16x16x32_bf16 v[108:111],a[52:55],a[236:239],v[108:111]\n"
"v_mul_f32 v151,v151,v95\n"
"v_cvt_pk_bf16_f32 v200,v144,v145\n"
"v_cvt_pk_bf16_f32 v201,v146,v147\n"
"v_cvt_pk_bf16_f32 v204,v148,v149\n"
"v_cvt_pk_bf16_f32 v205,v150,v151\n"
"s_waitcnt lgkmcnt(0)\n"
"v_mfma_f32_16x16x32_bf16 v[112:115],a[56:59],a[160:163],v[112:115]\n"
"ds_read_b64_tr_b16 a[32:33],v252 offset:25600\n"
"v_mul_f32 v144,v96,v96\n"
"v_mul_f32 v145,v97,v97\n"
"v_mul_f32 v146,v98,v98\n"
"v_mul_f32 v147,v99,v99\n"
"v_mul_f32 v148,v100,v100\n"
"v_mul_f32 v149,v101,v101\n"
"v_mfma_f32_16x16x32_bf16 v[112:115],a[60:63],a[176:179],v[112:115]\n"
"ds_read_b64_tr_b16 a[34:35],v252 offset:31744\n"
"v_mul_f32 v150,v102,v102\n"
"v_mul_f32 v151,v103,v103\n"
"v_fma_f32 v144,v144,s56,v184\n"
"v_fma_f32 v145,v145,s56,v184\n"
"v_fma_f32 v146,v146,s56,v184\n"
"v_fma_f32 v147,v147,s56,v184\n"
"v_mfma_f32_16x16x32_bf16 v[112:115],a[64:67],a[0:3],v[112:115]\n"
"ds_read_b64_tr_b16 a[36:37],v252 offset:37888\n"
"v_fma_f32 v148,v148,s56,v184\n"
"v_fma_f32 v149,v149,s56,v184\n"
"v_fma_f32 v150,v150,s56,v184\n"
"v_fma_f32 v151,v151,s56,v184\n"
"v_mul_f32 v144,v144,v96\n"
"v_mul_f32 v145,v145,v97\n"
"v_mfma_f32_16x16x32_bf16 v[112:115],a[68:71],a[16:19],v[112:115]\n"
"ds_read_b64_tr_b16 a[38:39],v252 offset:44032\n"
"v_mul_f32 v146,v146,v98\n"
"v_mul_f32 v147,v147,v99\n"
"v_mul_f32 v148,v148,v100\n"
"v_mul_f32 v149,v149,v101\n"
"v_mul_f32 v150,v150,v102\n"
"v_mul_f32 v151,v151,v103\n"
"v_mfma_f32_16x16x32_bf16 v[112:115],a[72:75],a[208:211],v[112:115]\n"
"ds_read_b64_tr_b16 a[40:41],v254 offset:1024\n"
"v_exp_f32 v144,v144\n"
"v_exp_f32 v145,v145\n"
"v_exp_f32 v146,v146\n"
"v_exp_f32 v147,v147\n"
"v_exp_f32 v148,v148\n"
"v_exp_f32 v149,v149\n"
"v_mfma_f32_16x16x32_bf16 v[112:115],a[76:79],a[224:227],v[112:115]\n"
"ds_read_b64_tr_b16 a[42:43],v254 offset:7168\n"
"v_exp_f32 v150,v150\n"
"v_exp_f32 v151,v151\n"
"v_add_f32 v144,1.0,v144\n"
"v_add_f32 v145,1.0,v145\n"
"v_add_f32 v146,1.0,v146\n"
"v_add_f32 v147,1.0,v147\n"
"v_mfma_f32_16x16x32_bf16 v[116:119],a[56:59],a[164:167],v[116:119]\n"
"ds_read_b64_tr_b16 a[44:45],v254 offset:13312\n"
"v_add_f32 v148,1.0,v148\n"
"v_add_f32 v149,1.0,v149\n"
"v_add_f32 v150,1.0,v150\n"
"v_add_f32 v151,1.0,v151\n"
"v_rcp_f32 v144,v144\n"
"v_rcp_f32 v145,v145\n"
"v_mfma_f32_16x16x32_bf16 v[116:119],a[60:63],a[180:183],v[116:119]\n"
"ds_read_b64_tr_b16 a[46:47],v254 offset:19456\n"
"v_rcp_f32 v146,v146\n"
"v_rcp_f32 v147,v147\n"
"v_rcp_f32 v148,v148\n"
"v_rcp_f32 v149,v149\n"
"v_rcp_f32 v150,v150\n"
"v_rcp_f32 v151,v151\n"
"v_mfma_f32_16x16x32_bf16 v[116:119],a[64:67],a[4:7],v[116:119]\n"
"ds_read_b64_tr_b16 a[48:49],v254 offset:25600\n"
"v_mul_f32 v144,v144,v96\n"
"v_mul_f32 v145,v145,v97\n"
"v_mul_f32 v146,v146,v98\n"
"v_mul_f32 v147,v147,v99\n"
"v_mul_f32 v148,v148,v100\n"
"v_mul_f32 v149,v149,v101\n"
"v_mfma_f32_16x16x32_bf16 v[116:119],a[68:71],a[20:23],v[116:119]\n"
"ds_read_b64_tr_b16 a[50:51],v254 offset:31744\n"
"v_mul_f32 v150,v150,v102\n"
"v_mul_f32 v151,v151,v103\n"
"v_cvt_pk_bf16_f32 v194,v144,v145\n"
"v_cvt_pk_bf16_f32 v195,v146,v147\n"
"v_cvt_pk_bf16_f32 v198,v148,v149\n"
"v_cvt_pk_bf16_f32 v199,v150,v151\n"
"v_mfma_f32_16x16x32_bf16 v[116:119],a[72:75],a[212:215],v[116:119]\n"
"ds_read_b64_tr_b16 a[52:53],v254 offset:37888\n"
"s_mov_b32 s41,12288\n"
"buffer_store_dwordx4 v[192:195],v209,s[84:87],s41 offen sc1\n"
"buffer_store_dwordx4 v[196:199],v209,s[84:87],s41 offen offset:1024 sc1\n"
"v_mul_f32 v144,v104,v104\n"
"v_mul_f32 v145,v105,v105\n"
"v_mul_f32 v146,v106,v106\n"
"v_mfma_f32_16x16x32_bf16 v[116:119],a[76:79],a[228:231],v[116:119]\n"
"ds_read_b64_tr_b16 a[54:55],v254 offset:44032\n"
"v_mul_f32 v147,v107,v107\n"
"v_mul_f32 v148,v108,v108\n"
"v_mul_f32 v149,v109,v109\n"
"v_mul_f32 v150,v110,v110\n"
"v_mul_f32 v151,v111,v111\n"
"v_fma_f32 v144,v144,s56,v184\n"
"v_mfma_f32_16x16x32_bf16 v[120:123],a[56:59],a[168:171],v[120:123]\n"
"s_add_u32 s40,s40,4096\n"
"v_fma_f32 v145,v145,s56,v184\n"
"v_fma_f32 v146,v146,s56,v184\n"
"v_fma_f32 v147,v147,s56,v184\n"
"v_fma_f32 v148,v148,s56,v184\n"
"v_fma_f32 v149,v149,s56,v184\n"
"v_fma_f32 v150,v150,s56,v184\n"
"v_mfma_f32_16x16x32_bf16 v[120:123],a[60:63],a[184:187],v[120:123]\n"
"v_fma_f32 v151,v151,s56,v184\n"
"v_mul_f32 v144,v144,v104\n"
"v_mul_f32 v145,v145,v105\n"
"v_mul_f32 v146,v146,v106\n"
"v_mul_f32 v147,v147,v107\n"
"v_mul_f32 v148,v148,v108\n"
"v_mfma_f32_16x16x32_bf16 v[120:123],a[64:67],a[8:11],v[120:123]\n"
"v_mul_f32 v149,v149,v109\n"
"v_mul_f32 v150,v150,v110\n"
"v_mul_f32 v151,v151,v111\n"
"v_exp_f32 v144,v144\n"
"v_exp_f32 v145,v145\n"
"v_exp_f32 v146,v146\n"
"v_mfma_f32_16x16x32_bf16 v[120:123],a[68:71],a[24:27],v[120:123]\n"
"v_exp_f32 v147,v147\n"
"v_exp_f32 v148,v148\n"
"v_exp_f32 v149,v149\n"
"v_exp_f32 v150,v150\n"
"v_exp_f32 v151,v151\n"
"v_add_f32 v144,1.0,v144\n"
"v_mfma_f32_16x16x32_bf16 v[120:123],a[72:75],a[216:219],v[120:123]\n"
"v_add_f32 v145,1.0,v145\n"
"v_add_f32 v146,1.0,v146\n"
"v_add_f32 v147,1.0,v147\n"
"v_add_f32 v148,1.0,v148\n"
"v_add_f32 v149,1.0,v149\n"
"v_add_f32 v150,1.0,v150\n"
"v_mfma_f32_16x16x32_bf16 v[120:123],a[76:79],a[232:235],v[120:123]\n"
"v_add_f32 v151,1.0,v151\n"
"v_rcp_f32 v144,v144\n"
"v_rcp_f32 v145,v145\n"
"v_rcp_f32 v146,v146\n"
"v_rcp_f32 v147,v147\n"
"v_rcp_f32 v148,v148\n"
"v_mfma_f32_16x16x32_bf16 v[124:127],a[56:59],a[172:175],v[124:127]\n"
"v_rcp_f32 v149,v149\n"
"v_rcp_f32 v150,v150\n"
"v_rcp_f32 v151,v151\n"
"v_mul_f32 v144,v144,v104\n"
"v_mul_f32 v145,v145,v105\n"
"v_mul_f32 v146,v146,v106\n"
"v_mfma_f32_16x16x32_bf16 v[124:127],a[60:63],a[188:191],v[124:127]\n"
"v_mul_f32 v147,v147,v107\n"
"v_mul_f32 v148,v148,v108\n"
"v_mul_f32 v149,v149,v109\n"
"v_mul_f32 v150,v150,v110\n"
"v_mul_f32 v151,v151,v111\n"
"v_cvt_pk_bf16_f32 v202,v144,v145\n"
"v_mfma_f32_16x16x32_bf16 v[124:127],a[64:67],a[12:15],v[124:127]\n"
"v_cvt_pk_bf16_f32 v203,v146,v147\n"
"v_cvt_pk_bf16_f32 v206,v148,v149\n"
"v_cvt_pk_bf16_f32 v207,v150,v151\n"
"s_mov_b32 s41,12288\n"
"buffer_store_dwordx4 v[200:203],v209,s[84:87],s41 offen offset:2048 sc1\n"
"buffer_store_dwordx4 v[204:207],v209,s[84:87],s41 offen offset:3072 sc1\n"
"v_mfma_f32_16x16x32_bf16 v[124:127],a[68:71],a[28:31],v[124:127]\n"
"v_mfma_f32_16x16x32_bf16 v[124:127],a[72:75],a[220:223],v[124:127]\n"
"v_mfma_f32_16x16x32_bf16 v[124:127],a[76:79],a[236:239],v[124:127]\n"
"s_waitcnt lgkmcnt(0)\n"
"v_mfma_f32_16x16x32_bf16 v[128:131],a[32:35],a[160:163],v[128:131]\n"
"v_mul_f32 v144,v112,v112\n"
"v_mul_f32 v145,v113,v113\n"
"v_mul_f32 v146,v114,v114\n"
"v_mul_f32 v147,v115,v115\n"
"v_mul_f32 v148,v116,v116\n"
"v_mfma_f32_16x16x32_bf16 v[128:131],a[36:39],a[176:179],v[128:131]\n"
"v_mul_f32 v149,v117,v117\n"
"v_mul_f32 v150,v118,v118\n"
"v_mul_f32 v151,v119,v119\n"
"v_fma_f32 v144,v144,s56,v184\n"
"v_fma_f32 v145,v145,s56,v184\n"
"v_mfma_f32_16x16x32_bf16 v[128:131],a[40:43],a[0:3],v[128:131]\n"
"v_fma_f32 v146,v146,s56,v184\n"
"v_fma_f32 v147,v147,s56,v184\n"
"v_fma_f32 v148,v148,s56,v184\n"
"v_fma_f32 v149,v149,s56,v184\n"
"v_fma_f32 v150,v150,s56,v184\n"
"v_mfma_f32_16x16x32_bf16 v[128:131],a[44:47],a[16:19],v[128:131]\n"
"v_fma_f32 v151,v151,s56,v184\n"
"v_mul_f32 v144,v144,v112\n"
"v_mul_f32 v145,v145,v113\n"
"v_mul_f32 v146,v146,v114\n"
"v_mul_f32 v147,v147,v115\n"
"v_mfma_f32_16x16x32_bf16 v[128:131],a[48:51],a[208:211],v[128:131]\n"
"v_mul_f32 v148,v148,v116\n"
"v_mul_f32 v149,v149,v117\n"
"v_mul_f32 v150,v150,v118\n"
"v_mul_f32 v151,v151,v119\n"
"v_exp_f32 v144,v144\n"
"v_mfma_f32_16x16x32_bf16 v[128:131],a[52:55],a[224:227],v[128:131]\n"
"v_exp_f32 v145,v145\n"
"v_exp_f32 v146,v146\n"
"v_exp_f32 v147,v147\n"
"v_exp_f32 v148,v148\n"
"v_exp_f32 v149,v149\n"
"v_mfma_f32_16x16x32_bf16 v[132:135],a[32:35],a[164:167],v[132:135]\n"
"v_exp_f32 v150,v150\n"
"v_exp_f32 v151,v151\n"
"v_add_f32 v144,1.0,v144\n"
"v_add_f32 v145,1.0,v145\n"
"v_add_f32 v146,1.0,v146\n"
"v_mfma_f32_16x16x32_bf16 v[132:135],a[36:39],a[180:183],v[132:135]\n"
"v_add_f32 v147,1.0,v147\n"
"v_add_f32 v148,1.0,v148\n"
"v_add_f32 v149,1.0,v149\n"
"v_add_f32 v150,1.0,v150\n"
"v_add_f32 v151,1.0,v151\n"
"v_mfma_f32_16x16x32_bf16 v[132:135],a[40:43],a[4:7],v[132:135]\n"
"v_rcp_f32 v144,v144\n"
"v_rcp_f32 v145,v145\n"
"v_rcp_f32 v146,v146\n"
"v_rcp_f32 v147,v147\n"
"v_rcp_f32 v148,v148\n"
"v_mfma_f32_16x16x32_bf16 v[132:135],a[44:47],a[20:23],v[132:135]\n"
"v_rcp_f32 v149,v149\n"
"v_rcp_f32 v150,v150\n"
"v_rcp_f32 v151,v151\n"
"v_mul_f32 v144,v144,v112\n"
"v_mul_f32 v145,v145,v113\n"
"v_mfma_f32_16x16x32_bf16 v[132:135],a[48:51],a[212:215],v[132:135]\n"
"v_mul_f32 v146,v146,v114\n"
"v_mul_f32 v147,v147,v115\n"
"v_mul_f32 v148,v148,v116\n"
"v_mul_f32 v149,v149,v117\n"
"v_mul_f32 v150,v150,v118\n"
"v_mfma_f32_16x16x32_bf16 v[132:135],a[52:55],a[228:231],v[132:135]\n"
"v_mul_f32 v151,v151,v119\n"
"v_cvt_pk_bf16_f32 v228,v144,v145\n"
"v_cvt_pk_bf16_f32 v229,v146,v147\n"
"v_cvt_pk_bf16_f32 v232,v148,v149\n"
"v_cvt_pk_bf16_f32 v233,v150,v151\n"
"v_mfma_f32_16x16x32_bf16 v[136:139],a[32:35],a[168:171],v[136:139]\n"
"v_mul_f32 v144,v120,v120\n"
"v_mul_f32 v145,v121,v121\n"
"v_mul_f32 v146,v122,v122\n"
"v_mul_f32 v147,v123,v123\n"
"v_mul_f32 v148,v124,v124\n"
"v_mfma_f32_16x16x32_bf16 v[136:139],a[36:39],a[184:187],v[136:139]\n"
"v_mul_f32 v149,v125,v125\n"
"v_mul_f32 v150,v126,v126\n"
"v_mul_f32 v151,v127,v127\n"
"v_fma_f32 v144,v144,s56,v184\n"
"v_fma_f32 v145,v145,s56,v184\n"
"v_mfma_f32_16x16x32_bf16 v[136:139],a[40:43],a[8:11],v[136:139]\n"
"v_fma_f32 v146,v146,s56,v184\n"
"v_fma_f32 v147,v147,s56,v184\n"
"v_fma_f32 v148,v148,s56,v184\n"
"v_fma_f32 v149,v149,s56,v184\n"
"v_fma_f32 v150,v150,s56,v184\n"
"v_mfma_f32_16x16x32_bf16 v[136:139],a[44:47],a[24:27],v[136:139]\n"
"v_fma_f32 v151,v151,s56,v184\n"
"v_mul_f32 v144,v144,v120\n"
"v_mul_f32 v145,v145,v121\n"
"v_mul_f32 v146,v146,v122\n"
"v_mul_f32 v147,v147,v123\n"
"v_mfma_f32_16x16x32_bf16 v[136:139],a[48:51],a[216:219],v[136:139]\n"
"v_mul_f32 v148,v148,v124\n"
"v_mul_f32 v149,v149,v125\n"
"v_mul_f32 v150,v150,v126\n"
"v_mul_f32 v151,v151,v127\n"
"v_exp_f32 v144,v144\n"
"v_mfma_f32_16x16x32_bf16 v[136:139],a[52:55],a[232:235],v[136:139]\n"
"v_exp_f32 v145,v145\n"
"v_exp_f32 v146,v146\n"
"v_exp_f32 v147,v147\n"
"v_exp_f32 v148,v148\n"
"v_exp_f32 v149,v149\n"
"v_mfma_f32_16x16x32_bf16 v[140:143],a[32:35],a[172:175],v[140:143]\n"
"v_exp_f32 v150,v150\n"
"v_exp_f32 v151,v151\n"
"v_add_f32 v144,1.0,v144\n"
"v_add_f32 v145,1.0,v145\n"
"v_add_f32 v146,1.0,v146\n"
"v_mfma_f32_16x16x32_bf16 v[140:143],a[36:39],a[188:191],v[140:143]\n"
"v_add_f32 v147,1.0,v147\n"
"v_add_f32 v148,1.0,v148\n"
"v_add_f32 v149,1.0,v149\n"
"v_add_f32 v150,1.0,v150\n"
"v_add_f32 v151,1.0,v151\n"
"v_mfma_f32_16x16x32_bf16 v[140:143],a[40:43],a[12:15],v[140:143]\n"
"v_rcp_f32 v144,v144\n"
"v_rcp_f32 v145,v145\n"
"v_rcp_f32 v146,v146\n"
"v_rcp_f32 v147,v147\n"
"v_rcp_f32 v148,v148\n"
"v_mfma_f32_16x16x32_bf16 v[140:143],a[44:47],a[28:31],v[140:143]\n"
"v_rcp_f32 v149,v149\n"
"v_rcp_f32 v150,v150\n"
"v_rcp_f32 v151,v151\n"
"v_mul_f32 v144,v144,v120\n"
"v_mul_f32 v145,v145,v121\n"
"v_mfma_f32_16x16x32_bf16 v[140:143],a[48:51],a[220:223],v[140:143]\n"
"v_mul_f32 v146,v146,v122\n"
"v_mul_f32 v147,v147,v123\n"
"v_mul_f32 v148,v148,v124\n"
"v_mul_f32 v149,v149,v125\n"
"v_mul_f32 v150,v150,v126\n"
"v_mfma_f32_16x16x32_bf16 v[140:143],a[52:55],a[236:239],v[140:143]\n"
"v_mul_f32 v151,v151,v127\n"
"v_cvt_pk_bf16_f32 v236,v144,v145\n"
"v_cvt_pk_bf16_f32 v237,v146,v147\n"
"v_cvt_pk_bf16_f32 v242,v148,v149\n"
"v_cvt_pk_bf16_f32 v243,v150,v151\n"
"s_barrier\n"
"s_nop 3\n"
"s_add_u32 m0,s51,90112\n"
"s_nop 0\n"
"buffer_load_dwordx4 v213,s[72:75],s39 offen lds\n"
"v_mul_f32 v144,v128,v128\n"
"v_mul_f32 v145,v129,v129\n"
"v_mul_f32 v146,v130,v130\n"
"v_mul_f32 v147,v131,v131\n"
"v_mul_f32 v148,v132,v132\n"
"v_mul_f32 v149,v133,v133\n"
"v_mul_f32 v150,v134,v134\n"
"v_mul_f32 v151,v135,v135\n"
"buffer_load_dwordx4 v214,s[72:75],s39 offen offset:1024 lds\n"
"v_fma_f32 v144,v144,s56,v184\n"
"v_fma_f32 v145,v145,s56,v184\n"
"v_fma_f32 v146,v146,s56,v184\n"
"v_fma_f32 v147,v147,s56,v184\n"
"v_fma_f32 v148,v148,s56,v184\n"
"v_fma_f32 v149,v149,s56,v184\n"
"v_fma_f32 v150,v150,s56,v184\n"
"v_fma_f32 v151,v151,s56,v184\n"
"buffer_load_dwordx4 v215,s[72:75],s39 offen offset:2048 lds\n"
"v_mul_f32 v144,v144,v128\n"
"v_mul_f32 v145,v145,v129\n"
"v_mul_f32 v146,v146,v130\n"
"v_mul_f32 v147,v147,v131\n"
"v_mul_f32 v148,v148,v132\n"
"v_mul_f32 v149,v149,v133\n"
"v_mul_f32 v150,v150,v134\n"
"v_mul_f32 v151,v151,v135\n"
"s_add_u32 s39,s39,s52\n"
"s_add_u32 m0,s51,93184\n"
"s_nop 0\n"
"buffer_load_dwordx4 v213,s[72:75],s39 offen lds\n"
"v_exp_f32 v144,v144\n"
"v_exp_f32 v145,v145\n"
"v_exp_f32 v146,v146\n"
"v_exp_f32 v147,v147\n"
"v_exp_f32 v148,v148\n"
"v_exp_f32 v149,v149\n"
"v_exp_f32 v150,v150\n"
"v_exp_f32 v151,v151\n"
"buffer_load_dwordx4 v214,s[72:75],s39 offen offset:1024 lds\n"
"v_add_f32 v144,1.0,v144\n"
"v_add_f32 v145,1.0,v145\n"
"v_add_f32 v146,1.0,v146\n"
"v_add_f32 v147,1.0,v147\n"
"v_add_f32 v148,1.0,v148\n"
"v_add_f32 v149,1.0,v149\n"
"v_add_f32 v150,1.0,v150\n"
"v_add_f32 v151,1.0,v151\n"
"buffer_load_dwordx4 v215,s[72:75],s39 offen offset:2048 lds\n"
"v_rcp_f32 v144,v144\n"
"v_rcp_f32 v145,v145\n"
"v_rcp_f32 v146,v146\n"
"v_rcp_f32 v147,v147\n"
"v_rcp_f32 v148,v148\n"
"v_rcp_f32 v149,v149\n"
"v_rcp_f32 v150,v150\n"
"v_rcp_f32 v151,v151\n"
"s_add_u32 s39,s39,s53\n"
"buffer_load_dwordx4 a[208:211],v209,s[88:91],s40 offen sc1\n"
"v_mul_f32 v144,v144,v128\n"
"v_mul_f32 v145,v145,v129\n"
"v_mul_f32 v146,v146,v130\n"
"v_mul_f32 v147,v147,v131\n"
"v_mul_f32 v148,v148,v132\n"
"v_mul_f32 v149,v149,v133\n"
"v_mul_f32 v150,v150,v134\n"
"v_mul_f32 v151,v151,v135\n"
"buffer_load_dwordx4 a[212:215],v209,s[88:91],s40 offen offset:1024 sc1\n"
"v_cvt_pk_bf16_f32 v230,v144,v145\n"
"v_cvt_pk_bf16_f32 v231,v146,v147\n"
"v_cvt_pk_bf16_f32 v234,v148,v149\n"
"v_cvt_pk_bf16_f32 v235,v150,v151\n"
"s_mov_b32 s41,16384\n"
"buffer_store_dwordx4 v[228:231],v209,s[84:87],s41 offen sc1\n"
"buffer_store_dwordx4 v[232:235],v209,s[84:87],s41 offen offset:1024 sc1\n"
"v_mul_f32 v144,v136,v136\n"
"buffer_load_dwordx4 a[216:219],v209,s[88:91],s40 offen offset:2048 sc1\n"
"v_mul_f32 v145,v137,v137\n"
"v_mul_f32 v146,v138,v138\n"
"v_mul_f32 v147,v139,v139\n"
"v_mul_f32 v148,v140,v140\n"
"v_mul_f32 v149,v141,v141\n"
"v_mul_f32 v150,v142,v142\n"
"v_mul_f32 v151,v143,v143\n"
"v_fma_f32 v144,v144,s56,v184\n"
"buffer_load_dwordx4 a[220:223],v209,s[88:91],s40 offen offset:3072 sc1\n"
"v_fma_f32 v145,v145,s56,v184\n"
"v_fma_f32 v146,v146,s56,v184\n"
"v_fma_f32 v147,v147,s56,v184\n"
"v_fma_f32 v148,v148,s56,v184\n"
"v_fma_f32 v149,v149,s56,v184\n"
"v_fma_f32 v150,v150,s56,v184\n"
"v_fma_f32 v151,v151,s56,v184\n"
"v_mul_f32 v144,v144,v136\n"
"s_add_u32 s40,s40,4096\n"
"buffer_load_dwordx4 a[224:227],v209,s[88:91],s40 offen sc1\n"
"v_mul_f32 v145,v145,v137\n"
"v_mul_f32 v146,v146,v138\n"
"v_mul_f32 v147,v147,v139\n"
"v_mul_f32 v148,v148,v140\n"
"v_mul_f32 v149,v149,v141\n"
"v_mul_f32 v150,v150,v142\n"
"v_mul_f32 v151,v151,v143\n"
"v_exp_f32 v144,v144\n"
"buffer_load_dwordx4 a[228:231],v209,s[88:91],s40 offen offset:1024 sc1\n"
"v_exp_f32 v145,v145\n"
"v_exp_f32 v146,v146\n"
"v_exp_f32 v147,v147\n"
"v_exp_f32 v148,v148\n"
"v_exp_f32 v149,v149\n"
"v_exp_f32 v150,v150\n"
"v_exp_f32 v151,v151\n"
"v_add_f32 v144,1.0,v144\n"
"buffer_load_dwordx4 a[232:235],v209,s[88:91],s40 offen offset:2048 sc1\n"
"v_add_f32 v145,1.0,v145\n"
"v_add_f32 v146,1.0,v146\n"
"v_add_f32 v147,1.0,v147\n"
"v_add_f32 v148,1.0,v148\n"
"v_add_f32 v149,1.0,v149\n"
"v_add_f32 v150,1.0,v150\n"
"v_add_f32 v151,1.0,v151\n"
"v_rcp_f32 v144,v144\n"
"buffer_load_dwordx4 a[236:239],v209,s[88:91],s40 offen offset:3072 sc1\n"
"v_rcp_f32 v145,v145\n"
"v_rcp_f32 v146,v146\n"
"v_rcp_f32 v147,v147\n"
"v_rcp_f32 v148,v148\n"
"v_rcp_f32 v149,v149\n"
"v_rcp_f32 v150,v150\n"
"v_rcp_f32 v151,v151\n"
"v_mul_f32 v144,v144,v136\n"
"s_add_u32 s40,s40,4096\n"
"v_mul_f32 v145,v145,v137\n"
"v_mul_f32 v146,v146,v138\n"
"v_mul_f32 v147,v147,v139\n"
"v_mul_f32 v148,v148,v140\n"
"v_mul_f32 v149,v149,v141\n"
"v_mul_f32 v150,v150,v142\n"
"v_mul_f32 v151,v151,v143\n"
"v_cvt_pk_bf16_f32 v238,v144,v145\n"
"v_cvt_pk_bf16_f32 v239,v146,v147\n"
"v_cvt_pk_bf16_f32 v244,v148,v149\n"
"v_cvt_pk_bf16_f32 v245,v150,v151\n"
"s_mov_b32 s41,16384\n"
"buffer_store_dwordx4 v[236:239],v209,s[84:87],s41 offen offset:2048 sc1\n"
"buffer_store_dwordx4 v[242:245],v209,s[84:87],s41 offen offset:3072 sc1\n"
"LM_GDONE:\n"
"v_readlane_b32 s84,v210,30\n"
"v_readlane_b32 s85,v210,31\n"
"s_mov_b32 s86,0x4800\n"
"s_mov_b32 s87,0x20000\n"
"v_readlane_b32 s49,v210,27\n"
"v_readlane_b32 s43,v210,43\n"
"s_cmp_lg_u32 s49,0\n"
"s_cselect_b32 s50,114688,0\n"
"s_cselect_b32 s62,0,65536\n"
"s_mov_b32 s63,114688\n"
"s_cselect_b32 s63,65536,s63\n"
"s_add_u32 s41,s43,0\n"
"s_and_b32 s42,s41,1\n"
"s_lshl_b32 s42,s42,5\n"
"s_lshr_b32 s41,s41,1\n"
"s_lshl_b32 s41,s41,8\n"
"v_xor_b32 v252,s42,v212\n"
"v_add3_u32 v252,v252,s41,v211\n"
"v_add_u32 v254,s62,v252\n"
"v_add_u32 v252,0,v252\n"
"s_add_u32 s41,s43,1\n"
"s_and_b32 s42,s41,1\n"
"s_lshl_b32 s42,s42,5\n"
"s_lshr_b32 s41,s41,1\n"
"s_lshl_b32 s41,s41,8\n"
"v_xor_b32 v253,s42,v212\n"
"v_add3_u32 v253,v253,s41,v211\n"
"v_add_u32 v255,s62,v253\n"
"v_add_u32 v253,0,v253\n"
"v_add_u32 v240,s63,v252\n"
"v_add_u32 v252,s50,v252\n"
"v_add_u32 v241,s63,v253\n"
"v_add_u32 v253,s50,v253\n"
"s_add_u32 s50,s50,s51\n"
"s_add_u32 s62,s62,s51\n"
"s_add_u32 s63,s63,s51\n"
"s_cmp_lg_u32 s49,0\n"
"s_cbranch_scc0 LM_5H\n"
"s_waitcnt vmcnt(58)\n"
"s_barrier\n"
"ds_read_b64_tr_b16 a[32:33],v254\n"
"ds_read_b64_tr_b16 a[34:35],v254 offset:6144\n"
"ds_read_b64_tr_b16 a[36:37],v254 offset:12288\n"
"ds_read_b64_tr_b16 a[38:39],v254 offset:18432\n"
"ds_read_b64_tr_b16 a[40:41],v255\n"
"ds_read_b64_tr_b16 a[42:43],v255 offset:6144\n"
"ds_read_b64_tr_b16 a[44:45],v255 offset:12288\n"
"ds_read_b64_tr_b16 a[46:47],v255 offset:18432\n"
"ds_read_b64_tr_b16 a[48:49],v254 offset:256\n"
"ds_read_b64_tr_b16 a[50:51],v254 offset:6400\n"
"ds_read_b64_tr_b16 a[52:53],v254 offset:12544\n"
"ds_read_b64_tr_b16 a[54:55],v254 offset:18688\n"
"s_waitcnt lgkmcnt(8)\n"
"v_mfma_f32_16x16x32_bf16 v[0:3],a[32:35],a[80:83],0\n"
"ds_read_b64_tr_b16 a[56:57],v255 offset:256\n"
"v_mfma_f32_16x16x32_bf16 v[0:3],a[36:39],a[96:99],v[0:3]\n"
"v_mfma_f32_16x16x32_bf16 v[4:7],a[32:35],a[84:87],0\n"
"v_readlane_b32 s39,v248,19\n"
"s_nop 4\n"
"s_add_u32 m0,s50,0\n"
"ds_read_b64_tr_b16 a[58:59],v255 offset:6400\n"
"v_mfma_f32_16x16x32_bf16 v[4:7],a[36:39],a[100:103],v[4:7]\n"
"buffer_load_dwordx4 v213,s[72:75],s39 offen lds\n"
"v_mfma_f32_16x16x32_bf16 v[8:11],a[32:35],a[88:91],0\n"
"ds_read_b64_tr_b16 a[60:61],v255 offset:12544\n"
"v_mfma_f32_16x16x32_bf16 v[8:11],a[36:39],a[104:107],v[8:11]\n"
"v_mfma_f32_16x16x32_bf16 v[12:15],a[32:35],a[92:95],0\n"
"ds_read_b64_tr_b16 a[62:63],v255 offset:18688\n"
"v_mfma_f32_16x16x32_bf16 v[12:15],a[36:39],a[108:111],v[12:15]\n"
"buffer_load_dwordx4 v214,s[72:75],s39 offen offset:1024 lds\n"
"s_waitcnt lgkmcnt(8)\n"
"v_mfma_f32_16x16x32_bf16 v[16:19],a[40:43],a[80:83],0\n"
"ds_read_b64_tr_b16 a[64:65],v254 offset:512\n"
"v_mfma_f32_16x16x32_bf16 v[16:19],a[44:47],a[96:99],v[16:19]\n"
"v_mfma_f32_16x16x32_bf16 v[20:23],a[40:43],a[84:87],0\n"
"ds_read_b64_tr_b16 a[66:67],v254 offset:6656\n"
"v_mfma_f32_16x16x32_bf16 v[20:23],a[44:47],a[100:103],v[20:23]\n"
"v_mfma_f32_16x16x32_bf16 v[24:27],a[40:43],a[88:91],0\n"
"ds_read_b64_tr_b16 a[68:69],v254 offset:12800\n"
"v_mfma_f32_16x16x32_bf16 v[24:27],a[44:47],a[104:107],v[24:27]\n"
"buffer_load_dwordx4 v215,s[72:75],s39 offen offset:2048 lds\n"
"v_mfma_f32_16x16x32_bf16 v[28:31],a[40:43],a[92:95],0\n"
"ds_read_b64_tr_b16 a[70:71],v254 offset:18944\n"
"v_mfma_f32_16x16x32_bf16 v[28:31],a[44:47],a[108:111],v[28:31]\n"
"s_waitcnt lgkmcnt(8)\n"
"v_mfma_f32_16x16x32_bf16 v[32:35],a[48:51],a[80:83],0\n"
"ds_read_b64_tr_b16 a[72:73],v255 offset:512\n"
"v_mfma_f32_16x16x32_bf16 v[32:35],a[52:55],a[96:99],v[32:35]\n"
"v_mfma_f32_16x16x32_bf16 v[36:39],a[48:51],a[84:87],0\n"
"s_add_u32 s39,s39,s52\n"
"s_add_u32 m0,s50,3072\n"
"ds_read_b64_tr_b16 a[74:75],v255 offset:6656\n"
"v_mfma_f32_16x16x32_bf16 v[36:39],a[52:55],a[100:103],v[36:39]\n"
"buffer_load_dwordx4 v213,s[72:75],s39 offen lds\n"
"v_mfma_f32_16x16x32_bf16 v[40:43],a[48:51],a[88:91],0\n"
"ds_read_b64_tr_b16 a[76:77],v255 offset:12800\n"
"v_mfma_f32_16x16x32_bf16 v[40:43],a[52:55],a[104:107],v[40:43]\n"
"v_mfma_f32_16x16x32_bf16 v[44:47],a[48:51],a[92:95],0\n"
"ds_read_b64_tr_b16 a[78:79],v255 offset:18944\n"
"v_mfma_f32_16x16x32_bf16 v[44:47],a[52:55],a[108:111],v[44:47]\n"
"buffer_load_dwordx4 v214,s[72:75],s39 offen offset:1024 lds\n"
"s_waitcnt lgkmcnt(8)\n"
"v_mfma_f32_16x16x32_bf16 v[48:51],a[56:59],a[80:83],0\n"
"ds_read_b64_tr_b16 a[32:33],v254 offset:768\n"
"v_mfma_f32_16x16x32_bf16 v[48:51],a[60:63],a[96:99],v[48:51]\n"
"v_mfma_f32_16x16x32_bf16 v[52:55],a[56:59],a[84:87],0\n"
"ds_read_b64_tr_b16 a[34:35],v254 offset:6912\n"
"v_mfma_f32_16x16x32_bf16 v[52:55],a[60:63],a[100:103],v[52:55]\n"
"v_mfma_f32_16x16x32_bf16 v[56:59],a[56:59],a[88:91],0\n"
"ds_read_b64_tr_b16 a[36:37],v254 offset:13056\n"
"v_mfma_f32_16x16x32_bf16 v[56:59],a[60:63],a[104:107],v[56:59]\n"
"buffer_load_dwordx4 v215,s[72:75],s39 offen offset:2048 lds\n"
"v_mfma_f32_16x16x32_bf16 v[60:63],a[56:59],a[92:95],0\n"
"ds_read_b64_tr_b16 a[38:39],v254 offset:19200\n"
"v_mfma_f32_16x16x32_bf16 v[60:63],a[60:63],a[108:111],v[60:63]\n"
"s_waitcnt lgkmcnt(8)\n"
"v_mfma_f32_16x16x32_bf16 v[64:67],a[64:67],a[80:83],0\n"
"s_add_u32 s39,s39,s53\n"
"v_readlane_b32 s40,v248,24\n"
"s_nop 4\n"
"ds_read_b64_tr_b16 a[40:41],v255 offset:768\n"
"v_mfma_f32_16x16x32_bf16 v[64:67],a[68:71],a[96:99],v[64:67]\n"
"buffer_load_dwordx4 a[160:163],v209,s[88:91],s40 offen sc1\n"
"v_mfma_f32_16x16x32_bf16 v[68:71],a[64:67],a[84:87],0\n"
"ds_read_b64_tr_b16 a[42:43],v255 offset:6912\n"
"v_mfma_f32_16x16x32_bf16 v[68:71],a[68:71],a[100:103],v[68:71]\n"
"v_mfma_f32_16x16x32_bf16 v[72:75],a[64:67],a[88:91],0\n"
"ds_read_b64_tr_b16 a[44:45],v255 offset:13056\n"
"v_mfma_f32_16x16x32_bf16 v[72:75],a[68:71],a[104:107],v[72:75]\n"
"v_mfma_f32_16x16x32_bf16 v[76:79],a[64:67],a[92:95],0\n"
"ds_read_b64_tr_b16 a[46:47],v255 offset:19200\n"
"v_mfma_f32_16x16x32_bf16 v[76:79],a[68:71],a[108:111],v[76:79]\n"
"buffer_load_dwordx4 a[164:167],v209,s[88:91],s40 offen offset:1024 sc1\n"
"s_waitcnt lgkmcnt(8)\n"
"v_mfma_f32_16x16x32_bf16 v[80:83],a[72:75],a[80:83],0\n"
"ds_read_b64_tr_b16 a[48:49],v254 offset:1024\n"
"v_mfma_f32_16x16x32_bf16 v[80:83],a[76:79],a[96:99],v[80:83]\n"
"v_mfma_f32_16x16x32_bf16 v[84:87],a[72:75],a[84:87],0\n"
"ds_read_b64_tr_b16 a[50:51],v254 offset:7168\n"
"v_mfma_f32_16x16x32_bf16 v[84:87],a[76:79],a[100:103],v[84:87]\n"
"buffer_load_dwordx4 a[168:171],v209,s[88:91],s40 offen offset:2048 sc1\n"
"v_mfma_f32_16x16x32_bf16 v[88:91],a[72:75],a[88:91],0\n"
"ds_read_b64_tr_b16 a[52:53],v254 offset:13312\n"
"v_mfma_f32_16x16x32_bf16 v[88:91],a[76:79],a[104:107],v[88:91]\n"
"v_mfma_f32_16x16x32_bf16 v[92:95],a[72:75],a[92:95],0\n"
"ds_read_b64_tr_b16 a[54:55],v254 offset:19456\n"
"v_mfma_f32_16x16x32_bf16 v[92:95],a[76:79],a[108:111],v[92:95]\n"
"s_waitcnt vmcnt(49) lgkmcnt(8)\n"
"s_barrier\n"
"v_mfma_f32_16x16x32_bf16 v[96:99],a[32:35],a[80:83],0\n"
"ds_read_b64_tr_b16 a[56:57],v254 offset:24576\n"
"v_mfma_f32_16x16x32_bf16 v[96:99],a[36:39],a[96:99],v[96:99]\n"
"buffer_load_dwordx4 a[172:175],v209,s[88:91],s40 offen offset:3072 sc1\n"
"v_mfma_f32_16x16x32_bf16 v[100:103],a[32:35],a[84:87],0\n"
"ds_read_b64_tr_b16 a[58:59],v254 offset:30720\n"
"v_mfma_f32_16x16x32_bf16 v[100:103],a[36:39],a[100:103],v[100:103]\n"
"v_mfma_f32_16x16x32_bf16 v[104:107],a[32:35],a[88:91],0\n"
"ds_read_b64_tr_b16 a[60:61],v254 offset:36864\n"
"v_mfma_f32_16x16x32_bf16 v[104:107],a[36:39],a[104:107],v[104:107]\n"
"v_mfma_f32_16x16x32_bf16 v[108:111],a[32:35],a[92:95],0\n"
"s_add_u32 s40,s40,4096\n"
"ds_read_b64_tr_b16 a[62:63],v254 offset:43008\n"
"v_mfma_f32_16x16x32_bf16 v[108:111],a[36:39],a[108:111],v[108:111]\n"
"buffer_load_dwordx4 a[176:179],v209,s[88:91],s40 offen sc1\n"
"s_waitcnt lgkmcnt(8)\n"
"v_mfma_f32_16x16x32_bf16 v[112:115],a[40:43],a[80:83],0\n"
"ds_read_b64_tr_b16 a[64:65],v255 offset:24576\n"
"v_mfma_f32_16x16x32_bf16 v[112:115],a[44:47],a[96:99],v[112:115]\n"
"v_mfma_f32_16x16x32_bf16 v[116:119],a[40:43],a[84:87],0\n"
"ds_read_b64_tr_b16 a[66:67],v255 offset:30720\n"
"v_mfma_f32_16x16x32_bf16 v[116:119],a[44:47],a[100:103],v[116:119]\n"
"buffer_load_dwordx4 a[180:183],v209,s[88:91],s40 offen offset:1024 sc1\n"
"v_mfma_f32_16x16x32_bf16 v[120:123],a[40:43],a[88:91],0\n"
"ds_read_b64_tr_b16 a[68:69],v255 offset:36864\n"
"v_mfma_f32_16x16x32_bf16 v[120:123],a[44:47],a[104:107],v[120:123]\n"
"v_mfma_f32_16x16x32_bf16 v[124:127],a[40:43],a[92:95],0\n"
"ds_read_b64_tr_b16 a[70:71],v255 offset:43008\n"
"v_mfma_f32_16x16x32_bf16 v[124:127],a[44:47],a[108:111],v[124:127]\n"
"s_waitcnt lgkmcnt(8)\n"
"v_mfma_f32_16x16x32_bf16 v[128:131],a[48:51],a[80:83],0\n"
"ds_read_b64_tr_b16 a[72:73],v254 offset:24832\n"
"v_mfma_f32_16x16x32_bf16 v[128:131],a[52:55],a[96:99],v[128:131]\n"
"buffer_load_dwordx4 a[184:187],v209,s[88:91],s40 offen offset:2048 sc1\n"
"v_mfma_f32_16x16x32_bf16 v[132:135],a[48:51],a[84:87],0\n"
"ds_read_b64_tr_b16 a[74:75],v254 offset:30976\n"
"v_mfma_f32_16x16x32_bf16 v[132:135],a[52:55],a[100:103],v[132:135]\n"
"v_mfma_f32_16x16x32_bf16 v[136:139],a[48:51],a[88:91],0\n"
"ds_read_b64_tr_b16 a[76:77],v254 offset:37120\n"
"v_mfma_f32_16x16x32_bf16 v[136:139],a[52:55],a[104:107],v[136:139]\n"
"buffer_load_dwordx4 a[188:191],v209,s[88:91],s40 offen offset:3072 sc1\n"
"v_mfma_f32_16x16x32_bf16 v[140:143],a[48:51],a[92:95],0\n"
"ds_read_b64_tr_b16 a[78:79],v254 offset:43264\n"
"v_mfma_f32_16x16x32_bf16 v[140:143],a[52:55],a[108:111],v[140:143]\n"
"s_add_u32 s40,s40,4096\n"
"s_waitcnt lgkmcnt(8)\n"
"v_mfma_f32_16x16x32_bf16 v[0:3],a[56:59],a[128:131],v[0:3]\n"
"ds_read_b64_tr_b16 a[32:33],v255 offset:24832\n"
"v_mfma_f32_16x16x32_bf16 v[0:3],a[60:63],a[144:147],v[0:3]\n"
"v_mfma_f32_16x16x32_bf16 v[4:7],a[56:59],a[132:135],v[4:7]\n"
"s_add_u32 m0,s50,24576\n"
"ds_read_b64_tr_b16 a[34:35],v255 offset:30976\n"
"v_mfma_f32_16x16x32_bf16 v[4:7],a[60:63],a[148:151],v[4:7]\n"
"buffer_load_dwordx4 v213,s[72:75],s39 offen lds\n"
"v_mfma_f32_16x16x32_bf16 v[8:11],a[56:59],a[136:139],v[8:11]\n"
"ds_read_b64_tr_b16 a[36:37],v255 offset:37120\n"
"v_mfma_f32_16x16x32_bf16 v[8:11],a[60:63],a[152:155],v[8:11]\n"
"v_mfma_f32_16x16x32_bf16 v[12:15],a[56:59],a[140:143],v[12:15]\n"
"ds_read_b64_tr_b16 a[38:39],v255 offset:43264\n"
"v_mfma_f32_16x16x32_bf16 v[12:15],a[60:63],a[156:159],v[12:15]\n"
"buffer_load_dwordx4 v214,s[72:75],s39 offen offset:1024 lds\n"
"s_waitcnt lgkmcnt(8)\n"
"v_mfma_f32_16x16x32_bf16 v[16:19],a[64:67],a[128:131],v[16:19]\n"
"ds_read_b64_tr_b16 a[40:41],v254 offset:25088\n"
"v_mfma_f32_16x16x32_bf16 v[16:19],a[68:71],a[144:147],v[16:19]\n"
"v_mfma_f32_16x16x32_bf16 v[20:23],a[64:67],a[132:135],v[20:23]\n"
"ds_read_b64_tr_b16 a[42:43],v254 offset:31232\n"
"v_mfma_f32_16x16x32_bf16 v[20:23],a[68:71],a[148:151],v[20:23]\n"
"v_mfma_f32_16x16x32_bf16 v[24:27],a[64:67],a[136:139],v[24:27]\n"
"ds_read_b64_tr_b16 a[44:45],v254 offset:37376\n"
"v_mfma_f32_16x16x32_bf16 v[24:27],a[68:71],a[152:155],v[24:27]\n"
"buffer_load_dwordx4 v215,s[72:75],s39 offen offset:2048 lds\n"
"v_mfma_f32_16x16x32_bf16 v[28:31],a[64:67],a[140:143],v[28:31]\n"
"ds_read_b64_tr_b16 a[46:47],v254 offset:43520\n"
"v_mfma_f32_16x16x32_bf16 v[28:31],a[68:71],a[156:159],v[28:31]\n"
"s_waitcnt lgkmcnt(8)\n"
"v_mfma_f32_16x16x32_bf16 v[32:35],a[72:75],a[128:131],v[32:35]\n"
"ds_read_b64_tr_b16 a[48:49],v255 offset:25088\n"
"v_mfma_f32_16x16x32_bf16 v[32:35],a[76:79],a[144:147],v[32:35]\n"
"v_mfma_f32_16x16x32_bf16 v[36:39],a[72:75],a[132:135],v[36:39]\n"
"s_add_u32 s39,s39,s52\n"
"s_add_u32 m0,s50,27648\n"
"ds_read_b64_tr_b16 a[50:51],v255 offset:31232\n"
"v_mfma_f32_16x16x32_bf16 v[36:39],a[76:79],a[148:151],v[36:39]\n"
"buffer_load_dwordx4 v213,s[72:75],s39 offen lds\n"
"v_mfma_f32_16x16x32_bf16 v[40:43],a[72:75],a[136:139],v[40:43]\n"
"ds_read_b64_tr_b16 a[52:53],v255 offset:37376\n"
"v_mfma_f32_16x16x32_bf16 v[40:43],a[76:79],a[152:155],v[40:43]\n"
"v_mfma_f32_16x16x32_bf16 v[44:47],a[72:75],a[140:143],v[44:47]\n"
"ds_read_b64_tr_b16 a[54:55],v255 offset:43520\n"
"v_mfma_f32_16x16x32_bf16 v[44:47],a[76:79],a[156:159],v[44:47]\n"
"buffer_load_dwordx4 v214,s[72:75],s39 offen offset:1024 lds\n"
"s_waitcnt lgkmcnt(8)\n"
"v_mfma_f32_16x16x32_bf16 v[48:51],a[32:35],a[128:131],v[48:51]\n"
"ds_read_b64_tr_b16 a[56:57],v254 offset:25344\n"
"v_mfma_f32_16x16x32_bf16 v[48:51],a[36:39],a[144:147],v[48:51]\n"
"v_mfma_f32_16x16x32_bf16 v[52:55],a[32:35],a[132:135],v[52:55]\n"
"ds_read_b64_tr_b16 a[58:59],v254 offset:31488\n"
"v_mfma_f32_16x16x32_bf16 v[52:55],a[36:39],a[148:151],v[52:55]\n"
"v_mfma_f32_16x16x32_bf16 v[56:59],a[32:35],a[136:139],v[56:59]\n"
"ds_read_b64_tr_b16 a[60:61],v254 offset:37632\n"
"v_mfma_f32_16x16x32_bf16 v[56:59],a[36:39],a[152:155],v[56:59]\n"
"buffer_load_dwordx4 v215,s[72:75],s39 offen offset:2048 lds\n"
"v_mfma_f32_16x16x32_bf16 v[60:63],a[32:35],a[140:143],v[60:63]\n"
"ds_read_b64_tr_b16 a[62:63],v254 offset:43776\n"
"v_mfma_f32_16x16x32_bf16 v[60:63],a[36:39],a[156:159],v[60:63]\n"
"s_waitcnt lgkmcnt(8)\n"
"v_mfma_f32_16x16x32_bf16 v[64:67],a[40:43],a[128:131],v[64:67]\n"
"s_add_u32 s39,s39,s53\n"
"ds_read_b64_tr_b16 a[64:65],v255 offset:25344\n"
"v_mfma_f32_16x16x32_bf16 v[64:67],a[44:47],a[144:147],v[64:67]\n"
"buffer_load_dwordx4 a[0:3],v209,s[88:91],s40 offen sc1\n"
"v_mfma_f32_16x16x32_bf16 v[68:71],a[40:43],a[132:135],v[68:71]\n"
"ds_read_b64_tr_b16 a[66:67],v255 offset:31488\n"
"v_mfma_f32_16x16x32_bf16 v[68:71],a[44:47],a[148:151],v[68:71]\n"
"v_mfma_f32_16x16x32_bf16 v[72:75],a[40:43],a[136:139],v[72:75]\n"
"ds_read_b64_tr_b16 a[68:69],v255 offset:37632\n"
"v_mfma_f32_16x16x32_bf16 v[72:75],a[44:47],a[152:155],v[72:75]\n"
"v_mfma_f32_16x16x32_bf16 v[76:79],a[40:43],a[140:143],v[76:79]\n"
"ds_read_b64_tr_b16 a[70:71],v255 offset:43776\n"
"v_mfma_f32_16x16x32_bf16 v[76:79],a[44:47],a[156:159],v[76:79]\n"
"buffer_load_dwordx4 a[4:7],v209,s[88:91],s40 offen offset:1024 sc1\n"
"s_waitcnt lgkmcnt(8)\n"
"v_mfma_f32_16x16x32_bf16 v[80:83],a[48:51],a[128:131],v[80:83]\n"
"ds_read_b64_tr_b16 a[72:73],v254 offset:25600\n"
"v_mfma_f32_16x16x32_bf16 v[80:83],a[52:55],a[144:147],v[80:83]\n"
"v_mfma_f32_16x16x32_bf16 v[84:87],a[48:51],a[132:135],v[84:87]\n"
"ds_read_b64_tr_b16 a[74:75],v254 offset:31744\n"
"v_mfma_f32_16x16x32_bf16 v[84:87],a[52:55],a[148:151],v[84:87]\n"
"buffer_load_dwordx4 a[8:11],v209,s[88:91],s40 offen offset:2048 sc1\n"
"v_mfma_f32_16x16x32_bf16 v[88:91],a[48:51],a[136:139],v[88:91]\n"
"ds_read_b64_tr_b16 a[76:77],v254 offset:37888\n"
"v_mfma_f32_16x16x32_bf16 v[88:91],a[52:55],a[152:155],v[88:91]\n"
"v_mfma_f32_16x16x32_bf16 v[92:95],a[48:51],a[140:143],v[92:95]\n"
"ds_read_b64_tr_b16 a[78:79],v254 offset:44032\n"
"v_mfma_f32_16x16x32_bf16 v[92:95],a[52:55],a[156:159],v[92:95]\n"
"s_waitcnt vmcnt(23) lgkmcnt(8)\n"
"s_barrier\n"
"v_readlane_b32 s43,v210,54\n"
"s_nop 0\n"
"s_lshl_b32 s43,s43,6\n"
"v_lshl_add_u32 v223,v209,2,s43\n"
"s_cmp_lg_u32 s48,0\n"
"s_cbranch_scc1 LM_FS17\n"
"s_mov_b64 exec,1\n"
"buffer_store_dwordx2 v[224:225],v223,s[68:71],0 offen sc1\n"
"s_mov_b64 exec,-1\n"
"LM_FS17:\n"
"v_mfma_f32_16x16x32_bf16 v[96:99],a[56:59],a[128:131],v[96:99]\n"
"ds_read_b64_tr_b16 a[32:33],v240\n"
"v_mfma_f32_16x16x32_bf16 v[96:99],a[60:63],a[144:147],v[96:99]\n"
"buffer_load_dwordx4 a[12:15],v209,s[88:91],s40 offen offset:3072 sc1\n"
"v_mfma_f32_16x16x32_bf16 v[100:103],a[56:59],a[132:135],v[100:103]\n"
"ds_read_b64_tr_b16 a[34:35],v240 offset:6144\n"
"v_mfma_f32_16x16x32_bf16 v[100:103],a[60:63],a[148:151],v[100:103]\n"
"v_mfma_f32_16x16x32_bf16 v[104:107],a[56:59],a[136:139],v[104:107]\n"
"ds_read_b64_tr_b16 a[36:37],v240 offset:12288\n"
"v_mfma_f32_16x16x32_bf16 v[104:107],a[60:63],a[152:155],v[104:107]\n"
"v_mfma_f32_16x16x32_bf16 v[108:111],a[56:59],a[140:143],v[108:111]\n"
"s_add_u32 s40,s40,4096\n"
"ds_read_b64_tr_b16 a[38:39],v240 offset:18432\n"
"v_mfma_f32_16x16x32_bf16 v[108:111],a[60:63],a[156:159],v[108:111]\n"
"buffer_load_dwordx4 a[16:19],v209,s[88:91],s40 offen sc1\n"
"s_waitcnt lgkmcnt(8)\n"
"v_mfma_f32_16x16x32_bf16 v[112:115],a[64:67],a[128:131],v[112:115]\n"
"ds_read_b64_tr_b16 a[40:41],v241\n"
"v_mfma_f32_16x16x32_bf16 v[112:115],a[68:71],a[144:147],v[112:115]\n"
"v_mfma_f32_16x16x32_bf16 v[116:119],a[64:67],a[132:135],v[116:119]\n"
"ds_read_b64_tr_b16 a[42:43],v241 offset:6144\n"
"v_mfma_f32_16x16x32_bf16 v[116:119],a[68:71],a[148:151],v[116:119]\n"
"buffer_load_dwordx4 a[20:23],v209,s[88:91],s40 offen offset:1024 sc1\n"
"v_mfma_f32_16x16x32_bf16 v[120:123],a[64:67],a[136:139],v[120:123]\n"
"ds_read_b64_tr_b16 a[44:45],v241 offset:12288\n"
"v_mfma_f32_16x16x32_bf16 v[120:123],a[68:71],a[152:155],v[120:123]\n"
"v_mfma_f32_16x16x32_bf16 v[124:127],a[64:67],a[140:143],v[124:127]\n"
"ds_read_b64_tr_b16 a[46:47],v241 offset:18432\n"
"v_mfma_f32_16x16x32_bf16 v[124:127],a[68:71],a[156:159],v[124:127]\n"
"s_waitcnt lgkmcnt(8)\n"
"v_mfma_f32_16x16x32_bf16 v[128:131],a[72:75],a[128:131],v[128:131]\n"
"ds_read_b64_tr_b16 a[48:49],v240 offset:256\n"
"v_mfma_f32_16x16x32_bf16 v[128:131],a[76:79],a[144:147],v[128:131]\n"
"buffer_load_dwordx4 a[24:27],v209,s[88:91],s40 offen offset:2048 sc1\n"
"v_mfma_f32_16x16x32_bf16 v[132:135],a[72:75],a[132:135],v[132:135]\n"
"ds_read_b64_tr_b16 a[50:51],v240 offset:6400\n"
"v_mfma_f32_16x16x32_bf16 v[132:135],a[76:79],a[148:151],v[132:135]\n"
"v_mfma_f32_16x16x32_bf16 v[136:139],a[72:75],a[136:139],v[136:139]\n"
"ds_read_b64_tr_b16 a[52:53],v240 offset:12544\n"
"v_mfma_f32_16x16x32_bf16 v[136:139],a[76:79],a[152:155],v[136:139]\n"
"buffer_load_dwordx4 a[28:31],v209,s[88:91],s40 offen offset:3072 sc1\n"
"v_mfma_f32_16x16x32_bf16 v[140:143],a[72:75],a[140:143],v[140:143]\n"
"ds_read_b64_tr_b16 a[54:55],v240 offset:18688\n"
"v_mfma_f32_16x16x32_bf16 v[140:143],a[76:79],a[156:159],v[140:143]\n"
"s_add_u32 s40,s40,4096\n"
"s_branch LM_5M\n"
"LM_5H:\n"
"s_mov_b32 s90,147456\n"
"v_mov_b32 v188,v191\n"
"v_add_u32 v189,512,v191\n"
"s_waitcnt vmcnt(63)\n"
"s_barrier\n"
"ds_read_b64_tr_b16 a[32:33],v252\n"
"ds_read_b64_tr_b16 a[34:35],v252 offset:6144\n"
"ds_read_b64_tr_b16 a[36:37],v252 offset:12288\n"
"ds_read_b64_tr_b16 a[38:39],v252 offset:18432\n"
"ds_read_b64_tr_b16 a[40:41],v253\n"
"ds_read_b64_tr_b16 a[42:43],v253 offset:6144\n"
"ds_read_b64_tr_b16 a[44:45],v253 offset:12288\n"
"ds_read_b64_tr_b16 a[46:47],v253 offset:18432\n"
"ds_read_b64_tr_b16 a[48:49],v252 offset:256\n"
"ds_read_b64_tr_b16 a[50:51],v252 offset:6400\n"
"ds_read_b64_tr_b16 a[52:53],v252 offset:12544\n"
"ds_read_b64_tr_b16 a[54:55],v252 offset:18688\n"
"s_waitcnt lgkmcnt(8)\n"
"v_mfma_f32_16x16x32_bf16 v[0:3],a[32:35],v[152:155],0\n"
"ds_read_b64_tr_b16 a[56:57],v253 offset:256\n"
"v_mfma_f32_16x16x32_bf16 v[0:3],a[36:39],v[168:171],v[0:3]\n"
"v_mfma_f32_16x16x32_bf16 v[4:7],a[32:35],v[156:159],0\n"
"ds_read_b64_tr_b16 a[58:59],v253 offset:6400\n"
"v_mfma_f32_16x16x32_bf16 v[4:7],a[36:39],v[172:175],v[4:7]\n"
"v_mfma_f32_16x16x32_bf16 v[8:11],a[32:35],v[160:163],0\n"
"ds_read_b64_tr_b16 a[60:61],v253 offset:12544\n"
"v_mfma_f32_16x16x32_bf16 v[8:11],a[36:39],v[176:179],v[8:11]\n"
"v_mfma_f32_16x16x32_bf16 v[12:15],a[32:35],v[164:167],0\n"
"v_readlane_b32 s39,v248,18\n"
"s_nop 4\n"
"s_add_u32 m0,s63,0\n"
"ds_read_b64_tr_b16 a[62:63],v253 offset:18688\n"
"v_mfma_f32_16x16x32_bf16 v[12:15],a[36:39],v[180:183],v[12:15]\n"
"buffer_load_dwordx4 v213,s[72:75],s39 offen lds\n"
"s_waitcnt lgkmcnt(8)\n"
"v_mfma_f32_16x16x32_bf16 v[16:19],a[40:43],v[152:155],0\n"
"ds_read_b64_tr_b16 a[64:65],v252 offset:512\n"
"v_mfma_f32_16x16x32_bf16 v[16:19],a[44:47],v[168:171],v[16:19]\n"
"v_mfma_f32_16x16x32_bf16 v[20:23],a[40:43],v[156:159],0\n"
"ds_read_b64_tr_b16 a[66:67],v252 offset:6656\n"
"v_mfma_f32_16x16x32_bf16 v[20:23],a[44:47],v[172:175],v[20:23]\n"
"v_mfma_f32_16x16x32_bf16 v[24:27],a[40:43],v[160:163],0\n"
"ds_read_b64_tr_b16 a[68:69],v252 offset:12800\n"
"v_mfma_f32_16x16x32_bf16 v[24:27],a[44:47],v[176:179],v[24:27]\n"
"v_mfma_f32_16x16x32_bf16 v[28:31],a[40:43],v[164:167],0\n"
"ds_read_b64_tr_b16 a[70:71],v252 offset:18944\n"
"v_mfma_f32_16x16x32_bf16 v[28:31],a[44:47],v[180:183],v[28:31]\n"
"s_waitcnt lgkmcnt(8)\n"
"v_mfma_f32_16x16x32_bf16 v[32:35],a[48:51],v[152:155],0\n"
"ds_read_b64_tr_b16 a[72:73],v253 offset:512\n"
"v_mfma_f32_16x16x32_bf16 v[32:35],a[52:55],v[168:171],v[32:35]\n"
"v_mfma_f32_16x16x32_bf16 v[36:39],a[48:51],v[156:159],0\n"
"ds_read_b64_tr_b16 a[74:75],v253 offset:6656\n"
"v_mfma_f32_16x16x32_bf16 v[36:39],a[52:55],v[172:175],v[36:39]\n"
"buffer_load_dwordx4 v214,s[72:75],s39 offen offset:1024 lds\n"
"v_mfma_f32_16x16x32_bf16 v[40:43],a[48:51],v[160:163],0\n"
"ds_read_b64_tr_b16 a[76:77],v253 offset:12800\n"
"v_mfma_f32_16x16x32_bf16 v[40:43],a[52:55],v[176:179],v[40:43]\n"
"v_mfma_f32_16x16x32_bf16 v[44:47],a[48:51],v[164:167],0\n"
"ds_read_b64_tr_b16 a[78:79],v253 offset:18944\n"
"v_mfma_f32_16x16x32_bf16 v[44:47],a[52:55],v[180:183],v[44:47]\n"
"s_waitcnt lgkmcnt(8)\n"
"v_mfma_f32_16x16x32_bf16 v[48:51],a[56:59],v[152:155],0\n"
"ds_read_b64_tr_b16 a[32:33],v252 offset:768\n"
"v_mfma_f32_16x16x32_bf16 v[48:51],a[60:63],v[168:171],v[48:51]\n"
"v_mfma_f32_16x16x32_bf16 v[52:55],a[56:59],v[156:159],0\n"
"ds_read_b64_tr_b16 a[34:35],v252 offset:6912\n"
"v_mfma_f32_16x16x32_bf16 v[52:55],a[60:63],v[172:175],v[52:55]\n"
"v_mfma_f32_16x16x32_bf16 v[56:59],a[56:59],v[160:163],0\n"
"ds_read_b64_tr_b16 a[36:37],v252 offset:13056\n"
"v_mfma_f32_16x16x32_bf16 v[56:59],a[60:63],v[176:179],v[56:59]\n"
"v_mfma_f32_16x16x32_bf16 v[60:63],a[56:59],v[164:167],0\n"
"ds_read_b64_tr_b16 a[38:39],v252 offset:19200\n"
"v_mfma_f32_16x16x32_bf16 v[60:63],a[60:63],v[180:183],v[60:63]\n"
"buffer_load_dwordx4 v215,s[72:75],s39 offen offset:2048 lds\n"
"s_waitcnt lgkmcnt(8)\n"
"v_mfma_f32_16x16x32_bf16 v[64:67],a[64:67],v[152:155],0\n"
"ds_read_b64_tr_b16 a[40:41],v253 offset:768\n"
"v_mfma_f32_16x16x32_bf16 v[64:67],a[68:71],v[168:171],v[64:67]\n"
"v_mfma_f32_16x16x32_bf16 v[68:71],a[64:67],v[156:159],0\n"
"ds_read_b64_tr_b16 a[42:43],v253 offset:6912\n"
"v_mfma_f32_16x16x32_bf16 v[68:71],a[68:71],v[172:175],v[68:71]\n"
"v_mfma_f32_16x16x32_bf16 v[72:75],a[64:67],v[160:163],0\n"
"ds_read_b64_tr_b16 a[44:45],v253 offset:13056\n"
"v_mfma_f32_16x16x32_bf16 v[72:75],a[68:71],v[176:179],v[72:75]\n"
"v_mfma_f32_16x16x32_bf16 v[76:79],a[64:67],v[164:167],0\n"
"ds_read_b64_tr_b16 a[46:47],v253 offset:19200\n"
"v_mfma_f32_16x16x32_bf16 v[76:79],a[68:71],v[180:183],v[76:79]\n"
"s_waitcnt lgkmcnt(8)\n"
"v_mfma_f32_16x16x32_bf16 v[80:83],a[72:75],v[152:155],0\n"
"ds_read_b64_tr_b16 a[48:49],v252 offset:1024\n"
"v_mfma_f32_16x16x32_bf16 v[80:83],a[76:79],v[168:171],v[80:83]\n"
"v_mfma_f32_16x16x32_bf16 v[84:87],a[72:75],v[156:159],0\n"
"s_add_u32 s39,s39,s52\n"
"s_add_u32 m0,s63,3072\n"
"ds_read_b64_tr_b16 a[50:51],v252 offset:7168\n"
"v_mfma_f32_16x16x32_bf16 v[84:87],a[76:79],v[172:175],v[84:87]\n"
"buffer_load_dwordx4 v213,s[72:75],s39 offen lds\n"
"v_mfma_f32_16x16x32_bf16 v[88:91],a[72:75],v[160:163],0\n"
"ds_read_b64_tr_b16 a[52:53],v252 offset:13312\n"
"v_mfma_f32_16x16x32_bf16 v[88:91],a[76:79],v[176:179],v[88:91]\n"
"v_mfma_f32_16x16x32_bf16 v[92:95],a[72:75],v[164:167],0\n"
"ds_read_b64_tr_b16 a[54:55],v252 offset:19456\n"
"v_mfma_f32_16x16x32_bf16 v[92:95],a[76:79],v[180:183],v[92:95]\n"
"s_waitcnt vmcnt(4) lgkmcnt(8)\n"
"s_barrier\n"
"v_readlane_b32 s43,v210,54\n"
"s_nop 0\n"
"s_lshl_b32 s43,s43,6\n"
"v_lshl_add_u32 v223,v209,2,s43\n"
"s_cmp_lg_u32 s48,0\n"
"s_cbranch_scc1 LM_FS18\n"
"s_mov_b64 exec,1\n"
"buffer_store_dwordx2 v[224:225],v223,s[68:71],0 offen sc1\n"
"s_mov_b64 exec,-1\n"
"LM_FS18:\n"
"v_mfma_f32_16x16x32_bf16 v[96:99],a[32:35],v[152:155],0\n"
"ds_read_b64_tr_b16 a[56:57],v252 offset:24576\n"
"v_mfma_f32_16x16x32_bf16 v[96:99],a[36:39],v[168:171],v[96:99]\n"
"v_mfma_f32_16x16x32_bf16 v[100:103],a[32:35],v[156:159],0\n"
"ds_read_b64_tr_b16 a[58:59],v252 offset:30720\n"
"v_mfma_f32_16x16x32_bf16 v[100:103],a[36:39],v[172:175],v[100:103]\n"
"v_mfma_f32_16x16x32_bf16 v[104:107],a[32:35],v[160:163],0\n"
"ds_read_b64_tr_b16 a[60:61],v252 offset:36864\n"
"v_mfma_f32_16x16x32_bf16 v[104:107],a[36:39],v[176:179],v[104:107]\n"
"v_mfma_f32_16x16x32_bf16 v[108:111],a[32:35],v[164:167],0\n"
"ds_read_b64_tr_b16 a[62:63],v252 offset:43008\n"
"v_mfma_f32_16x16x32_bf16 v[108:111],a[36:39],v[180:183],v[108:111]\n"
"buffer_load_dwordx4 v214,s[72:75],s39 offen offset:1024 lds\n"
"s_waitcnt lgkmcnt(8)\n"
"v_mfma_f32_16x16x32_bf16 v[112:115],a[40:43],v[152:155],0\n"
"ds_read_b64_tr_b16 a[64:65],v253 offset:24576\n"
"v_mfma_f32_16x16x32_bf16 v[112:115],a[44:47],v[168:171],v[112:115]\n"
"v_mfma_f32_16x16x32_bf16 v[116:119],a[40:43],v[156:159],0\n"
"ds_read_b64_tr_b16 a[66:67],v253 offset:30720\n"
"v_mfma_f32_16x16x32_bf16 v[116:119],a[44:47],v[172:175],v[116:119]\n"
"v_mfma_f32_16x16x32_bf16 v[120:123],a[40:43],v[160:163],0\n"
"ds_read_b64_tr_b16 a[68:69],v253 offset:36864\n"
"v_mfma_f32_16x16x32_bf16 v[120:123],a[44:47],v[176:179],v[120:123]\n"
"v_mfma_f32_16x16x32_bf16 v[124:127],a[40:43],v[164:167],0\n"
"ds_read_b64_tr_b16 a[70:71],v253 offset:43008\n"
"v_mfma_f32_16x16x32_bf16 v[124:127],a[44:47],v[180:183],v[124:127]\n"
"s_waitcnt lgkmcnt(8)\n"
"v_mfma_f32_16x16x32_bf16 v[128:131],a[48:51],v[152:155],0\n"
"ds_read_b64_tr_b16 a[72:73],v252 offset:24832\n"
"v_mfma_f32_16x16x32_bf16 v[128:131],a[52:55],v[168:171],v[128:131]\n"
"v_mfma_f32_16x16x32_bf16 v[132:135],a[48:51],v[156:159],0\n"
"ds_read_b64_tr_b16 a[74:75],v252 offset:30976\n"
"v_mfma_f32_16x16x32_bf16 v[132:135],a[52:55],v[172:175],v[132:135]\n"
"buffer_load_dwordx4 v215,s[72:75],s39 offen offset:2048 lds\n"
"v_mfma_f32_16x16x32_bf16 v[136:139],a[48:51],v[160:163],0\n"
"ds_read_b64_tr_b16 a[76:77],v252 offset:37120\n"
"v_mfma_f32_16x16x32_bf16 v[136:139],a[52:55],v[176:179],v[136:139]\n"
"v_mfma_f32_16x16x32_bf16 v[140:143],a[48:51],v[164:167],0\n"
"ds_read_b64_tr_b16 a[78:79],v252 offset:43264\n"
"v_mfma_f32_16x16x32_bf16 v[140:143],a[52:55],v[180:183],v[140:143]\n"
"s_add_u32 s39,s39,s53\n"
"s_waitcnt lgkmcnt(8)\n"
"v_mfma_f32_16x16x32_bf16 v[0:3],a[56:59],v[192:195],v[0:3]\n"
"ds_read_b64_tr_b16 a[32:33],v253 offset:24832\n"
"v_mfma_f32_16x16x32_bf16 v[0:3],a[60:63],v[228:231],v[0:3]\n"
"v_mfma_f32_16x16x32_bf16 v[4:7],a[56:59],v[196:199],v[4:7]\n"
"ds_read_b64_tr_b16 a[34:35],v253 offset:30976\n"
"v_mfma_f32_16x16x32_bf16 v[4:7],a[60:63],v[232:235],v[4:7]\n"
"v_mfma_f32_16x16x32_bf16 v[8:11],a[56:59],v[200:203],v[8:11]\n"
"ds_read_b64_tr_b16 a[36:37],v253 offset:37120\n"
"v_mfma_f32_16x16x32_bf16 v[8:11],a[60:63],v[236:239],v[8:11]\n"
"v_mfma_f32_16x16x32_bf16 v[12:15],a[56:59],v[204:207],v[12:15]\n"
"s_add_u32 m0,s63,24576\n"
"ds_read_b64_tr_b16 a[38:39],v253 offset:43264\n"
"v_mfma_f32_16x16x32_bf16 v[12:15],a[60:63],v[242:245],v[12:15]\n"
"buffer_load_dwordx4 v213,s[72:75],s39 offen lds\n"
"s_waitcnt lgkmcnt(8)\n"
"v_mfma_f32_16x16x32_bf16 v[16:19],a[64:67],v[192:195],v[16:19]\n"
"ds_read_b64_tr_b16 a[40:41],v252 offset:25088\n"
"v_mfma_f32_16x16x32_bf16 v[16:19],a[68:71],v[228:231],v[16:19]\n"
"v_mfma_f32_16x16x32_bf16 v[20:23],a[64:67],v[196:199],v[20:23]\n"
"ds_read_b64_tr_b16 a[42:43],v252 offset:31232\n"
"v_mfma_f32_16x16x32_bf16 v[20:23],a[68:71],v[232:235],v[20:23]\n"
"v_mfma_f32_16x16x32_bf16 v[24:27],a[64:67],v[200:203],v[24:27]\n"
"ds_read_b64_tr_b16 a[44:45],v252 offset:37376\n"
"v_mfma_f32_16x16x32_bf16 v[24:27],a[68:71],v[236:239],v[24:27]\n"
"v_mfma_f32_16x16x32_bf16 v[28:31],a[64:67],v[204:207],v[28:31]\n"
"ds_read_b64_tr_b16 a[46:47],v252 offset:43520\n"
"v_mfma_f32_16x16x32_bf16 v[28:31],a[68:71],v[242:245],v[28:31]\n"
"s_waitcnt lgkmcnt(8)\n"
"v_mfma_f32_16x16x32_bf16 v[32:35],a[72:75],v[192:195],v[32:35]\n"
"ds_read_b64_tr_b16 a[48:49],v253 offset:25088\n"
"v_mfma_f32_16x16x32_bf16 v[32:35],a[76:79],v[228:231],v[32:35]\n"
"v_mfma_f32_16x16x32_bf16 v[36:39],a[72:75],v[196:199],v[36:39]\n"
"ds_read_b64_tr_b16 a[50:51],v253 offset:31232\n"
"v_mfma_f32_16x16x32_bf16 v[36:39],a[76:79],v[232:235],v[36:39]\n"
"buffer_load_dwordx4 v214,s[72:75],s39 offen offset:1024 lds\n"
"v_mfma_f32_16x16x32_bf16 v[40:43],a[72:75],v[200:203],v[40:43]\n"
"ds_read_b64_tr_b16 a[52:53],v253 offset:37376\n"
"v_mfma_f32_16x16x32_bf16 v[40:43],a[76:79],v[236:239],v[40:43]\n"
"v_mfma_f32_16x16x32_bf16 v[44:47],a[72:75],v[204:207],v[44:47]\n"
"ds_read_b64_tr_b16 a[54:55],v253 offset:43520\n"
"v_mfma_f32_16x16x32_bf16 v[44:47],a[76:79],v[242:245],v[44:47]\n"
"s_waitcnt lgkmcnt(8)\n"
"v_mfma_f32_16x16x32_bf16 v[48:51],a[32:35],v[192:195],v[48:51]\n"
"ds_read_b64_tr_b16 a[56:57],v252 offset:25344\n"
"v_mfma_f32_16x16x32_bf16 v[48:51],a[36:39],v[228:231],v[48:51]\n"
"v_mfma_f32_16x16x32_bf16 v[52:55],a[32:35],v[196:199],v[52:55]\n"
"ds_read_b64_tr_b16 a[58:59],v252 offset:31488\n"
"v_mfma_f32_16x16x32_bf16 v[52:55],a[36:39],v[232:235],v[52:55]\n"
"v_mfma_f32_16x16x32_bf16 v[56:59],a[32:35],v[200:203],v[56:59]\n"
"ds_read_b64_tr_b16 a[60:61],v252 offset:37632\n"
"v_mfma_f32_16x16x32_bf16 v[56:59],a[36:39],v[236:239],v[56:59]\n"
"v_mfma_f32_16x16x32_bf16 v[60:63],a[32:35],v[204:207],v[60:63]\n"
"ds_read_b64_tr_b16 a[62:63],v252 offset:43776\n"
"v_mfma_f32_16x16x32_bf16 v[60:63],a[36:39],v[242:245],v[60:63]\n"
"buffer_load_dwordx4 v215,s[72:75],s39 offen offset:2048 lds\n"
"s_waitcnt lgkmcnt(8)\n"
"v_mfma_f32_16x16x32_bf16 v[64:67],a[40:43],v[192:195],v[64:67]\n"
"ds_read_b64_tr_b16 a[64:65],v253 offset:25344\n"
"v_mfma_f32_16x16x32_bf16 v[64:67],a[44:47],v[228:231],v[64:67]\n"
"v_mfma_f32_16x16x32_bf16 v[68:71],a[40:43],v[196:199],v[68:71]\n"
"ds_read_b64_tr_b16 a[66:67],v253 offset:31488\n"
"v_mfma_f32_16x16x32_bf16 v[68:71],a[44:47],v[232:235],v[68:71]\n"
"v_mfma_f32_16x16x32_bf16 v[72:75],a[40:43],v[200:203],v[72:75]\n"
"ds_read_b64_tr_b16 a[68:69],v253 offset:37632\n"
"v_mfma_f32_16x16x32_bf16 v[72:75],a[44:47],v[236:239],v[72:75]\n"
"v_mfma_f32_16x16x32_bf16 v[76:79],a[40:43],v[204:207],v[76:79]\n"
"ds_read_b64_tr_b16 a[70:71],v253 offset:43776\n"
"v_mfma_f32_16x16x32_bf16 v[76:79],a[44:47],v[242:245],v[76:79]\n"
"v_readlane_b32 s43,v210,55\n"
"s_and_b32 s43,s43,0xffff\n"
"s_add_u32 s43,s43,256\n"
"s_lshl_b32 s43,s43,6\n"
"v_lshl_add_u32 v223,v209,2,s43\n"
"s_mov_b64 s[46:47],0xf\n"
"buffer_load_dwordx2 v[226:227],v223,s[68:71],0 offen sc1\n"
"s_waitcnt lgkmcnt(8)\n"
"v_mfma_f32_16x16x32_bf16 v[80:83],a[48:51],v[192:195],v[80:83]\n"
"ds_read_b64_tr_b16 a[72:73],v252 offset:25600\n"
"v_mfma_f32_16x16x32_bf16 v[80:83],a[52:55],v[228:231],v[80:83]\n"
"v_mfma_f32_16x16x32_bf16 v[84:87],a[48:51],v[196:199],v[84:87]\n"
"s_add_u32 s39,s39,s52\n"
"s_add_u32 m0,s63,27648\n"
"ds_read_b64_tr_b16 a[74:75],v252 offset:31744\n"
"v_mfma_f32_16x16x32_bf16 v[84:87],a[52:55],v[232:235],v[84:87]\n"
"buffer_load_dwordx4 v213,s[72:75],s39 offen lds\n"
"v_mfma_f32_16x16x32_bf16 v[88:91],a[48:51],v[200:203],v[88:91]\n"
"ds_read_b64_tr_b16 a[76:77],v252 offset:37888\n"
"v_mfma_f32_16x16x32_bf16 v[88:91],a[52:55],v[236:239],v[88:91]\n"
"v_mfma_f32_16x16x32_bf16 v[92:95],a[48:51],v[204:207],v[92:95]\n"
"ds_read_b64_tr_b16 a[78:79],v252 offset:44032\n"
"v_mfma_f32_16x16x32_bf16 v[92:95],a[52:55],v[242:245],v[92:95]\n"
"s_waitcnt vmcnt(43) lgkmcnt(8)\n"
"s_barrier\n"
"v_mfma_f32_16x16x32_bf16 v[96:99],a[56:59],v[192:195],v[96:99]\n"
"ds_read_b64_tr_b16 a[32:33],v254\n"
"v_mfma_f32_16x16x32_bf16 v[96:99],a[60:63],v[228:231],v[96:99]\n"
"v_mfma_f32_16x16x32_bf16 v[100:103],a[56:59],v[196:199],v[100:103]\n"
"ds_read_b64_tr_b16 a[34:35],v254 offset:6144\n"
"v_mfma_f32_16x16x32_bf16 v[100:103],a[60:63],v[232:235],v[100:103]\n"
"v_mfma_f32_16x16x32_bf16 v[104:107],a[56:59],v[200:203],v[104:107]\n"
"ds_read_b64_tr_b16 a[36:37],v254 offset:12288\n"
"v_mfma_f32_16x16x32_bf16 v[104:107],a[60:63],v[236:239],v[104:107]\n"
"v_mfma_f32_16x16x32_bf16 v[108:111],a[56:59],v[204:207],v[108:111]\n"
"ds_read_b64_tr_b16 a[38:39],v254 offset:18432\n"
"v_mfma_f32_16x16x32_bf16 v[108:111],a[60:63],v[242:245],v[108:111]\n"
"buffer_load_dwordx4 v214,s[72:75],s39 offen offset:1024 lds\n"
"s_waitcnt lgkmcnt(8)\n"
"v_mfma_f32_16x16x32_bf16 v[112:115],a[64:67],v[192:195],v[112:115]\n"
"ds_read_b64_tr_b16 a[40:41],v255\n"
"v_mfma_f32_16x16x32_bf16 v[112:115],a[68:71],v[228:231],v[112:115]\n"
"v_mfma_f32_16x16x32_bf16 v[116:119],a[64:67],v[196:199],v[116:119]\n"
"ds_read_b64_tr_b16 a[42:43],v255 offset:6144\n"
"v_mfma_f32_16x16x32_bf16 v[116:119],a[68:71],v[232:235],v[116:119]\n"
"v_mfma_f32_16x16x32_bf16 v[120:123],a[64:67],v[200:203],v[120:123]\n"
"ds_read_b64_tr_b16 a[44:45],v255 offset:12288\n"
"v_mfma_f32_16x16x32_bf16 v[120:123],a[68:71],v[236:239],v[120:123]\n"
"v_mfma_f32_16x16x32_bf16 v[124:127],a[64:67],v[204:207],v[124:127]\n"
"ds_read_b64_tr_b16 a[46:47],v255 offset:18432\n"
"v_mfma_f32_16x16x32_bf16 v[124:127],a[68:71],v[242:245],v[124:127]\n"
"s_waitcnt lgkmcnt(8)\n"
"v_mfma_f32_16x16x32_bf16 v[128:131],a[72:75],v[192:195],v[128:131]\n"
"ds_read_b64_tr_b16 a[48:49],v254 offset:256\n"
"v_mfma_f32_16x16x32_bf16 v[128:131],a[76:79],v[228:231],v[128:131]\n"
"v_mfma_f32_16x16x32_bf16 v[132:135],a[72:75],v[196:199],v[132:135]\n"
"ds_read_b64_tr_b16 a[50:51],v254 offset:6400\n"
"v_mfma_f32_16x16x32_bf16 v[132:135],a[76:79],v[232:235],v[132:135]\n"
"buffer_load_dwordx4 v215,s[72:75],s39 offen offset:2048 lds\n"
"v_mfma_f32_16x16x32_bf16 v[136:139],a[72:75],v[200:203],v[136:139]\n"
"ds_read_b64_tr_b16 a[52:53],v254 offset:12544\n"
"v_mfma_f32_16x16x32_bf16 v[136:139],a[76:79],v[236:239],v[136:139]\n"
"v_mfma_f32_16x16x32_bf16 v[140:143],a[72:75],v[204:207],v[140:143]\n"
"ds_read_b64_tr_b16 a[54:55],v254 offset:18688\n"
"v_mfma_f32_16x16x32_bf16 v[140:143],a[76:79],v[242:245],v[140:143]\n"
"s_add_u32 s39,s39,s53\n"
"s_mov_b32 s55,0x40000\n"
"s_waitcnt vmcnt(3)\n"
"LM_POLL19:\n"
"v_cmp_ne_u64 vcc,v[226:227],v[224:225]\n"
"s_nop 1\n"
"s_and_b64 s[44:45],vcc,s[46:47]\n"
"s_cbranch_scc0 LM_POK20\n"
"s_sleep 1\n"
"buffer_load_dwordx2 v[226:227],v223,s[68:71],0 offen sc1\n"
"s_waitcnt vmcnt(0)\n"
"s_sub_u32 s55,s55,1\n"
"s_cbranch_scc0 LM_POLL19\n"
"LM_POK20:\n"
"v_readlane_b32 s40,v248,22\n"
"s_nop 4\n"
"buffer_load_dwordx4 a[80:83],v209,s[88:91],s40 offen sc1\n"
"buffer_load_dwordx4 a[84:87],v209,s[88:91],s40 offen offset:1024 sc1\n"
"buffer_load_dwordx4 a[88:91],v209,s[88:91],s40 offen offset:2048 sc1\n"
"buffer_load_dwordx4 a[92:95],v209,s[88:91],s40 offen offset:3072 sc1\n"
"s_add_u32 s40,s40,4096\n"
"buffer_load_dwordx4 a[96:99],v209,s[88:91],s40 offen sc1\n"
"buffer_load_dwordx4 a[100:103],v209,s[88:91],s40 offen offset:1024 sc1\n"
"buffer_load_dwordx4 a[104:107],v209,s[88:91],s40 offen offset:2048 sc1\n"
"buffer_load_dwordx4 a[108:111],v209,s[88:91],s40 offen offset:3072 sc1\n"
"s_add_u32 s40,s40,4096\n"
"buffer_load_dwordx4 a[128:131],v209,s[88:91],s40 offen sc1\n"
"buffer_load_dwordx4 a[132:135],v209,s[88:91],s40 offen offset:1024 sc1\n"
"buffer_load_dwordx4 a[136:139],v209,s[88:91],s40 offen offset:2048 sc1\n"
"buffer_load_dwordx4 a[140:143],v209,s[88:91],s40 offen offset:3072 sc1\n"
"s_add_u32 s40,s40,4096\n"
"buffer_load_dwordx4 a[144:147],v209,s[88:91],s40 offen sc1\n"
"buffer_load_dwordx4 a[148:151],v209,s[88:91],s40 offen offset:1024 sc1\n"
"buffer_load_dwordx4 a[152:155],v209,s[88:91],s40 offen offset:2048 sc1\n"
"buffer_load_dwordx4 a[156:159],v209,s[88:91],s40 offen offset:3072 sc1\n"
"s_add_u32 s40,s40,4096\n"
"s_waitcnt vmcnt(8)\n"
"s_waitcnt lgkmcnt(8)\n"
"v_mfma_f32_16x16x32_bf16 v[0:3],a[32:35],a[80:83],v[0:3]\n"
"v_readlane_b32 s40,v248,23\n"
"s_nop 4\n"
"ds_read_b64_tr_b16 a[56:57],v255 offset:256\n"
"v_mfma_f32_16x16x32_bf16 v[0:3],a[36:39],a[96:99],v[0:3]\n"
"buffer_load_dwordx4 a[192:195],v209,s[88:91],s40 offen sc1\n"
"v_mfma_f32_16x16x32_bf16 v[4:7],a[32:35],a[84:87],v[4:7]\n"
"ds_read_b64_tr_b16 a[58:59],v255 offset:6400\n"
"v_mfma_f32_16x16x32_bf16 v[4:7],a[36:39],a[100:103],v[4:7]\n"
"v_mfma_f32_16x16x32_bf16 v[8:11],a[32:35],a[88:91],v[8:11]\n"
"ds_read_b64_tr_b16 a[60:61],v255 offset:12544\n"
"v_mfma_f32_16x16x32_bf16 v[8:11],a[36:39],a[104:107],v[8:11]\n"
"buffer_load_dwordx4 a[196:199],v209,s[88:91],s40 offen offset:1024 sc1\n"
"v_mfma_f32_16x16x32_bf16 v[12:15],a[32:35],a[92:95],v[12:15]\n"
"ds_read_b64_tr_b16 a[62:63],v255 offset:18688\n"
"v_mfma_f32_16x16x32_bf16 v[12:15],a[36:39],a[108:111],v[12:15]\n"
"s_waitcnt lgkmcnt(8)\n"
"v_mfma_f32_16x16x32_bf16 v[16:19],a[40:43],a[80:83],v[16:19]\n"
"ds_read_b64_tr_b16 a[64:65],v254 offset:512\n"
"v_mfma_f32_16x16x32_bf16 v[16:19],a[44:47],a[96:99],v[16:19]\n"
"buffer_load_dwordx4 a[200:203],v209,s[88:91],s40 offen offset:2048 sc1\n"
"v_mfma_f32_16x16x32_bf16 v[20:23],a[40:43],a[84:87],v[20:23]\n"
"ds_read_b64_tr_b16 a[66:67],v254 offset:6656\n"
"v_mfma_f32_16x16x32_bf16 v[20:23],a[44:47],a[100:103],v[20:23]\n"
"buffer_load_dwordx4 a[204:207],v209,s[88:91],s40 offen offset:3072 sc1\n"
"v_mfma_f32_16x16x32_bf16 v[24:27],a[40:43],a[88:91],v[24:27]\n"
"ds_read_b64_tr_b16 a[68:69],v254 offset:12800\n"
"v_mfma_f32_16x16x32_bf16 v[24:27],a[44:47],a[104:107],v[24:27]\n"
"v_mfma_f32_16x16x32_bf16 v[28:31],a[40:43],a[92:95],v[28:31]\n"
"s_add_u32 s40,s40,4096\n"
"ds_read_b64_tr_b16 a[70:71],v254 offset:18944\n"
"v_mfma_f32_16x16x32_bf16 v[28:31],a[44:47],a[108:111],v[28:31]\n"
"buffer_load_dwordx4 a[240:243],v209,s[88:91],s40 offen sc1\n"
"s_waitcnt lgkmcnt(8)\n"
"v_mfma_f32_16x16x32_bf16 v[32:35],a[48:51],a[80:83],v[32:35]\n"
"ds_read_b64_tr_b16 a[72:73],v255 offset:512\n"
"v_mfma_f32_16x16x32_bf16 v[32:35],a[52:55],a[96:99],v[32:35]\n"
"v_mfma_f32_16x16x32_bf16 v[36:39],a[48:51],a[84:87],v[36:39]\n"
"ds_read_b64_tr_b16 a[74:75],v255 offset:6656\n"
"v_mfma_f32_16x16x32_bf16 v[36:39],a[52:55],a[100:103],v[36:39]\n"
"buffer_load_dwordx4 a[244:247],v209,s[88:91],s40 offen offset:1024 sc1\n"
"v_mfma_f32_16x16x32_bf16 v[40:43],a[48:51],a[88:91],v[40:43]\n"
"ds_read_b64_tr_b16 a[76:77],v255 offset:12800\n"
"v_mfma_f32_16x16x32_bf16 v[40:43],a[52:55],a[104:107],v[40:43]\n"
"buffer_load_dwordx4 a[248:251],v209,s[88:91],s40 offen offset:2048 sc1\n"
"v_mfma_f32_16x16x32_bf16 v[44:47],a[48:51],a[92:95],v[44:47]\n"
"ds_read_b64_tr_b16 a[78:79],v255 offset:18944\n"
"v_mfma_f32_16x16x32_bf16 v[44:47],a[52:55],a[108:111],v[44:47]\n"
"s_waitcnt lgkmcnt(8)\n"
"v_mfma_f32_16x16x32_bf16 v[48:51],a[56:59],a[80:83],v[48:51]\n"
"ds_read_b64_tr_b16 a[32:33],v254 offset:768\n"
"v_mfma_f32_16x16x32_bf16 v[48:51],a[60:63],a[96:99],v[48:51]\n"
"buffer_load_dwordx4 a[252:255],v209,s[88:91],s40 offen offset:3072 sc1\n"
"v_mfma_f32_16x16x32_bf16 v[52:55],a[56:59],a[84:87],v[52:55]\n"
"s_add_u32 s40,s40,4096\n"
"ds_read_b64_tr_b16 a[34:35],v254 offset:6912\n"
"v_mfma_f32_16x16x32_bf16 v[52:55],a[60:63],a[100:103],v[52:55]\n"
"buffer_load_dwordx4 a[208:211],v209,s[88:91],s40 offen sc1\n"
"v_mfma_f32_16x16x32_bf16 v[56:59],a[56:59],a[88:91],v[56:59]\n"
"ds_read_b64_tr_b16 a[36:37],v254 offset:13056\n"
"v_mfma_f32_16x16x32_bf16 v[56:59],a[60:63],a[104:107],v[56:59]\n"
"v_mfma_f32_16x16x32_bf16 v[60:63],a[56:59],a[92:95],v[60:63]\n"
"ds_read_b64_tr_b16 a[38:39],v254 offset:19200\n"
"v_mfma_f32_16x16x32_bf16 v[60:63],a[60:63],a[108:111],v[60:63]\n"
"buffer_load_dwordx4 a[212:215],v209,s[88:91],s40 offen offset:1024 sc1\n"
"s_waitcnt lgkmcnt(8)\n"
"v_mfma_f32_16x16x32_bf16 v[64:67],a[64:67],a[80:83],v[64:67]\n"
"ds_read_b64_tr_b16 a[40:41],v255 offset:768\n"
"v_mfma_f32_16x16x32_bf16 v[64:67],a[68:71],a[96:99],v[64:67]\n"
"v_mfma_f32_16x16x32_bf16 v[68:71],a[64:67],a[84:87],v[68:71]\n"
"ds_read_b64_tr_b16 a[42:43],v255 offset:6912\n"
"v_mfma_f32_16x16x32_bf16 v[68:71],a[68:71],a[100:103],v[68:71]\n"
"buffer_load_dwordx4 a[216:219],v209,s[88:91],s40 offen offset:2048 sc1\n"
"v_mfma_f32_16x16x32_bf16 v[72:75],a[64:67],a[88:91],v[72:75]\n"
"ds_read_b64_tr_b16 a[44:45],v255 offset:13056\n"
"v_mfma_f32_16x16x32_bf16 v[72:75],a[68:71],a[104:107],v[72:75]\n"
"buffer_load_dwordx4 a[220:223],v209,s[88:91],s40 offen offset:3072 sc1\n"
"v_mfma_f32_16x16x32_bf16 v[76:79],a[64:67],a[92:95],v[76:79]\n"
"ds_read_b64_tr_b16 a[46:47],v255 offset:19200\n"
"v_mfma_f32_16x16x32_bf16 v[76:79],a[68:71],a[108:111],v[76:79]\n"
"s_waitcnt lgkmcnt(8)\n"
"v_mfma_f32_16x16x32_bf16 v[80:83],a[72:75],a[80:83],v[80:83]\n"
"s_add_u32 s40,s40,4096\n"
"ds_read_b64_tr_b16 a[48:49],v254 offset:1024\n"
"v_mfma_f32_16x16x32_bf16 v[80:83],a[76:79],a[96:99],v[80:83]\n"
"buffer_load_dwordx4 a[224:227],v209,s[88:91],s40 offen sc1\n"
"v_mfma_f32_16x16x32_bf16 v[84:87],a[72:75],a[84:87],v[84:87]\n"
"ds_read_b64_tr_b16 a[50:51],v254 offset:7168\n"
"v_mfma_f32_16x16x32_bf16 v[84:87],a[76:79],a[100:103],v[84:87]\n"
"v_mfma_f32_16x16x32_bf16 v[88:91],a[72:75],a[88:91],v[88:91]\n"
"ds_read_b64_tr_b16 a[52:53],v254 offset:13312\n"
"v_mfma_f32_16x16x32_bf16 v[88:91],a[76:79],a[104:107],v[88:91]\n"
"buffer_load_dwordx4 a[228:231],v209,s[88:91],s40 offen offset:1024 sc1\n"
"v_mfma_f32_16x16x32_bf16 v[92:95],a[72:75],a[92:95],v[92:95]\n"
"ds_read_b64_tr_b16 a[54:55],v254 offset:19456\n"
"v_mfma_f32_16x16x32_bf16 v[92:95],a[76:79],a[108:111],v[92:95]\n"
"buffer_load_dwordx4 a[232:235],v209,s[88:91],s40 offen offset:2048 sc1\n"
"s_waitcnt vmcnt(15) lgkmcnt(8)\n"
"s_barrier\n"
"v_mfma_f32_16x16x32_bf16 v[96:99],a[32:35],a[80:83],v[96:99]\n"
"ds_read_b64_tr_b16 a[56:57],v254 offset:24576\n"
"v_mfma_f32_16x16x32_bf16 v[96:99],a[36:39],a[96:99],v[96:99]\n"
"v_mfma_f32_16x16x32_bf16 v[100:103],a[32:35],a[84:87],v[100:103]\n"
"ds_read_b64_tr_b16 a[58:59],v254 offset:30720\n"
"v_mfma_f32_16x16x32_bf16 v[100:103],a[36:39],a[100:103],v[100:103]\n"
"buffer_load_dwordx4 a[236:239],v209,s[88:91],s40 offen offset:3072 sc1\n"
"v_mfma_f32_16x16x32_bf16 v[104:107],a[32:35],a[88:91],v[104:107]\n"
"ds_read_b64_tr_b16 a[60:61],v254 offset:36864\n"
"v_mfma_f32_16x16x32_bf16 v[104:107],a[36:39],a[104:107],v[104:107]\n"
"v_mfma_f32_16x16x32_bf16 v[108:111],a[32:35],a[92:95],v[108:111]\n"
"s_add_u32 s40,s40,4096\n"
"v_readlane_b32 s39,v248,19\n"
"s_nop 4\n"
"s_add_u32 m0,s50,0\n"
"ds_read_b64_tr_b16 a[62:63],v254 offset:43008\n"
"v_mfma_f32_16x16x32_bf16 v[108:111],a[36:39],a[108:111],v[108:111]\n"
"buffer_load_dwordx4 v213,s[72:75],s39 offen lds\n"
"s_waitcnt lgkmcnt(8)\n"
"v_mfma_f32_16x16x32_bf16 v[112:115],a[40:43],a[80:83],v[112:115]\n"
"ds_read_b64_tr_b16 a[64:65],v255 offset:24576\n"
"v_mfma_f32_16x16x32_bf16 v[112:115],a[44:47],a[96:99],v[112:115]\n"
"buffer_load_dwordx4 v214,s[72:75],s39 offen offset:1024 lds\n"
"v_mfma_f32_16x16x32_bf16 v[116:119],a[40:43],a[84:87],v[116:119]\n"
"ds_read_b64_tr_b16 a[66:67],v255 offset:30720\n"
"v_mfma_f32_16x16x32_bf16 v[116:119],a[44:47],a[100:103],v[116:119]\n"
"v_mfma_f32_16x16x32_bf16 v[120:123],a[40:43],a[88:91],v[120:123]\n"
"ds_read_b64_tr_b16 a[68:69],v255 offset:36864\n"
"v_mfma_f32_16x16x32_bf16 v[120:123],a[44:47],a[104:107],v[120:123]\n"
"buffer_load_dwordx4 v215,s[72:75],s39 offen offset:2048 lds\n"
"v_mfma_f32_16x16x32_bf16 v[124:127],a[40:43],a[92:95],v[124:127]\n"
"s_add_u32 s39,s39,s52\n"
"s_add_u32 m0,s50,3072\n"
"ds_read_b64_tr_b16 a[70:71],v255 offset:43008\n"
"v_mfma_f32_16x16x32_bf16 v[124:127],a[44:47],a[108:111],v[124:127]\n"
"buffer_load_dwordx4 v213,s[72:75],s39 offen lds\n"
"s_waitcnt lgkmcnt(8)\n"
"v_mfma_f32_16x16x32_bf16 v[128:131],a[48:51],a[80:83],v[128:131]\n"
"ds_read_b64_tr_b16 a[72:73],v254 offset:24832\n"
"v_mfma_f32_16x16x32_bf16 v[128:131],a[52:55],a[96:99],v[128:131]\n"
"v_mfma_f32_16x16x32_bf16 v[132:135],a[48:51],a[84:87],v[132:135]\n"
"ds_read_b64_tr_b16 a[74:75],v254 offset:30976\n"
"v_mfma_f32_16x16x32_bf16 v[132:135],a[52:55],a[100:103],v[132:135]\n"
"buffer_load_dwordx4 v214,s[72:75],s39 offen offset:1024 lds\n"
"v_mfma_f32_16x16x32_bf16 v[136:139],a[48:51],a[88:91],v[136:139]\n"
"ds_read_b64_tr_b16 a[76:77],v254 offset:37120\n"
"v_mfma_f32_16x16x32_bf16 v[136:139],a[52:55],a[104:107],v[136:139]\n"
"v_mfma_f32_16x16x32_bf16 v[140:143],a[48:51],a[92:95],v[140:143]\n"
"ds_read_b64_tr_b16 a[78:79],v254 offset:43264\n"
"v_mfma_f32_16x16x32_bf16 v[140:143],a[52:55],a[108:111],v[140:143]\n"
"buffer_load_dwordx4 v215,s[72:75],s39 offen offset:2048 lds\n"
"s_add_u32 s39,s39,s53\n"
"s_waitcnt lgkmcnt(8)\n"
"v_mfma_f32_16x16x32_bf16 v[0:3],a[56:59],a[128:131],v[0:3]\n"
"v_readlane_b32 s40,v248,24\n"
"s_nop 4\n"
"ds_read_b64_tr_b16 a[32:33],v255 offset:24832\n"
"v_mfma_f32_16x16x32_bf16 v[0:3],a[60:63],a[144:147],v[0:3]\n"
"buffer_load_dwordx4 a[160:163],v209,s[88:91],s40 offen sc1\n"
"v_mfma_f32_16x16x32_bf16 v[4:7],a[56:59],a[132:135],v[4:7]\n"
"ds_read_b64_tr_b16 a[34:35],v255 offset:30976\n"
"v_mfma_f32_16x16x32_bf16 v[4:7],a[60:63],a[148:151],v[4:7]\n"
"v_mfma_f32_16x16x32_bf16 v[8:11],a[56:59],a[136:139],v[8:11]\n"
"ds_read_b64_tr_b16 a[36:37],v255 offset:37120\n"
"v_mfma_f32_16x16x32_bf16 v[8:11],a[60:63],a[152:155],v[8:11]\n"
"buffer_load_dwordx4 a[164:167],v209,s[88:91],s40 offen offset:1024 sc1\n"
"v_mfma_f32_16x16x32_bf16 v[12:15],a[56:59],a[140:143],v[12:15]\n"
"ds_read_b64_tr_b16 a[38:39],v255 offset:43264\n"
"v_mfma_f32_16x16x32_bf16 v[12:15],a[60:63],a[156:159],v[12:15]\n"
"s_waitcnt lgkmcnt(8)\n"
"v_mfma_f32_16x16x32_bf16 v[16:19],a[64:67],a[128:131],v[16:19]\n"
"ds_read_b64_tr_b16 a[40:41],v254 offset:25088\n"
"v_mfma_f32_16x16x32_bf16 v[16:19],a[68:71],a[144:147],v[16:19]\n"
"buffer_load_dwordx4 a[168:171],v209,s[88:91],s40 offen offset:2048 sc1\n"
"v_mfma_f32_16x16x32_bf16 v[20:23],a[64:67],a[132:135],v[20:23]\n"
"ds_read_b64_tr_b16 a[42:43],v254 offset:31232\n"
"v_mfma_f32_16x16x32_bf16 v[20:23],a[68:71],a[148:151],v[20:23]\n"
"buffer_load_dwordx4 a[172:175],v209,s[88:91],s40 offen offset:3072 sc1\n"
"v_mfma_f32_16x16x32_bf16 v[24:27],a[64:67],a[136:139],v[24:27]\n"
"ds_read_b64_tr_b16 a[44:45],v254 offset:37376\n"
"v_mfma_f32_16x16x32_bf16 v[24:27],a[68:71],a[152:155],v[24:27]\n"
"v_mfma_f32_16x16x32_bf16 v[28:31],a[64:67],a[140:143],v[28:31]\n"
"s_add_u32 s40,s40,4096\n"
"ds_read_b64_tr_b16 a[46:47],v254 offset:43520\n"
"v_mfma_f32_16x16x32_bf16 v[28:31],a[68:71],a[156:159],v[28:31]\n"
"buffer_load_dwordx4 a[176:179],v209,s[88:91],s40 offen sc1\n"
"s_waitcnt lgkmcnt(8)\n"
"v_mfma_f32_16x16x32_bf16 v[32:35],a[72:75],a[128:131],v[32:35]\n"
"ds_read_b64_tr_b16 a[48:49],v255 offset:25088\n"
"v_mfma_f32_16x16x32_bf16 v[32:35],a[76:79],a[144:147],v[32:35]\n"
"v_mfma_f32_16x16x32_bf16 v[36:39],a[72:75],a[132:135],v[36:39]\n"
"ds_read_b64_tr_b16 a[50:51],v255 offset:31232\n"
"v_mfma_f32_16x16x32_bf16 v[36:39],a[76:79],a[148:151],v[36:39]\n"
"buffer_load_dwordx4 a[180:183],v209,s[88:91],s40 offen offset:1024 sc1\n"
"v_mfma_f32_16x16x32_bf16 v[40:43],a[72:75],a[136:139],v[40:43]\n"
"ds_read_b64_tr_b16 a[52:53],v255 offset:37376\n"
"v_mfma_f32_16x16x32_bf16 v[40:43],a[76:79],a[152:155],v[40:43]\n"
"buffer_load_dwordx4 a[184:187],v209,s[88:91],s40 offen offset:2048 sc1\n"
"v_mfma_f32_16x16x32_bf16 v[44:47],a[72:75],a[140:143],v[44:47]\n"
"ds_read_b64_tr_b16 a[54:55],v255 offset:43520\n"
"v_mfma_f32_16x16x32_bf16 v[44:47],a[76:79],a[156:159],v[44:47]\n"
"s_waitcnt lgkmcnt(8)\n"
"v_mfma_f32_16x16x32_bf16 v[48:51],a[32:35],a[128:131],v[48:51]\n"
"ds_read_b64_tr_b16 a[56:57],v254 offset:25344\n"
"v_mfma_f32_16x16x32_bf16 v[48:51],a[36:39],a[144:147],v[48:51]\n"
"buffer_load_dwordx4 a[188:191],v209,s[88:91],s40 offen offset:3072 sc1\n"
"v_mfma_f32_16x16x32_bf16 v[52:55],a[32:35],a[132:135],v[52:55]\n"
"s_add_u32 s40,s40,4096\n"
"s_add_u32 m0,s50,24576\n"
"ds_read_b64_tr_b16 a[58:59],v254 offset:31488\n"
"v_mfma_f32_16x16x32_bf16 v[52:55],a[36:39],a[148:151],v[52:55]\n"
"buffer_load_dwordx4 v213,s[72:75],s39 offen lds\n"
"v_mfma_f32_16x16x32_bf16 v[56:59],a[32:35],a[136:139],v[56:59]\n"
"ds_read_b64_tr_b16 a[60:61],v254 offset:37632\n"
"v_mfma_f32_16x16x32_bf16 v[56:59],a[36:39],a[152:155],v[56:59]\n"
"v_mfma_f32_16x16x32_bf16 v[60:63],a[32:35],a[140:143],v[60:63]\n"
"ds_read_b64_tr_b16 a[62:63],v254 offset:43776\n"
"v_mfma_f32_16x16x32_bf16 v[60:63],a[36:39],a[156:159],v[60:63]\n"
"buffer_load_dwordx4 v214,s[72:75],s39 offen offset:1024 lds\n"
"s_waitcnt lgkmcnt(8)\n"
"v_mfma_f32_16x16x32_bf16 v[64:67],a[40:43],a[128:131],v[64:67]\n"
"ds_read_b64_tr_b16 a[64:65],v255 offset:25344\n"
"v_mfma_f32_16x16x32_bf16 v[64:67],a[44:47],a[144:147],v[64:67]\n"
"v_mfma_f32_16x16x32_bf16 v[68:71],a[40:43],a[132:135],v[68:71]\n"
"ds_read_b64_tr_b16 a[66:67],v255 offset:31488\n"
"v_mfma_f32_16x16x32_bf16 v[68:71],a[44:47],a[148:151],v[68:71]\n"
"buffer_load_dwordx4 v215,s[72:75],s39 offen offset:2048 lds\n"
"v_mfma_f32_16x16x32_bf16 v[72:75],a[40:43],a[136:139],v[72:75]\n"
"s_add_u32 s39,s39,s52\n"
"s_add_u32 m0,s50,27648\n"
"ds_read_b64_tr_b16 a[68:69],v255 offset:37632\n"
"v_mfma_f32_16x16x32_bf16 v[72:75],a[44:47],a[152:155],v[72:75]\n"
"buffer_load_dwordx4 v213,s[72:75],s39 offen lds\n"
"v_mfma_f32_16x16x32_bf16 v[76:79],a[40:43],a[140:143],v[76:79]\n"
"ds_read_b64_tr_b16 a[70:71],v255 offset:43776\n"
"v_mfma_f32_16x16x32_bf16 v[76:79],a[44:47],a[156:159],v[76:79]\n"
"s_waitcnt lgkmcnt(8)\n"
"v_mfma_f32_16x16x32_bf16 v[80:83],a[48:51],a[128:131],v[80:83]\n"
"ds_read_b64_tr_b16 a[72:73],v254 offset:25600\n"
"v_mfma_f32_16x16x32_bf16 v[80:83],a[52:55],a[144:147],v[80:83]\n"
"buffer_load_dwordx4 v214,s[72:75],s39 offen offset:1024 lds\n"
"v_mfma_f32_16x16x32_bf16 v[84:87],a[48:51],a[132:135],v[84:87]\n"
"ds_read_b64_tr_b16 a[74:75],v254 offset:31744\n"
"v_mfma_f32_16x16x32_bf16 v[84:87],a[52:55],a[148:151],v[84:87]\n"
"v_mfma_f32_16x16x32_bf16 v[88:91],a[48:51],a[136:139],v[88:91]\n"
"ds_read_b64_tr_b16 a[76:77],v254 offset:37888\n"
"v_mfma_f32_16x16x32_bf16 v[88:91],a[52:55],a[152:155],v[88:91]\n"
"buffer_load_dwordx4 v215,s[72:75],s39 offen offset:2048 lds\n"
"v_mfma_f32_16x16x32_bf16 v[92:95],a[48:51],a[140:143],v[92:95]\n"
"s_add_u32 s39,s39,s53\n"
"ds_read_b64_tr_b16 a[78:79],v254 offset:44032\n"
"v_mfma_f32_16x16x32_bf16 v[92:95],a[52:55],a[156:159],v[92:95]\n"
"buffer_load_dwordx4 a[0:3],v209,s[88:91],s40 offen sc1\n"
"s_waitcnt vmcnt(29) lgkmcnt(8)\n"
"s_barrier\n"
"v_mfma_f32_16x16x32_bf16 v[96:99],a[56:59],a[128:131],v[96:99]\n"
"ds_read_b64_tr_b16 a[32:33],v240\n"
"v_mfma_f32_16x16x32_bf16 v[96:99],a[60:63],a[144:147],v[96:99]\n"
"v_mfma_f32_16x16x32_bf16 v[100:103],a[56:59],a[132:135],v[100:103]\n"
"ds_read_b64_tr_b16 a[34:35],v240 offset:6144\n"
"v_mfma_f32_16x16x32_bf16 v[100:103],a[60:63],a[148:151],v[100:103]\n"
"buffer_load_dwordx4 a[4:7],v209,s[88:91],s40 offen offset:1024 sc1\n"
"v_mfma_f32_16x16x32_bf16 v[104:107],a[56:59],a[136:139],v[104:107]\n"
"ds_read_b64_tr_b16 a[36:37],v240 offset:12288\n"
"v_mfma_f32_16x16x32_bf16 v[104:107],a[60:63],a[152:155],v[104:107]\n"
"v_mfma_f32_16x16x32_bf16 v[108:111],a[56:59],a[140:143],v[108:111]\n"
"ds_read_b64_tr_b16 a[38:39],v240 offset:18432\n"
"v_mfma_f32_16x16x32_bf16 v[108:111],a[60:63],a[156:159],v[108:111]\n"
"buffer_load_dwordx4 a[8:11],v209,s[88:91],s40 offen offset:2048 sc1\n"
"s_waitcnt lgkmcnt(8)\n"
"v_mfma_f32_16x16x32_bf16 v[112:115],a[64:67],a[128:131],v[112:115]\n"
"ds_read_b64_tr_b16 a[40:41],v241\n"
"v_mfma_f32_16x16x32_bf16 v[112:115],a[68:71],a[144:147],v[112:115]\n"
"buffer_load_dwordx4 a[12:15],v209,s[88:91],s40 offen offset:3072 sc1\n"
"v_mfma_f32_16x16x32_bf16 v[116:119],a[64:67],a[132:135],v[116:119]\n"
"ds_read_b64_tr_b16 a[42:43],v241 offset:6144\n"
"v_mfma_f32_16x16x32_bf16 v[116:119],a[68:71],a[148:151],v[116:119]\n"
"v_mfma_f32_16x16x32_bf16 v[120:123],a[64:67],a[136:139],v[120:123]\n"
"s_add_u32 s40,s40,4096\n"
"ds_read_b64_tr_b16 a[44:45],v241 offset:12288\n"
"v_mfma_f32_16x16x32_bf16 v[120:123],a[68:71],a[152:155],v[120:123]\n"
"buffer_load_dwordx4 a[16:19],v209,s[88:91],s40 offen sc1\n"
"v_mfma_f32_16x16x32_bf16 v[124:127],a[64:67],a[140:143],v[124:127]\n"
"ds_read_b64_tr_b16 a[46:47],v241 offset:18432\n"
"v_mfma_f32_16x16x32_bf16 v[124:127],a[68:71],a[156:159],v[124:127]\n"
"buffer_load_dwordx4 a[20:23],v209,s[88:91],s40 offen offset:1024 sc1\n"
"s_waitcnt lgkmcnt(8)\n"
"v_mfma_f32_16x16x32_bf16 v[128:131],a[72:75],a[128:131],v[128:131]\n"
"ds_read_b64_tr_b16 a[48:49],v240 offset:256\n"
"v_mfma_f32_16x16x32_bf16 v[128:131],a[76:79],a[144:147],v[128:131]\n"
"v_mfma_f32_16x16x32_bf16 v[132:135],a[72:75],a[132:135],v[132:135]\n"
"ds_read_b64_tr_b16 a[50:51],v240 offset:6400\n"
"v_mfma_f32_16x16x32_bf16 v[132:135],a[76:79],a[148:151],v[132:135]\n"
"buffer_load_dwordx4 a[24:27],v209,s[88:91],s40 offen offset:2048 sc1\n"
"v_mfma_f32_16x16x32_bf16 v[136:139],a[72:75],a[136:139],v[136:139]\n"
"ds_read_b64_tr_b16 a[52:53],v240 offset:12544\n"
"v_mfma_f32_16x16x32_bf16 v[136:139],a[76:79],a[152:155],v[136:139]\n"
"v_mfma_f32_16x16x32_bf16 v[140:143],a[72:75],a[140:143],v[140:143]\n"
"ds_read_b64_tr_b16 a[54:55],v240 offset:18688\n"
"v_mfma_f32_16x16x32_bf16 v[140:143],a[76:79],a[156:159],v[140:143]\n"
"buffer_load_dwordx4 a[28:31],v209,s[88:91],s40 offen offset:3072 sc1\n"
"s_add_u32 s40,s40,4096\n"
"LM_5M:\n"
"s_waitcnt lgkmcnt(8)\n"
"v_mfma_f32_16x16x32_bf16 v[0:3],a[32:35],a[192:195],v[0:3]\n"
"v_readlane_b32 s39,v248,20\n"
"s_nop 4\n"
"s_add_u32 m0,s62,0\n"
"ds_read_b64_tr_b16 a[56:57],v241 offset:256\n"
"v_mfma_f32_16x16x32_bf16 v[0:3],a[36:39],a[240:243],v[0:3]\n"
"buffer_load_dwordx4 v213,s[72:75],s39 offen lds\n"
"v_mfma_f32_16x16x32_bf16 v[4:7],a[32:35],a[196:199],v[4:7]\n"
"ds_read_b64_tr_b16 a[58:59],v241 offset:6400\n"
"v_mfma_f32_16x16x32_bf16 v[4:7],a[36:39],a[244:247],v[4:7]\n"
"v_mfma_f32_16x16x32_bf16 v[8:11],a[32:35],a[200:203],v[8:11]\n"
"ds_read_b64_tr_b16 a[60:61],v241 offset:12544\n"
"v_mfma_f32_16x16x32_bf16 v[8:11],a[36:39],a[248:251],v[8:11]\n"
"buffer_load_dwordx4 v214,s[72:75],s39 offen offset:1024 lds\n"
"v_mfma_f32_16x16x32_bf16 v[12:15],a[32:35],a[204:207],v[12:15]\n"
"ds_read_b64_tr_b16 a[62:63],v241 offset:18688\n"
"v_mfma_f32_16x16x32_bf16 v[12:15],a[36:39],a[252:255],v[12:15]\n"
"s_waitcnt lgkmcnt(8)\n"
"v_mfma_f32_16x16x32_bf16 v[16:19],a[40:43],a[192:195],v[16:19]\n"
"ds_read_b64_tr_b16 a[64:65],v240 offset:512\n"
"v_mfma_f32_16x16x32_bf16 v[16:19],a[44:47],a[240:243],v[16:19]\n"
"buffer_load_dwordx4 v215,s[72:75],s39 offen offset:2048 lds\n"
"v_mfma_f32_16x16x32_bf16 v[20:23],a[40:43],a[196:199],v[20:23]\n"
"s_add_u32 s39,s39,s52\n"
"s_add_u32 m0,s62,3072\n"
"ds_read_b64_tr_b16 a[66:67],v240 offset:6656\n"
"v_mfma_f32_16x16x32_bf16 v[20:23],a[44:47],a[244:247],v[20:23]\n"
"buffer_load_dwordx4 v213,s[72:75],s39 offen lds\n"
"v_mfma_f32_16x16x32_bf16 v[24:27],a[40:43],a[200:203],v[24:27]\n"
"ds_read_b64_tr_b16 a[68:69],v240 offset:12800\n"
"v_mfma_f32_16x16x32_bf16 v[24:27],a[44:47],a[248:251],v[24:27]\n"
"v_mfma_f32_16x16x32_bf16 v[28:31],a[40:43],a[204:207],v[28:31]\n"
"ds_read_b64_tr_b16 a[70:71],v240 offset:18944\n"
"v_mfma_f32_16x16x32_bf16 v[28:31],a[44:47],a[252:255],v[28:31]\n"
"buffer_load_dwordx4 v214,s[72:75],s39 offen offset:1024 lds\n"
"s_waitcnt lgkmcnt(8)\n"
"v_mfma_f32_16x16x32_bf16 v[32:35],a[48:51],a[192:195],v[32:35]\n"
"ds_read_b64_tr_b16 a[72:73],v241 offset:512\n"
"v_mfma_f32_16x16x32_bf16 v[32:35],a[52:55],a[240:243],v[32:35]\n"
"v_mfma_f32_16x16x32_bf16 v[36:39],a[48:51],a[196:199],v[36:39]\n"
"ds_read_b64_tr_b16 a[74:75],v241 offset:6656\n"
"v_mfma_f32_16x16x32_bf16 v[36:39],a[52:55],a[244:247],v[36:39]\n"
"buffer_load_dwordx4 v215,s[72:75],s39 offen offset:2048 lds\n"
"v_mfma_f32_16x16x32_bf16 v[40:43],a[48:51],a[200:203],v[40:43]\n"
"s_add_u32 s39,s39,s53\n"
"v_readlane_b32 s40,v248,25\n"
"s_nop 4\n"
"ds_read_b64_tr_b16 a[76:77],v241 offset:12800\n"
"v_mfma_f32_16x16x32_bf16 v[40:43],a[52:55],a[248:251],v[40:43]\n"
"buffer_load_dwordx2 a[80:81],v188,s[88:91],s40 offen sc1\n"
"v_mfma_f32_16x16x32_bf16 v[44:47],a[48:51],a[204:207],v[44:47]\n"
"ds_read_b64_tr_b16 a[78:79],v241 offset:18944\n"
"v_mfma_f32_16x16x32_bf16 v[44:47],a[52:55],a[252:255],v[44:47]\n"
"v_readlane_b32 s43,v210,55\n"
"s_and_b32 s43,s43,0xffff\n"
"s_add_u32 s43,s43,256\n"
"s_lshl_b32 s43,s43,6\n"
"v_lshl_add_u32 v223,v209,2,s43\n"
"v_readlane_b32 s46,v210,29\n"
"s_mov_b32 s47,0\n"
"buffer_load_dwordx2 v[226:227],v223,s[68:71],0 offen sc1\n"
"s_waitcnt lgkmcnt(8)\n"
"v_mfma_f32_16x16x32_bf16 v[48:51],a[56:59],a[192:195],v[48:51]\n"
"ds_read_b64_tr_b16 a[32:33],v240 offset:768\n"
"v_mfma_f32_16x16x32_bf16 v[48:51],a[60:63],a[240:243],v[48:51]\n"
"buffer_load_dwordx2 a[82:83],v189,s[88:91],s40 offen sc1\n"
"v_mfma_f32_16x16x32_bf16 v[52:55],a[56:59],a[196:199],v[52:55]\n"
"ds_read_b64_tr_b16 a[34:35],v240 offset:6912\n"
"v_mfma_f32_16x16x32_bf16 v[52:55],a[60:63],a[244:247],v[52:55]\n"
"buffer_load_dwordx2 a[84:85],v188,s[88:91],s40 offen offset:1024 sc1\n"
"v_mfma_f32_16x16x32_bf16 v[56:59],a[56:59],a[200:203],v[56:59]\n"
"ds_read_b64_tr_b16 a[36:37],v240 offset:13056\n"
"v_mfma_f32_16x16x32_bf16 v[56:59],a[60:63],a[248:251],v[56:59]\n"
"v_mfma_f32_16x16x32_bf16 v[60:63],a[56:59],a[204:207],v[60:63]\n"
"ds_read_b64_tr_b16 a[38:39],v240 offset:19200\n"
"v_mfma_f32_16x16x32_bf16 v[60:63],a[60:63],a[252:255],v[60:63]\n"
"buffer_load_dwordx2 a[86:87],v189,s[88:91],s40 offen offset:1024 sc1\n"
"s_waitcnt lgkmcnt(8)\n"
"v_mfma_f32_16x16x32_bf16 v[64:67],a[64:67],a[192:195],v[64:67]\n"
"ds_read_b64_tr_b16 a[40:41],v241 offset:768\n"
"v_mfma_f32_16x16x32_bf16 v[64:67],a[68:71],a[240:243],v[64:67]\n"
"v_mfma_f32_16x16x32_bf16 v[68:71],a[64:67],a[196:199],v[68:71]\n"
"ds_read_b64_tr_b16 a[42:43],v241 offset:6912\n"
"v_mfma_f32_16x16x32_bf16 v[68:71],a[68:71],a[244:247],v[68:71]\n"
"buffer_load_dwordx2 a[88:89],v188,s[88:91],s40 offen offset:2048 sc1\n"
"v_mfma_f32_16x16x32_bf16 v[72:75],a[64:67],a[200:203],v[72:75]\n"
"ds_read_b64_tr_b16 a[44:45],v241 offset:13056\n"
"v_mfma_f32_16x16x32_bf16 v[72:75],a[68:71],a[248:251],v[72:75]\n"
"buffer_load_dwordx2 a[90:91],v189,s[88:91],s40 offen offset:2048 sc1\n"
"v_mfma_f32_16x16x32_bf16 v[76:79],a[64:67],a[204:207],v[76:79]\n"
"ds_read_b64_tr_b16 a[46:47],v241 offset:19200\n"
"v_mfma_f32_16x16x32_bf16 v[76:79],a[68:71],a[252:255],v[76:79]\n"
"s_waitcnt lgkmcnt(8)\n"
"v_mfma_f32_16x16x32_bf16 v[80:83],a[72:75],a[192:195],v[80:83]\n"
"ds_read_b64_tr_b16 a[48:49],v240 offset:1024\n"
"v_mfma_f32_16x16x32_bf16 v[80:83],a[76:79],a[240:243],v[80:83]\n"
"buffer_load_dwordx2 a[92:93],v188,s[88:91],s40 offen offset:3072 sc1\n"
"v_mfma_f32_16x16x32_bf16 v[84:87],a[72:75],a[196:199],v[84:87]\n"
"ds_read_b64_tr_b16 a[50:51],v240 offset:7168\n"
"v_mfma_f32_16x16x32_bf16 v[84:87],a[76:79],a[244:247],v[84:87]\n"
"v_mfma_f32_16x16x32_bf16 v[88:91],a[72:75],a[200:203],v[88:91]\n"
"ds_read_b64_tr_b16 a[52:53],v240 offset:13312\n"
"v_mfma_f32_16x16x32_bf16 v[88:91],a[76:79],a[248:251],v[88:91]\n"
"buffer_load_dwordx2 a[94:95],v189,s[88:91],s40 offen offset:3072 sc1\n"
"v_mfma_f32_16x16x32_bf16 v[92:95],a[72:75],a[204:207],v[92:95]\n"
"s_add_u32 s40,s40,4096\n"
"v_readlane_b32 s40,v248,26\n"
"s_nop 4\n"
"ds_read_b64_tr_b16 a[54:55],v240 offset:19456\n"
"v_mfma_f32_16x16x32_bf16 v[92:95],a[76:79],a[252:255],v[92:95]\n"
"buffer_load_dwordx2 a[96:97],v188,s[88:91],s40 offen sc1\n"
"s_waitcnt vmcnt(44) lgkmcnt(8)\n"
"s_barrier\n"
"v_mfma_f32_16x16x32_bf16 v[96:99],a[32:35],a[192:195],v[96:99]\n"
"ds_read_b64_tr_b16 a[56:57],v240 offset:24576\n"
"v_mfma_f32_16x16x32_bf16 v[96:99],a[36:39],a[240:243],v[96:99]\n"
"v_mfma_f32_16x16x32_bf16 v[100:103],a[32:35],a[196:199],v[100:103]\n"
"ds_read_b64_tr_b16 a[58:59],v240 offset:30720\n"
"v_mfma_f32_16x16x32_bf16 v[100:103],a[36:39],a[244:247],v[100:103]\n"
"buffer_load_dwordx2 a[98:99],v189,s[88:91],s40 offen sc1\n"
"v_mfma_f32_16x16x32_bf16 v[104:107],a[32:35],a[200:203],v[104:107]\n"
"ds_read_b64_tr_b16 a[60:61],v240 offset:36864\n"
"v_mfma_f32_16x16x32_bf16 v[104:107],a[36:39],a[248:251],v[104:107]\n"
"v_mfma_f32_16x16x32_bf16 v[108:111],a[32:35],a[204:207],v[108:111]\n"
"ds_read_b64_tr_b16 a[62:63],v240 offset:43008\n"
"v_mfma_f32_16x16x32_bf16 v[108:111],a[36:39],a[252:255],v[108:111]\n"
"buffer_load_dwordx2 a[100:101],v188,s[88:91],s40 offen offset:1024 sc1\n"
"s_waitcnt lgkmcnt(8)\n"
"v_mfma_f32_16x16x32_bf16 v[112:115],a[40:43],a[192:195],v[112:115]\n"
"ds_read_b64_tr_b16 a[64:65],v241 offset:24576\n"
"v_mfma_f32_16x16x32_bf16 v[112:115],a[44:47],a[240:243],v[112:115]\n"
"buffer_load_dwordx2 a[102:103],v189,s[88:91],s40 offen offset:1024 sc1\n"
"v_mfma_f32_16x16x32_bf16 v[116:119],a[40:43],a[196:199],v[116:119]\n"
"ds_read_b64_tr_b16 a[66:67],v241 offset:30720\n"
"v_mfma_f32_16x16x32_bf16 v[116:119],a[44:47],a[244:247],v[116:119]\n"
"v_mfma_f32_16x16x32_bf16 v[120:123],a[40:43],a[200:203],v[120:123]\n"
"ds_read_b64_tr_b16 a[68:69],v241 offset:36864\n"
"v_mfma_f32_16x16x32_bf16 v[120:123],a[44:47],a[248:251],v[120:123]\n"
"buffer_load_dwordx2 a[104:105],v188,s[88:91],s40 offen offset:2048 sc1\n"
"v_mfma_f32_16x16x32_bf16 v[124:127],a[40:43],a[204:207],v[124:127]\n"
"ds_read_b64_tr_b16 a[70:71],v241 offset:43008\n"
"v_mfma_f32_16x16x32_bf16 v[124:127],a[44:47],a[252:255],v[124:127]\n"
"buffer_load_dwordx2 a[106:107],v189,s[88:91],s40 offen offset:2048 sc1\n"
"s_waitcnt lgkmcnt(8)\n"
"v_mfma_f32_16x16x32_bf16 v[128:131],a[48:51],a[192:195],v[128:131]\n"
"ds_read_b64_tr_b16 a[72:73],v240 offset:24832\n"
"v_mfma_f32_16x16x32_bf16 v[128:131],a[52:55],a[240:243],v[128:131]\n"
"v_mfma_f32_16x16x32_bf16 v[132:135],a[48:51],a[196:199],v[132:135]\n"
"ds_read_b64_tr_b16 a[74:75],v240 offset:30976\n"
"v_mfma_f32_16x16x32_bf16 v[132:135],a[52:55],a[244:247],v[132:135]\n"
"buffer_load_dwordx2 a[108:109],v188,s[88:91],s40 offen offset:3072 sc1\n"
"v_mfma_f32_16x16x32_bf16 v[136:139],a[48:51],a[200:203],v[136:139]\n"
"ds_read_b64_tr_b16 a[76:77],v240 offset:37120\n"
"v_mfma_f32_16x16x32_bf16 v[136:139],a[52:55],a[248:251],v[136:139]\n"
"v_mfma_f32_16x16x32_bf16 v[140:143],a[48:51],a[204:207],v[140:143]\n"
"ds_read_b64_tr_b16 a[78:79],v240 offset:43264\n"
"v_mfma_f32_16x16x32_bf16 v[140:143],a[52:55],a[252:255],v[140:143]\n"
"buffer_load_dwordx2 a[110:111],v189,s[88:91],s40 offen offset:3072 sc1\n"
"s_add_u32 s40,s40,4096\n"
"s_waitcnt lgkmcnt(8)\n"
"v_mfma_f32_16x16x32_bf16 v[0:3],a[56:59],a[208:211],v[0:3]\n"
"ds_read_b64_tr_b16 a[32:33],v241 offset:24832\n"
"v_mfma_f32_16x16x32_bf16 v[0:3],a[60:63],a[224:227],v[0:3]\n"
"v_mfma_f32_16x16x32_bf16 v[4:7],a[56:59],a[212:215],v[4:7]\n"
"v_readlane_b32 s39,v248,21\n"
"s_nop 4\n"
"s_add_u32 m0,s62,24576\n"
"ds_read_b64_tr_b16 a[34:35],v241 offset:30976\n"
"v_mfma_f32_16x16x32_bf16 v[4:7],a[60:63],a[228:231],v[4:7]\n"
"buffer_load_dwordx4 v213,s[72:75],s39 offen lds\n"
"v_mfma_f32_16x16x32_bf16 v[8:11],a[56:59],a[216:219],v[8:11]\n"
"ds_read_b64_tr_b16 a[36:37],v241 offset:37120\n"
"v_mfma_f32_16x16x32_bf16 v[8:11],a[60:63],a[232:235],v[8:11]\n"
"v_mfma_f32_16x16x32_bf16 v[12:15],a[56:59],a[220:223],v[12:15]\n"
"ds_read_b64_tr_b16 a[38:39],v241 offset:43264\n"
"v_mfma_f32_16x16x32_bf16 v[12:15],a[60:63],a[236:239],v[12:15]\n"
"buffer_load_dwordx4 v214,s[72:75],s39 offen offset:1024 lds\n"
"s_waitcnt lgkmcnt(8)\n"
"v_mfma_f32_16x16x32_bf16 v[16:19],a[64:67],a[208:211],v[16:19]\n"
"ds_read_b64_tr_b16 a[40:41],v240 offset:25088\n"
"v_mfma_f32_16x16x32_bf16 v[16:19],a[68:71],a[224:227],v[16:19]\n"
"v_mfma_f32_16x16x32_bf16 v[20:23],a[64:67],a[212:215],v[20:23]\n"
"ds_read_b64_tr_b16 a[42:43],v240 offset:31232\n"
"v_mfma_f32_16x16x32_bf16 v[20:23],a[68:71],a[228:231],v[20:23]\n"
"v_mfma_f32_16x16x32_bf16 v[24:27],a[64:67],a[216:219],v[24:27]\n"
"ds_read_b64_tr_b16 a[44:45],v240 offset:37376\n"
"v_mfma_f32_16x16x32_bf16 v[24:27],a[68:71],a[232:235],v[24:27]\n"
"buffer_load_dwordx4 v215,s[72:75],s39 offen offset:2048 lds\n"
"v_mfma_f32_16x16x32_bf16 v[28:31],a[64:67],a[220:223],v[28:31]\n"
"ds_read_b64_tr_b16 a[46:47],v240 offset:43520\n"
"v_mfma_f32_16x16x32_bf16 v[28:31],a[68:71],a[236:239],v[28:31]\n"
"s_waitcnt lgkmcnt(8)\n"
"v_mfma_f32_16x16x32_bf16 v[32:35],a[72:75],a[208:211],v[32:35]\n"
"ds_read_b64_tr_b16 a[48:49],v241 offset:25088\n"
"v_mfma_f32_16x16x32_bf16 v[32:35],a[76:79],a[224:227],v[32:35]\n"
"v_mfma_f32_16x16x32_bf16 v[36:39],a[72:75],a[212:215],v[36:39]\n"
"s_add_u32 s39,s39,s52\n"
"s_add_u32 m0,s62,27648\n"
"ds_read_b64_tr_b16 a[50:51],v241 offset:31232\n"
"v_mfma_f32_16x16x32_bf16 v[36:39],a[76:79],a[228:231],v[36:39]\n"
"buffer_load_dwordx4 v213,s[72:75],s39 offen lds\n"
"v_mfma_f32_16x16x32_bf16 v[40:43],a[72:75],a[216:219],v[40:43]\n"
"ds_read_b64_tr_b16 a[52:53],v241 offset:37376\n"
"v_mfma_f32_16x16x32_bf16 v[40:43],a[76:79],a[232:235],v[40:43]\n"
"v_mfma_f32_16x16x32_bf16 v[44:47],a[72:75],a[220:223],v[44:47]\n"
"ds_read_b64_tr_b16 a[54:55],v241 offset:43520\n"
"v_mfma_f32_16x16x32_bf16 v[44:47],a[76:79],a[236:239],v[44:47]\n"
"buffer_load_dwordx4 v214,s[72:75],s39 offen offset:1024 lds\n"
"s_waitcnt lgkmcnt(8)\n"
"v_mfma_f32_16x16x32_bf16 v[48:51],a[32:35],a[208:211],v[48:51]\n"
"ds_read_b64_tr_b16 a[56:57],v240 offset:25344\n"
"v_mfma_f32_16x16x32_bf16 v[48:51],a[36:39],a[224:227],v[48:51]\n"
"v_mfma_f32_16x16x32_bf16 v[52:55],a[32:35],a[212:215],v[52:55]\n"
"ds_read_b64_tr_b16 a[58:59],v240 offset:31488\n"
"v_mfma_f32_16x16x32_bf16 v[52:55],a[36:39],a[228:231],v[52:55]\n"
"v_mfma_f32_16x16x32_bf16 v[56:59],a[32:35],a[216:219],v[56:59]\n"
"ds_read_b64_tr_b16 a[60:61],v240 offset:37632\n"
"v_mfma_f32_16x16x32_bf16 v[56:59],a[36:39],a[232:235],v[56:59]\n"
"buffer_load_dwordx4 v215,s[72:75],s39 offen offset:2048 lds\n"
"v_mfma_f32_16x16x32_bf16 v[60:63],a[32:35],a[220:223],v[60:63]\n"
"ds_read_b64_tr_b16 a[62:63],v240 offset:43776\n"
"v_mfma_f32_16x16x32_bf16 v[60:63],a[36:39],a[236:239],v[60:63]\n"
"s_waitcnt lgkmcnt(8)\n"
"v_mfma_f32_16x16x32_bf16 v[64:67],a[40:43],a[208:211],v[64:67]\n"
"s_add_u32 s39,s39,s53\n"
"s_mov_b32 s55,0x40000\n"
"s_waitcnt vmcnt(21)\n"
"LM_POLL21:\n"
"v_cmp_ne_u64 vcc,v[226:227],v[224:225]\n"
"s_nop 1\n"
"s_and_b64 s[44:45],vcc,s[46:47]\n"
"s_cbranch_scc0 LM_POK22\n"
"s_sleep 1\n"
"buffer_load_dwordx2 v[226:227],v223,s[68:71],0 offen sc1\n"
"s_waitcnt vmcnt(0)\n"
"s_sub_u32 s55,s55,1\n"
"s_cbranch_scc0 LM_POLL21\n"
"LM_POK22:\n"
"v_readlane_b32 s40,v248,27\n"
"s_nop 4\n"
"ds_read_b64_tr_b16 a[64:65],v241 offset:25344\n"
"v_mfma_f32_16x16x32_bf16 v[64:67],a[44:47],a[224:227],v[64:67]\n"
"buffer_load_dwordx4 a[128:131],v209,s[88:91],s40 offen sc1\n"
"v_mfma_f32_16x16x32_bf16 v[68:71],a[40:43],a[212:215],v[68:71]\n"
"ds_read_b64_tr_b16 a[66:67],v241 offset:31488\n"
"v_mfma_f32_16x16x32_bf16 v[68:71],a[44:47],a[228:231],v[68:71]\n"
"v_mfma_f32_16x16x32_bf16 v[72:75],a[40:43],a[216:219],v[72:75]\n"
"ds_read_b64_tr_b16 a[68:69],v241 offset:37632\n"
"v_mfma_f32_16x16x32_bf16 v[72:75],a[44:47],a[232:235],v[72:75]\n"
"v_mfma_f32_16x16x32_bf16 v[76:79],a[40:43],a[220:223],v[76:79]\n"
"ds_read_b64_tr_b16 a[70:71],v241 offset:43776\n"
"v_mfma_f32_16x16x32_bf16 v[76:79],a[44:47],a[236:239],v[76:79]\n"
"buffer_load_dwordx4 a[132:135],v209,s[88:91],s40 offen offset:1024 sc1\n"
"s_waitcnt lgkmcnt(8)\n"
"v_mfma_f32_16x16x32_bf16 v[80:83],a[48:51],a[208:211],v[80:83]\n"
"ds_read_b64_tr_b16 a[72:73],v240 offset:25600\n"
"v_mfma_f32_16x16x32_bf16 v[80:83],a[52:55],a[224:227],v[80:83]\n"
"v_mfma_f32_16x16x32_bf16 v[84:87],a[48:51],a[212:215],v[84:87]\n"
"ds_read_b64_tr_b16 a[74:75],v240 offset:31744\n"
"v_mfma_f32_16x16x32_bf16 v[84:87],a[52:55],a[228:231],v[84:87]\n"
"buffer_load_dwordx4 a[136:139],v209,s[88:91],s40 offen offset:2048 sc1\n"
"v_mfma_f32_16x16x32_bf16 v[88:91],a[48:51],a[216:219],v[88:91]\n"
"ds_read_b64_tr_b16 a[76:77],v240 offset:37888\n"
"v_mfma_f32_16x16x32_bf16 v[88:91],a[52:55],a[232:235],v[88:91]\n"
"v_mfma_f32_16x16x32_bf16 v[92:95],a[48:51],a[220:223],v[92:95]\n"
"ds_read_b64_tr_b16 a[78:79],v240 offset:44032\n"
"v_mfma_f32_16x16x32_bf16 v[92:95],a[52:55],a[236:239],v[92:95]\n"
"s_waitcnt vmcnt(46) lgkmcnt(8)\n"
"s_barrier\n"
"v_mfma_f32_16x16x32_bf16 v[96:99],a[56:59],a[208:211],v[96:99]\n"
"ds_read_b64_tr_b16 a[32:33],v252\n"
"v_mfma_f32_16x16x32_bf16 v[96:99],a[60:63],a[224:227],v[96:99]\n"
"buffer_load_dwordx4 a[140:143],v209,s[88:91],s40 offen offset:3072 sc1\n"
"v_mfma_f32_16x16x32_bf16 v[100:103],a[56:59],a[212:215],v[100:103]\n"
"ds_read_b64_tr_b16 a[34:35],v252 offset:6144\n"
"v_mfma_f32_16x16x32_bf16 v[100:103],a[60:63],a[228:231],v[100:103]\n"
"v_mfma_f32_16x16x32_bf16 v[104:107],a[56:59],a[216:219],v[104:107]\n"
"ds_read_b64_tr_b16 a[36:37],v252 offset:12288\n"
"v_mfma_f32_16x16x32_bf16 v[104:107],a[60:63],a[232:235],v[104:107]\n"
"v_mfma_f32_16x16x32_bf16 v[108:111],a[56:59],a[220:223],v[108:111]\n"
"s_add_u32 s40,s40,4096\n"
"ds_read_b64_tr_b16 a[38:39],v252 offset:18432\n"
"v_mfma_f32_16x16x32_bf16 v[108:111],a[60:63],a[236:239],v[108:111]\n"
"buffer_load_dwordx4 a[144:147],v209,s[88:91],s40 offen sc1\n"
"s_waitcnt lgkmcnt(8)\n"
"v_mfma_f32_16x16x32_bf16 v[112:115],a[64:67],a[208:211],v[112:115]\n"
"ds_read_b64_tr_b16 a[40:41],v253\n"
"v_mfma_f32_16x16x32_bf16 v[112:115],a[68:71],a[224:227],v[112:115]\n"
"v_mfma_f32_16x16x32_bf16 v[116:119],a[64:67],a[212:215],v[116:119]\n"
"ds_read_b64_tr_b16 a[42:43],v253 offset:6144\n"
"v_mfma_f32_16x16x32_bf16 v[116:119],a[68:71],a[228:231],v[116:119]\n"
"buffer_load_dwordx4 a[148:151],v209,s[88:91],s40 offen offset:1024 sc1\n"
"v_mfma_f32_16x16x32_bf16 v[120:123],a[64:67],a[216:219],v[120:123]\n"
"ds_read_b64_tr_b16 a[44:45],v253 offset:12288\n"
"v_mfma_f32_16x16x32_bf16 v[120:123],a[68:71],a[232:235],v[120:123]\n"
"v_mfma_f32_16x16x32_bf16 v[124:127],a[64:67],a[220:223],v[124:127]\n"
"ds_read_b64_tr_b16 a[46:47],v253 offset:18432\n"
"v_mfma_f32_16x16x32_bf16 v[124:127],a[68:71],a[236:239],v[124:127]\n"
"s_waitcnt lgkmcnt(8)\n"
"v_mfma_f32_16x16x32_bf16 v[128:131],a[72:75],a[208:211],v[128:131]\n"
"ds_read_b64_tr_b16 a[48:49],v252 offset:256\n"
"v_mfma_f32_16x16x32_bf16 v[128:131],a[76:79],a[224:227],v[128:131]\n"
"buffer_load_dwordx4 a[152:155],v209,s[88:91],s40 offen offset:2048 sc1\n"
"v_mfma_f32_16x16x32_bf16 v[132:135],a[72:75],a[212:215],v[132:135]\n"
"ds_read_b64_tr_b16 a[50:51],v252 offset:6400\n"
"v_mfma_f32_16x16x32_bf16 v[132:135],a[76:79],a[228:231],v[132:135]\n"
"v_mfma_f32_16x16x32_bf16 v[136:139],a[72:75],a[216:219],v[136:139]\n"
"ds_read_b64_tr_b16 a[52:53],v252 offset:12544\n"
"v_mfma_f32_16x16x32_bf16 v[136:139],a[76:79],a[232:235],v[136:139]\n"
"buffer_load_dwordx4 a[156:159],v209,s[88:91],s40 offen offset:3072 sc1\n"
"v_mfma_f32_16x16x32_bf16 v[140:143],a[72:75],a[220:223],v[140:143]\n"
"ds_read_b64_tr_b16 a[54:55],v252 offset:18688\n"
"v_mfma_f32_16x16x32_bf16 v[140:143],a[76:79],a[236:239],v[140:143]\n"
"s_add_u32 s40,s40,4096\n"
"s_waitcnt lgkmcnt(8)\n"
"v_mfma_f32_16x16x32_bf16 v[0:3],a[32:35],a[160:163],v[0:3]\n"
"ds_read_b64_tr_b16 a[56:57],v253 offset:256\n"
"v_mfma_f32_16x16x32_bf16 v[0:3],a[36:39],a[176:179],v[0:3]\n"
"v_mfma_f32_16x16x32_bf16 v[4:7],a[32:35],a[164:167],v[4:7]\n"
"s_add_u32 m0,s63,0\n"
"ds_read_b64_tr_b16 a[58:59],v253 offset:6400\n"
"v_mfma_f32_16x16x32_bf16 v[4:7],a[36:39],a[180:183],v[4:7]\n"
"buffer_load_dwordx4 v213,s[72:75],s39 offen lds\n"
"v_mfma_f32_16x16x32_bf16 v[8:11],a[32:35],a[168:171],v[8:11]\n"
"ds_read_b64_tr_b16 a[60:61],v253 offset:12544\n"
"v_mfma_f32_16x16x32_bf16 v[8:11],a[36:39],a[184:187],v[8:11]\n"
"v_mfma_f32_16x16x32_bf16 v[12:15],a[32:35],a[172:175],v[12:15]\n"
"ds_read_b64_tr_b16 a[62:63],v253 offset:18688\n"
"v_mfma_f32_16x16x32_bf16 v[12:15],a[36:39],a[188:191],v[12:15]\n"
"buffer_load_dwordx4 v214,s[72:75],s39 offen offset:1024 lds\n"
"s_waitcnt lgkmcnt(8)\n"
"v_mfma_f32_16x16x32_bf16 v[16:19],a[40:43],a[160:163],v[16:19]\n"
"ds_read_b64_tr_b16 a[64:65],v252 offset:512\n"
"v_mfma_f32_16x16x32_bf16 v[16:19],a[44:47],a[176:179],v[16:19]\n"
"v_mfma_f32_16x16x32_bf16 v[20:23],a[40:43],a[164:167],v[20:23]\n"
"ds_read_b64_tr_b16 a[66:67],v252 offset:6656\n"
"v_mfma_f32_16x16x32_bf16 v[20:23],a[44:47],a[180:183],v[20:23]\n"
"v_mfma_f32_16x16x32_bf16 v[24:27],a[40:43],a[168:171],v[24:27]\n"
"ds_read_b64_tr_b16 a[68:69],v252 offset:12800\n"
"v_mfma_f32_16x16x32_bf16 v[24:27],a[44:47],a[184:187],v[24:27]\n"
"buffer_load_dwordx4 v215,s[72:75],s39 offen offset:2048 lds\n"
"v_mfma_f32_16x16x32_bf16 v[28:31],a[40:43],a[172:175],v[28:31]\n"
"ds_read_b64_tr_b16 a[70:71],v252 offset:18944\n"
"v_mfma_f32_16x16x32_bf16 v[28:31],a[44:47],a[188:191],v[28:31]\n"
"s_waitcnt lgkmcnt(8)\n"
"v_mfma_f32_16x16x32_bf16 v[32:35],a[48:51],a[160:163],v[32:35]\n"
"ds_read_b64_tr_b16 a[72:73],v253 offset:512\n"
"v_mfma_f32_16x16x32_bf16 v[32:35],a[52:55],a[176:179],v[32:35]\n"
"v_mfma_f32_16x16x32_bf16 v[36:39],a[48:51],a[164:167],v[36:39]\n"
"s_add_u32 s39,s39,s52\n"
"s_add_u32 m0,s63,3072\n"
"ds_read_b64_tr_b16 a[74:75],v253 offset:6656\n"
"v_mfma_f32_16x16x32_bf16 v[36:39],a[52:55],a[180:183],v[36:39]\n"
"buffer_load_dwordx4 v213,s[72:75],s39 offen lds\n"
"v_mfma_f32_16x16x32_bf16 v[40:43],a[48:51],a[168:171],v[40:43]\n"
"ds_read_b64_tr_b16 a[76:77],v253 offset:12800\n"
"v_mfma_f32_16x16x32_bf16 v[40:43],a[52:55],a[184:187],v[40:43]\n"
"v_mfma_f32_16x16x32_bf16 v[44:47],a[48:51],a[172:175],v[44:47]\n"
"ds_read_b64_tr_b16 a[78:79],v253 offset:18944\n"
"v_mfma_f32_16x16x32_bf16 v[44:47],a[52:55],a[188:191],v[44:47]\n"
"buffer_load_dwordx4 v214,s[72:75],s39 offen offset:1024 lds\n"
"s_waitcnt lgkmcnt(8)\n"
"v_mfma_f32_16x16x32_bf16 v[48:51],a[56:59],a[160:163],v[48:51]\n"
"ds_read_b64_tr_b16 a[32:33],v252 offset:768\n"
"v_mfma_f32_16x16x32_bf16 v[48:51],a[60:63],a[176:179],v[48:51]\n"
"v_mfma_f32_16x16x32_bf16 v[52:55],a[56:59],a[164:167],v[52:55]\n"
"ds_read_b64_tr_b16 a[34:35],v252 offset:6912\n"
"v_mfma_f32_16x16x32_bf16 v[52:55],a[60:63],a[180:183],v[52:55]\n"
"v_mfma_f32_16x16x32_bf16 v[56:59],a[56:59],a[168:171],v[56:59]\n"
"ds_read_b64_tr_b16 a[36:37],v252 offset:13056\n"
"v_mfma_f32_16x16x32_bf16 v[56:59],a[60:63],a[184:187],v[56:59]\n"
"buffer_load_dwordx4 v215,s[72:75],s39 offen offset:2048 lds\n"
"v_mfma_f32_16x16x32_bf16 v[60:63],a[56:59],a[172:175],v[60:63]\n"
"ds_read_b64_tr_b16 a[38:39],v252 offset:19200\n"
"v_mfma_f32_16x16x32_bf16 v[60:63],a[60:63],a[188:191],v[60:63]\n"
"s_waitcnt lgkmcnt(8)\n"
"v_mfma_f32_16x16x32_bf16 v[64:67],a[64:67],a[160:163],v[64:67]\n"
"s_add_u32 s39,s39,s53\n"
"ds_read_b64_tr_b16 a[40:41],v253 offset:768\n"
"v_mfma_f32_16x16x32_bf16 v[64:67],a[68:71],a[176:179],v[64:67]\n"
"buffer_load_dwordx4 a[192:195],v209,s[88:91],s40 offen sc1\n"
"v_mfma_f32_16x16x32_bf16 v[68:71],a[64:67],a[164:167],v[68:71]\n"
"ds_read_b64_tr_b16 a[42:43],v253 offset:6912\n"
"v_mfma_f32_16x16x32_bf16 v[68:71],a[68:71],a[180:183],v[68:71]\n"
"v_mfma_f32_16x16x32_bf16 v[72:75],a[64:67],a[168:171],v[72:75]\n"
"ds_read_b64_tr_b16 a[44:45],v253 offset:13056\n"
"v_mfma_f32_16x16x32_bf16 v[72:75],a[68:71],a[184:187],v[72:75]\n"
"v_mfma_f32_16x16x32_bf16 v[76:79],a[64:67],a[172:175],v[76:79]\n"
"ds_read_b64_tr_b16 a[46:47],v253 offset:19200\n"
"v_mfma_f32_16x16x32_bf16 v[76:79],a[68:71],a[188:191],v[76:79]\n"
"buffer_load_dwordx4 a[196:199],v209,s[88:91],s40 offen offset:1024 sc1\n"
"s_waitcnt lgkmcnt(8)\n"
"v_mfma_f32_16x16x32_bf16 v[80:83],a[72:75],a[160:163],v[80:83]\n"
"ds_read_b64_tr_b16 a[48:49],v252 offset:1024\n"
"v_mfma_f32_16x16x32_bf16 v[80:83],a[76:79],a[176:179],v[80:83]\n"
"v_mfma_f32_16x16x32_bf16 v[84:87],a[72:75],a[164:167],v[84:87]\n"
"ds_read_b64_tr_b16 a[50:51],v252 offset:7168\n"
"v_mfma_f32_16x16x32_bf16 v[84:87],a[76:79],a[180:183],v[84:87]\n"
"buffer_load_dwordx4 a[200:203],v209,s[88:91],s40 offen offset:2048 sc1\n"
"v_mfma_f32_16x16x32_bf16 v[88:91],a[72:75],a[168:171],v[88:91]\n"
"ds_read_b64_tr_b16 a[52:53],v252 offset:13312\n"
"v_mfma_f32_16x16x32_bf16 v[88:91],a[76:79],a[184:187],v[88:91]\n"
"v_mfma_f32_16x16x32_bf16 v[92:95],a[72:75],a[172:175],v[92:95]\n"
"ds_read_b64_tr_b16 a[54:55],v252 offset:19456\n"
"v_mfma_f32_16x16x32_bf16 v[92:95],a[76:79],a[188:191],v[92:95]\n"
"s_waitcnt vmcnt(46) lgkmcnt(8)\n"
"s_barrier\n"
"v_mfma_f32_16x16x32_bf16 v[96:99],a[32:35],a[160:163],v[96:99]\n"
"ds_read_b64_tr_b16 a[56:57],v252 offset:24576\n"
"v_mfma_f32_16x16x32_bf16 v[96:99],a[36:39],a[176:179],v[96:99]\n"
"buffer_load_dwordx4 a[204:207],v209,s[88:91],s40 offen offset:3072 sc1\n"
"v_mfma_f32_16x16x32_bf16 v[100:103],a[32:35],a[164:167],v[100:103]\n"
"ds_read_b64_tr_b16 a[58:59],v252 offset:30720\n"
"v_mfma_f32_16x16x32_bf16 v[100:103],a[36:39],a[180:183],v[100:103]\n"
"v_mfma_f32_16x16x32_bf16 v[104:107],a[32:35],a[168:171],v[104:107]\n"
"ds_read_b64_tr_b16 a[60:61],v252 offset:36864\n"
"v_mfma_f32_16x16x32_bf16 v[104:107],a[36:39],a[184:187],v[104:107]\n"
"v_mfma_f32_16x16x32_bf16 v[108:111],a[32:35],a[172:175],v[108:111]\n"
"s_add_u32 s40,s40,4096\n"
"ds_read_b64_tr_b16 a[62:63],v252 offset:43008\n"
"v_mfma_f32_16x16x32_bf16 v[108:111],a[36:39],a[188:191],v[108:111]\n"
"buffer_load_dwordx4 a[240:243],v209,s[88:91],s40 offen sc1\n"
"s_waitcnt lgkmcnt(8)\n"
"v_mfma_f32_16x16x32_bf16 v[112:115],a[40:43],a[160:163],v[112:115]\n"
"ds_read_b64_tr_b16 a[64:65],v253 offset:24576\n"
"v_mfma_f32_16x16x32_bf16 v[112:115],a[44:47],a[176:179],v[112:115]\n"
"v_mfma_f32_16x16x32_bf16 v[116:119],a[40:43],a[164:167],v[116:119]\n"
"ds_read_b64_tr_b16 a[66:67],v253 offset:30720\n"
"v_mfma_f32_16x16x32_bf16 v[116:119],a[44:47],a[180:183],v[116:119]\n"
"buffer_load_dwordx4 a[244:247],v209,s[88:91],s40 offen offset:1024 sc1\n"
"v_mfma_f32_16x16x32_bf16 v[120:123],a[40:43],a[168:171],v[120:123]\n"
"ds_read_b64_tr_b16 a[68:69],v253 offset:36864\n"
"v_mfma_f32_16x16x32_bf16 v[120:123],a[44:47],a[184:187],v[120:123]\n"
"v_mfma_f32_16x16x32_bf16 v[124:127],a[40:43],a[172:175],v[124:127]\n"
"ds_read_b64_tr_b16 a[70:71],v253 offset:43008\n"
"v_mfma_f32_16x16x32_bf16 v[124:127],a[44:47],a[188:191],v[124:127]\n"
"s_waitcnt lgkmcnt(8)\n"
"v_mfma_f32_16x16x32_bf16 v[128:131],a[48:51],a[160:163],v[128:131]\n"
"ds_read_b64_tr_b16 a[72:73],v252 offset:24832\n"
"v_mfma_f32_16x16x32_bf16 v[128:131],a[52:55],a[176:179],v[128:131]\n"
"buffer_load_dwordx4 a[248:251],v209,s[88:91],s40 offen offset:2048 sc1\n"
"v_mfma_f32_16x16x32_bf16 v[132:135],a[48:51],a[164:167],v[132:135]\n"
"ds_read_b64_tr_b16 a[74:75],v252 offset:30976\n"
"v_mfma_f32_16x16x32_bf16 v[132:135],a[52:55],a[180:183],v[132:135]\n"
"v_mfma_f32_16x16x32_bf16 v[136:139],a[48:51],a[168:171],v[136:139]\n"
"ds_read_b64_tr_b16 a[76:77],v252 offset:37120\n"
"v_mfma_f32_16x16x32_bf16 v[136:139],a[52:55],a[184:187],v[136:139]\n"
"buffer_load_dwordx4 a[252:255],v209,s[88:91],s40 offen offset:3072 sc1\n"
"v_mfma_f32_16x16x32_bf16 v[140:143],a[48:51],a[172:175],v[140:143]\n"
"ds_read_b64_tr_b16 a[78:79],v252 offset:43264\n"
"v_mfma_f32_16x16x32_bf16 v[140:143],a[52:55],a[188:191],v[140:143]\n"
"s_add_u32 s40,s40,4096\n"
"s_waitcnt lgkmcnt(8)\n"
"v_mfma_f32_16x16x32_bf16 v[0:3],a[56:59],a[0:3],v[0:3]\n"
"ds_read_b64_tr_b16 a[32:33],v253 offset:24832\n"
"v_mfma_f32_16x16x32_bf16 v[0:3],a[60:63],a[16:19],v[0:3]\n"
"v_mfma_f32_16x16x32_bf16 v[4:7],a[56:59],a[4:7],v[4:7]\n"
"s_add_u32 m0,s63,24576\n"
"ds_read_b64_tr_b16 a[34:35],v253 offset:30976\n"
"v_mfma_f32_16x16x32_bf16 v[4:7],a[60:63],a[20:23],v[4:7]\n"
"buffer_load_dwordx4 v213,s[72:75],s39 offen lds\n"
"v_mfma_f32_16x16x32_bf16 v[8:11],a[56:59],a[8:11],v[8:11]\n"
"ds_read_b64_tr_b16 a[36:37],v253 offset:37120\n"
"v_mfma_f32_16x16x32_bf16 v[8:11],a[60:63],a[24:27],v[8:11]\n"
"v_mfma_f32_16x16x32_bf16 v[12:15],a[56:59],a[12:15],v[12:15]\n"
"ds_read_b64_tr_b16 a[38:39],v253 offset:43264\n"
"v_mfma_f32_16x16x32_bf16 v[12:15],a[60:63],a[28:31],v[12:15]\n"
"buffer_load_dwordx4 v214,s[72:75],s39 offen offset:1024 lds\n"
"s_waitcnt lgkmcnt(8)\n"
"v_mfma_f32_16x16x32_bf16 v[16:19],a[64:67],a[0:3],v[16:19]\n"
"ds_read_b64_tr_b16 a[40:41],v252 offset:25088\n"
"v_mfma_f32_16x16x32_bf16 v[16:19],a[68:71],a[16:19],v[16:19]\n"
"v_mfma_f32_16x16x32_bf16 v[20:23],a[64:67],a[4:7],v[20:23]\n"
"ds_read_b64_tr_b16 a[42:43],v252 offset:31232\n"
"v_mfma_f32_16x16x32_bf16 v[20:23],a[68:71],a[20:23],v[20:23]\n"
"buffer_load_dwordx4 v215,s[72:75],s39 offen offset:2048 lds\n"
"v_mfma_f32_16x16x32_bf16 v[24:27],a[64:67],a[8:11],v[24:27]\n"
"ds_read_b64_tr_b16 a[44:45],v252 offset:37376\n"
"v_mfma_f32_16x16x32_bf16 v[24:27],a[68:71],a[24:27],v[24:27]\n"
"v_mfma_f32_16x16x32_bf16 v[28:31],a[64:67],a[12:15],v[28:31]\n"
"s_add_u32 s39,s39,s52\n"
"s_add_u32 m0,s63,27648\n"
"ds_read_b64_tr_b16 a[46:47],v252 offset:43520\n"
"v_mfma_f32_16x16x32_bf16 v[28:31],a[68:71],a[28:31],v[28:31]\n"
"buffer_load_dwordx4 v213,s[72:75],s39 offen lds\n"
"s_waitcnt lgkmcnt(8)\n"
"v_mfma_f32_16x16x32_bf16 v[32:35],a[72:75],a[0:3],v[32:35]\n"
"ds_read_b64_tr_b16 a[48:49],v253 offset:25088\n"
"v_mfma_f32_16x16x32_bf16 v[32:35],a[76:79],a[16:19],v[32:35]\n"
"v_mfma_f32_16x16x32_bf16 v[36:39],a[72:75],a[4:7],v[36:39]\n"
"ds_read_b64_tr_b16 a[50:51],v253 offset:31232\n"
"v_mfma_f32_16x16x32_bf16 v[36:39],a[76:79],a[20:23],v[36:39]\n"
"buffer_load_dwordx4 v214,s[72:75],s39 offen offset:1024 lds\n"
"v_mfma_f32_16x16x32_bf16 v[40:43],a[72:75],a[8:11],v[40:43]\n"
"ds_read_b64_tr_b16 a[52:53],v253 offset:37376\n"
"v_mfma_f32_16x16x32_bf16 v[40:43],a[76:79],a[24:27],v[40:43]\n"
"v_mfma_f32_16x16x32_bf16 v[44:47],a[72:75],a[12:15],v[44:47]\n"
"ds_read_b64_tr_b16 a[54:55],v253 offset:43520\n"
"v_mfma_f32_16x16x32_bf16 v[44:47],a[76:79],a[28:31],v[44:47]\n"
"buffer_load_dwordx4 v215,s[72:75],s39 offen offset:2048 lds\n"
"s_waitcnt lgkmcnt(8)\n"
"v_mfma_f32_16x16x32_bf16 v[48:51],a[32:35],a[0:3],v[48:51]\n"
"ds_read_b64_tr_b16 a[56:57],v252 offset:25344\n"
"v_mfma_f32_16x16x32_bf16 v[48:51],a[36:39],a[16:19],v[48:51]\n"
"v_mfma_f32_16x16x32_bf16 v[52:55],a[32:35],a[4:7],v[52:55]\n"
"s_add_u32 s39,s39,s53\n"
"ds_read_b64_tr_b16 a[58:59],v252 offset:31488\n"
"v_mfma_f32_16x16x32_bf16 v[52:55],a[36:39],a[20:23],v[52:55]\n"
"buffer_load_dwordx2 a[208:209],v188,s[88:91],s40 offen sc1\n"
"v_mfma_f32_16x16x32_bf16 v[56:59],a[32:35],a[8:11],v[56:59]\n"
"ds_read_b64_tr_b16 a[60:61],v252 offset:37632\n"
"v_mfma_f32_16x16x32_bf16 v[56:59],a[36:39],a[24:27],v[56:59]\n"
"v_mfma_f32_16x16x32_bf16 v[60:63],a[32:35],a[12:15],v[60:63]\n"
"ds_read_b64_tr_b16 a[62:63],v252 offset:43776\n"
"v_mfma_f32_16x16x32_bf16 v[60:63],a[36:39],a[28:31],v[60:63]\n"
"buffer_load_dwordx2 a[210:211],v189,s[88:91],s40 offen sc1\n"
"s_waitcnt lgkmcnt(8)\n"
"v_mfma_f32_16x16x32_bf16 v[64:67],a[40:43],a[0:3],v[64:67]\n"
"ds_read_b64_tr_b16 a[64:65],v253 offset:25344\n"
"v_mfma_f32_16x16x32_bf16 v[64:67],a[44:47],a[16:19],v[64:67]\n"
"v_mfma_f32_16x16x32_bf16 v[68:71],a[40:43],a[4:7],v[68:71]\n"
"ds_read_b64_tr_b16 a[66:67],v253 offset:31488\n"
"v_mfma_f32_16x16x32_bf16 v[68:71],a[44:47],a[20:23],v[68:71]\n"
"buffer_load_dwordx2 a[212:213],v188,s[88:91],s40 offen offset:1024 sc1\n"
"v_mfma_f32_16x16x32_bf16 v[72:75],a[40:43],a[8:11],v[72:75]\n"
"ds_read_b64_tr_b16 a[68:69],v253 offset:37632\n"
"v_mfma_f32_16x16x32_bf16 v[72:75],a[44:47],a[24:27],v[72:75]\n"
"v_mfma_f32_16x16x32_bf16 v[76:79],a[40:43],a[12:15],v[76:79]\n"
"ds_read_b64_tr_b16 a[70:71],v253 offset:43776\n"
"v_mfma_f32_16x16x32_bf16 v[76:79],a[44:47],a[28:31],v[76:79]\n"
"buffer_load_dwordx2 a[214:215],v189,s[88:91],s40 offen offset:1024 sc1\n"
"s_waitcnt lgkmcnt(8)\n"
"v_mfma_f32_16x16x32_bf16 v[80:83],a[48:51],a[0:3],v[80:83]\n"
"ds_read_b64_tr_b16 a[72:73],v252 offset:25600\n"
"v_mfma_f32_16x16x32_bf16 v[80:83],a[52:55],a[16:19],v[80:83]\n"
"v_mfma_f32_16x16x32_bf16 v[84:87],a[48:51],a[4:7],v[84:87]\n"
"ds_read_b64_tr_b16 a[74:75],v252 offset:31744\n"
"v_mfma_f32_16x16x32_bf16 v[84:87],a[52:55],a[20:23],v[84:87]\n"
"buffer_load_dwordx2 a[216:217],v188,s[88:91],s40 offen offset:2048 sc1\n"
"v_mfma_f32_16x16x32_bf16 v[88:91],a[48:51],a[8:11],v[88:91]\n"
"ds_read_b64_tr_b16 a[76:77],v252 offset:37888\n"
"v_mfma_f32_16x16x32_bf16 v[88:91],a[52:55],a[24:27],v[88:91]\n"
"v_mfma_f32_16x16x32_bf16 v[92:95],a[48:51],a[12:15],v[92:95]\n"
"ds_read_b64_tr_b16 a[78:79],v252 offset:44032\n"
"v_mfma_f32_16x16x32_bf16 v[92:95],a[52:55],a[28:31],v[92:95]\n"
"buffer_load_dwordx2 a[218:219],v189,s[88:91],s40 offen offset:2048 sc1\n"
"s_waitcnt vmcnt(40) lgkmcnt(8)\n"
"s_barrier\n"
"v_mfma_f32_16x16x32_bf16 v[96:99],a[56:59],a[0:3],v[96:99]\n"
"ds_read_b64_tr_b16 a[32:33],v254\n"
"v_mfma_f32_16x16x32_bf16 v[96:99],a[60:63],a[16:19],v[96:99]\n"
"v_mfma_f32_16x16x32_bf16 v[100:103],a[56:59],a[4:7],v[100:103]\n"
"ds_read_b64_tr_b16 a[34:35],v254 offset:6144\n"
"v_mfma_f32_16x16x32_bf16 v[100:103],a[60:63],a[20:23],v[100:103]\n"
"buffer_load_dwordx2 a[220:221],v188,s[88:91],s40 offen offset:3072 sc1\n"
"v_mfma_f32_16x16x32_bf16 v[104:107],a[56:59],a[8:11],v[104:107]\n"
"ds_read_b64_tr_b16 a[36:37],v254 offset:12288\n"
"v_mfma_f32_16x16x32_bf16 v[104:107],a[60:63],a[24:27],v[104:107]\n"
"v_mfma_f32_16x16x32_bf16 v[108:111],a[56:59],a[12:15],v[108:111]\n"
"ds_read_b64_tr_b16 a[38:39],v254 offset:18432\n"
"v_mfma_f32_16x16x32_bf16 v[108:111],a[60:63],a[28:31],v[108:111]\n"
"buffer_load_dwordx2 a[222:223],v189,s[88:91],s40 offen offset:3072 sc1\n"
"s_waitcnt lgkmcnt(8)\n"
"v_mfma_f32_16x16x32_bf16 v[112:115],a[64:67],a[0:3],v[112:115]\n"
"ds_read_b64_tr_b16 a[40:41],v255\n"
"v_mfma_f32_16x16x32_bf16 v[112:115],a[68:71],a[16:19],v[112:115]\n"
"v_mfma_f32_16x16x32_bf16 v[116:119],a[64:67],a[4:7],v[116:119]\n"
"s_add_u32 s40,s40,4096\n"
"ds_read_b64_tr_b16 a[42:43],v255 offset:6144\n"
"v_mfma_f32_16x16x32_bf16 v[116:119],a[68:71],a[20:23],v[116:119]\n"
"buffer_load_dwordx4 a[224:227],v209,s[88:91],s40 offen sc1\n"
"v_mfma_f32_16x16x32_bf16 v[120:123],a[64:67],a[8:11],v[120:123]\n"
"ds_read_b64_tr_b16 a[44:45],v255 offset:12288\n"
"v_mfma_f32_16x16x32_bf16 v[120:123],a[68:71],a[24:27],v[120:123]\n"
"v_mfma_f32_16x16x32_bf16 v[124:127],a[64:67],a[12:15],v[124:127]\n"
"ds_read_b64_tr_b16 a[46:47],v255 offset:18432\n"
"v_mfma_f32_16x16x32_bf16 v[124:127],a[68:71],a[28:31],v[124:127]\n"
"buffer_load_dwordx4 a[228:231],v209,s[88:91],s40 offen offset:1024 sc1\n"
"s_waitcnt lgkmcnt(8)\n"
"v_mfma_f32_16x16x32_bf16 v[128:131],a[72:75],a[0:3],v[128:131]\n"
"ds_read_b64_tr_b16 a[48:49],v254 offset:256\n"
"v_mfma_f32_16x16x32_bf16 v[128:131],a[76:79],a[16:19],v[128:131]\n"
"v_mfma_f32_16x16x32_bf16 v[132:135],a[72:75],a[4:7],v[132:135]\n"
"ds_read_b64_tr_b16 a[50:51],v254 offset:6400\n"
"v_mfma_f32_16x16x32_bf16 v[132:135],a[76:79],a[20:23],v[132:135]\n"
"buffer_load_dwordx4 a[232:235],v209,s[88:91],s40 offen offset:2048 sc1\n"
"v_mfma_f32_16x16x32_bf16 v[136:139],a[72:75],a[8:11],v[136:139]\n"
"ds_read_b64_tr_b16 a[52:53],v254 offset:12544\n"
"v_mfma_f32_16x16x32_bf16 v[136:139],a[76:79],a[24:27],v[136:139]\n"
"v_mfma_f32_16x16x32_bf16 v[140:143],a[72:75],a[12:15],v[140:143]\n"
"ds_read_b64_tr_b16 a[54:55],v254 offset:18688\n"
"v_mfma_f32_16x16x32_bf16 v[140:143],a[76:79],a[28:31],v[140:143]\n"
"buffer_load_dwordx4 a[236:239],v209,s[88:91],s40 offen offset:3072 sc1\n"
"s_add_u32 s40,s40,4096\n"
"s_waitcnt lgkmcnt(8)\n"
"v_mfma_f32_16x16x32_bf16 v[0:3],a[32:35],a[80:83],v[0:3]\n"
"ds_read_b64_tr_b16 a[56:57],v255 offset:256\n"
"v_mfma_f32_16x16x32_bf16 v[0:3],a[36:39],a[96:99],v[0:3]\n"
"v_mfma_f32_16x16x32_bf16 v[4:7],a[32:35],a[84:87],v[4:7]\n"
"s_add_u32 m0,s50,0\n"
"ds_read_b64_tr_b16 a[58:59],v255 offset:6400\n"
"v_mfma_f32_16x16x32_bf16 v[4:7],a[36:39],a[100:103],v[4:7]\n"
"buffer_load_dwordx4 v213,s[72:75],s39 offen lds\n"
"v_mfma_f32_16x16x32_bf16 v[8:11],a[32:35],a[88:91],v[8:11]\n"
"ds_read_b64_tr_b16 a[60:61],v255 offset:12544\n"
"v_mfma_f32_16x16x32_bf16 v[8:11],a[36:39],a[104:107],v[8:11]\n"
"v_mfma_f32_16x16x32_bf16 v[12:15],a[32:35],a[92:95],v[12:15]\n"
"ds_read_b64_tr_b16 a[62:63],v255 offset:18688\n"
"v_mfma_f32_16x16x32_bf16 v[12:15],a[36:39],a[108:111],v[12:15]\n"
"buffer_load_dwordx4 v214,s[72:75],s39 offen offset:1024 lds\n"
"s_waitcnt lgkmcnt(8)\n"
"v_mfma_f32_16x16x32_bf16 v[16:19],a[40:43],a[80:83],v[16:19]\n"
"ds_read_b64_tr_b16 a[64:65],v254 offset:512\n"
"v_mfma_f32_16x16x32_bf16 v[16:19],a[44:47],a[96:99],v[16:19]\n"
"v_mfma_f32_16x16x32_bf16 v[20:23],a[40:43],a[84:87],v[20:23]\n"
"ds_read_b64_tr_b16 a[66:67],v254 offset:6656\n"
"v_mfma_f32_16x16x32_bf16 v[20:23],a[44:47],a[100:103],v[20:23]\n"
"v_mfma_f32_16x16x32_bf16 v[24:27],a[40:43],a[88:91],v[24:27]\n"
"ds_read_b64_tr_b16 a[68:69],v254 offset:12800\n"
"v_mfma_f32_16x16x32_bf16 v[24:27],a[44:47],a[104:107],v[24:27]\n"
"buffer_load_dwordx4 v215,s[72:75],s39 offen offset:2048 lds\n"
"v_mfma_f32_16x16x32_bf16 v[28:31],a[40:43],a[92:95],v[28:31]\n"
"ds_read_b64_tr_b16 a[70:71],v254 offset:18944\n"
"v_mfma_f32_16x16x32_bf16 v[28:31],a[44:47],a[108:111],v[28:31]\n"
"s_waitcnt lgkmcnt(8)\n"
"v_mfma_f32_16x16x32_bf16 v[32:35],a[48:51],a[80:83],v[32:35]\n"
"ds_read_b64_tr_b16 a[72:73],v255 offset:512\n"
"v_mfma_f32_16x16x32_bf16 v[32:35],a[52:55],a[96:99],v[32:35]\n"
"v_mfma_f32_16x16x32_bf16 v[36:39],a[48:51],a[84:87],v[36:39]\n"
"s_add_u32 s39,s39,s52\n"
"s_add_u32 m0,s50,3072\n"
"ds_read_b64_tr_b16 a[74:75],v255 offset:6656\n"
"v_mfma_f32_16x16x32_bf16 v[36:39],a[52:55],a[100:103],v[36:39]\n"
"buffer_load_dwordx4 v213,s[72:75],s39 offen lds\n"
"v_mfma_f32_16x16x32_bf16 v[40:43],a[48:51],a[88:91],v[40:43]\n"
"ds_read_b64_tr_b16 a[76:77],v255 offset:12800\n"
"v_mfma_f32_16x16x32_bf16 v[40:43],a[52:55],a[104:107],v[40:43]\n"
"v_mfma_f32_16x16x32_bf16 v[44:47],a[48:51],a[92:95],v[44:47]\n"
"ds_read_b64_tr_b16 a[78:79],v255 offset:18944\n"
"v_mfma_f32_16x16x32_bf16 v[44:47],a[52:55],a[108:111],v[44:47]\n"
"buffer_load_dwordx4 v214,s[72:75],s39 offen offset:1024 lds\n"
"s_waitcnt lgkmcnt(8)\n"
"v_mfma_f32_16x16x32_bf16 v[48:51],a[56:59],a[80:83],v[48:51]\n"
"ds_read_b64_tr_b16 a[32:33],v254 offset:768\n"
"v_mfma_f32_16x16x32_bf16 v[48:51],a[60:63],a[96:99],v[48:51]\n"
"v_mfma_f32_16x16x32_bf16 v[52:55],a[56:59],a[84:87],v[52:55]\n"
"ds_read_b64_tr_b16 a[34:35],v254 offset:6912\n"
"v_mfma_f32_16x16x32_bf16 v[52:55],a[60:63],a[100:103],v[52:55]\n"
"v_mfma_f32_16x16x32_bf16 v[56:59],a[56:59],a[88:91],v[56:59]\n"
"ds_read_b64_tr_b16 a[36:37],v254 offset:13056\n"
"v_mfma_f32_16x16x32_bf16 v[56:59],a[60:63],a[104:107],v[56:59]\n"
"buffer_load_dwordx4 v215,s[72:75],s39 offen offset:2048 lds\n"
"v_mfma_f32_16x16x32_bf16 v[60:63],a[56:59],a[92:95],v[60:63]\n"
"ds_read_b64_tr_b16 a[38:39],v254 offset:19200\n"
"v_mfma_f32_16x16x32_bf16 v[60:63],a[60:63],a[108:111],v[60:63]\n"
"s_waitcnt lgkmcnt(8)\n"
"v_mfma_f32_16x16x32_bf16 v[64:67],a[64:67],a[80:83],v[64:67]\n"
"s_add_u32 s39,s39,s53\n"
"ds_read_b64_tr_b16 a[40:41],v255 offset:768\n"
"v_mfma_f32_16x16x32_bf16 v[64:67],a[68:71],a[96:99],v[64:67]\n"
"buffer_load_dwordx4 a[160:163],v209,s[88:91],s40 offen sc1\n"
"v_mfma_f32_16x16x32_bf16 v[68:71],a[64:67],a[84:87],v[68:71]\n"
"ds_read_b64_tr_b16 a[42:43],v255 offset:6912\n"
"v_mfma_f32_16x16x32_bf16 v[68:71],a[68:71],a[100:103],v[68:71]\n"
"v_mfma_f32_16x16x32_bf16 v[72:75],a[64:67],a[88:91],v[72:75]\n"
"ds_read_b64_tr_b16 a[44:45],v255 offset:13056\n"
"v_mfma_f32_16x16x32_bf16 v[72:75],a[68:71],a[104:107],v[72:75]\n"
"v_mfma_f32_16x16x32_bf16 v[76:79],a[64:67],a[92:95],v[76:79]\n"
"ds_read_b64_tr_b16 a[46:47],v255 offset:19200\n"
"v_mfma_f32_16x16x32_bf16 v[76:79],a[68:71],a[108:111],v[76:79]\n"
"buffer_load_dwordx4 a[164:167],v209,s[88:91],s40 offen offset:1024 sc1\n"
"s_waitcnt lgkmcnt(8)\n"
"v_mfma_f32_16x16x32_bf16 v[80:83],a[72:75],a[80:83],v[80:83]\n"
"ds_read_b64_tr_b16 a[48:49],v254 offset:1024\n"
"v_mfma_f32_16x16x32_bf16 v[80:83],a[76:79],a[96:99],v[80:83]\n"
"v_mfma_f32_16x16x32_bf16 v[84:87],a[72:75],a[84:87],v[84:87]\n"
"ds_read_b64_tr_b16 a[50:51],v254 offset:7168\n"
"v_mfma_f32_16x16x32_bf16 v[84:87],a[76:79],a[100:103],v[84:87]\n"
"buffer_load_dwordx4 a[168:171],v209,s[88:91],s40 offen offset:2048 sc1\n"
"v_mfma_f32_16x16x32_bf16 v[88:91],a[72:75],a[88:91],v[88:91]\n"
"ds_read_b64_tr_b16 a[52:53],v254 offset:13312\n"
"v_mfma_f32_16x16x32_bf16 v[88:91],a[76:79],a[104:107],v[88:91]\n"
"v_mfma_f32_16x16x32_bf16 v[92:95],a[72:75],a[92:95],v[92:95]\n"
"ds_read_b64_tr_b16 a[54:55],v254 offset:19456\n"
"v_mfma_f32_16x16x32_bf16 v[92:95],a[76:79],a[108:111],v[92:95]\n"
"s_waitcnt vmcnt(41) lgkmcnt(8)\n"
"s_barrier\n"
"v_mfma_f32_16x16x32_bf16 v[96:99],a[32:35],a[80:83],v[96:99]\n"
"ds_read_b64_tr_b16 a[56:57],v254 offset:24576\n"
"v_mfma_f32_16x16x32_bf16 v[96:99],a[36:39],a[96:99],v[96:99]\n"
"buffer_load_dwordx4 a[172:175],v209,s[88:91],s40 offen offset:3072 sc1\n"
"v_mfma_f32_16x16x32_bf16 v[100:103],a[32:35],a[84:87],v[100:103]\n"
"ds_read_b64_tr_b16 a[58:59],v254 offset:30720\n"
"v_mfma_f32_16x16x32_bf16 v[100:103],a[36:39],a[100:103],v[100:103]\n"
"v_mfma_f32_16x16x32_bf16 v[104:107],a[32:35],a[88:91],v[104:107]\n"
"ds_read_b64_tr_b16 a[60:61],v254 offset:36864\n"
"v_mfma_f32_16x16x32_bf16 v[104:107],a[36:39],a[104:107],v[104:107]\n"
"v_mfma_f32_16x16x32_bf16 v[108:111],a[32:35],a[92:95],v[108:111]\n"
"s_add_u32 s40,s40,4096\n"
"ds_read_b64_tr_b16 a[62:63],v254 offset:43008\n"
"v_mfma_f32_16x16x32_bf16 v[108:111],a[36:39],a[108:111],v[108:111]\n"
"buffer_load_dwordx4 a[176:179],v209,s[88:91],s40 offen sc1\n"
"s_waitcnt lgkmcnt(8)\n"
"v_mfma_f32_16x16x32_bf16 v[112:115],a[40:43],a[80:83],v[112:115]\n"
"ds_read_b64_tr_b16 a[64:65],v255 offset:24576\n"
"v_mfma_f32_16x16x32_bf16 v[112:115],a[44:47],a[96:99],v[112:115]\n"
"v_mfma_f32_16x16x32_bf16 v[116:119],a[40:43],a[84:87],v[116:119]\n"
"ds_read_b64_tr_b16 a[66:67],v255 offset:30720\n"
"v_mfma_f32_16x16x32_bf16 v[116:119],a[44:47],a[100:103],v[116:119]\n"
"buffer_load_dwordx4 a[180:183],v209,s[88:91],s40 offen offset:1024 sc1\n"
"v_mfma_f32_16x16x32_bf16 v[120:123],a[40:43],a[88:91],v[120:123]\n"
"ds_read_b64_tr_b16 a[68:69],v255 offset:36864\n"
"v_mfma_f32_16x16x32_bf16 v[120:123],a[44:47],a[104:107],v[120:123]\n"
"v_mfma_f32_16x16x32_bf16 v[124:127],a[40:43],a[92:95],v[124:127]\n"
"ds_read_b64_tr_b16 a[70:71],v255 offset:43008\n"
"v_mfma_f32_16x16x32_bf16 v[124:127],a[44:47],a[108:111],v[124:127]\n"
"s_waitcnt lgkmcnt(8)\n"
"v_mfma_f32_16x16x32_bf16 v[128:131],a[48:51],a[80:83],v[128:131]\n"
"ds_read_b64_tr_b16 a[72:73],v254 offset:24832\n"
"v_mfma_f32_16x16x32_bf16 v[128:131],a[52:55],a[96:99],v[128:131]\n"
"buffer_load_dwordx4 a[184:187],v209,s[88:91],s40 offen offset:2048 sc1\n"
"v_mfma_f32_16x16x32_bf16 v[132:135],a[48:51],a[84:87],v[132:135]\n"
"ds_read_b64_tr_b16 a[74:75],v254 offset:30976\n"
"v_mfma_f32_16x16x32_bf16 v[132:135],a[52:55],a[100:103],v[132:135]\n"
"v_mfma_f32_16x16x32_bf16 v[136:139],a[48:51],a[88:91],v[136:139]\n"
"ds_read_b64_tr_b16 a[76:77],v254 offset:37120\n"
"v_mfma_f32_16x16x32_bf16 v[136:139],a[52:55],a[104:107],v[136:139]\n"
"buffer_load_dwordx4 a[188:191],v209,s[88:91],s40 offen offset:3072 sc1\n"
"v_mfma_f32_16x16x32_bf16 v[140:143],a[48:51],a[92:95],v[140:143]\n"
"ds_read_b64_tr_b16 a[78:79],v254 offset:43264\n"
"v_mfma_f32_16x16x32_bf16 v[140:143],a[52:55],a[108:111],v[140:143]\n"
"s_add_u32 s40,s40,4096\n"
"s_waitcnt lgkmcnt(8)\n"
"v_mfma_f32_16x16x32_bf16 v[0:3],a[56:59],a[128:131],v[0:3]\n"
"ds_read_b64_tr_b16 a[32:33],v255 offset:24832\n"
"v_mfma_f32_16x16x32_bf16 v[0:3],a[60:63],a[144:147],v[0:3]\n"
"v_mfma_f32_16x16x32_bf16 v[4:7],a[56:59],a[132:135],v[4:7]\n"
"s_add_u32 m0,s50,24576\n"
"ds_read_b64_tr_b16 a[34:35],v255 offset:30976\n"
"v_mfma_f32_16x16x32_bf16 v[4:7],a[60:63],a[148:151],v[4:7]\n"
"buffer_load_dwordx4 v213,s[72:75],s39 offen lds\n"
"v_mfma_f32_16x16x32_bf16 v[8:11],a[56:59],a[136:139],v[8:11]\n"
"ds_read_b64_tr_b16 a[36:37],v255 offset:37120\n"
"v_mfma_f32_16x16x32_bf16 v[8:11],a[60:63],a[152:155],v[8:11]\n"
"v_mfma_f32_16x16x32_bf16 v[12:15],a[56:59],a[140:143],v[12:15]\n"
"ds_read_b64_tr_b16 a[38:39],v255 offset:43264\n"
"v_mfma_f32_16x16x32_bf16 v[12:15],a[60:63],a[156:159],v[12:15]\n"
"buffer_load_dwordx4 v214,s[72:75],s39 offen offset:1024 lds\n"
"s_waitcnt lgkmcnt(8)\n"
"v_mfma_f32_16x16x32_bf16 v[16:19],a[64:67],a[128:131],v[16:19]\n"
"ds_read_b64_tr_b16 a[40:41],v254 offset:25088\n"
"v_mfma_f32_16x16x32_bf16 v[16:19],a[68:71],a[144:147],v[16:19]\n"
"v_mfma_f32_16x16x32_bf16 v[20:23],a[64:67],a[132:135],v[20:23]\n"
"ds_read_b64_tr_b16 a[42:43],v254 offset:31232\n"
"v_mfma_f32_16x16x32_bf16 v[20:23],a[68:71],a[148:151],v[20:23]\n"
"v_mfma_f32_16x16x32_bf16 v[24:27],a[64:67],a[136:139],v[24:27]\n"
"ds_read_b64_tr_b16 a[44:45],v254 offset:37376\n"
"v_mfma_f32_16x16x32_bf16 v[24:27],a[68:71],a[152:155],v[24:27]\n"
"buffer_load_dwordx4 v215,s[72:75],s39 offen offset:2048 lds\n"
"v_mfma_f32_16x16x32_bf16 v[28:31],a[64:67],a[140:143],v[28:31]\n"
"ds_read_b64_tr_b16 a[46:47],v254 offset:43520\n"
"v_mfma_f32_16x16x32_bf16 v[28:31],a[68:71],a[156:159],v[28:31]\n"
"s_waitcnt lgkmcnt(8)\n"
"v_mfma_f32_16x16x32_bf16 v[32:35],a[72:75],a[128:131],v[32:35]\n"
"ds_read_b64_tr_b16 a[48:49],v255 offset:25088\n"
"v_mfma_f32_16x16x32_bf16 v[32:35],a[76:79],a[144:147],v[32:35]\n"
"v_mfma_f32_16x16x32_bf16 v[36:39],a[72:75],a[132:135],v[36:39]\n"
"s_add_u32 s39,s39,s52\n"
"s_add_u32 m0,s50,27648\n"
"ds_read_b64_tr_b16 a[50:51],v255 offset:31232\n"
"v_mfma_f32_16x16x32_bf16 v[36:39],a[76:79],a[148:151],v[36:39]\n"
"buffer_load_dwordx4 v213,s[72:75],s39 offen lds\n"
"v_mfma_f32_16x16x32_bf16 v[40:43],a[72:75],a[136:139],v[40:43]\n"
"ds_read_b64_tr_b16 a[52:53],v255 offset:37376\n"
"v_mfma_f32_16x16x32_bf16 v[40:43],a[76:79],a[152:155],v[40:43]\n"
"v_mfma_f32_16x16x32_bf16 v[44:47],a[72:75],a[140:143],v[44:47]\n"
"ds_read_b64_tr_b16 a[54:55],v255 offset:43520\n"
"v_mfma_f32_16x16x32_bf16 v[44:47],a[76:79],a[156:159],v[44:47]\n"
"buffer_load_dwordx4 v214,s[72:75],s39 offen offset:1024 lds\n"
"s_waitcnt lgkmcnt(8)\n"
"v_mfma_f32_16x16x32_bf16 v[48:51],a[32:35],a[128:131],v[48:51]\n"
"ds_read_b64_tr_b16 a[56:57],v254 offset:25344\n"
"v_mfma_f32_16x16x32_bf16 v[48:51],a[36:39],a[144:147],v[48:51]\n"
"v_mfma_f32_16x16x32_bf16 v[52:55],a[32:35],a[132:135],v[52:55]\n"
"ds_read_b64_tr_b16 a[58:59],v254 offset:31488\n"
"v_mfma_f32_16x16x32_bf16 v[52:55],a[36:39],a[148:151],v[52:55]\n"
"v_mfma_f32_16x16x32_bf16 v[56:59],a[32:35],a[136:139],v[56:59]\n"
"ds_read_b64_tr_b16 a[60:61],v254 offset:37632\n"
"v_mfma_f32_16x16x32_bf16 v[56:59],a[36:39],a[152:155],v[56:59]\n"
"buffer_load_dwordx4 v215,s[72:75],s39 offen offset:2048 lds\n"
"v_mfma_f32_16x16x32_bf16 v[60:63],a[32:35],a[140:143],v[60:63]\n"
"ds_read_b64_tr_b16 a[62:63],v254 offset:43776\n"
"v_mfma_f32_16x16x32_bf16 v[60:63],a[36:39],a[156:159],v[60:63]\n"
"v_readlane_b32 s41,v248,8\n"
"s_nop 0\n"
"s_cmp_lg_u32 s41,3\n"
"s_cbranch_scc1 LM_NOEP\n"
"v_readlane_b32 s43,v210,55\n"
"s_and_b32 s43,s43,0x0\n"
"s_add_u32 s43,s43,1024\n"
"s_lshl_b32 s43,s43,6\n"
"v_lshl_add_u32 v223,v209,2,s43\n"
"s_mov_b64 s[46:47],-1\n"
"buffer_load_dwordx2 v[226:227],v223,s[68:71],0 offen sc1\n"
"s_mov_b32 s55,0x40000\n"
"s_waitcnt vmcnt(0)\n"
"LM_POLL23:\n"
"v_cmp_ne_u64 vcc,v[226:227],v[224:225]\n"
"s_nop 1\n"
"s_and_b64 s[44:45],vcc,s[46:47]\n"
"s_cbranch_scc0 LM_POK24\n"
"s_sleep 1\n"
"buffer_load_dwordx2 v[226:227],v223,s[68:71],0 offen sc1\n"
"s_waitcnt vmcnt(0)\n"
"s_sub_u32 s55,s55,1\n"
"s_cbranch_scc0 LM_POLL23\n"
"LM_POK24:\n"
"v_readlane_b32 s43,v210,55\n"
"s_and_b32 s43,s43,0x0\n"
"s_add_u32 s43,s43,1088\n"
"s_lshl_b32 s43,s43,6\n"
"v_lshl_add_u32 v223,v209,2,s43\n"
"s_mov_b64 s[46:47],-1\n"
"buffer_load_dwordx2 v[226:227],v223,s[68:71],0 offen sc1\n"
"s_mov_b32 s55,0x40000\n"
"s_waitcnt vmcnt(0)\n"
"LM_POLL25:\n"
"v_cmp_ne_u64 vcc,v[226:227],v[224:225]\n"
"s_nop 1\n"
"s_and_b64 s[44:45],vcc,s[46:47]\n"
"s_cbranch_scc0 LM_POK26\n"
"s_sleep 1\n"
"buffer_load_dwordx2 v[226:227],v223,s[68:71],0 offen sc1\n"
"s_waitcnt vmcnt(0)\n"
"s_sub_u32 s55,s55,1\n"
"s_cbranch_scc0 LM_POLL25\n"
"LM_POK26:\n"
"v_readlane_b32 s43,v210,55\n"
"s_and_b32 s43,s43,0x0\n"
"s_add_u32 s43,s43,1152\n"
"s_lshl_b32 s43,s43,6\n"
"v_lshl_add_u32 v223,v209,2,s43\n"
"s_mov_b64 s[46:47],-1\n"
"buffer_load_dwordx2 v[226:227],v223,s[68:71],0 offen sc1\n"
"s_mov_b32 s55,0x40000\n"
"s_waitcnt vmcnt(0)\n"
"LM_POLL27:\n"
"v_cmp_ne_u64 vcc,v[226:227],v[224:225]\n"
"s_nop 1\n"
"s_and_b64 s[44:45],vcc,s[46:47]\n"
"s_cbranch_scc0 LM_POK28\n"
"s_sleep 1\n"
"buffer_load_dwordx2 v[226:227],v223,s[68:71],0 offen sc1\n"
"s_waitcnt vmcnt(0)\n"
"s_sub_u32 s55,s55,1\n"
"s_cbranch_scc0 LM_POLL27\n"
"LM_POK28:\n"
"v_readlane_b32 s43,v210,55\n"
"s_and_b32 s43,s43,0x0\n"
"s_add_u32 s43,s43,1216\n"
"s_lshl_b32 s43,s43,6\n"
"v_lshl_add_u32 v223,v209,2,s43\n"
"s_mov_b64 s[46:47],-1\n"
"buffer_load_dwordx2 v[226:227],v223,s[68:71],0 offen sc1\n"
"s_mov_b32 s55,0x40000\n"
"s_waitcnt vmcnt(0)\n"
"LM_POLL29:\n"
"v_cmp_ne_u64 vcc,v[226:227],v[224:225]\n"
"s_nop 1\n"
"s_and_b64 s[44:45],vcc,s[46:47]\n"
"s_cbranch_scc0 LM_POK30\n"
"s_sleep 1\n"
"buffer_load_dwordx2 v[226:227],v223,s[68:71],0 offen sc1\n"
"s_waitcnt vmcnt(0)\n"
"s_sub_u32 s55,s55,1\n"
"s_cbranch_scc0 LM_POLL29\n"
"LM_POK30:\n"
"v_mov_b32 v223,16320\n"
"s_mov_b64 exec,1\n"
"buffer_store_dwordx2 v[224:225],v223,s[68:71],0 offen sc1\n"
"s_mov_b64 exec,-1\n"
"LM_NOEP:\n"
"s_waitcnt lgkmcnt(8)\n"
"v_mfma_f32_16x16x32_bf16 v[64:67],a[40:43],a[128:131],v[64:67]\n"
"s_add_u32 s39,s39,s53\n"
"ds_read_b64_tr_b16 a[64:65],v255 offset:25344\n"
"v_mfma_f32_16x16x32_bf16 v[64:67],a[44:47],a[144:147],v[64:67]\n"
"buffer_load_dwordx4 a[0:3],v209,s[88:91],s40 offen sc1\n"
"v_mfma_f32_16x16x32_bf16 v[68:71],a[40:43],a[132:135],v[68:71]\n"
"ds_read_b64_tr_b16 a[66:67],v255 offset:31488\n"
"v_mfma_f32_16x16x32_bf16 v[68:71],a[44:47],a[148:151],v[68:71]\n"
"v_mfma_f32_16x16x32_bf16 v[72:75],a[40:43],a[136:139],v[72:75]\n"
"ds_read_b64_tr_b16 a[68:69],v255 offset:37632\n"
"v_mfma_f32_16x16x32_bf16 v[72:75],a[44:47],a[152:155],v[72:75]\n"
"v_mfma_f32_16x16x32_bf16 v[76:79],a[40:43],a[140:143],v[76:79]\n"
"ds_read_b64_tr_b16 a[70:71],v255 offset:43776\n"
"v_mfma_f32_16x16x32_bf16 v[76:79],a[44:47],a[156:159],v[76:79]\n"
"buffer_load_dwordx4 a[4:7],v209,s[88:91],s40 offen offset:1024 sc1\n"
"s_waitcnt lgkmcnt(8)\n"
"v_mfma_f32_16x16x32_bf16 v[80:83],a[48:51],a[128:131],v[80:83]\n"
"ds_read_b64_tr_b16 a[72:73],v254 offset:25600\n"
"v_mfma_f32_16x16x32_bf16 v[80:83],a[52:55],a[144:147],v[80:83]\n"
"v_mfma_f32_16x16x32_bf16 v[84:87],a[48:51],a[132:135],v[84:87]\n"
"ds_read_b64_tr_b16 a[74:75],v254 offset:31744\n"
"v_mfma_f32_16x16x32_bf16 v[84:87],a[52:55],a[148:151],v[84:87]\n"
"buffer_load_dwordx4 a[8:11],v209,s[88:91],s40 offen offset:2048 sc1\n"
"v_mfma_f32_16x16x32_bf16 v[88:91],a[48:51],a[136:139],v[88:91]\n"
"ds_read_b64_tr_b16 a[76:77],v254 offset:37888\n"
"v_mfma_f32_16x16x32_bf16 v[88:91],a[52:55],a[152:155],v[88:91]\n"
"v_mfma_f32_16x16x32_bf16 v[92:95],a[48:51],a[140:143],v[92:95]\n"
"ds_read_b64_tr_b16 a[78:79],v254 offset:44032\n"
"v_mfma_f32_16x16x32_bf16 v[92:95],a[52:55],a[156:159],v[92:95]\n"
"s_waitcnt vmcnt(41) lgkmcnt(8)\n"
"s_barrier\n"
"v_mfma_f32_16x16x32_bf16 v[96:99],a[56:59],a[128:131],v[96:99]\n"
"ds_read_b64_tr_b16 a[32:33],v240\n"
"v_mfma_f32_16x16x32_bf16 v[96:99],a[60:63],a[144:147],v[96:99]\n"
"buffer_load_dwordx4 a[12:15],v209,s[88:91],s40 offen offset:3072 sc1\n"
"v_mfma_f32_16x16x32_bf16 v[100:103],a[56:59],a[132:135],v[100:103]\n"
"ds_read_b64_tr_b16 a[34:35],v240 offset:6144\n"
"v_mfma_f32_16x16x32_bf16 v[100:103],a[60:63],a[148:151],v[100:103]\n"
"v_mfma_f32_16x16x32_bf16 v[104:107],a[56:59],a[136:139],v[104:107]\n"
"ds_read_b64_tr_b16 a[36:37],v240 offset:12288\n"
"v_mfma_f32_16x16x32_bf16 v[104:107],a[60:63],a[152:155],v[104:107]\n"
"v_mfma_f32_16x16x32_bf16 v[108:111],a[56:59],a[140:143],v[108:111]\n"
"s_add_u32 s40,s40,4096\n"
"ds_read_b64_tr_b16 a[38:39],v240 offset:18432\n"
"v_mfma_f32_16x16x32_bf16 v[108:111],a[60:63],a[156:159],v[108:111]\n"
"buffer_load_dwordx4 a[16:19],v209,s[88:91],s40 offen sc1\n"
"s_waitcnt lgkmcnt(8)\n"
"v_mfma_f32_16x16x32_bf16 v[112:115],a[64:67],a[128:131],v[112:115]\n"
"ds_read_b64_tr_b16 a[40:41],v241\n"
"v_mfma_f32_16x16x32_bf16 v[112:115],a[68:71],a[144:147],v[112:115]\n"
"v_mfma_f32_16x16x32_bf16 v[116:119],a[64:67],a[132:135],v[116:119]\n"
"ds_read_b64_tr_b16 a[42:43],v241 offset:6144\n"
"v_mfma_f32_16x16x32_bf16 v[116:119],a[68:71],a[148:151],v[116:119]\n"
"buffer_load_dwordx4 a[20:23],v209,s[88:91],s40 offen offset:1024 sc1\n"
"v_mfma_f32_16x16x32_bf16 v[120:123],a[64:67],a[136:139],v[120:123]\n"
"ds_read_b64_tr_b16 a[44:45],v241 offset:12288\n"
"v_mfma_f32_16x16x32_bf16 v[120:123],a[68:71],a[152:155],v[120:123]\n"
"v_mfma_f32_16x16x32_bf16 v[124:127],a[64:67],a[140:143],v[124:127]\n"
"ds_read_b64_tr_b16 a[46:47],v241 offset:18432\n"
"v_mfma_f32_16x16x32_bf16 v[124:127],a[68:71],a[156:159],v[124:127]\n"
"s_waitcnt lgkmcnt(8)\n"
"v_mfma_f32_16x16x32_bf16 v[128:131],a[72:75],a[128:131],v[128:131]\n"
"ds_read_b64_tr_b16 a[48:49],v240 offset:256\n"
"v_mfma_f32_16x16x32_bf16 v[128:131],a[76:79],a[144:147],v[128:131]\n"
"buffer_load_dwordx4 a[24:27],v209,s[88:91],s40 offen offset:2048 sc1\n"
"v_mfma_f32_16x16x32_bf16 v[132:135],a[72:75],a[132:135],v[132:135]\n"
"ds_read_b64_tr_b16 a[50:51],v240 offset:6400\n"
"v_mfma_f32_16x16x32_bf16 v[132:135],a[76:79],a[148:151],v[132:135]\n"
"v_mfma_f32_16x16x32_bf16 v[136:139],a[72:75],a[136:139],v[136:139]\n"
"ds_read_b64_tr_b16 a[52:53],v240 offset:12544\n"
"v_mfma_f32_16x16x32_bf16 v[136:139],a[76:79],a[152:155],v[136:139]\n"
"buffer_load_dwordx4 a[28:31],v209,s[88:91],s40 offen offset:3072 sc1\n"
"v_mfma_f32_16x16x32_bf16 v[140:143],a[72:75],a[140:143],v[140:143]\n"
"ds_read_b64_tr_b16 a[54:55],v240 offset:18688\n"
"v_mfma_f32_16x16x32_bf16 v[140:143],a[76:79],a[156:159],v[140:143]\n"
"s_add_u32 s40,s40,4096\n"
"s_waitcnt lgkmcnt(8)\n"
"v_mfma_f32_16x16x32_bf16 v[0:3],a[32:35],a[192:195],v[0:3]\n"
"ds_read_b64_tr_b16 a[56:57],v241 offset:256\n"
"v_mfma_f32_16x16x32_bf16 v[0:3],a[36:39],a[240:243],v[0:3]\n"
"v_mfma_f32_16x16x32_bf16 v[4:7],a[32:35],a[196:199],v[4:7]\n"
"s_add_u32 m0,s62,0\n"
"ds_read_b64_tr_b16 a[58:59],v241 offset:6400\n"
"v_mfma_f32_16x16x32_bf16 v[4:7],a[36:39],a[244:247],v[4:7]\n"
"buffer_load_dwordx4 v213,s[72:75],s39 offen lds\n"
"v_mfma_f32_16x16x32_bf16 v[8:11],a[32:35],a[200:203],v[8:11]\n"
"ds_read_b64_tr_b16 a[60:61],v241 offset:12544\n"
"v_mfma_f32_16x16x32_bf16 v[8:11],a[36:39],a[248:251],v[8:11]\n"
"v_mfma_f32_16x16x32_bf16 v[12:15],a[32:35],a[204:207],v[12:15]\n"
"ds_read_b64_tr_b16 a[62:63],v241 offset:18688\n"
"v_mfma_f32_16x16x32_bf16 v[12:15],a[36:39],a[252:255],v[12:15]\n"
"buffer_load_dwordx4 v214,s[72:75],s39 offen offset:1024 lds\n"
"s_waitcnt lgkmcnt(8)\n"
"v_mfma_f32_16x16x32_bf16 v[16:19],a[40:43],a[192:195],v[16:19]\n"
"ds_read_b64_tr_b16 a[64:65],v240 offset:512\n"
"v_mfma_f32_16x16x32_bf16 v[16:19],a[44:47],a[240:243],v[16:19]\n"
"v_mfma_f32_16x16x32_bf16 v[20:23],a[40:43],a[196:199],v[20:23]\n"
"ds_read_b64_tr_b16 a[66:67],v240 offset:6656\n"
"v_mfma_f32_16x16x32_bf16 v[20:23],a[44:47],a[244:247],v[20:23]\n"
"v_mfma_f32_16x16x32_bf16 v[24:27],a[40:43],a[200:203],v[24:27]\n"
"ds_read_b64_tr_b16 a[68:69],v240 offset:12800\n"
"v_mfma_f32_16x16x32_bf16 v[24:27],a[44:47],a[248:251],v[24:27]\n"
"buffer_load_dwordx4 v215,s[72:75],s39 offen offset:2048 lds\n"
"v_mfma_f32_16x16x32_bf16 v[28:31],a[40:43],a[204:207],v[28:31]\n"
"ds_read_b64_tr_b16 a[70:71],v240 offset:18944\n"
"v_mfma_f32_16x16x32_bf16 v[28:31],a[44:47],a[252:255],v[28:31]\n"
"s_waitcnt lgkmcnt(8)\n"
"v_mfma_f32_16x16x32_bf16 v[32:35],a[48:51],a[192:195],v[32:35]\n"
"ds_read_b64_tr_b16 a[72:73],v241 offset:512\n"
"v_mfma_f32_16x16x32_bf16 v[32:35],a[52:55],a[240:243],v[32:35]\n"
"v_mfma_f32_16x16x32_bf16 v[36:39],a[48:51],a[196:199],v[36:39]\n"
"s_add_u32 s39,s39,s52\n"
"s_add_u32 m0,s62,3072\n"
"ds_read_b64_tr_b16 a[74:75],v241 offset:6656\n"
"v_mfma_f32_16x16x32_bf16 v[36:39],a[52:55],a[244:247],v[36:39]\n"
"buffer_load_dwordx4 v213,s[72:75],s39 offen lds\n"
"v_mfma_f32_16x16x32_bf16 v[40:43],a[48:51],a[200:203],v[40:43]\n"
"ds_read_b64_tr_b16 a[76:77],v241 offset:12800\n"
"v_mfma_f32_16x16x32_bf16 v[40:43],a[52:55],a[248:251],v[40:43]\n"
"v_mfma_f32_16x16x32_bf16 v[44:47],a[48:51],a[204:207],v[44:47]\n"
"ds_read_b64_tr_b16 a[78:79],v241 offset:18944\n"
"v_mfma_f32_16x16x32_bf16 v[44:47],a[52:55],a[252:255],v[44:47]\n"
"buffer_load_dwordx4 v214,s[72:75],s39 offen offset:1024 lds\n"
"s_waitcnt lgkmcnt(8)\n"
"v_mfma_f32_16x16x32_bf16 v[48:51],a[56:59],a[192:195],v[48:51]\n"
"ds_read_b64_tr_b16 a[32:33],v240 offset:768\n"
"v_mfma_f32_16x16x32_bf16 v[48:51],a[60:63],a[240:243],v[48:51]\n"
"v_mfma_f32_16x16x32_bf16 v[52:55],a[56:59],a[196:199],v[52:55]\n"
"ds_read_b64_tr_b16 a[34:35],v240 offset:6912\n"
"v_mfma_f32_16x16x32_bf16 v[52:55],a[60:63],a[244:247],v[52:55]\n"
"v_mfma_f32_16x16x32_bf16 v[56:59],a[56:59],a[200:203],v[56:59]\n"
"ds_read_b64_tr_b16 a[36:37],v240 offset:13056\n"
"v_mfma_f32_16x16x32_bf16 v[56:59],a[60:63],a[248:251],v[56:59]\n"
"buffer_load_dwordx4 v215,s[72:75],s39 offen offset:2048 lds\n"
"v_mfma_f32_16x16x32_bf16 v[60:63],a[56:59],a[204:207],v[60:63]\n"
"ds_read_b64_tr_b16 a[38:39],v240 offset:19200\n"
"v_mfma_f32_16x16x32_bf16 v[60:63],a[60:63],a[252:255],v[60:63]\n"
"s_waitcnt lgkmcnt(8)\n"
"v_mfma_f32_16x16x32_bf16 v[64:67],a[64:67],a[192:195],v[64:67]\n"
"s_add_u32 s39,s39,s53\n"
"ds_read_b64_tr_b16 a[40:41],v241 offset:768\n"
"v_mfma_f32_16x16x32_bf16 v[64:67],a[68:71],a[240:243],v[64:67]\n"
"buffer_load_dwordx4 a[80:83],v209,s[88:91],s40 offen sc1\n"
"v_mfma_f32_16x16x32_bf16 v[68:71],a[64:67],a[196:199],v[68:71]\n"
"ds_read_b64_tr_b16 a[42:43],v241 offset:6912\n"
"v_mfma_f32_16x16x32_bf16 v[68:71],a[68:71],a[244:247],v[68:71]\n"
"v_mfma_f32_16x16x32_bf16 v[72:75],a[64:67],a[200:203],v[72:75]\n"
"ds_read_b64_tr_b16 a[44:45],v241 offset:13056\n"
"v_mfma_f32_16x16x32_bf16 v[72:75],a[68:71],a[248:251],v[72:75]\n"
"v_mfma_f32_16x16x32_bf16 v[76:79],a[64:67],a[204:207],v[76:79]\n"
"ds_read_b64_tr_b16 a[46:47],v241 offset:19200\n"
"v_mfma_f32_16x16x32_bf16 v[76:79],a[68:71],a[252:255],v[76:79]\n"
"buffer_load_dwordx4 a[84:87],v209,s[88:91],s40 offen offset:1024 sc1\n"
"s_waitcnt lgkmcnt(8)\n"
"v_mfma_f32_16x16x32_bf16 v[80:83],a[72:75],a[192:195],v[80:83]\n"
"ds_read_b64_tr_b16 a[48:49],v240 offset:1024\n"
"v_mfma_f32_16x16x32_bf16 v[80:83],a[76:79],a[240:243],v[80:83]\n"
"v_mfma_f32_16x16x32_bf16 v[84:87],a[72:75],a[196:199],v[84:87]\n"
"ds_read_b64_tr_b16 a[50:51],v240 offset:7168\n"
"v_mfma_f32_16x16x32_bf16 v[84:87],a[76:79],a[244:247],v[84:87]\n"
"buffer_load_dwordx4 a[88:91],v209,s[88:91],s40 offen offset:2048 sc1\n"
"v_mfma_f32_16x16x32_bf16 v[88:91],a[72:75],a[200:203],v[88:91]\n"
"ds_read_b64_tr_b16 a[52:53],v240 offset:13312\n"
"v_mfma_f32_16x16x32_bf16 v[88:91],a[76:79],a[248:251],v[88:91]\n"
"v_mfma_f32_16x16x32_bf16 v[92:95],a[72:75],a[204:207],v[92:95]\n"
"ds_read_b64_tr_b16 a[54:55],v240 offset:19456\n"
"v_mfma_f32_16x16x32_bf16 v[92:95],a[76:79],a[252:255],v[92:95]\n"
"s_waitcnt vmcnt(37) lgkmcnt(8)\n"
"s_barrier\n"
"v_mfma_f32_16x16x32_bf16 v[96:99],a[32:35],a[192:195],v[96:99]\n"
"ds_read_b64_tr_b16 a[56:57],v240 offset:24576\n"
"v_mfma_f32_16x16x32_bf16 v[96:99],a[36:39],a[240:243],v[96:99]\n"
"buffer_load_dwordx4 a[92:95],v209,s[88:91],s40 offen offset:3072 sc1\n"
"v_mfma_f32_16x16x32_bf16 v[100:103],a[32:35],a[196:199],v[100:103]\n"
"ds_read_b64_tr_b16 a[58:59],v240 offset:30720\n"
"v_mfma_f32_16x16x32_bf16 v[100:103],a[36:39],a[244:247],v[100:103]\n"
"v_mfma_f32_16x16x32_bf16 v[104:107],a[32:35],a[200:203],v[104:107]\n"
"ds_read_b64_tr_b16 a[60:61],v240 offset:36864\n"
"v_mfma_f32_16x16x32_bf16 v[104:107],a[36:39],a[248:251],v[104:107]\n"
"v_mfma_f32_16x16x32_bf16 v[108:111],a[32:35],a[204:207],v[108:111]\n"
"s_add_u32 s40,s40,4096\n"
"ds_read_b64_tr_b16 a[62:63],v240 offset:43008\n"
"v_mfma_f32_16x16x32_bf16 v[108:111],a[36:39],a[252:255],v[108:111]\n"
"buffer_load_dwordx4 a[96:99],v209,s[88:91],s40 offen sc1\n"
"s_waitcnt lgkmcnt(8)\n"
"v_mfma_f32_16x16x32_bf16 v[112:115],a[40:43],a[192:195],v[112:115]\n"
"ds_read_b64_tr_b16 a[64:65],v241 offset:24576\n"
"v_mfma_f32_16x16x32_bf16 v[112:115],a[44:47],a[240:243],v[112:115]\n"
"v_mfma_f32_16x16x32_bf16 v[116:119],a[40:43],a[196:199],v[116:119]\n"
"ds_read_b64_tr_b16 a[66:67],v241 offset:30720\n"
"v_mfma_f32_16x16x32_bf16 v[116:119],a[44:47],a[244:247],v[116:119]\n"
"buffer_load_dwordx4 a[100:103],v209,s[88:91],s40 offen offset:1024 sc1\n"
"v_mfma_f32_16x16x32_bf16 v[120:123],a[40:43],a[200:203],v[120:123]\n"
"ds_read_b64_tr_b16 a[68:69],v241 offset:36864\n"
"v_mfma_f32_16x16x32_bf16 v[120:123],a[44:47],a[248:251],v[120:123]\n"
"v_mfma_f32_16x16x32_bf16 v[124:127],a[40:43],a[204:207],v[124:127]\n"
"ds_read_b64_tr_b16 a[70:71],v241 offset:43008\n"
"v_mfma_f32_16x16x32_bf16 v[124:127],a[44:47],a[252:255],v[124:127]\n"
"s_waitcnt lgkmcnt(8)\n"
"v_mfma_f32_16x16x32_bf16 v[128:131],a[48:51],a[192:195],v[128:131]\n"
"ds_read_b64_tr_b16 a[72:73],v240 offset:24832\n"
"v_mfma_f32_16x16x32_bf16 v[128:131],a[52:55],a[240:243],v[128:131]\n"
"buffer_load_dwordx4 a[104:107],v209,s[88:91],s40 offen offset:2048 sc1\n"
"v_mfma_f32_16x16x32_bf16 v[132:135],a[48:51],a[196:199],v[132:135]\n"
"ds_read_b64_tr_b16 a[74:75],v240 offset:30976\n"
"v_mfma_f32_16x16x32_bf16 v[132:135],a[52:55],a[244:247],v[132:135]\n"
"v_mfma_f32_16x16x32_bf16 v[136:139],a[48:51],a[200:203],v[136:139]\n"
"ds_read_b64_tr_b16 a[76:77],v240 offset:37120\n"
"v_mfma_f32_16x16x32_bf16 v[136:139],a[52:55],a[248:251],v[136:139]\n"
"buffer_load_dwordx4 a[108:111],v209,s[88:91],s40 offen offset:3072 sc1\n"
"v_mfma_f32_16x16x32_bf16 v[140:143],a[48:51],a[204:207],v[140:143]\n"
"ds_read_b64_tr_b16 a[78:79],v240 offset:43264\n"
"v_mfma_f32_16x16x32_bf16 v[140:143],a[52:55],a[252:255],v[140:143]\n"
"s_add_u32 s40,s40,4096\n"
"s_waitcnt lgkmcnt(8)\n"
"v_mfma_f32_16x16x32_bf16 v[0:3],a[56:59],a[208:211],v[0:3]\n"
"ds_read_b64_tr_b16 a[32:33],v241 offset:24832\n"
"v_mfma_f32_16x16x32_bf16 v[0:3],a[60:63],a[224:227],v[0:3]\n"
"v_mfma_f32_16x16x32_bf16 v[4:7],a[56:59],a[212:215],v[4:7]\n"
"s_add_u32 m0,s62,24576\n"
"ds_read_b64_tr_b16 a[34:35],v241 offset:30976\n"
"v_mfma_f32_16x16x32_bf16 v[4:7],a[60:63],a[228:231],v[4:7]\n"
"buffer_load_dwordx4 v213,s[72:75],s39 offen lds\n"
"v_mfma_f32_16x16x32_bf16 v[8:11],a[56:59],a[216:219],v[8:11]\n"
"ds_read_b64_tr_b16 a[36:37],v241 offset:37120\n"
"v_mfma_f32_16x16x32_bf16 v[8:11],a[60:63],a[232:235],v[8:11]\n"
"v_mfma_f32_16x16x32_bf16 v[12:15],a[56:59],a[220:223],v[12:15]\n"
"ds_read_b64_tr_b16 a[38:39],v241 offset:43264\n"
"v_mfma_f32_16x16x32_bf16 v[12:15],a[60:63],a[236:239],v[12:15]\n"
"buffer_load_dwordx4 v214,s[72:75],s39 offen offset:1024 lds\n"
"s_waitcnt lgkmcnt(8)\n"
"v_mfma_f32_16x16x32_bf16 v[16:19],a[64:67],a[208:211],v[16:19]\n"
"ds_read_b64_tr_b16 a[40:41],v240 offset:25088\n"
"v_mfma_f32_16x16x32_bf16 v[16:19],a[68:71],a[224:227],v[16:19]\n"
"v_mfma_f32_16x16x32_bf16 v[20:23],a[64:67],a[212:215],v[20:23]\n"
"ds_read_b64_tr_b16 a[42:43],v240 offset:31232\n"
"v_mfma_f32_16x16x32_bf16 v[20:23],a[68:71],a[228:231],v[20:23]\n"
"buffer_load_dwordx4 v215,s[72:75],s39 offen offset:2048 lds\n"
"v_mfma_f32_16x16x32_bf16 v[24:27],a[64:67],a[216:219],v[24:27]\n"
"ds_read_b64_tr_b16 a[44:45],v240 offset:37376\n"
"v_mfma_f32_16x16x32_bf16 v[24:27],a[68:71],a[232:235],v[24:27]\n"
"v_mfma_f32_16x16x32_bf16 v[28:31],a[64:67],a[220:223],v[28:31]\n"
"s_add_u32 s39,s39,s52\n"
"s_add_u32 m0,s62,27648\n"
"ds_read_b64_tr_b16 a[46:47],v240 offset:43520\n"
"v_mfma_f32_16x16x32_bf16 v[28:31],a[68:71],a[236:239],v[28:31]\n"
"buffer_load_dwordx4 v213,s[72:75],s39 offen lds\n"
"s_waitcnt lgkmcnt(8)\n"
"v_mfma_f32_16x16x32_bf16 v[32:35],a[72:75],a[208:211],v[32:35]\n"
"ds_read_b64_tr_b16 a[48:49],v241 offset:25088\n"
"v_mfma_f32_16x16x32_bf16 v[32:35],a[76:79],a[224:227],v[32:35]\n"
"v_mfma_f32_16x16x32_bf16 v[36:39],a[72:75],a[212:215],v[36:39]\n"
"ds_read_b64_tr_b16 a[50:51],v241 offset:31232\n"
"v_mfma_f32_16x16x32_bf16 v[36:39],a[76:79],a[228:231],v[36:39]\n"
"buffer_load_dwordx4 v214,s[72:75],s39 offen offset:1024 lds\n"
"v_mfma_f32_16x16x32_bf16 v[40:43],a[72:75],a[216:219],v[40:43]\n"
"ds_read_b64_tr_b16 a[52:53],v241 offset:37376\n"
"v_mfma_f32_16x16x32_bf16 v[40:43],a[76:79],a[232:235],v[40:43]\n"
"v_mfma_f32_16x16x32_bf16 v[44:47],a[72:75],a[220:223],v[44:47]\n"
"ds_read_b64_tr_b16 a[54:55],v241 offset:43520\n"
"v_mfma_f32_16x16x32_bf16 v[44:47],a[76:79],a[236:239],v[44:47]\n"
"buffer_load_dwordx4 v215,s[72:75],s39 offen offset:2048 lds\n"
"s_waitcnt lgkmcnt(8)\n"
"v_mfma_f32_16x16x32_bf16 v[48:51],a[32:35],a[208:211],v[48:51]\n"
"ds_read_b64_tr_b16 a[56:57],v240 offset:25344\n"
"v_mfma_f32_16x16x32_bf16 v[48:51],a[36:39],a[224:227],v[48:51]\n"
"v_mfma_f32_16x16x32_bf16 v[52:55],a[32:35],a[212:215],v[52:55]\n"
"s_add_u32 s39,s39,s53\n"
"ds_read_b64_tr_b16 a[58:59],v240 offset:31488\n"
"v_mfma_f32_16x16x32_bf16 v[52:55],a[36:39],a[228:231],v[52:55]\n"
"buffer_load_dwordx4 a[128:131],v209,s[88:91],s40 offen sc1\n"
"v_mfma_f32_16x16x32_bf16 v[56:59],a[32:35],a[216:219],v[56:59]\n"
"ds_read_b64_tr_b16 a[60:61],v240 offset:37632\n"
"v_mfma_f32_16x16x32_bf16 v[56:59],a[36:39],a[232:235],v[56:59]\n"
"v_mfma_f32_16x16x32_bf16 v[60:63],a[32:35],a[220:223],v[60:63]\n"
"ds_read_b64_tr_b16 a[62:63],v240 offset:43776\n"
"v_mfma_f32_16x16x32_bf16 v[60:63],a[36:39],a[236:239],v[60:63]\n"
"buffer_load_dwordx4 a[132:135],v209,s[88:91],s40 offen offset:1024 sc1\n"
"s_waitcnt lgkmcnt(8)\n"
"v_mfma_f32_16x16x32_bf16 v[64:67],a[40:43],a[208:211],v[64:67]\n"
"ds_read_b64_tr_b16 a[64:65],v241 offset:25344\n"
"v_mfma_f32_16x16x32_bf16 v[64:67],a[44:47],a[224:227],v[64:67]\n"
"v_mfma_f32_16x16x32_bf16 v[68:71],a[40:43],a[212:215],v[68:71]\n"
"ds_read_b64_tr_b16 a[66:67],v241 offset:31488\n"
"v_mfma_f32_16x16x32_bf16 v[68:71],a[44:47],a[228:231],v[68:71]\n"
"buffer_load_dwordx4 a[136:139],v209,s[88:91],s40 offen offset:2048 sc1\n"
"v_mfma_f32_16x16x32_bf16 v[72:75],a[40:43],a[216:219],v[72:75]\n"
"ds_read_b64_tr_b16 a[68:69],v241 offset:37632\n"
"v_mfma_f32_16x16x32_bf16 v[72:75],a[44:47],a[232:235],v[72:75]\n"
"v_mfma_f32_16x16x32_bf16 v[76:79],a[40:43],a[220:223],v[76:79]\n"
"ds_read_b64_tr_b16 a[70:71],v241 offset:43776\n"
"v_mfma_f32_16x16x32_bf16 v[76:79],a[44:47],a[236:239],v[76:79]\n"
"buffer_load_dwordx4 a[140:143],v209,s[88:91],s40 offen offset:3072 sc1\n"
"s_waitcnt lgkmcnt(8)\n"
"v_mfma_f32_16x16x32_bf16 v[80:83],a[48:51],a[208:211],v[80:83]\n"
"ds_read_b64_tr_b16 a[72:73],v240 offset:25600\n"
"v_mfma_f32_16x16x32_bf16 v[80:83],a[52:55],a[224:227],v[80:83]\n"
"v_mfma_f32_16x16x32_bf16 v[84:87],a[48:51],a[212:215],v[84:87]\n"
"s_add_u32 s40,s40,4096\n"
"ds_read_b64_tr_b16 a[74:75],v240 offset:31744\n"
"v_mfma_f32_16x16x32_bf16 v[84:87],a[52:55],a[228:231],v[84:87]\n"
"buffer_load_dwordx2 a[144:145],v188,s[88:91],s40 offen sc1\n"
"v_mfma_f32_16x16x32_bf16 v[88:91],a[48:51],a[216:219],v[88:91]\n"
"ds_read_b64_tr_b16 a[76:77],v240 offset:37888\n"
"v_mfma_f32_16x16x32_bf16 v[88:91],a[52:55],a[232:235],v[88:91]\n"
"v_mfma_f32_16x16x32_bf16 v[92:95],a[48:51],a[220:223],v[92:95]\n"
"ds_read_b64_tr_b16 a[78:79],v240 offset:44032\n"
"v_mfma_f32_16x16x32_bf16 v[92:95],a[52:55],a[236:239],v[92:95]\n"
"buffer_load_dwordx2 a[146:147],v189,s[88:91],s40 offen sc1\n"
"s_waitcnt vmcnt(40) lgkmcnt(8)\n"
"s_barrier\n"
"v_mfma_f32_16x16x32_bf16 v[96:99],a[56:59],a[208:211],v[96:99]\n"
"ds_read_b64_tr_b16 a[32:33],v252\n"
"v_mfma_f32_16x16x32_bf16 v[96:99],a[60:63],a[224:227],v[96:99]\n"
"v_mfma_f32_16x16x32_bf16 v[100:103],a[56:59],a[212:215],v[100:103]\n"
"ds_read_b64_tr_b16 a[34:35],v252 offset:6144\n"
"v_mfma_f32_16x16x32_bf16 v[100:103],a[60:63],a[228:231],v[100:103]\n"
"buffer_load_dwordx2 a[148:149],v188,s[88:91],s40 offen offset:1024 sc1\n"
"v_mfma_f32_16x16x32_bf16 v[104:107],a[56:59],a[216:219],v[104:107]\n"
"ds_read_b64_tr_b16 a[36:37],v252 offset:12288\n"
"v_mfma_f32_16x16x32_bf16 v[104:107],a[60:63],a[232:235],v[104:107]\n"
"v_mfma_f32_16x16x32_bf16 v[108:111],a[56:59],a[220:223],v[108:111]\n"
"ds_read_b64_tr_b16 a[38:39],v252 offset:18432\n"
"v_mfma_f32_16x16x32_bf16 v[108:111],a[60:63],a[236:239],v[108:111]\n"
"buffer_load_dwordx2 a[150:151],v189,s[88:91],s40 offen offset:1024 sc1\n"
"s_waitcnt lgkmcnt(8)\n"
"v_mfma_f32_16x16x32_bf16 v[112:115],a[64:67],a[208:211],v[112:115]\n"
"ds_read_b64_tr_b16 a[40:41],v253\n"
"v_mfma_f32_16x16x32_bf16 v[112:115],a[68:71],a[224:227],v[112:115]\n"
"v_mfma_f32_16x16x32_bf16 v[116:119],a[64:67],a[212:215],v[116:119]\n"
"ds_read_b64_tr_b16 a[42:43],v253 offset:6144\n"
"v_mfma_f32_16x16x32_bf16 v[116:119],a[68:71],a[228:231],v[116:119]\n"
"buffer_load_dwordx2 a[152:153],v188,s[88:91],s40 offen offset:2048 sc1\n"
"v_mfma_f32_16x16x32_bf16 v[120:123],a[64:67],a[216:219],v[120:123]\n"
"ds_read_b64_tr_b16 a[44:45],v253 offset:12288\n"
"v_mfma_f32_16x16x32_bf16 v[120:123],a[68:71],a[232:235],v[120:123]\n"
"v_mfma_f32_16x16x32_bf16 v[124:127],a[64:67],a[220:223],v[124:127]\n"
"ds_read_b64_tr_b16 a[46:47],v253 offset:18432\n"
"v_mfma_f32_16x16x32_bf16 v[124:127],a[68:71],a[236:239],v[124:127]\n"
"buffer_load_dwordx2 a[154:155],v189,s[88:91],s40 offen offset:2048 sc1\n"
"s_waitcnt lgkmcnt(8)\n"
"v_mfma_f32_16x16x32_bf16 v[128:131],a[72:75],a[208:211],v[128:131]\n"
"ds_read_b64_tr_b16 a[48:49],v252 offset:256\n"
"v_mfma_f32_16x16x32_bf16 v[128:131],a[76:79],a[224:227],v[128:131]\n"
"v_mfma_f32_16x16x32_bf16 v[132:135],a[72:75],a[212:215],v[132:135]\n"
"ds_read_b64_tr_b16 a[50:51],v252 offset:6400\n"
"v_mfma_f32_16x16x32_bf16 v[132:135],a[76:79],a[228:231],v[132:135]\n"
"buffer_load_dwordx2 a[156:157],v188,s[88:91],s40 offen offset:3072 sc1\n"
"v_mfma_f32_16x16x32_bf16 v[136:139],a[72:75],a[216:219],v[136:139]\n"
"ds_read_b64_tr_b16 a[52:53],v252 offset:12544\n"
"v_mfma_f32_16x16x32_bf16 v[136:139],a[76:79],a[232:235],v[136:139]\n"
"v_mfma_f32_16x16x32_bf16 v[140:143],a[72:75],a[220:223],v[140:143]\n"
"ds_read_b64_tr_b16 a[54:55],v252 offset:18688\n"
"v_mfma_f32_16x16x32_bf16 v[140:143],a[76:79],a[236:239],v[140:143]\n"
"buffer_load_dwordx2 a[158:159],v189,s[88:91],s40 offen offset:3072 sc1\n"
"s_add_u32 s40,s40,4096\n"
"s_waitcnt lgkmcnt(8)\n"
"v_mfma_f32_16x16x32_bf16 v[0:3],a[32:35],a[160:163],v[0:3]\n"
"ds_read_b64_tr_b16 a[56:57],v253 offset:256\n"
"v_mfma_f32_16x16x32_bf16 v[0:3],a[36:39],a[176:179],v[0:3]\n"
"v_mfma_f32_16x16x32_bf16 v[4:7],a[32:35],a[164:167],v[4:7]\n"
"s_add_u32 m0,s63,0\n"
"ds_read_b64_tr_b16 a[58:59],v253 offset:6400\n"
"v_mfma_f32_16x16x32_bf16 v[4:7],a[36:39],a[180:183],v[4:7]\n"
"buffer_load_dwordx4 v213,s[72:75],s39 offen lds\n"
"v_mfma_f32_16x16x32_bf16 v[8:11],a[32:35],a[168:171],v[8:11]\n"
"ds_read_b64_tr_b16 a[60:61],v253 offset:12544\n"
"v_mfma_f32_16x16x32_bf16 v[8:11],a[36:39],a[184:187],v[8:11]\n"
"v_mfma_f32_16x16x32_bf16 v[12:15],a[32:35],a[172:175],v[12:15]\n"
"ds_read_b64_tr_b16 a[62:63],v253 offset:18688\n"
"v_mfma_f32_16x16x32_bf16 v[12:15],a[36:39],a[188:191],v[12:15]\n"
"buffer_load_dwordx4 v214,s[72:75],s39 offen offset:1024 lds\n"
"s_waitcnt lgkmcnt(8)\n"
"v_mfma_f32_16x16x32_bf16 v[16:19],a[40:43],a[160:163],v[16:19]\n"
"ds_read_b64_tr_b16 a[64:65],v252 offset:512\n"
"v_mfma_f32_16x16x32_bf16 v[16:19],a[44:47],a[176:179],v[16:19]\n"
"v_mfma_f32_16x16x32_bf16 v[20:23],a[40:43],a[164:167],v[20:23]\n"
"ds_read_b64_tr_b16 a[66:67],v252 offset:6656\n"
"v_mfma_f32_16x16x32_bf16 v[20:23],a[44:47],a[180:183],v[20:23]\n"
"v_mfma_f32_16x16x32_bf16 v[24:27],a[40:43],a[168:171],v[24:27]\n"
"ds_read_b64_tr_b16 a[68:69],v252 offset:12800\n"
"v_mfma_f32_16x16x32_bf16 v[24:27],a[44:47],a[184:187],v[24:27]\n"
"buffer_load_dwordx4 v215,s[72:75],s39 offen offset:2048 lds\n"
"v_mfma_f32_16x16x32_bf16 v[28:31],a[40:43],a[172:175],v[28:31]\n"
"ds_read_b64_tr_b16 a[70:71],v252 offset:18944\n"
"v_mfma_f32_16x16x32_bf16 v[28:31],a[44:47],a[188:191],v[28:31]\n"
"s_waitcnt lgkmcnt(8)\n"
"v_mfma_f32_16x16x32_bf16 v[32:35],a[48:51],a[160:163],v[32:35]\n"
"ds_read_b64_tr_b16 a[72:73],v253 offset:512\n"
"v_mfma_f32_16x16x32_bf16 v[32:35],a[52:55],a[176:179],v[32:35]\n"
"v_mfma_f32_16x16x32_bf16 v[36:39],a[48:51],a[164:167],v[36:39]\n"
"s_add_u32 s39,s39,s52\n"
"s_add_u32 m0,s63,3072\n"
"ds_read_b64_tr_b16 a[74:75],v253 offset:6656\n"
"v_mfma_f32_16x16x32_bf16 v[36:39],a[52:55],a[180:183],v[36:39]\n"
"buffer_load_dwordx4 v213,s[72:75],s39 offen lds\n"
"v_mfma_f32_16x16x32_bf16 v[40:43],a[48:51],a[168:171],v[40:43]\n"
"ds_read_b64_tr_b16 a[76:77],v253 offset:12800\n"
"v_mfma_f32_16x16x32_bf16 v[40:43],a[52:55],a[184:187],v[40:43]\n"
"v_mfma_f32_16x16x32_bf16 v[44:47],a[48:51],a[172:175],v[44:47]\n"
"ds_read_b64_tr_b16 a[78:79],v253 offset:18944\n"
"v_mfma_f32_16x16x32_bf16 v[44:47],a[52:55],a[188:191],v[44:47]\n"
"buffer_load_dwordx4 v214,s[72:75],s39 offen offset:1024 lds\n"
"s_waitcnt lgkmcnt(8)\n"
"v_mfma_f32_16x16x32_bf16 v[48:51],a[56:59],a[160:163],v[48:51]\n"
"ds_read_b64_tr_b16 a[32:33],v252 offset:768\n"
"v_mfma_f32_16x16x32_bf16 v[48:51],a[60:63],a[176:179],v[48:51]\n"
"v_mfma_f32_16x16x32_bf16 v[52:55],a[56:59],a[164:167],v[52:55]\n"
"ds_read_b64_tr_b16 a[34:35],v252 offset:6912\n"
"v_mfma_f32_16x16x32_bf16 v[52:55],a[60:63],a[180:183],v[52:55]\n"
"v_mfma_f32_16x16x32_bf16 v[56:59],a[56:59],a[168:171],v[56:59]\n"
"ds_read_b64_tr_b16 a[36:37],v252 offset:13056\n"
"v_mfma_f32_16x16x32_bf16 v[56:59],a[60:63],a[184:187],v[56:59]\n"
"buffer_load_dwordx4 v215,s[72:75],s39 offen offset:2048 lds\n"
"v_mfma_f32_16x16x32_bf16 v[60:63],a[56:59],a[172:175],v[60:63]\n"
"ds_read_b64_tr_b16 a[38:39],v252 offset:19200\n"
"v_mfma_f32_16x16x32_bf16 v[60:63],a[60:63],a[188:191],v[60:63]\n"
"s_waitcnt lgkmcnt(8)\n"
"v_mfma_f32_16x16x32_bf16 v[64:67],a[64:67],a[160:163],v[64:67]\n"
"s_add_u32 s39,s39,s53\n"
"ds_read_b64_tr_b16 a[40:41],v253 offset:768\n"
"v_mfma_f32_16x16x32_bf16 v[64:67],a[68:71],a[176:179],v[64:67]\n"
"buffer_load_dwordx4 a[192:195],v209,s[88:91],s40 offen sc1\n"
"v_mfma_f32_16x16x32_bf16 v[68:71],a[64:67],a[164:167],v[68:71]\n"
"ds_read_b64_tr_b16 a[42:43],v253 offset:6912\n"
"v_mfma_f32_16x16x32_bf16 v[68:71],a[68:71],a[180:183],v[68:71]\n"
"v_mfma_f32_16x16x32_bf16 v[72:75],a[64:67],a[168:171],v[72:75]\n"
"ds_read_b64_tr_b16 a[44:45],v253 offset:13056\n"
"v_mfma_f32_16x16x32_bf16 v[72:75],a[68:71],a[184:187],v[72:75]\n"
"v_mfma_f32_16x16x32_bf16 v[76:79],a[64:67],a[172:175],v[76:79]\n"
"ds_read_b64_tr_b16 a[46:47],v253 offset:19200\n"
"v_mfma_f32_16x16x32_bf16 v[76:79],a[68:71],a[188:191],v[76:79]\n"
"buffer_load_dwordx4 a[196:199],v209,s[88:91],s40 offen offset:1024 sc1\n"
"s_waitcnt lgkmcnt(8)\n"
"v_mfma_f32_16x16x32_bf16 v[80:83],a[72:75],a[160:163],v[80:83]\n"
"ds_read_b64_tr_b16 a[48:49],v252 offset:1024\n"
"v_mfma_f32_16x16x32_bf16 v[80:83],a[76:79],a[176:179],v[80:83]\n"
"v_mfma_f32_16x16x32_bf16 v[84:87],a[72:75],a[164:167],v[84:87]\n"
"ds_read_b64_tr_b16 a[50:51],v252 offset:7168\n"
"v_mfma_f32_16x16x32_bf16 v[84:87],a[76:79],a[180:183],v[84:87]\n"
"buffer_load_dwordx4 a[200:203],v209,s[88:91],s40 offen offset:2048 sc1\n"
"v_mfma_f32_16x16x32_bf16 v[88:91],a[72:75],a[168:171],v[88:91]\n"
"ds_read_b64_tr_b16 a[52:53],v252 offset:13312\n"
"v_mfma_f32_16x16x32_bf16 v[88:91],a[76:79],a[184:187],v[88:91]\n"
"v_mfma_f32_16x16x32_bf16 v[92:95],a[72:75],a[172:175],v[92:95]\n"
"ds_read_b64_tr_b16 a[54:55],v252 offset:19456\n"
"v_mfma_f32_16x16x32_bf16 v[92:95],a[76:79],a[188:191],v[92:95]\n"
"s_waitcnt vmcnt(41) lgkmcnt(8)\n"
"s_barrier\n"
"v_mfma_f32_16x16x32_bf16 v[96:99],a[32:35],a[160:163],v[96:99]\n"
"ds_read_b64_tr_b16 a[56:57],v252 offset:24576\n"
"v_mfma_f32_16x16x32_bf16 v[96:99],a[36:39],a[176:179],v[96:99]\n"
"buffer_load_dwordx4 a[204:207],v209,s[88:91],s40 offen offset:3072 sc1\n"
"v_mfma_f32_16x16x32_bf16 v[100:103],a[32:35],a[164:167],v[100:103]\n"
"ds_read_b64_tr_b16 a[58:59],v252 offset:30720\n"
"v_mfma_f32_16x16x32_bf16 v[100:103],a[36:39],a[180:183],v[100:103]\n"
"v_mfma_f32_16x16x32_bf16 v[104:107],a[32:35],a[168:171],v[104:107]\n"
"ds_read_b64_tr_b16 a[60:61],v252 offset:36864\n"
"v_mfma_f32_16x16x32_bf16 v[104:107],a[36:39],a[184:187],v[104:107]\n"
"v_mfma_f32_16x16x32_bf16 v[108:111],a[32:35],a[172:175],v[108:111]\n"
"s_add_u32 s40,s40,4096\n"
"ds_read_b64_tr_b16 a[62:63],v252 offset:43008\n"
"v_mfma_f32_16x16x32_bf16 v[108:111],a[36:39],a[188:191],v[108:111]\n"
"buffer_load_dwordx4 a[240:243],v209,s[88:91],s40 offen sc1\n"
"s_waitcnt lgkmcnt(8)\n"
"v_mfma_f32_16x16x32_bf16 v[112:115],a[40:43],a[160:163],v[112:115]\n"
"ds_read_b64_tr_b16 a[64:65],v253 offset:24576\n"
"v_mfma_f32_16x16x32_bf16 v[112:115],a[44:47],a[176:179],v[112:115]\n"
"v_mfma_f32_16x16x32_bf16 v[116:119],a[40:43],a[164:167],v[116:119]\n"
"ds_read_b64_tr_b16 a[66:67],v253 offset:30720\n"
"v_mfma_f32_16x16x32_bf16 v[116:119],a[44:47],a[180:183],v[116:119]\n"
"buffer_load_dwordx4 a[244:247],v209,s[88:91],s40 offen offset:1024 sc1\n"
"v_mfma_f32_16x16x32_bf16 v[120:123],a[40:43],a[168:171],v[120:123]\n"
"ds_read_b64_tr_b16 a[68:69],v253 offset:36864\n"
"v_mfma_f32_16x16x32_bf16 v[120:123],a[44:47],a[184:187],v[120:123]\n"
"v_mfma_f32_16x16x32_bf16 v[124:127],a[40:43],a[172:175],v[124:127]\n"
"ds_read_b64_tr_b16 a[70:71],v253 offset:43008\n"
"v_mfma_f32_16x16x32_bf16 v[124:127],a[44:47],a[188:191],v[124:127]\n"
"s_waitcnt lgkmcnt(8)\n"
"v_mfma_f32_16x16x32_bf16 v[128:131],a[48:51],a[160:163],v[128:131]\n"
"ds_read_b64_tr_b16 a[72:73],v252 offset:24832\n"
"v_mfma_f32_16x16x32_bf16 v[128:131],a[52:55],a[176:179],v[128:131]\n"
"buffer_load_dwordx4 a[248:251],v209,s[88:91],s40 offen offset:2048 sc1\n"
"v_mfma_f32_16x16x32_bf16 v[132:135],a[48:51],a[164:167],v[132:135]\n"
"ds_read_b64_tr_b16 a[74:75],v252 offset:30976\n"
"v_mfma_f32_16x16x32_bf16 v[132:135],a[52:55],a[180:183],v[132:135]\n"
"v_mfma_f32_16x16x32_bf16 v[136:139],a[48:51],a[168:171],v[136:139]\n"
"ds_read_b64_tr_b16 a[76:77],v252 offset:37120\n"
"v_mfma_f32_16x16x32_bf16 v[136:139],a[52:55],a[184:187],v[136:139]\n"
"buffer_load_dwordx4 a[252:255],v209,s[88:91],s40 offen offset:3072 sc1\n"
"v_mfma_f32_16x16x32_bf16 v[140:143],a[48:51],a[172:175],v[140:143]\n"
"ds_read_b64_tr_b16 a[78:79],v252 offset:43264\n"
"v_mfma_f32_16x16x32_bf16 v[140:143],a[52:55],a[188:191],v[140:143]\n"
"s_add_u32 s40,s40,4096\n"
"s_waitcnt lgkmcnt(8)\n"
"v_mfma_f32_16x16x32_bf16 v[0:3],a[56:59],a[0:3],v[0:3]\n"
"ds_read_b64_tr_b16 a[32:33],v253 offset:24832\n"
"v_mfma_f32_16x16x32_bf16 v[0:3],a[60:63],a[16:19],v[0:3]\n"
"v_mfma_f32_16x16x32_bf16 v[4:7],a[56:59],a[4:7],v[4:7]\n"
"s_add_u32 m0,s63,24576\n"
"ds_read_b64_tr_b16 a[34:35],v253 offset:30976\n"
"v_mfma_f32_16x16x32_bf16 v[4:7],a[60:63],a[20:23],v[4:7]\n"
"buffer_load_dwordx4 v213,s[72:75],s39 offen lds\n"
"v_mfma_f32_16x16x32_bf16 v[8:11],a[56:59],a[8:11],v[8:11]\n"
"ds_read_b64_tr_b16 a[36:37],v253 offset:37120\n"
"v_mfma_f32_16x16x32_bf16 v[8:11],a[60:63],a[24:27],v[8:11]\n"
"v_mfma_f32_16x16x32_bf16 v[12:15],a[56:59],a[12:15],v[12:15]\n"
"ds_read_b64_tr_b16 a[38:39],v253 offset:43264\n"
"v_mfma_f32_16x16x32_bf16 v[12:15],a[60:63],a[28:31],v[12:15]\n"
"buffer_load_dwordx4 v214,s[72:75],s39 offen offset:1024 lds\n"
"s_waitcnt lgkmcnt(8)\n"
"v_mfma_f32_16x16x32_bf16 v[16:19],a[64:67],a[0:3],v[16:19]\n"
"ds_read_b64_tr_b16 a[40:41],v252 offset:25088\n"
"v_mfma_f32_16x16x32_bf16 v[16:19],a[68:71],a[16:19],v[16:19]\n"
"v_mfma_f32_16x16x32_bf16 v[20:23],a[64:67],a[4:7],v[20:23]\n"
"ds_read_b64_tr_b16 a[42:43],v252 offset:31232\n"
"v_mfma_f32_16x16x32_bf16 v[20:23],a[68:71],a[20:23],v[20:23]\n"
"v_mfma_f32_16x16x32_bf16 v[24:27],a[64:67],a[8:11],v[24:27]\n"
"ds_read_b64_tr_b16 a[44:45],v252 offset:37376\n"
"v_mfma_f32_16x16x32_bf16 v[24:27],a[68:71],a[24:27],v[24:27]\n"
"buffer_load_dwordx4 v215,s[72:75],s39 offen offset:2048 lds\n"
"v_mfma_f32_16x16x32_bf16 v[28:31],a[64:67],a[12:15],v[28:31]\n"
"ds_read_b64_tr_b16 a[46:47],v252 offset:43520\n"
"v_mfma_f32_16x16x32_bf16 v[28:31],a[68:71],a[28:31],v[28:31]\n"
"s_waitcnt lgkmcnt(8)\n"
"v_mfma_f32_16x16x32_bf16 v[32:35],a[72:75],a[0:3],v[32:35]\n"
"ds_read_b64_tr_b16 a[48:49],v253 offset:25088\n"
"v_mfma_f32_16x16x32_bf16 v[32:35],a[76:79],a[16:19],v[32:35]\n"
"v_mfma_f32_16x16x32_bf16 v[36:39],a[72:75],a[4:7],v[36:39]\n"
"s_add_u32 s39,s39,s52\n"
"s_add_u32 m0,s63,27648\n"
"ds_read_b64_tr_b16 a[50:51],v253 offset:31232\n"
"v_mfma_f32_16x16x32_bf16 v[36:39],a[76:79],a[20:23],v[36:39]\n"
"buffer_load_dwordx4 v213,s[72:75],s39 offen lds\n"
"v_mfma_f32_16x16x32_bf16 v[40:43],a[72:75],a[8:11],v[40:43]\n"
"ds_read_b64_tr_b16 a[52:53],v253 offset:37376\n"
"v_mfma_f32_16x16x32_bf16 v[40:43],a[76:79],a[24:27],v[40:43]\n"
"v_mfma_f32_16x16x32_bf16 v[44:47],a[72:75],a[12:15],v[44:47]\n"
"ds_read_b64_tr_b16 a[54:55],v253 offset:43520\n"
"v_mfma_f32_16x16x32_bf16 v[44:47],a[76:79],a[28:31],v[44:47]\n"
"buffer_load_dwordx4 v214,s[72:75],s39 offen offset:1024 lds\n"
"s_waitcnt lgkmcnt(8)\n"
"v_mfma_f32_16x16x32_bf16 v[48:51],a[32:35],a[0:3],v[48:51]\n"
"ds_read_b64_tr_b16 a[56:57],v252 offset:25344\n"
"v_mfma_f32_16x16x32_bf16 v[48:51],a[36:39],a[16:19],v[48:51]\n"
"v_mfma_f32_16x16x32_bf16 v[52:55],a[32:35],a[4:7],v[52:55]\n"
"ds_read_b64_tr_b16 a[58:59],v252 offset:31488\n"
"v_mfma_f32_16x16x32_bf16 v[52:55],a[36:39],a[20:23],v[52:55]\n"
"v_mfma_f32_16x16x32_bf16 v[56:59],a[32:35],a[8:11],v[56:59]\n"
"ds_read_b64_tr_b16 a[60:61],v252 offset:37632\n"
"v_mfma_f32_16x16x32_bf16 v[56:59],a[36:39],a[24:27],v[56:59]\n"
"buffer_load_dwordx4 v215,s[72:75],s39 offen offset:2048 lds\n"
"v_mfma_f32_16x16x32_bf16 v[60:63],a[32:35],a[12:15],v[60:63]\n"
"ds_read_b64_tr_b16 a[62:63],v252 offset:43776\n"
"v_mfma_f32_16x16x32_bf16 v[60:63],a[36:39],a[28:31],v[60:63]\n"
"s_waitcnt lgkmcnt(8)\n"
"v_mfma_f32_16x16x32_bf16 v[64:67],a[40:43],a[0:3],v[64:67]\n"
"s_add_u32 s39,s39,s53\n"
"ds_read_b64_tr_b16 a[64:65],v253 offset:25344\n"
"v_mfma_f32_16x16x32_bf16 v[64:67],a[44:47],a[16:19],v[64:67]\n"
"buffer_load_dwordx4 a[208:211],v209,s[88:91],s40 offen sc1\n"
"v_mfma_f32_16x16x32_bf16 v[68:71],a[40:43],a[4:7],v[68:71]\n"
"ds_read_b64_tr_b16 a[66:67],v253 offset:31488\n"
"v_mfma_f32_16x16x32_bf16 v[68:71],a[44:47],a[20:23],v[68:71]\n"
"v_mfma_f32_16x16x32_bf16 v[72:75],a[40:43],a[8:11],v[72:75]\n"
"ds_read_b64_tr_b16 a[68:69],v253 offset:37632\n"
"v_mfma_f32_16x16x32_bf16 v[72:75],a[44:47],a[24:27],v[72:75]\n"
"v_mfma_f32_16x16x32_bf16 v[76:79],a[40:43],a[12:15],v[76:79]\n"
"ds_read_b64_tr_b16 a[70:71],v253 offset:43776\n"
"v_mfma_f32_16x16x32_bf16 v[76:79],a[44:47],a[28:31],v[76:79]\n"
"buffer_load_dwordx4 a[212:215],v209,s[88:91],s40 offen offset:1024 sc1\n"
"s_waitcnt lgkmcnt(8)\n"
"v_mfma_f32_16x16x32_bf16 v[80:83],a[48:51],a[0:3],v[80:83]\n"
"ds_read_b64_tr_b16 a[72:73],v252 offset:25600\n"
"v_mfma_f32_16x16x32_bf16 v[80:83],a[52:55],a[16:19],v[80:83]\n"
"v_mfma_f32_16x16x32_bf16 v[84:87],a[48:51],a[4:7],v[84:87]\n"
"ds_read_b64_tr_b16 a[74:75],v252 offset:31744\n"
"v_mfma_f32_16x16x32_bf16 v[84:87],a[52:55],a[20:23],v[84:87]\n"
"buffer_load_dwordx4 a[216:219],v209,s[88:91],s40 offen offset:2048 sc1\n"
"v_mfma_f32_16x16x32_bf16 v[88:91],a[48:51],a[8:11],v[88:91]\n"
"ds_read_b64_tr_b16 a[76:77],v252 offset:37888\n"
"v_mfma_f32_16x16x32_bf16 v[88:91],a[52:55],a[24:27],v[88:91]\n"
"v_mfma_f32_16x16x32_bf16 v[92:95],a[48:51],a[12:15],v[92:95]\n"
"ds_read_b64_tr_b16 a[78:79],v252 offset:44032\n"
"v_mfma_f32_16x16x32_bf16 v[92:95],a[52:55],a[28:31],v[92:95]\n"
"s_waitcnt vmcnt(41) lgkmcnt(8)\n"
"s_barrier\n"
"v_mfma_f32_16x16x32_bf16 v[96:99],a[56:59],a[0:3],v[96:99]\n"
"ds_read_b64_tr_b16 a[32:33],v254\n"
"v_mfma_f32_16x16x32_bf16 v[96:99],a[60:63],a[16:19],v[96:99]\n"
"buffer_load_dwordx4 a[220:223],v209,s[88:91],s40 offen offset:3072 sc1\n"
"v_mfma_f32_16x16x32_bf16 v[100:103],a[56:59],a[4:7],v[100:103]\n"
"ds_read_b64_tr_b16 a[34:35],v254 offset:6144\n"
"v_mfma_f32_16x16x32_bf16 v[100:103],a[60:63],a[20:23],v[100:103]\n"
"v_mfma_f32_16x16x32_bf16 v[104:107],a[56:59],a[8:11],v[104:107]\n"
"ds_read_b64_tr_b16 a[36:37],v254 offset:12288\n"
"v_mfma_f32_16x16x32_bf16 v[104:107],a[60:63],a[24:27],v[104:107]\n"
"v_mfma_f32_16x16x32_bf16 v[108:111],a[56:59],a[12:15],v[108:111]\n"
"s_add_u32 s40,s40,4096\n"
"ds_read_b64_tr_b16 a[38:39],v254 offset:18432\n"
"v_mfma_f32_16x16x32_bf16 v[108:111],a[60:63],a[28:31],v[108:111]\n"
"buffer_load_dwordx4 a[224:227],v209,s[88:91],s40 offen sc1\n"
"s_waitcnt lgkmcnt(8)\n"
"v_mfma_f32_16x16x32_bf16 v[112:115],a[64:67],a[0:3],v[112:115]\n"
"ds_read_b64_tr_b16 a[40:41],v255\n"
"v_mfma_f32_16x16x32_bf16 v[112:115],a[68:71],a[16:19],v[112:115]\n"
"v_mfma_f32_16x16x32_bf16 v[116:119],a[64:67],a[4:7],v[116:119]\n"
"ds_read_b64_tr_b16 a[42:43],v255 offset:6144\n"
"v_mfma_f32_16x16x32_bf16 v[116:119],a[68:71],a[20:23],v[116:119]\n"
"buffer_load_dwordx4 a[228:231],v209,s[88:91],s40 offen offset:1024 sc1\n"
"v_mfma_f32_16x16x32_bf16 v[120:123],a[64:67],a[8:11],v[120:123]\n"
"ds_read_b64_tr_b16 a[44:45],v255 offset:12288\n"
"v_mfma_f32_16x16x32_bf16 v[120:123],a[68:71],a[24:27],v[120:123]\n"
"v_mfma_f32_16x16x32_bf16 v[124:127],a[64:67],a[12:15],v[124:127]\n"
"ds_read_b64_tr_b16 a[46:47],v255 offset:18432\n"
"v_mfma_f32_16x16x32_bf16 v[124:127],a[68:71],a[28:31],v[124:127]\n"
"s_waitcnt lgkmcnt(8)\n"
"v_mfma_f32_16x16x32_bf16 v[128:131],a[72:75],a[0:3],v[128:131]\n"
"ds_read_b64_tr_b16 a[48:49],v254 offset:256\n"
"v_mfma_f32_16x16x32_bf16 v[128:131],a[76:79],a[16:19],v[128:131]\n"
"buffer_load_dwordx4 a[232:235],v209,s[88:91],s40 offen offset:2048 sc1\n"
"v_mfma_f32_16x16x32_bf16 v[132:135],a[72:75],a[4:7],v[132:135]\n"
"ds_read_b64_tr_b16 a[50:51],v254 offset:6400\n"
"v_mfma_f32_16x16x32_bf16 v[132:135],a[76:79],a[20:23],v[132:135]\n"
"v_mfma_f32_16x16x32_bf16 v[136:139],a[72:75],a[8:11],v[136:139]\n"
"ds_read_b64_tr_b16 a[52:53],v254 offset:12544\n"
"v_mfma_f32_16x16x32_bf16 v[136:139],a[76:79],a[24:27],v[136:139]\n"
"buffer_load_dwordx4 a[236:239],v209,s[88:91],s40 offen offset:3072 sc1\n"
"v_mfma_f32_16x16x32_bf16 v[140:143],a[72:75],a[12:15],v[140:143]\n"
"ds_read_b64_tr_b16 a[54:55],v254 offset:18688\n"
"v_mfma_f32_16x16x32_bf16 v[140:143],a[76:79],a[28:31],v[140:143]\n"
"s_add_u32 s40,s40,4096\n"
"v_readlane_b32 s76,v248,0\n"
"v_readlane_b32 s77,v248,1\n"
"s_mov_b32 s78,0x240000\n"
"s_mov_b32 s79,0x20000\n"
"v_readlane_b32 s80,v248,2\n"
"v_readlane_b32 s81,v248,3\n"
"s_mov_b32 s82,0x240000\n"
"s_mov_b32 s83,0x20000\n"
"v_readlane_b32 s92,v248,4\n"
"v_readlane_b32 s36,v248,5\n"
"v_readlane_b32 s56,v248,6\n"
"s_add_u32 s93,s92,4096\n"
"s_add_u32 s94,s92,8192\n"
"s_add_u32 s37,s36,8192\n"
"s_add_u32 s38,s36,16384\n"
"s_add_u32 s57,s56,128\n"
"s_add_u32 s58,s56,256\n"
"s_cmp_eq_u32 s48,0\n"
"s_cselect_b32 s94,s94,0x80000000\n"
"s_cselect_b32 s38,s38,0x80000000\n"
"s_cselect_b32 s58,s58,0x80000000\n"
"v_bfe_u32 v236,v209,4,4\n"
"v_lshrrev_b32 v237,8,v209\n"
"v_mul_u32_u24 v190,1152,v236\n"
"v_lshl_add_u32 v190,v237,3,v190\n"
"v_readlane_b32 s43,v210,56\n"
"s_and_b32 s43,s43,0xfff8\n"
"s_add_u32 s43,s43,0\n"
"s_lshl_b32 s43,s43,6\n"
"v_lshl_add_u32 v223,v209,2,s43\n"
"v_bfe_u32 v236,v209,7,1\n"
"v_mul_u32_u24 v236,15872,v236\n"
"v_add_u32 v222,v223,v236\n"
"v_readlane_b32 s35,v248,7\n"
"s_mov_b32 s60,0\n"
"s_andn2_b32 s35,0xff,s35\n"
"s_waitcnt lgkmcnt(8)\n"
"v_mfma_f32_16x16x32_bf16 v[0:3],a[32:35],a[80:83],v[0:3]\n"
"ds_read_b64_tr_b16 a[56:57],v255 offset:256\n"
"v_mfma_f32_16x16x32_bf16 v[0:3],a[36:39],a[96:99],v[0:3]\n"
"v_mfma_f32_16x16x32_bf16 v[4:7],a[32:35],a[84:87],v[4:7]\n"
"s_add_u32 s42,s56,0\n"
"ds_read_b64_tr_b16 a[58:59],v255 offset:6400\n"
"v_mfma_f32_16x16x32_bf16 v[4:7],a[36:39],a[100:103],v[4:7]\n"
"buffer_load_dwordx2 v[192:193],v190,s[76:79],s42 offen\n"
"v_mfma_f32_16x16x32_bf16 v[8:11],a[32:35],a[88:91],v[8:11]\n"
"ds_read_b64_tr_b16 a[60:61],v255 offset:12544\n"
"v_mfma_f32_16x16x32_bf16 v[8:11],a[36:39],a[104:107],v[8:11]\n"
"v_mfma_f32_16x16x32_bf16 v[12:15],a[32:35],a[92:95],v[12:15]\n"
"ds_read_b64_tr_b16 a[62:63],v255 offset:18688\n"
"v_mfma_f32_16x16x32_bf16 v[12:15],a[36:39],a[108:111],v[12:15]\n"
"s_waitcnt lgkmcnt(8)\n"
"v_mfma_f32_16x16x32_bf16 v[16:19],a[40:43],a[80:83],v[16:19]\n"
"s_add_u32 s42,s56,18432\n"
"ds_read_b64_tr_b16 a[64:65],v254 offset:512\n"
"v_mfma_f32_16x16x32_bf16 v[16:19],a[44:47],a[96:99],v[16:19]\n"
"buffer_load_dwordx2 v[194:195],v190,s[76:79],s42 offen\n"
"v_mfma_f32_16x16x32_bf16 v[20:23],a[40:43],a[84:87],v[20:23]\n"
"ds_read_b64_tr_b16 a[66:67],v254 offset:6656\n"
"v_mfma_f32_16x16x32_bf16 v[20:23],a[44:47],a[100:103],v[20:23]\n"
"v_mfma_f32_16x16x32_bf16 v[24:27],a[40:43],a[88:91],v[24:27]\n"
"ds_read_b64_tr_b16 a[68:69],v254 offset:12800\n"
"v_mfma_f32_16x16x32_bf16 v[24:27],a[44:47],a[104:107],v[24:27]\n"
"v_mfma_f32_16x16x32_bf16 v[28:31],a[40:43],a[92:95],v[28:31]\n"
"s_add_u32 s42,s57,0\n"
"ds_read_b64_tr_b16 a[70:71],v254 offset:18944\n"
"v_mfma_f32_16x16x32_bf16 v[28:31],a[44:47],a[108:111],v[28:31]\n"
"buffer_load_dwordx2 v[196:197],v190,s[76:79],s42 offen\n"
"s_waitcnt lgkmcnt(8)\n"
"v_mfma_f32_16x16x32_bf16 v[32:35],a[48:51],a[80:83],v[32:35]\n"
"ds_read_b64_tr_b16 a[72:73],v255 offset:512\n"
"v_mfma_f32_16x16x32_bf16 v[32:35],a[52:55],a[96:99],v[32:35]\n"
"v_mfma_f32_16x16x32_bf16 v[36:39],a[48:51],a[84:87],v[36:39]\n"
"ds_read_b64_tr_b16 a[74:75],v255 offset:6656\n"
"v_mfma_f32_16x16x32_bf16 v[36:39],a[52:55],a[100:103],v[36:39]\n"
"v_mfma_f32_16x16x32_bf16 v[40:43],a[48:51],a[88:91],v[40:43]\n"
"s_add_u32 s42,s57,18432\n"
"ds_read_b64_tr_b16 a[76:77],v255 offset:12800\n"
"v_mfma_f32_16x16x32_bf16 v[40:43],a[52:55],a[104:107],v[40:43]\n"
"buffer_load_dwordx2 v[198:199],v190,s[76:79],s42 offen\n"
"v_mfma_f32_16x16x32_bf16 v[44:47],a[48:51],a[92:95],v[44:47]\n"
"ds_read_b64_tr_b16 a[78:79],v255 offset:18944\n"
"v_mfma_f32_16x16x32_bf16 v[44:47],a[52:55],a[108:111],v[44:47]\n"
"s_waitcnt lgkmcnt(8)\n"
"v_mfma_f32_16x16x32_bf16 v[48:51],a[56:59],a[80:83],v[48:51]\n"
"ds_read_b64_tr_b16 a[32:33],v254 offset:768\n"
"v_mfma_f32_16x16x32_bf16 v[48:51],a[60:63],a[96:99],v[48:51]\n"
"v_mfma_f32_16x16x32_bf16 v[52:55],a[56:59],a[84:87],v[52:55]\n"
"s_add_u32 s42,s58,0\n"
"ds_read_b64_tr_b16 a[34:35],v254 offset:6912\n"
"v_mfma_f32_16x16x32_bf16 v[52:55],a[60:63],a[100:103],v[52:55]\n"
"buffer_load_dwordx2 v[200:201],v190,s[76:79],s42 offen\n"
"v_mfma_f32_16x16x32_bf16 v[56:59],a[56:59],a[88:91],v[56:59]\n"
"ds_read_b64_tr_b16 a[36:37],v254 offset:13056\n"
"v_mfma_f32_16x16x32_bf16 v[56:59],a[60:63],a[104:107],v[56:59]\n"
"v_mfma_f32_16x16x32_bf16 v[60:63],a[56:59],a[92:95],v[60:63]\n"
"ds_read_b64_tr_b16 a[38:39],v254 offset:19200\n"
"v_mfma_f32_16x16x32_bf16 v[60:63],a[60:63],a[108:111],v[60:63]\n"
"s_waitcnt lgkmcnt(8)\n"
"v_mfma_f32_16x16x32_bf16 v[64:67],a[64:67],a[80:83],v[64:67]\n"
"s_add_u32 s42,s58,18432\n"
"ds_read_b64_tr_b16 a[40:41],v255 offset:768\n"
"v_mfma_f32_16x16x32_bf16 v[64:67],a[68:71],a[96:99],v[64:67]\n"
"buffer_load_dwordx2 v[202:203],v190,s[76:79],s42 offen\n"
"v_mfma_f32_16x16x32_bf16 v[68:71],a[64:67],a[84:87],v[68:71]\n"
"ds_read_b64_tr_b16 a[42:43],v255 offset:6912\n"
"v_mfma_f32_16x16x32_bf16 v[68:71],a[68:71],a[100:103],v[68:71]\n"
"v_mfma_f32_16x16x32_bf16 v[72:75],a[64:67],a[88:91],v[72:75]\n"
"ds_read_b64_tr_b16 a[44:45],v255 offset:13056\n"
"v_mfma_f32_16x16x32_bf16 v[72:75],a[68:71],a[104:107],v[72:75]\n"
"v_mfma_f32_16x16x32_bf16 v[76:79],a[64:67],a[92:95],v[76:79]\n"
"s_nop 0\n"
"ds_read_b64_tr_b16 a[46:47],v255 offset:19200\n"
"v_mfma_f32_16x16x32_bf16 v[76:79],a[68:71],a[108:111],v[76:79]\n"
"buffer_load_dwordx2 a[112:113],v209,s[64:67],s36 offen sc1\n"
"s_waitcnt lgkmcnt(8)\n"
"v_mfma_f32_16x16x32_bf16 v[80:83],a[72:75],a[80:83],v[80:83]\n"
"ds_read_b64_tr_b16 a[48:49],v254 offset:1024\n"
"v_mfma_f32_16x16x32_bf16 v[80:83],a[76:79],a[96:99],v[80:83]\n"
"v_mfma_f32_16x16x32_bf16 v[84:87],a[72:75],a[84:87],v[84:87]\n"
"ds_read_b64_tr_b16 a[50:51],v254 offset:7168\n"
"v_mfma_f32_16x16x32_bf16 v[84:87],a[76:79],a[100:103],v[84:87]\n"
"v_mfma_f32_16x16x32_bf16 v[88:91],a[72:75],a[88:91],v[88:91]\n"
"s_nop 0\n"
"ds_read_b64_tr_b16 a[52:53],v254 offset:13312\n"
"v_mfma_f32_16x16x32_bf16 v[88:91],a[76:79],a[104:107],v[88:91]\n"
"buffer_load_dwordx2 a[114:115],v209,s[64:67],s36 offen offset:1024 sc1\n"
"v_mfma_f32_16x16x32_bf16 v[92:95],a[72:75],a[92:95],v[92:95]\n"
"ds_read_b64_tr_b16 a[54:55],v254 offset:19456\n"
"v_mfma_f32_16x16x32_bf16 v[92:95],a[76:79],a[108:111],v[92:95]\n"
"s_waitcnt vmcnt(8) lgkmcnt(8)\n"
"s_barrier\n"
"v_mfma_f32_16x16x32_bf16 v[96:99],a[32:35],a[80:83],v[96:99]\n"
"v_mfma_f32_16x16x32_bf16 v[96:99],a[36:39],a[96:99],v[96:99]\n"
"v_mfma_f32_16x16x32_bf16 v[100:103],a[32:35],a[84:87],v[100:103]\n"
"s_nop 0\n"
"v_mfma_f32_16x16x32_bf16 v[100:103],a[36:39],a[100:103],v[100:103]\n"
"buffer_load_dwordx2 a[116:117],v209,s[64:67],s37 offen sc1\n"
"v_mfma_f32_16x16x32_bf16 v[104:107],a[32:35],a[88:91],v[104:107]\n"
"v_mfma_f32_16x16x32_bf16 v[104:107],a[36:39],a[104:107],v[104:107]\n"
"v_mfma_f32_16x16x32_bf16 v[108:111],a[32:35],a[92:95],v[108:111]\n"
"v_mfma_f32_16x16x32_bf16 v[108:111],a[36:39],a[108:111],v[108:111]\n"
"s_waitcnt lgkmcnt(4)\n"
"v_mfma_f32_16x16x32_bf16 v[112:115],a[40:43],a[80:83],v[112:115]\n"
"s_nop 0\n"
"v_mfma_f32_16x16x32_bf16 v[112:115],a[44:47],a[96:99],v[112:115]\n"
"buffer_load_dwordx2 a[118:119],v209,s[64:67],s37 offen offset:1024 sc1\n"
"v_mfma_f32_16x16x32_bf16 v[116:119],a[40:43],a[84:87],v[116:119]\n"
"v_mfma_f32_16x16x32_bf16 v[116:119],a[44:47],a[100:103],v[116:119]\n"
"v_mfma_f32_16x16x32_bf16 v[120:123],a[40:43],a[88:91],v[120:123]\n"
"v_mfma_f32_16x16x32_bf16 v[120:123],a[44:47],a[104:107],v[120:123]\n"
"v_mfma_f32_16x16x32_bf16 v[124:127],a[40:43],a[92:95],v[124:127]\n"
"s_nop 0\n"
"v_mfma_f32_16x16x32_bf16 v[124:127],a[44:47],a[108:111],v[124:127]\n"
"buffer_load_dwordx2 a[120:121],v209,s[64:67],s38 offen sc1\n"
"s_waitcnt lgkmcnt(0)\n"
"v_mfma_f32_16x16x32_bf16 v[128:131],a[48:51],a[80:83],v[128:131]\n"
"v_mfma_f32_16x16x32_bf16 v[128:131],a[52:55],a[96:99],v[128:131]\n"
"v_mfma_f32_16x16x32_bf16 v[132:135],a[48:51],a[84:87],v[132:135]\n"
"v_mfma_f32_16x16x32_bf16 v[132:135],a[52:55],a[100:103],v[132:135]\n"
"v_mfma_f32_16x16x32_bf16 v[136:139],a[48:51],a[88:91],v[136:139]\n"
"s_nop 0\n"
"v_mfma_f32_16x16x32_bf16 v[136:139],a[52:55],a[104:107],v[136:139]\n"
"buffer_load_dwordx2 a[122:123],v209,s[64:67],s38 offen offset:1024 sc1\n"
"v_mfma_f32_16x16x32_bf16 v[140:143],a[48:51],a[92:95],v[140:143]\n"
"v_mfma_f32_16x16x32_bf16 v[140:143],a[52:55],a[108:111],v[140:143]\n"
"ds_read_b64_tr_b16 a[32:33],v254 offset:24576\n"
"ds_read_b64_tr_b16 a[34:35],v254 offset:30720\n"
"ds_read_b64_tr_b16 a[36:37],v254 offset:36864\n"
"ds_read_b64_tr_b16 a[38:39],v254 offset:43008\n"
"ds_read_b64_tr_b16 a[40:41],v240\n"
"ds_read_b64_tr_b16 a[42:43],v240 offset:6144\n"
"ds_read_b64_tr_b16 a[44:45],v240 offset:12288\n"
"ds_read_b64_tr_b16 a[46:47],v240 offset:18432\n"
"ds_read_b64_tr_b16 a[48:49],v240 offset:24576\n"
"ds_read_b64_tr_b16 a[50:51],v240 offset:30720\n"
"ds_read_b64_tr_b16 a[52:53],v240 offset:36864\n"
"ds_read_b64_tr_b16 a[54:55],v240 offset:43008\n"
"s_waitcnt lgkmcnt(0)\n"
"v_mfma_f32_16x16x32_bf16 v[0:3],a[32:35],a[128:131],v[0:3]\n"
"ds_read_b64_tr_b16 a[56:57],v255 offset:24576\n"
"v_mfma_f32_16x16x32_bf16 v[0:3],a[36:39],a[144:147],v[0:3]\n"
"ds_read_b64_tr_b16 a[58:59],v255 offset:30720\n"
"v_mfma_f32_16x16x32_bf16 v[0:3],a[40:43],a[192:195],v[0:3]\n"
"ds_read_b64_tr_b16 a[60:61],v255 offset:36864\n"
"v_mfma_f32_16x16x32_bf16 v[0:3],a[44:47],a[240:243],v[0:3]\n"
"ds_read_b64_tr_b16 a[62:63],v255 offset:43008\n"
"v_mfma_f32_16x16x32_bf16 v[0:3],a[48:51],a[208:211],v[0:3]\n"
"ds_read_b64_tr_b16 a[64:65],v241\n"
"v_mfma_f32_16x16x32_bf16 v[0:3],a[52:55],a[224:227],v[0:3]\n"
"ds_read_b64_tr_b16 a[66:67],v241 offset:6144\n"
"v_mfma_f32_16x16x32_bf16 v[4:7],a[32:35],a[132:135],v[4:7]\n"
"ds_read_b64_tr_b16 a[68:69],v241 offset:12288\n"
"v_mfma_f32_16x16x32_bf16 v[4:7],a[36:39],a[148:151],v[4:7]\n"
"ds_read_b64_tr_b16 a[70:71],v241 offset:18432\n"
"v_mfma_f32_16x16x32_bf16 v[4:7],a[40:43],a[196:199],v[4:7]\n"
"ds_read_b64_tr_b16 a[72:73],v241 offset:24576\n"
"v_mfma_f32_16x16x32_bf16 v[4:7],a[44:47],a[244:247],v[4:7]\n"
"ds_read_b64_tr_b16 a[74:75],v241 offset:30720\n"
"v_mfma_f32_16x16x32_bf16 v[4:7],a[48:51],a[212:215],v[4:7]\n"
"ds_read_b64_tr_b16 a[76:77],v241 offset:36864\n"
"v_mfma_f32_16x16x32_bf16 v[4:7],a[52:55],a[228:231],v[4:7]\n"
"ds_read_b64_tr_b16 a[78:79],v241 offset:43008\n"
"v_mfma_f32_16x16x32_bf16 v[8:11],a[32:35],a[136:139],v[8:11]\n"
"v_mfma_f32_16x16x32_bf16 v[8:11],a[36:39],a[152:155],v[8:11]\n"
"v_mfma_f32_16x16x32_bf16 v[8:11],a[40:43],a[200:203],v[8:11]\n"
"v_mfma_f32_16x16x32_bf16 v[8:11],a[44:47],a[248:251],v[8:11]\n"
"v_mfma_f32_16x16x32_bf16 v[8:11],a[48:51],a[216:219],v[8:11]\n"
"v_mfma_f32_16x16x32_bf16 v[8:11],a[52:55],a[232:235],v[8:11]\n"
"v_mfma_f32_16x16x32_bf16 v[12:15],a[32:35],a[140:143],v[12:15]\n"
"v_mfma_f32_16x16x32_bf16 v[12:15],a[36:39],a[156:159],v[12:15]\n"
"v_mfma_f32_16x16x32_bf16 v[12:15],a[40:43],a[204:207],v[12:15]\n"
"v_mfma_f32_16x16x32_bf16 v[12:15],a[44:47],a[252:255],v[12:15]\n"
"v_mfma_f32_16x16x32_bf16 v[12:15],a[48:51],a[220:223],v[12:15]\n"
"v_mfma_f32_16x16x32_bf16 v[12:15],a[52:55],a[236:239],v[12:15]\n"
"s_waitcnt lgkmcnt(0)\n"
"v_mfma_f32_16x16x32_bf16 v[16:19],a[56:59],a[128:131],v[16:19]\n"
"ds_read_b64_tr_b16 a[32:33],v254 offset:24832\n"
"s_nop 0\n"
"v_mfma_f32_16x16x32_bf16 v[16:19],a[60:63],a[144:147],v[16:19]\n"
"ds_read_b64_tr_b16 a[34:35],v254 offset:30976\n"
"s_nop 0\n"
"v_mfma_f32_16x16x32_bf16 v[16:19],a[64:67],a[192:195],v[16:19]\n"
"ds_read_b64_tr_b16 a[36:37],v254 offset:37120\n"
"s_nop 0\n"
"v_mfma_f32_16x16x32_bf16 v[16:19],a[68:71],a[240:243],v[16:19]\n"
"ds_read_b64_tr_b16 a[38:39],v254 offset:43264\n"
"s_nop 0\n"
"v_mfma_f32_16x16x32_bf16 v[16:19],a[72:75],a[208:211],v[16:19]\n"
"ds_read_b64_tr_b16 a[40:41],v240 offset:256\n"
"v_cvt_pk_bf16_f32 v144,v0,v1\n"
"v_mfma_f32_16x16x32_bf16 v[16:19],a[76:79],a[224:227],v[16:19]\n"
"ds_read_b64_tr_b16 a[42:43],v240 offset:6400\n"
"v_cvt_pk_bf16_f32 v145,v2,v3\n"
"v_mfma_f32_16x16x32_bf16 v[20:23],a[56:59],a[132:135],v[20:23]\n"
"ds_read_b64_tr_b16 a[44:45],v240 offset:12544\n"
"v_cvt_pk_bf16_f32 v146,v4,v5\n"
"v_mfma_f32_16x16x32_bf16 v[20:23],a[60:63],a[148:151],v[20:23]\n"
"ds_read_b64_tr_b16 a[46:47],v240 offset:18688\n"
"v_cvt_pk_bf16_f32 v147,v6,v7\n"
"v_mfma_f32_16x16x32_bf16 v[20:23],a[64:67],a[196:199],v[20:23]\n"
"ds_read_b64_tr_b16 a[48:49],v240 offset:24832\n"
"v_cvt_pk_bf16_f32 v148,v8,v9\n"
"v_mfma_f32_16x16x32_bf16 v[20:23],a[68:71],a[244:247],v[20:23]\n"
"ds_read_b64_tr_b16 a[50:51],v240 offset:30976\n"
"v_cvt_pk_bf16_f32 v149,v10,v11\n"
"v_mfma_f32_16x16x32_bf16 v[20:23],a[72:75],a[212:215],v[20:23]\n"
"ds_read_b64_tr_b16 a[52:53],v240 offset:37120\n"
"v_cvt_pk_bf16_f32 v150,v12,v13\n"
"v_mfma_f32_16x16x32_bf16 v[20:23],a[76:79],a[228:231],v[20:23]\n"
"ds_read_b64_tr_b16 a[54:55],v240 offset:43264\n"
"v_cvt_pk_bf16_f32 v151,v14,v15\n"
"v_mfma_f32_16x16x32_bf16 v[24:27],a[56:59],a[136:139],v[24:27]\n"
"s_mov_b32 s41,0\n"
"v_mfma_f32_16x16x32_bf16 v[24:27],a[60:63],a[152:155],v[24:27]\n"
"buffer_store_dwordx4 v[144:147],v209,s[84:87],s41 offen sc1\n"
"v_mfma_f32_16x16x32_bf16 v[24:27],a[64:67],a[200:203],v[24:27]\n"
"s_mov_b32 s41,8192\n"
"v_mfma_f32_16x16x32_bf16 v[24:27],a[68:71],a[248:251],v[24:27]\n"
"buffer_store_dwordx4 v[148:151],v209,s[84:87],s41 offen offset:1024 sc1\n"
"v_mfma_f32_16x16x32_bf16 v[24:27],a[72:75],a[216:219],v[24:27]\n"
"v_mfma_f32_16x16x32_bf16 v[24:27],a[76:79],a[232:235],v[24:27]\n"
"v_mfma_f32_16x16x32_bf16 v[28:31],a[56:59],a[140:143],v[28:31]\n"
"v_mfma_f32_16x16x32_bf16 v[28:31],a[60:63],a[156:159],v[28:31]\n"
"v_mfma_f32_16x16x32_bf16 v[28:31],a[64:67],a[204:207],v[28:31]\n"
"v_mfma_f32_16x16x32_bf16 v[28:31],a[68:71],a[252:255],v[28:31]\n"
"v_mfma_f32_16x16x32_bf16 v[28:31],a[72:75],a[220:223],v[28:31]\n"
"v_mfma_f32_16x16x32_bf16 v[28:31],a[76:79],a[236:239],v[28:31]\n"
"s_waitcnt lgkmcnt(0)\n"
"v_mfma_f32_16x16x32_bf16 v[32:35],a[32:35],a[128:131],v[32:35]\n"
"ds_read_b64_tr_b16 a[56:57],v255 offset:24832\n"
"s_nop 0\n"
"v_mfma_f32_16x16x32_bf16 v[32:35],a[36:39],a[144:147],v[32:35]\n"
"ds_read_b64_tr_b16 a[58:59],v255 offset:30976\n"
"s_nop 0\n"
"v_mfma_f32_16x16x32_bf16 v[32:35],a[40:43],a[192:195],v[32:35]\n"
"ds_read_b64_tr_b16 a[60:61],v255 offset:37120\n"
"s_nop 0\n"
"v_mfma_f32_16x16x32_bf16 v[32:35],a[44:47],a[240:243],v[32:35]\n"
"ds_read_b64_tr_b16 a[62:63],v255 offset:43264\n"
"s_nop 0\n"
"v_mfma_f32_16x16x32_bf16 v[32:35],a[48:51],a[208:211],v[32:35]\n"
"ds_read_b64_tr_b16 a[64:65],v241 offset:256\n"
"v_cvt_pk_bf16_f32 v152,v16,v17\n"
"v_mfma_f32_16x16x32_bf16 v[32:35],a[52:55],a[224:227],v[32:35]\n"
"ds_read_b64_tr_b16 a[66:67],v241 offset:6400\n"
"v_cvt_pk_bf16_f32 v153,v18,v19\n"
"v_mfma_f32_16x16x32_bf16 v[36:39],a[32:35],a[132:135],v[36:39]\n"
"ds_read_b64_tr_b16 a[68:69],v241 offset:12544\n"
"v_cvt_pk_bf16_f32 v154,v20,v21\n"
"v_mfma_f32_16x16x32_bf16 v[36:39],a[36:39],a[148:151],v[36:39]\n"
"ds_read_b64_tr_b16 a[70:71],v241 offset:18688\n"
"v_cvt_pk_bf16_f32 v155,v22,v23\n"
"v_mfma_f32_16x16x32_bf16 v[36:39],a[40:43],a[196:199],v[36:39]\n"
"ds_read_b64_tr_b16 a[72:73],v241 offset:24832\n"
"v_cvt_pk_bf16_f32 v156,v24,v25\n"
"v_mfma_f32_16x16x32_bf16 v[36:39],a[44:47],a[244:247],v[36:39]\n"
"ds_read_b64_tr_b16 a[74:75],v241 offset:30976\n"
"v_cvt_pk_bf16_f32 v157,v26,v27\n"
"v_mfma_f32_16x16x32_bf16 v[36:39],a[48:51],a[212:215],v[36:39]\n"
"ds_read_b64_tr_b16 a[76:77],v241 offset:37120\n"
"v_cvt_pk_bf16_f32 v158,v28,v29\n"
"v_mfma_f32_16x16x32_bf16 v[36:39],a[52:55],a[228:231],v[36:39]\n"
"ds_read_b64_tr_b16 a[78:79],v241 offset:43264\n"
"v_cvt_pk_bf16_f32 v159,v30,v31\n"
"v_mfma_f32_16x16x32_bf16 v[40:43],a[32:35],a[136:139],v[40:43]\n"
"s_mov_b32 s41,0\n"
"v_mfma_f32_16x16x32_bf16 v[40:43],a[36:39],a[152:155],v[40:43]\n"
"buffer_store_dwordx4 v[152:155],v209,s[84:87],s41 offen offset:1024 sc1\n"
"v_mfma_f32_16x16x32_bf16 v[40:43],a[40:43],a[200:203],v[40:43]\n"
"s_mov_b32 s41,8192\n"
"v_mfma_f32_16x16x32_bf16 v[40:43],a[44:47],a[248:251],v[40:43]\n"
"buffer_store_dwordx4 v[156:159],v209,s[84:87],s41 offen offset:2048 sc1\n"
"v_mfma_f32_16x16x32_bf16 v[40:43],a[48:51],a[216:219],v[40:43]\n"
"v_mfma_f32_16x16x32_bf16 v[40:43],a[52:55],a[232:235],v[40:43]\n"
"v_mfma_f32_16x16x32_bf16 v[44:47],a[32:35],a[140:143],v[44:47]\n"
"v_mfma_f32_16x16x32_bf16 v[44:47],a[36:39],a[156:159],v[44:47]\n"
"v_mfma_f32_16x16x32_bf16 v[44:47],a[40:43],a[204:207],v[44:47]\n"
"v_mfma_f32_16x16x32_bf16 v[44:47],a[44:47],a[252:255],v[44:47]\n"
"v_mfma_f32_16x16x32_bf16 v[44:47],a[48:51],a[220:223],v[44:47]\n"
"v_mfma_f32_16x16x32_bf16 v[44:47],a[52:55],a[236:239],v[44:47]\n"
"s_waitcnt lgkmcnt(0)\n"
"v_mfma_f32_16x16x32_bf16 v[48:51],a[56:59],a[128:131],v[48:51]\n"
"ds_read_b64_tr_b16 a[32:33],v254 offset:25088\n"
"s_nop 0\n"
"v_mfma_f32_16x16x32_bf16 v[48:51],a[60:63],a[144:147],v[48:51]\n"
"ds_read_b64_tr_b16 a[34:35],v254 offset:31232\n"
"s_nop 0\n"
"v_mfma_f32_16x16x32_bf16 v[48:51],a[64:67],a[192:195],v[48:51]\n"
"ds_read_b64_tr_b16 a[36:37],v254 offset:37376\n"
"s_nop 0\n"
"v_mfma_f32_16x16x32_bf16 v[48:51],a[68:71],a[240:243],v[48:51]\n"
"ds_read_b64_tr_b16 a[38:39],v254 offset:43520\n"
"s_nop 0\n"
"v_mfma_f32_16x16x32_bf16 v[48:51],a[72:75],a[208:211],v[48:51]\n"
"ds_read_b64_tr_b16 a[40:41],v240 offset:512\n"
"v_cvt_pk_bf16_f32 v160,v32,v33\n"
"v_mfma_f32_16x16x32_bf16 v[48:51],a[76:79],a[224:227],v[48:51]\n"
"ds_read_b64_tr_b16 a[42:43],v240 offset:6656\n"
"v_cvt_pk_bf16_f32 v161,v34,v35\n"
"v_mfma_f32_16x16x32_bf16 v[52:55],a[56:59],a[132:135],v[52:55]\n"
"ds_read_b64_tr_b16 a[44:45],v240 offset:12800\n"
"v_cvt_pk_bf16_f32 v162,v36,v37\n"
"v_mfma_f32_16x16x32_bf16 v[52:55],a[60:63],a[148:151],v[52:55]\n"
"ds_read_b64_tr_b16 a[46:47],v240 offset:18944\n"
"v_cvt_pk_bf16_f32 v163,v38,v39\n"
"v_mfma_f32_16x16x32_bf16 v[52:55],a[64:67],a[196:199],v[52:55]\n"
"ds_read_b64_tr_b16 a[48:49],v240 offset:25088\n"
"v_cvt_pk_bf16_f32 v164,v40,v41\n"
"v_mfma_f32_16x16x32_bf16 v[52:55],a[68:71],a[244:247],v[52:55]\n"
"ds_read_b64_tr_b16 a[50:51],v240 offset:31232\n"
"v_cvt_pk_bf16_f32 v165,v42,v43\n"
"v_mfma_f32_16x16x32_bf16 v[52:55],a[72:75],a[212:215],v[52:55]\n"
"ds_read_b64_tr_b16 a[52:53],v240 offset:37376\n"
"v_cvt_pk_bf16_f32 v166,v44,v45\n"
"v_mfma_f32_16x16x32_bf16 v[52:55],a[76:79],a[228:231],v[52:55]\n"
"ds_read_b64_tr_b16 a[54:55],v240 offset:43520\n"
"v_cvt_pk_bf16_f32 v167,v46,v47\n"
"v_mfma_f32_16x16x32_bf16 v[56:59],a[56:59],a[136:139],v[56:59]\n"
"s_mov_b32 s41,0\n"
"v_mfma_f32_16x16x32_bf16 v[56:59],a[60:63],a[152:155],v[56:59]\n"
"buffer_store_dwordx4 v[160:163],v209,s[84:87],s41 offen offset:2048 sc1\n"
"v_mfma_f32_16x16x32_bf16 v[56:59],a[64:67],a[200:203],v[56:59]\n"
"s_mov_b32 s41,8192\n"
"v_mfma_f32_16x16x32_bf16 v[56:59],a[68:71],a[248:251],v[56:59]\n"
"buffer_store_dwordx4 v[164:167],v209,s[84:87],s41 offen offset:3072 sc1\n"
"v_mfma_f32_16x16x32_bf16 v[56:59],a[72:75],a[216:219],v[56:59]\n"
"v_mfma_f32_16x16x32_bf16 v[56:59],a[76:79],a[232:235],v[56:59]\n"
"v_mfma_f32_16x16x32_bf16 v[60:63],a[56:59],a[140:143],v[60:63]\n"
"v_mfma_f32_16x16x32_bf16 v[60:63],a[60:63],a[156:159],v[60:63]\n"
"v_mfma_f32_16x16x32_bf16 v[60:63],a[64:67],a[204:207],v[60:63]\n"
"v_mfma_f32_16x16x32_bf16 v[60:63],a[68:71],a[252:255],v[60:63]\n"
"v_mfma_f32_16x16x32_bf16 v[60:63],a[72:75],a[220:223],v[60:63]\n"
"v_mfma_f32_16x16x32_bf16 v[60:63],a[76:79],a[236:239],v[60:63]\n"
"s_waitcnt lgkmcnt(0)\n"
"v_mfma_f32_16x16x32_bf16 v[64:67],a[32:35],a[128:131],v[64:67]\n"
"ds_read_b64_tr_b16 a[56:57],v255 offset:25088\n"
"s_nop 0\n"
"v_mfma_f32_16x16x32_bf16 v[64:67],a[36:39],a[144:147],v[64:67]\n"
"ds_read_b64_tr_b16 a[58:59],v255 offset:31232\n"
"s_nop 0\n"
"v_mfma_f32_16x16x32_bf16 v[64:67],a[40:43],a[192:195],v[64:67]\n"
"ds_read_b64_tr_b16 a[60:61],v255 offset:37376\n"
"s_nop 0\n"
"v_mfma_f32_16x16x32_bf16 v[64:67],a[44:47],a[240:243],v[64:67]\n"
"ds_read_b64_tr_b16 a[62:63],v255 offset:43520\n"
"s_nop 0\n"
"v_mfma_f32_16x16x32_bf16 v[64:67],a[48:51],a[208:211],v[64:67]\n"
"ds_read_b64_tr_b16 a[64:65],v241 offset:512\n"
"v_cvt_pk_bf16_f32 v168,v48,v49\n"
"v_mfma_f32_16x16x32_bf16 v[64:67],a[52:55],a[224:227],v[64:67]\n"
"ds_read_b64_tr_b16 a[66:67],v241 offset:6656\n"
"v_cvt_pk_bf16_f32 v169,v50,v51\n"
"v_mfma_f32_16x16x32_bf16 v[68:71],a[32:35],a[132:135],v[68:71]\n"
"ds_read_b64_tr_b16 a[68:69],v241 offset:12800\n"
"v_cvt_pk_bf16_f32 v170,v52,v53\n"
"v_mfma_f32_16x16x32_bf16 v[68:71],a[36:39],a[148:151],v[68:71]\n"
"ds_read_b64_tr_b16 a[70:71],v241 offset:18944\n"
"v_cvt_pk_bf16_f32 v171,v54,v55\n"
"v_mfma_f32_16x16x32_bf16 v[68:71],a[40:43],a[196:199],v[68:71]\n"
"ds_read_b64_tr_b16 a[72:73],v241 offset:25088\n"
"v_cvt_pk_bf16_f32 v172,v56,v57\n"
"v_mfma_f32_16x16x32_bf16 v[68:71],a[44:47],a[244:247],v[68:71]\n"
"ds_read_b64_tr_b16 a[74:75],v241 offset:31232\n"
"v_cvt_pk_bf16_f32 v173,v58,v59\n"
"v_mfma_f32_16x16x32_bf16 v[68:71],a[48:51],a[212:215],v[68:71]\n"
"ds_read_b64_tr_b16 a[76:77],v241 offset:37376\n"
"v_cvt_pk_bf16_f32 v174,v60,v61\n"
"v_mfma_f32_16x16x32_bf16 v[68:71],a[52:55],a[228:231],v[68:71]\n"
"ds_read_b64_tr_b16 a[78:79],v241 offset:43520\n"
"v_cvt_pk_bf16_f32 v175,v62,v63\n"
"v_mfma_f32_16x16x32_bf16 v[72:75],a[32:35],a[136:139],v[72:75]\n"
"s_mov_b32 s41,0\n"
"v_mfma_f32_16x16x32_bf16 v[72:75],a[36:39],a[152:155],v[72:75]\n"
"buffer_store_dwordx4 v[168:171],v209,s[84:87],s41 offen offset:3072 sc1\n"
"v_mfma_f32_16x16x32_bf16 v[72:75],a[40:43],a[200:203],v[72:75]\n"
"s_mov_b32 s41,12288\n"
"v_mfma_f32_16x16x32_bf16 v[72:75],a[44:47],a[248:251],v[72:75]\n"
"buffer_store_dwordx4 v[172:175],v209,s[84:87],s41 offen sc1\n"
"v_mfma_f32_16x16x32_bf16 v[72:75],a[48:51],a[216:219],v[72:75]\n"
"v_mfma_f32_16x16x32_bf16 v[72:75],a[52:55],a[232:235],v[72:75]\n"
"v_mfma_f32_16x16x32_bf16 v[76:79],a[32:35],a[140:143],v[76:79]\n"
"v_mfma_f32_16x16x32_bf16 v[76:79],a[36:39],a[156:159],v[76:79]\n"
"v_mfma_f32_16x16x32_bf16 v[76:79],a[40:43],a[204:207],v[76:79]\n"
"v_mfma_f32_16x16x32_bf16 v[76:79],a[44:47],a[252:255],v[76:79]\n"
"v_mfma_f32_16x16x32_bf16 v[76:79],a[48:51],a[220:223],v[76:79]\n"
"v_mfma_f32_16x16x32_bf16 v[76:79],a[52:55],a[236:239],v[76:79]\n"
"s_waitcnt lgkmcnt(0)\n"
"v_mfma_f32_16x16x32_bf16 v[80:83],a[56:59],a[128:131],v[80:83]\n"
"ds_read_b64_tr_b16 a[32:33],v254 offset:25344\n"
"s_nop 0\n"
"v_mfma_f32_16x16x32_bf16 v[80:83],a[60:63],a[144:147],v[80:83]\n"
"ds_read_b64_tr_b16 a[34:35],v254 offset:31488\n"
"s_nop 0\n"
"v_mfma_f32_16x16x32_bf16 v[80:83],a[64:67],a[192:195],v[80:83]\n"
"ds_read_b64_tr_b16 a[36:37],v254 offset:37632\n"
"s_nop 0\n"
"v_mfma_f32_16x16x32_bf16 v[80:83],a[68:71],a[240:243],v[80:83]\n"
"ds_read_b64_tr_b16 a[38:39],v254 offset:43776\n"
"s_nop 0\n"
"v_mfma_f32_16x16x32_bf16 v[80:83],a[72:75],a[208:211],v[80:83]\n"
"ds_read_b64_tr_b16 a[40:41],v240 offset:768\n"
"v_cvt_pk_bf16_f32 v176,v64,v65\n"
"v_mfma_f32_16x16x32_bf16 v[80:83],a[76:79],a[224:227],v[80:83]\n"
"ds_read_b64_tr_b16 a[42:43],v240 offset:6912\n"
"v_cvt_pk_bf16_f32 v177,v66,v67\n"
"v_mfma_f32_16x16x32_bf16 v[84:87],a[56:59],a[132:135],v[84:87]\n"
"ds_read_b64_tr_b16 a[44:45],v240 offset:13056\n"
"v_cvt_pk_bf16_f32 v178,v68,v69\n"
"v_mfma_f32_16x16x32_bf16 v[84:87],a[60:63],a[148:151],v[84:87]\n"
"ds_read_b64_tr_b16 a[46:47],v240 offset:19200\n"
"v_cvt_pk_bf16_f32 v179,v70,v71\n"
"v_mfma_f32_16x16x32_bf16 v[84:87],a[64:67],a[196:199],v[84:87]\n"
"ds_read_b64_tr_b16 a[48:49],v240 offset:25344\n"
"v_cvt_pk_bf16_f32 v180,v72,v73\n"
"v_mfma_f32_16x16x32_bf16 v[84:87],a[68:71],a[244:247],v[84:87]\n"
"ds_read_b64_tr_b16 a[50:51],v240 offset:31488\n"
"v_cvt_pk_bf16_f32 v181,v74,v75\n"
"v_mfma_f32_16x16x32_bf16 v[84:87],a[72:75],a[212:215],v[84:87]\n"
"ds_read_b64_tr_b16 a[52:53],v240 offset:37632\n"
"v_cvt_pk_bf16_f32 v182,v76,v77\n"
"v_mfma_f32_16x16x32_bf16 v[84:87],a[76:79],a[228:231],v[84:87]\n"
"ds_read_b64_tr_b16 a[54:55],v240 offset:43776\n"
"v_cvt_pk_bf16_f32 v183,v78,v79\n"
"v_mfma_f32_16x16x32_bf16 v[88:91],a[56:59],a[136:139],v[88:91]\n"
"s_mov_b32 s41,4096\n"
"v_mfma_f32_16x16x32_bf16 v[88:91],a[60:63],a[152:155],v[88:91]\n"
"buffer_store_dwordx4 v[176:179],v209,s[84:87],s41 offen sc1\n"
"v_mfma_f32_16x16x32_bf16 v[88:91],a[64:67],a[200:203],v[88:91]\n"
"s_mov_b32 s41,12288\n"
"v_mfma_f32_16x16x32_bf16 v[88:91],a[68:71],a[248:251],v[88:91]\n"
"buffer_store_dwordx4 v[180:183],v209,s[84:87],s41 offen offset:1024 sc1\n"
"v_mfma_f32_16x16x32_bf16 v[88:91],a[72:75],a[216:219],v[88:91]\n"
"v_mfma_f32_16x16x32_bf16 v[88:91],a[76:79],a[232:235],v[88:91]\n"
"v_mfma_f32_16x16x32_bf16 v[92:95],a[56:59],a[140:143],v[92:95]\n"
"v_mfma_f32_16x16x32_bf16 v[92:95],a[60:63],a[156:159],v[92:95]\n"
"v_mfma_f32_16x16x32_bf16 v[92:95],a[64:67],a[204:207],v[92:95]\n"
"v_mfma_f32_16x16x32_bf16 v[92:95],a[68:71],a[252:255],v[92:95]\n"
"v_mfma_f32_16x16x32_bf16 v[92:95],a[72:75],a[220:223],v[92:95]\n"
"v_mfma_f32_16x16x32_bf16 v[92:95],a[76:79],a[236:239],v[92:95]\n"
"s_waitcnt lgkmcnt(0)\n"
"v_mfma_f32_16x16x32_bf16 v[96:99],a[32:35],a[128:131],v[96:99]\n"
"ds_read_b64_tr_b16 a[56:57],v255 offset:25344\n"
"s_nop 0\n"
"v_mfma_f32_16x16x32_bf16 v[96:99],a[36:39],a[144:147],v[96:99]\n"
"ds_read_b64_tr_b16 a[58:59],v255 offset:31488\n"
"s_nop 0\n"
"v_mfma_f32_16x16x32_bf16 v[96:99],a[40:43],a[192:195],v[96:99]\n"
"ds_read_b64_tr_b16 a[60:61],v255 offset:37632\n"
"s_nop 0\n"
"v_mfma_f32_16x16x32_bf16 v[96:99],a[44:47],a[240:243],v[96:99]\n"
"ds_read_b64_tr_b16 a[62:63],v255 offset:43776\n"
"s_nop 0\n"
"v_mfma_f32_16x16x32_bf16 v[96:99],a[48:51],a[208:211],v[96:99]\n"
"ds_read_b64_tr_b16 a[64:65],v241 offset:768\n"
"v_cvt_pk_bf16_f32 v144,v80,v81\n"
"v_mfma_f32_16x16x32_bf16 v[96:99],a[52:55],a[224:227],v[96:99]\n"
"ds_read_b64_tr_b16 a[66:67],v241 offset:6912\n"
"v_cvt_pk_bf16_f32 v145,v82,v83\n"
"v_mfma_f32_16x16x32_bf16 v[100:103],a[32:35],a[132:135],v[100:103]\n"
"ds_read_b64_tr_b16 a[68:69],v241 offset:13056\n"
"v_cvt_pk_bf16_f32 v146,v84,v85\n"
"v_mfma_f32_16x16x32_bf16 v[100:103],a[36:39],a[148:151],v[100:103]\n"
"ds_read_b64_tr_b16 a[70:71],v241 offset:19200\n"
"v_cvt_pk_bf16_f32 v147,v86,v87\n"
"v_mfma_f32_16x16x32_bf16 v[100:103],a[40:43],a[196:199],v[100:103]\n"
"ds_read_b64_tr_b16 a[72:73],v241 offset:25344\n"
"v_cvt_pk_bf16_f32 v148,v88,v89\n"
"v_mfma_f32_16x16x32_bf16 v[100:103],a[44:47],a[244:247],v[100:103]\n"
"ds_read_b64_tr_b16 a[74:75],v241 offset:31488\n"
"v_cvt_pk_bf16_f32 v149,v90,v91\n"
"v_mfma_f32_16x16x32_bf16 v[100:103],a[48:51],a[212:215],v[100:103]\n"
"ds_read_b64_tr_b16 a[76:77],v241 offset:37632\n"
"v_cvt_pk_bf16_f32 v150,v92,v93\n"
"v_mfma_f32_16x16x32_bf16 v[100:103],a[52:55],a[228:231],v[100:103]\n"
"ds_read_b64_tr_b16 a[78:79],v241 offset:43776\n"
"v_cvt_pk_bf16_f32 v151,v94,v95\n"
"v_mfma_f32_16x16x32_bf16 v[104:107],a[32:35],a[136:139],v[104:107]\n"
"s_mov_b32 s41,4096\n"
"v_mfma_f32_16x16x32_bf16 v[104:107],a[36:39],a[152:155],v[104:107]\n"
"buffer_store_dwordx4 v[144:147],v209,s[84:87],s41 offen offset:1024 sc1\n"
"v_mfma_f32_16x16x32_bf16 v[104:107],a[40:43],a[200:203],v[104:107]\n"
"s_mov_b32 s41,12288\n"
"v_mfma_f32_16x16x32_bf16 v[104:107],a[44:47],a[248:251],v[104:107]\n"
"buffer_store_dwordx4 v[148:151],v209,s[84:87],s41 offen offset:2048 sc1\n"
"v_mfma_f32_16x16x32_bf16 v[104:107],a[48:51],a[216:219],v[104:107]\n"
"v_mfma_f32_16x16x32_bf16 v[104:107],a[52:55],a[232:235],v[104:107]\n"
"v_mfma_f32_16x16x32_bf16 v[108:111],a[32:35],a[140:143],v[108:111]\n"
"v_mfma_f32_16x16x32_bf16 v[108:111],a[36:39],a[156:159],v[108:111]\n"
"v_mfma_f32_16x16x32_bf16 v[108:111],a[40:43],a[204:207],v[108:111]\n"
"v_mfma_f32_16x16x32_bf16 v[108:111],a[44:47],a[252:255],v[108:111]\n"
"v_mfma_f32_16x16x32_bf16 v[108:111],a[48:51],a[220:223],v[108:111]\n"
"v_mfma_f32_16x16x32_bf16 v[108:111],a[52:55],a[236:239],v[108:111]\n"
"s_waitcnt vmcnt(4)\n"
"s_barrier\n"
"v_readlane_b32 s43,v210,56\n"
"s_and_b32 s43,s43,0xffff\n"
"s_add_u32 s43,s43,256\n"
"s_lshl_b32 s43,s43,6\n"
"v_lshl_add_u32 v223,v209,2,s43\n"
"s_cmp_lg_u32 s48,3\n"
"s_cbranch_scc1 LM_FS31\n"
"s_mov_b64 exec,1\n"
"buffer_store_dwordx2 v[224:225],v223,s[68:71],0 offen sc1\n"
"s_mov_b64 exec,-1\n"
"LM_FS31:\n"
"s_waitcnt lgkmcnt(0)\n"
"v_mfma_f32_16x16x32_bf16 v[112:115],a[56:59],a[128:131],v[112:115]\n"
"ds_read_b64_tr_b16 a[32:33],v254 offset:25600\n"
"s_nop 0\n"
"v_mfma_f32_16x16x32_bf16 v[112:115],a[60:63],a[144:147],v[112:115]\n"
"ds_read_b64_tr_b16 a[34:35],v254 offset:31744\n"
"s_nop 0\n"
"v_mfma_f32_16x16x32_bf16 v[112:115],a[64:67],a[192:195],v[112:115]\n"
"ds_read_b64_tr_b16 a[36:37],v254 offset:37888\n"
"s_nop 0\n"
"v_mfma_f32_16x16x32_bf16 v[112:115],a[68:71],a[240:243],v[112:115]\n"
"ds_read_b64_tr_b16 a[38:39],v254 offset:44032\n"
"s_nop 0\n"
"v_mfma_f32_16x16x32_bf16 v[112:115],a[72:75],a[208:211],v[112:115]\n"
"ds_read_b64_tr_b16 a[40:41],v240 offset:1024\n"
"v_cvt_pk_bf16_f32 v152,v96,v97\n"
"v_mfma_f32_16x16x32_bf16 v[112:115],a[76:79],a[224:227],v[112:115]\n"
"ds_read_b64_tr_b16 a[42:43],v240 offset:7168\n"
"v_cvt_pk_bf16_f32 v153,v98,v99\n"
"v_mfma_f32_16x16x32_bf16 v[116:119],a[56:59],a[132:135],v[116:119]\n"
"ds_read_b64_tr_b16 a[44:45],v240 offset:13312\n"
"v_cvt_pk_bf16_f32 v154,v100,v101\n"
"v_mfma_f32_16x16x32_bf16 v[116:119],a[60:63],a[148:151],v[116:119]\n"
"ds_read_b64_tr_b16 a[46:47],v240 offset:19456\n"
"v_cvt_pk_bf16_f32 v155,v102,v103\n"
"v_mfma_f32_16x16x32_bf16 v[116:119],a[64:67],a[196:199],v[116:119]\n"
"ds_read_b64_tr_b16 a[48:49],v240 offset:25600\n"
"v_cvt_pk_bf16_f32 v156,v104,v105\n"
"v_mfma_f32_16x16x32_bf16 v[116:119],a[68:71],a[244:247],v[116:119]\n"
"ds_read_b64_tr_b16 a[50:51],v240 offset:31744\n"
"v_cvt_pk_bf16_f32 v157,v106,v107\n"
"v_mfma_f32_16x16x32_bf16 v[116:119],a[72:75],a[212:215],v[116:119]\n"
"ds_read_b64_tr_b16 a[52:53],v240 offset:37888\n"
"v_cvt_pk_bf16_f32 v158,v108,v109\n"
"v_mfma_f32_16x16x32_bf16 v[116:119],a[76:79],a[228:231],v[116:119]\n"
"ds_read_b64_tr_b16 a[54:55],v240 offset:44032\n"
"v_cvt_pk_bf16_f32 v159,v110,v111\n"
"v_mfma_f32_16x16x32_bf16 v[120:123],a[56:59],a[136:139],v[120:123]\n"
"s_mov_b32 s41,4096\n"
"v_mfma_f32_16x16x32_bf16 v[120:123],a[60:63],a[152:155],v[120:123]\n"
"buffer_store_dwordx4 v[152:155],v209,s[84:87],s41 offen offset:2048 sc1\n"
"v_mfma_f32_16x16x32_bf16 v[120:123],a[64:67],a[200:203],v[120:123]\n"
"s_mov_b32 s41,12288\n"
"v_mfma_f32_16x16x32_bf16 v[120:123],a[68:71],a[248:251],v[120:123]\n"
"buffer_store_dwordx4 v[156:159],v209,s[84:87],s41 offen offset:3072 sc1\n"
"v_mfma_f32_16x16x32_bf16 v[120:123],a[72:75],a[216:219],v[120:123]\n"
"v_mfma_f32_16x16x32_bf16 v[120:123],a[76:79],a[232:235],v[120:123]\n"
"v_mfma_f32_16x16x32_bf16 v[124:127],a[56:59],a[140:143],v[124:127]\n"
"v_mfma_f32_16x16x32_bf16 v[124:127],a[60:63],a[156:159],v[124:127]\n"
"v_mfma_f32_16x16x32_bf16 v[124:127],a[64:67],a[204:207],v[124:127]\n"
"v_mfma_f32_16x16x32_bf16 v[124:127],a[68:71],a[252:255],v[124:127]\n"
"v_mfma_f32_16x16x32_bf16 v[124:127],a[72:75],a[220:223],v[124:127]\n"
"v_mfma_f32_16x16x32_bf16 v[124:127],a[76:79],a[236:239],v[124:127]\n"
"s_waitcnt lgkmcnt(0)\n"
"v_mfma_f32_16x16x32_bf16 v[128:131],a[32:35],a[128:131],v[128:131]\n"
"s_nop 0\n"
"v_mfma_f32_16x16x32_bf16 v[128:131],a[36:39],a[144:147],v[128:131]\n"
"s_nop 0\n"
"v_mfma_f32_16x16x32_bf16 v[128:131],a[40:43],a[192:195],v[128:131]\n"
"s_nop 0\n"
"v_mfma_f32_16x16x32_bf16 v[128:131],a[44:47],a[240:243],v[128:131]\n"
"s_nop 0\n"
"v_mfma_f32_16x16x32_bf16 v[128:131],a[48:51],a[208:211],v[128:131]\n"
"v_cvt_pk_bf16_f32 v160,v112,v113\n"
"v_mfma_f32_16x16x32_bf16 v[128:131],a[52:55],a[224:227],v[128:131]\n"
"v_cvt_pk_bf16_f32 v161,v114,v115\n"
"v_mfma_f32_16x16x32_bf16 v[132:135],a[32:35],a[132:135],v[132:135]\n"
"v_cvt_pk_bf16_f32 v162,v116,v117\n"
"v_mfma_f32_16x16x32_bf16 v[132:135],a[36:39],a[148:151],v[132:135]\n"
"v_cvt_pk_bf16_f32 v163,v118,v119\n"
"v_mfma_f32_16x16x32_bf16 v[132:135],a[40:43],a[196:199],v[132:135]\n"
"v_cvt_pk_bf16_f32 v164,v120,v121\n"
"v_mfma_f32_16x16x32_bf16 v[132:135],a[44:47],a[244:247],v[132:135]\n"
"v_cvt_pk_bf16_f32 v165,v122,v123\n"
"v_mfma_f32_16x16x32_bf16 v[132:135],a[48:51],a[212:215],v[132:135]\n"
"v_cvt_pk_bf16_f32 v166,v124,v125\n"
"v_mfma_f32_16x16x32_bf16 v[132:135],a[52:55],a[228:231],v[132:135]\n"
"v_cvt_pk_bf16_f32 v167,v126,v127\n"
"v_mfma_f32_16x16x32_bf16 v[136:139],a[32:35],a[136:139],v[136:139]\n"
"s_mov_b32 s41,4096\n"
"v_mfma_f32_16x16x32_bf16 v[136:139],a[36:39],a[152:155],v[136:139]\n"
"buffer_store_dwordx4 v[160:163],v209,s[84:87],s41 offen offset:3072 sc1\n"
"v_mfma_f32_16x16x32_bf16 v[136:139],a[40:43],a[200:203],v[136:139]\n"
"s_mov_b32 s41,16384\n"
"v_mfma_f32_16x16x32_bf16 v[136:139],a[44:47],a[248:251],v[136:139]\n"
"buffer_store_dwordx4 v[164:167],v209,s[84:87],s41 offen sc1\n"
"v_mfma_f32_16x16x32_bf16 v[136:139],a[48:51],a[216:219],v[136:139]\n"
"v_mfma_f32_16x16x32_bf16 v[136:139],a[52:55],a[232:235],v[136:139]\n"
"v_mfma_f32_16x16x32_bf16 v[140:143],a[32:35],a[140:143],v[140:143]\n"
"v_mfma_f32_16x16x32_bf16 v[140:143],a[36:39],a[156:159],v[140:143]\n"
"v_mfma_f32_16x16x32_bf16 v[140:143],a[40:43],a[204:207],v[140:143]\n"
"v_mfma_f32_16x16x32_bf16 v[140:143],a[44:47],a[252:255],v[140:143]\n"
"v_mfma_f32_16x16x32_bf16 v[140:143],a[48:51],a[220:223],v[140:143]\n"
"v_mfma_f32_16x16x32_bf16 v[140:143],a[52:55],a[236:239],v[140:143]\n"
"s_nop 7\n"
"s_nop 0\n"
"s_nop 0\n"
"s_nop 0\n"
"s_nop 0\n"
"v_cvt_pk_bf16_f32 v168,v128,v129\n"
"v_cvt_pk_bf16_f32 v169,v130,v131\n"
"v_cvt_pk_bf16_f32 v170,v132,v133\n"
"v_cvt_pk_bf16_f32 v171,v134,v135\n"
"v_cvt_pk_bf16_f32 v172,v136,v137\n"
"v_cvt_pk_bf16_f32 v173,v138,v139\n"
"v_cvt_pk_bf16_f32 v174,v140,v141\n"
"v_cvt_pk_bf16_f32 v175,v142,v143\n"
"s_mov_b32 s41,8192\n"
"buffer_store_dwordx4 v[168:171],v209,s[84:87],s41 offen sc1\n"
"s_mov_b32 s41,16384\n"
"buffer_store_dwordx4 v[172:175],v209,s[84:87],s41 offen offset:1024 sc1\n"
"s_nop 1\n"
"v_bfe_u32 v236,v209,4,4\n"
"v_lshrrev_b32 v237,8,v209\n"
"v_and_b32 v238,16,v209\n"
"v_mov_b32 v239,0x3f80\n"
"v_lshlrev_b32 v238,v238,v239\n"
"v_lshrrev_b32 v236,1,v236\n"
"v_lshlrev_b32 v237,1,v237\n"
"v_mov_b32 v216,0\n"
"v_mov_b32 v217,0\n"
"v_mov_b32 v220,0\n"
"v_mov_b32 v221,0\n"
"v_cmp_eq_u32 vcc,v236,v237\n"
"s_nop 1\n"
"v_cndmask_b32 v218,0,v238,vcc\n"
"v_add_u32 v237,1,v237\n"
"v_cmp_eq_u32 vcc,v236,v237\n"
"s_nop 1\n"
"v_cndmask_b32 v219,0,v238,vcc\n"
"v_add_u32 v237,1,v237\n"
"s_nop 1\n"
"v_mfma_f32_16x16x32_bf16 v[120:123],v[218:221],v[192:195],0\n"
"v_mfma_f32_16x16x32_bf16 v[144:147],v[218:221],a[112:115],0\n"
"v_mfma_f32_16x16x32_bf16 v[124:127],v[216:219],v[192:195],0\n"
"v_mfma_f32_16x16x32_bf16 v[148:151],v[216:219],a[112:115],0\n"
"v_mfma_f32_16x16x32_bf16 v[128:131],v[218:221],v[196:199],0\n"
"v_mfma_f32_16x16x32_bf16 v[152:155],v[218:221],a[116:119],0\n"
"v_mfma_f32_16x16x32_bf16 v[132:135],v[216:219],v[196:199],0\n"
"v_mfma_f32_16x16x32_bf16 v[156:159],v[216:219],a[116:119],0\n"
"v_mfma_f32_16x16x32_bf16 v[136:139],v[218:221],v[200:203],0\n"
"v_mfma_f32_16x16x32_bf16 v[160:163],v[218:221],a[120:123],0\n"
"v_mfma_f32_16x16x32_bf16 v[140:143],v[216:219],v[200:203],0\n"
"v_mfma_f32_16x16x32_bf16 v[164:167],v[216:219],a[120:123],0\n"
"buffer_load_dwordx2 v[226:227],v222,s[68:71],0 offen sc1\n"
"s_waitcnt vmcnt(0)\n"
"s_barrier\n"
"v_readlane_b32 s43,v210,56\n"
"s_nop 0\n"
"s_lshl_b32 s43,s43,6\n"
"v_lshl_add_u32 v223,v209,2,s43\n"
"s_cmp_lg_u32 s48,3\n"
"s_cbranch_scc1 LM_FS32\n"
"s_mov_b64 exec,1\n"
"buffer_store_dwordx2 v[224:225],v223,s[68:71],0 offen sc1\n"
"s_mov_b64 exec,-1\n"
"LM_FS32:\n"
"v_cmp_eq_u64 vcc,v[226:227],v[224:225]\n"
"s_nop 1\n"
"s_and_b32 s42,vcc_lo,0xffff\n"
"s_or_b32 s42,s42,s35\n"
"s_lshl_b32 s41,s42,8\n"
"s_or_b32 s42,s42,s41\n"
"s_and_b32 s42,s42,0xffff\n"
"s_andn2_b32 s42,s42,s60\n"
"s_or_b32 s60,s60,s42\n"
"s_bitcmp1_b32 s42,8\n"
"s_cbranch_scc0 LM_SL33\n"
"s_add_u32 s40,s92,0\n"
"buffer_load_dwordx4 a[160:163],v209,s[64:67],s40 offen sc1\n"
"LM_SL33:\n"
"s_bitcmp1_b32 s42,9\n"
"s_cbranch_scc0 LM_SL34\n"
"s_add_u32 s40,s92,2359296\n"
"buffer_load_dwordx4 a[164:167],v209,s[64:67],s40 offen sc1\n"
"LM_SL34:\n"
"s_bitcmp1_b32 s42,10\n"
"s_cbranch_scc0 LM_SL35\n"
"s_add_u32 s40,s92,4718592\n"
"buffer_load_dwordx4 a[168:171],v209,s[64:67],s40 offen sc1\n"
"LM_SL35:\n"
"s_bitcmp1_b32 s42,11\n"
"s_cbranch_scc0 LM_SL36\n"
"s_add_u32 s40,s92,7077888\n"
"buffer_load_dwordx4 a[172:175],v209,s[64:67],s40 offen sc1\n"
"LM_SL36:\n"
"s_bitcmp1_b32 s42,12\n"
"s_cbranch_scc0 LM_SL37\n"
"s_add_u32 s40,s92,9437184\n"
"buffer_load_dwordx4 a[176:179],v209,s[64:67],s40 offen sc1\n"
"LM_SL37:\n"
"s_bitcmp1_b32 s42,13\n"
"s_cbranch_scc0 LM_SL38\n"
"s_add_u32 s40,s92,11796480\n"
"buffer_load_dwordx4 a[180:183],v209,s[64:67],s40 offen sc1\n"
"LM_SL38:\n"
"s_bitcmp1_b32 s42,14\n"
"s_cbranch_scc0 LM_SL39\n"
"s_add_u32 s40,s92,14155776\n"
"buffer_load_dwordx4 a[184:187],v209,s[64:67],s40 offen sc1\n"
"LM_SL39:\n"
"s_bitcmp1_b32 s42,15\n"
"s_cbranch_scc0 LM_SL40\n"
"s_add_u32 s40,s92,16515072\n"
"buffer_load_dwordx4 a[188:191],v209,s[64:67],s40 offen sc1\n"
"LM_SL40:\n"
"s_bitcmp1_b32 s42,0\n"
"s_cbranch_scc0 LM_SL41\n"
"s_add_u32 s40,s93,0\n"
"buffer_load_dwordx4 a[80:83],v209,s[64:67],s40 offen sc1\n"
"s_add_u32 s40,s94,0\n"
"buffer_load_dwordx4 a[0:3],v209,s[64:67],s40 offen sc1\n"
"LM_SL41:\n"
"s_bitcmp1_b32 s42,1\n"
"s_cbranch_scc0 LM_SL42\n"
"s_add_u32 s40,s93,2359296\n"
"buffer_load_dwordx4 a[84:87],v209,s[64:67],s40 offen sc1\n"
"s_add_u32 s40,s94,2359296\n"
"buffer_load_dwordx4 a[4:7],v209,s[64:67],s40 offen sc1\n"
"LM_SL42:\n"
"s_bitcmp1_b32 s42,2\n"
"s_cbranch_scc0 LM_SL43\n"
"s_add_u32 s40,s93,4718592\n"
"buffer_load_dwordx4 a[88:91],v209,s[64:67],s40 offen sc1\n"
"s_add_u32 s40,s94,4718592\n"
"buffer_load_dwordx4 a[8:11],v209,s[64:67],s40 offen sc1\n"
"LM_SL43:\n"
"s_bitcmp1_b32 s42,3\n"
"s_cbranch_scc0 LM_SL44\n"
"s_add_u32 s40,s93,7077888\n"
"buffer_load_dwordx4 a[92:95],v209,s[64:67],s40 offen sc1\n"
"s_add_u32 s40,s94,7077888\n"
"buffer_load_dwordx4 a[12:15],v209,s[64:67],s40 offen sc1\n"
"LM_SL44:\n"
"s_bitcmp1_b32 s42,4\n"
"s_cbranch_scc0 LM_SL45\n"
"s_add_u32 s40,s93,9437184\n"
"buffer_load_dwordx4 a[96:99],v209,s[64:67],s40 offen sc1\n"
"s_add_u32 s40,s94,9437184\n"
"buffer_load_dwordx4 a[16:19],v209,s[64:67],s40 offen sc1\n"
"LM_SL45:\n"
"s_bitcmp1_b32 s42,5\n"
"s_cbranch_scc0 LM_SL46\n"
"s_add_u32 s40,s93,11796480\n"
"buffer_load_dwordx4 a[100:103],v209,s[64:67],s40 offen sc1\n"
"s_add_u32 s40,s94,11796480\n"
"buffer_load_dwordx4 a[20:23],v209,s[64:67],s40 offen sc1\n"
"LM_SL46:\n"
"s_bitcmp1_b32 s42,6\n"
"s_cbranch_scc0 LM_SL47\n"
"s_add_u32 s40,s93,14155776\n"
"buffer_load_dwordx4 a[104:107],v209,s[64:67],s40 offen sc1\n"
"s_add_u32 s40,s94,14155776\n"
"buffer_load_dwordx4 a[24:27],v209,s[64:67],s40 offen sc1\n"
"LM_SL47:\n"
"s_bitcmp1_b32 s42,7\n"
"s_cbranch_scc0 LM_SL48\n"
"s_add_u32 s40,s93,16515072\n"
"buffer_load_dwordx4 a[108:111],v209,s[64:67],s40 offen sc1\n"
"s_add_u32 s40,s94,16515072\n"
"buffer_load_dwordx4 a[28:31],v209,s[64:67],s40 offen sc1\n"
"LM_SL48:\n"
"buffer_load_dwordx2 v[226:227],v222,s[68:71],0 offen sc1\n"
"s_mov_b32 s55,0x40000\n"
"LM_FP:\n"
"s_cmp_eq_u32 s60,0xffff\n"
"s_cbranch_scc1 LM_FD\n"
"s_waitcnt vmcnt(0)\n"
"v_cmp_eq_u64 vcc,v[226:227],v[224:225]\n"
"s_nop 1\n"
"s_and_b32 s42,vcc_lo,0xffff\n"
"s_lshl_b32 s41,s42,8\n"
"s_or_b32 s42,s42,s41\n"
"s_and_b32 s42,s42,0xffff\n"
"s_andn2_b32 s42,s42,s60\n"
"s_or_b32 s60,s60,s42\n"
"s_bitcmp1_b32 s42,8\n"
"s_cbranch_scc0 LM_SL49\n"
"s_add_u32 s40,s92,0\n"
"buffer_load_dwordx4 a[160:163],v209,s[64:67],s40 offen sc1\n"
"LM_SL49:\n"
"s_bitcmp1_b32 s42,9\n"
"s_cbranch_scc0 LM_SL50\n"
"s_add_u32 s40,s92,2359296\n"
"buffer_load_dwordx4 a[164:167],v209,s[64:67],s40 offen sc1\n"
"LM_SL50:\n"
"s_bitcmp1_b32 s42,10\n"
"s_cbranch_scc0 LM_SL51\n"
"s_add_u32 s40,s92,4718592\n"
"buffer_load_dwordx4 a[168:171],v209,s[64:67],s40 offen sc1\n"
"LM_SL51:\n"
"s_bitcmp1_b32 s42,11\n"
"s_cbranch_scc0 LM_SL52\n"
"s_add_u32 s40,s92,7077888\n"
"buffer_load_dwordx4 a[172:175],v209,s[64:67],s40 offen sc1\n"
"LM_SL52:\n"
"s_bitcmp1_b32 s42,12\n"
"s_cbranch_scc0 LM_SL53\n"
"s_add_u32 s40,s92,9437184\n"
"buffer_load_dwordx4 a[176:179],v209,s[64:67],s40 offen sc1\n"
"LM_SL53:\n"
"s_bitcmp1_b32 s42,13\n"
"s_cbranch_scc0 LM_SL54\n"
"s_add_u32 s40,s92,11796480\n"
"buffer_load_dwordx4 a[180:183],v209,s[64:67],s40 offen sc1\n"
"LM_SL54:\n"
"s_bitcmp1_b32 s42,14\n"
"s_cbranch_scc0 LM_SL55\n"
"s_add_u32 s40,s92,14155776\n"
"buffer_load_dwordx4 a[184:187],v209,s[64:67],s40 offen sc1\n"
"LM_SL55:\n"
"s_bitcmp1_b32 s42,15\n"
"s_cbranch_scc0 LM_SL56\n"
"s_add_u32 s40,s92,16515072\n"
"buffer_load_dwordx4 a[188:191],v209,s[64:67],s40 offen sc1\n"
"LM_SL56:\n"
"s_bitcmp1_b32 s42,0\n"
"s_cbranch_scc0 LM_SL57\n"
"s_add_u32 s40,s93,0\n"
"buffer_load_dwordx4 a[80:83],v209,s[64:67],s40 offen sc1\n"
"s_add_u32 s40,s94,0\n"
"buffer_load_dwordx4 a[0:3],v209,s[64:67],s40 offen sc1\n"
"LM_SL57:\n"
"s_bitcmp1_b32 s42,1\n"
"s_cbranch_scc0 LM_SL58\n"
"s_add_u32 s40,s93,2359296\n"
"buffer_load_dwordx4 a[84:87],v209,s[64:67],s40 offen sc1\n"
"s_add_u32 s40,s94,2359296\n"
"buffer_load_dwordx4 a[4:7],v209,s[64:67],s40 offen sc1\n"
"LM_SL58:\n"
"s_bitcmp1_b32 s42,2\n"
"s_cbranch_scc0 LM_SL59\n"
"s_add_u32 s40,s93,4718592\n"
"buffer_load_dwordx4 a[88:91],v209,s[64:67],s40 offen sc1\n"
"s_add_u32 s40,s94,4718592\n"
"buffer_load_dwordx4 a[8:11],v209,s[64:67],s40 offen sc1\n"
"LM_SL59:\n"
"s_bitcmp1_b32 s42,3\n"
"s_cbranch_scc0 LM_SL60\n"
"s_add_u32 s40,s93,7077888\n"
"buffer_load_dwordx4 a[92:95],v209,s[64:67],s40 offen sc1\n"
"s_add_u32 s40,s94,7077888\n"
"buffer_load_dwordx4 a[12:15],v209,s[64:67],s40 offen sc1\n"
"LM_SL60:\n"
"s_bitcmp1_b32 s42,4\n"
"s_cbranch_scc0 LM_SL61\n"
"s_add_u32 s40,s93,9437184\n"
"buffer_load_dwordx4 a[96:99],v209,s[64:67],s40 offen sc1\n"
"s_add_u32 s40,s94,9437184\n"
"buffer_load_dwordx4 a[16:19],v209,s[64:67],s40 offen sc1\n"
"LM_SL61:\n"
"s_bitcmp1_b32 s42,5\n"
"s_cbranch_scc0 LM_SL62\n"
"s_add_u32 s40,s93,11796480\n"
"buffer_load_dwordx4 a[100:103],v209,s[64:67],s40 offen sc1\n"
"s_add_u32 s40,s94,11796480\n"
"buffer_load_dwordx4 a[20:23],v209,s[64:67],s40 offen sc1\n"
"LM_SL62:\n"
"s_bitcmp1_b32 s42,6\n"
"s_cbranch_scc0 LM_SL63\n"
"s_add_u32 s40,s93,14155776\n"
"buffer_load_dwordx4 a[104:107],v209,s[64:67],s40 offen sc1\n"
"s_add_u32 s40,s94,14155776\n"
"buffer_load_dwordx4 a[24:27],v209,s[64:67],s40 offen sc1\n"
"LM_SL63:\n"
"s_bitcmp1_b32 s42,7\n"
"s_cbranch_scc0 LM_SL64\n"
"s_add_u32 s40,s93,16515072\n"
"buffer_load_dwordx4 a[108:111],v209,s[64:67],s40 offen sc1\n"
"s_add_u32 s40,s94,16515072\n"
"buffer_load_dwordx4 a[28:31],v209,s[64:67],s40 offen sc1\n"
"LM_SL64:\n"
"buffer_load_dwordx2 v[226:227],v222,s[68:71],0 offen sc1\n"
"s_sub_u32 s55,s55,1\n"
"s_cbranch_scc0 LM_FP\n"
"LM_FD:\n"
"s_waitcnt vmcnt(1)\n"
"s_mov_b32 s43,0xbfb8aa3b\n"
"v_mfma_f32_16x16x32_bf16 v[144:147],v[218:221],a[160:163],v[144:147]\n"
"v_mfma_f32_16x16x32_bf16 v[144:147],v[218:221],a[168:171],v[144:147]\n"
"v_mfma_f32_16x16x32_bf16 v[144:147],v[218:221],a[176:179],v[144:147]\n"
"v_mfma_f32_16x16x32_bf16 v[144:147],v[218:221],a[184:187],v[144:147]\n"
"v_mfma_f32_16x16x32_bf16 v[96:99],v[218:221],a[164:167],0\n"
"v_mfma_f32_16x16x32_bf16 v[96:99],v[218:221],a[172:175],v[96:99]\n"
"v_mfma_f32_16x16x32_bf16 v[96:99],v[218:221],a[180:183],v[96:99]\n"
"v_mfma_f32_16x16x32_bf16 v[96:99],v[218:221],a[188:191],v[96:99]\n"
"v_mfma_f32_16x16x32_bf16 v[148:151],v[216:219],a[160:163],v[148:151]\n"
"v_mfma_f32_16x16x32_bf16 v[148:151],v[216:219],a[168:171],v[148:151]\n"
"v_mfma_f32_16x16x32_bf16 v[148:151],v[216:219],a[176:179],v[148:151]\n"
"v_mfma_f32_16x16x32_bf16 v[148:151],v[216:219],a[184:187],v[148:151]\n"
"v_mfma_f32_16x16x32_bf16 v[100:103],v[216:219],a[164:167],0\n"
"v_mfma_f32_16x16x32_bf16 v[100:103],v[216:219],a[172:175],v[100:103]\n"
"v_mfma_f32_16x16x32_bf16 v[100:103],v[216:219],a[180:183],v[100:103]\n"
"v_mfma_f32_16x16x32_bf16 v[100:103],v[216:219],a[188:191],v[100:103]\n"
"v_mfma_f32_16x16x32_bf16 v[152:155],v[218:221],a[80:83],v[152:155]\n"
"v_mfma_f32_16x16x32_bf16 v[152:155],v[218:221],a[88:91],v[152:155]\n"
"v_mfma_f32_16x16x32_bf16 v[152:155],v[218:221],a[96:99],v[152:155]\n"
"v_mfma_f32_16x16x32_bf16 v[152:155],v[218:221],a[104:107],v[152:155]\n"
"v_mfma_f32_16x16x32_bf16 v[104:107],v[218:221],a[84:87],0\n"
"v_mfma_f32_16x16x32_bf16 v[104:107],v[218:221],a[92:95],v[104:107]\n"
"v_mfma_f32_16x16x32_bf16 v[104:107],v[218:221],a[100:103],v[104:107]\n"
"v_mfma_f32_16x16x32_bf16 v[104:107],v[218:221],a[108:111],v[104:107]\n"
"v_mfma_f32_16x16x32_bf16 v[156:159],v[216:219],a[80:83],v[156:159]\n"
"v_mfma_f32_16x16x32_bf16 v[156:159],v[216:219],a[88:91],v[156:159]\n"
"v_mfma_f32_16x16x32_bf16 v[156:159],v[216:219],a[96:99],v[156:159]\n"
"v_mfma_f32_16x16x32_bf16 v[156:159],v[216:219],a[104:107],v[156:159]\n"
"v_mfma_f32_16x16x32_bf16 v[108:111],v[216:219],a[84:87],0\n"
"v_mfma_f32_16x16x32_bf16 v[108:111],v[216:219],a[92:95],v[108:111]\n"
"v_mfma_f32_16x16x32_bf16 v[108:111],v[216:219],a[100:103],v[108:111]\n"
"v_mfma_f32_16x16x32_bf16 v[108:111],v[216:219],a[108:111],v[108:111]\n"
"v_mul_f32 v168,s43,v144\n"
"v_mul_f32 v169,s43,v145\n"
"v_mul_f32 v170,s43,v146\n"
"v_mul_f32 v171,s43,v147\n"
"v_mul_f32 v172,s43,v148\n"
"v_mul_f32 v173,s43,v149\n"
"v_mul_f32 v174,s43,v150\n"
"v_mul_f32 v175,s43,v151\n"
"v_exp_f32 v168,v168\n"
"v_exp_f32 v169,v169\n"
"v_exp_f32 v170,v170\n"
"v_exp_f32 v171,v171\n"
"v_exp_f32 v172,v172\n"
"v_exp_f32 v173,v173\n"
"v_exp_f32 v174,v174\n"
"v_exp_f32 v175,v175\n"
"v_add_f32 v168,1.0,v168\n"
"v_add_f32 v169,1.0,v169\n"
"v_add_f32 v170,1.0,v170\n"
"v_add_f32 v171,1.0,v171\n"
"v_add_f32 v172,1.0,v172\n"
"v_add_f32 v173,1.0,v173\n"
"v_add_f32 v174,1.0,v174\n"
"v_add_f32 v175,1.0,v175\n"
"v_rcp_f32 v168,v168\n"
"v_rcp_f32 v169,v169\n"
"v_rcp_f32 v170,v170\n"
"v_rcp_f32 v171,v171\n"
"v_rcp_f32 v172,v172\n"
"v_rcp_f32 v173,v173\n"
"v_rcp_f32 v174,v174\n"
"v_rcp_f32 v175,v175\n"
"v_mul_f32 v168,v168,v144\n"
"v_mul_f32 v169,v169,v145\n"
"v_mul_f32 v170,v170,v146\n"
"v_mul_f32 v171,v171,v147\n"
"v_mul_f32 v172,v172,v148\n"
"v_mul_f32 v173,v173,v149\n"
"v_mul_f32 v174,v174,v150\n"
"v_mul_f32 v175,v175,v151\n"
"v_fma_f32 v168,v168,v96,v120\n"
"v_fma_f32 v169,v169,v97,v121\n"
"v_fma_f32 v170,v170,v98,v122\n"
"v_fma_f32 v171,v171,v99,v123\n"
"v_fma_f32 v172,v172,v100,v124\n"
"v_fma_f32 v173,v173,v101,v125\n"
"v_fma_f32 v174,v174,v102,v126\n"
"v_fma_f32 v175,v175,v103,v127\n"
"v_cvt_pk_bf16_f32 v96,v168,v169\n"
"v_cvt_pk_bf16_f32 v97,v170,v171\n"
"v_cvt_pk_bf16_f32 v98,v172,v173\n"
"v_cvt_pk_bf16_f32 v99,v174,v175\n"
"s_add_u32 s41,s56,0\n"
"buffer_store_dwordx2 v[96:97],v190,s[80:83],s41 offen\n"
"s_add_u32 s41,s56,18432\n"
"buffer_store_dwordx2 v[98:99],v190,s[80:83],s41 offen\n"
"v_mfma_f32_16x16x32_bf16 v[160:163],v[218:221],a[0:3],v[160:163]\n"
"v_mfma_f32_16x16x32_bf16 v[160:163],v[218:221],a[8:11],v[160:163]\n"
"v_mfma_f32_16x16x32_bf16 v[160:163],v[218:221],a[16:19],v[160:163]\n"
"v_mfma_f32_16x16x32_bf16 v[160:163],v[218:221],a[24:27],v[160:163]\n"
"v_mfma_f32_16x16x32_bf16 v[112:115],v[218:221],a[4:7],0\n"
"v_mfma_f32_16x16x32_bf16 v[112:115],v[218:221],a[12:15],v[112:115]\n"
"v_mfma_f32_16x16x32_bf16 v[112:115],v[218:221],a[20:23],v[112:115]\n"
"v_mfma_f32_16x16x32_bf16 v[112:115],v[218:221],a[28:31],v[112:115]\n"
"v_mfma_f32_16x16x32_bf16 v[164:167],v[216:219],a[0:3],v[164:167]\n"
"v_mfma_f32_16x16x32_bf16 v[164:167],v[216:219],a[8:11],v[164:167]\n"
"v_mfma_f32_16x16x32_bf16 v[164:167],v[216:219],a[16:19],v[164:167]\n"
"v_mfma_f32_16x16x32_bf16 v[164:167],v[216:219],a[24:27],v[164:167]\n"
"v_mfma_f32_16x16x32_bf16 v[116:119],v[216:219],a[4:7],0\n"
"v_mfma_f32_16x16x32_bf16 v[116:119],v[216:219],a[12:15],v[116:119]\n"
"v_mfma_f32_16x16x32_bf16 v[116:119],v[216:219],a[20:23],v[116:119]\n"
"v_mfma_f32_16x16x32_bf16 v[116:119],v[216:219],a[28:31],v[116:119]\n"
"v_mul_f32 v168,s43,v152\n"
"v_mul_f32 v169,s43,v153\n"
"v_mul_f32 v170,s43,v154\n"
"v_mul_f32 v171,s43,v155\n"
"v_mul_f32 v172,s43,v156\n"
"v_mul_f32 v173,s43,v157\n"
"v_mul_f32 v174,s43,v158\n"
"v_mul_f32 v175,s43,v159\n"
"v_exp_f32 v168,v168\n"
"v_exp_f32 v169,v169\n"
"v_exp_f32 v170,v170\n"
"v_exp_f32 v171,v171\n"
"v_exp_f32 v172,v172\n"
"v_exp_f32 v173,v173\n"
"v_exp_f32 v174,v174\n"
"v_exp_f32 v175,v175\n"
"v_add_f32 v168,1.0,v168\n"
"v_add_f32 v169,1.0,v169\n"
"v_add_f32 v170,1.0,v170\n"
"v_add_f32 v171,1.0,v171\n"
"v_add_f32 v172,1.0,v172\n"
"v_add_f32 v173,1.0,v173\n"
"v_add_f32 v174,1.0,v174\n"
"v_add_f32 v175,1.0,v175\n"
"v_rcp_f32 v168,v168\n"
"v_rcp_f32 v169,v169\n"
"v_rcp_f32 v170,v170\n"
"v_rcp_f32 v171,v171\n"
"v_rcp_f32 v172,v172\n"
"v_rcp_f32 v173,v173\n"
"v_rcp_f32 v174,v174\n"
"v_rcp_f32 v175,v175\n"
"v_mul_f32 v168,v168,v152\n"
"v_mul_f32 v169,v169,v153\n"
"v_mul_f32 v170,v170,v154\n"
"v_mul_f32 v171,v171,v155\n"
"v_mul_f32 v172,v172,v156\n"
"v_mul_f32 v173,v173,v157\n"
"v_mul_f32 v174,v174,v158\n"
"v_mul_f32 v175,v175,v159\n"
"v_fma_f32 v168,v168,v104,v128\n"
"v_fma_f32 v169,v169,v105,v129\n"
"v_fma_f32 v170,v170,v106,v130\n"
"v_fma_f32 v171,v171,v107,v131\n"
"v_fma_f32 v172,v172,v108,v132\n"
"v_fma_f32 v173,v173,v109,v133\n"
"v_fma_f32 v174,v174,v110,v134\n"
"v_fma_f32 v175,v175,v111,v135\n"
"v_cvt_pk_bf16_f32 v104,v168,v169\n"
"v_cvt_pk_bf16_f32 v105,v170,v171\n"
"v_cvt_pk_bf16_f32 v106,v172,v173\n"
"v_cvt_pk_bf16_f32 v107,v174,v175\n"
"s_add_u32 s41,s57,0\n"
"buffer_store_dwordx2 v[104:105],v190,s[80:83],s41 offen\n"
"s_add_u32 s41,s57,18432\n"
"buffer_store_dwordx2 v[106:107],v190,s[80:83],s41 offen\n"
"s_nop 7\n"
"v_mul_f32 v168,s43,v160\n"
"v_mul_f32 v169,s43,v161\n"
"v_mul_f32 v170,s43,v162\n"
"v_mul_f32 v171,s43,v163\n"
"v_mul_f32 v172,s43,v164\n"
"v_mul_f32 v173,s43,v165\n"
"v_mul_f32 v174,s43,v166\n"
"v_mul_f32 v175,s43,v167\n"
"v_exp_f32 v168,v168\n"
"v_exp_f32 v169,v169\n"
"v_exp_f32 v170,v170\n"
"v_exp_f32 v171,v171\n"
"v_exp_f32 v172,v172\n"
"v_exp_f32 v173,v173\n"
"v_exp_f32 v174,v174\n"
"v_exp_f32 v175,v175\n"
"v_add_f32 v168,1.0,v168\n"
"v_add_f32 v169,1.0,v169\n"
"v_add_f32 v170,1.0,v170\n"
"v_add_f32 v171,1.0,v171\n"
"v_add_f32 v172,1.0,v172\n"
"v_add_f32 v173,1.0,v173\n"
"v_add_f32 v174,1.0,v174\n"
"v_add_f32 v175,1.0,v175\n"
"v_rcp_f32 v168,v168\n"
"v_rcp_f32 v169,v169\n"
"v_rcp_f32 v170,v170\n"
"v_rcp_f32 v171,v171\n"
"v_rcp_f32 v172,v172\n"
"v_rcp_f32 v173,v173\n"
"v_rcp_f32 v174,v174\n"
"v_rcp_f32 v175,v175\n"
"v_mul_f32 v168,v168,v160\n"
"v_mul_f32 v169,v169,v161\n"
"v_mul_f32 v170,v170,v162\n"
"v_mul_f32 v171,v171,v163\n"
"v_mul_f32 v172,v172,v164\n"
"v_mul_f32 v173,v173,v165\n"
"v_mul_f32 v174,v174,v166\n"
"v_mul_f32 v175,v175,v167\n"
"v_fma_f32 v168,v168,v112,v136\n"
"v_fma_f32 v169,v169,v113,v137\n"
"v_fma_f32 v170,v170,v114,v138\n"
"v_fma_f32 v171,v171,v115,v139\n"
"v_fma_f32 v172,v172,v116,v140\n"
"v_fma_f32 v173,v173,v117,v141\n"
"v_fma_f32 v174,v174,v118,v142\n"
"v_fma_f32 v175,v175,v119,v143\n"
"v_cvt_pk_bf16_f32 v112,v168,v169\n"
"v_cvt_pk_bf16_f32 v113,v170,v171\n"
"v_cvt_pk_bf16_f32 v114,v172,v173\n"
"v_cvt_pk_bf16_f32 v115,v174,v175\n"
"s_add_u32 s41,s58,0\n"
"buffer_store_dwordx2 v[112:113],v190,s[80:83],s41 offen\n"
"s_add_u32 s41,s58,18432\n"
"buffer_store_dwordx2 v[114:115],v190,s[80:83],s41 offen\n"
      : "+{v223}"(xfl), "+{v226}"(csink), "+{v228}"(ho2[0]), "+{v229}"(ho2[1]), "+{v230}"(ho2[2]), "+{v231}"(ho2[3])
      : "{v192}"(kb[0]), "{v193}"(kb[1]), "{v194}"(kb[2]), "{v195}"(kb[3]), "{v196}"(vb[0]), "{v197}"(vb[1]),
         "{v198}"(kd[0]), "{v199}"(kd[1]), "{v200}"(kd[2]), "{v201}"(kd[3]), "{v202}"(vd[0]), "{v203}"(vd[1]), "{v204}"(wb),
         "{v205}"(wqd[0]), "{v206}"(wqd[1]), "{v207}"(wqd[2]), "{v208}"(wqd[3]), "{v209}"(l16), "{v210}"(pv), "{v211}"(t1), "{v212}"(pg),
         "{v213}"(wd1[0]), "{v214}"(wd1[1]), "{v215}"(wd1[2]), "{v216}"(wd9[0]), "{v217}"(wd9[1]), "{v218}"(wd9[2]),
         "{v219}"(ho[0]), "{v220}"(ho[1]), "{v221}"(ho[2]), "{v222}"(ho[3]), "{v[224:225]}"(tag), "{v248}"(pv2)
      : "memory", "vcc", "scc", "v0", "v1", "v2", "v3", "v4", "v5", "v6", "v7", "v8", "v9", "v10", "v11", "v12", "v13", "v14", "v15", "v16", "v17", "v18", "v19", "v20", "v21", "v22", "v23", "v24", "v25", "v26", "v27", "v28", "v29", "v30", "v31", "v32", "v33", "v34", "v35", "v36", "v37", "v38", "v39", "v40", "v41", "v42", "v43", "v44", "v45", "v46", "v47", "v48", "v49", "v50", "v51", "v52", "v53", "v54", "v55", "v56", "v57", "v58", "v59", "v60", "v61", "v62", "v63", "v64", "v65", "v66", "v67", "v68", "v69", "v70", "v71", "v72", "v73", "v74", "v75", "v76", "v77", "v78", "v79", "v80", "v81", "v82", "v83", "v84", "v85", "v86", "v87", "v88", "v89", "v90", "v91", "v92", "v93", "v94", "v95", "v96", "v97", "v98", "v99", "v100", "v101", "v102", "v103", "v104", "v105", "v106", "v107", "v108", "v109", "v110", "v111", "v112", "v113", "v114", "v115", "v116", "v117", "v118", "v119", "v120", "v121", "v122", "v123", "v124", "v125", "v126", "v127", "v128", "v129", "v130", "v131", "v132", "v133", "v134", "v135", "v136", "v137", "v138", "v139", "v140", "v141", "v142", "v143", "v144", "v145", "v146", "v147", "v148", "v149", "v150", "v151", "v152", "v153", "v154", "v155", "v156", "v157", "v158", "v159", "v160", "v161", "v162", "v163", "v164", "v165", "v166", "v167", "v168", "v169", "v170", "v171", "v172", "v173", "v174", "v175", "v176", "v177", "v178", "v179", "v180", "v181", "v182", "v183", "v184", "v185", "v186", "v187", "v188", "v189", "v190", "v191", "v227", "v232", "v233", "v234", "v235", "v236", "v237", "v238", "v239", "v240", "v241", "v242", "v243", "v244", "v245", "v246", "v247", "v252", "v253", "v254", "v255", "a0", "a1", "a2", "a3", "a4", "a5", "a6", "a7", "a8", "a9", "a10", "a11", "a12", "a13", "a14", "a15", "a16", "a17", "a18", "a19", "a20", "a21", "a22", "a23", "a24", "a25", "a26", "a27", "a28", "a29", "a30", "a31", "a32", "a33", "a34", "a35", "a36", "a37", "a38", "a39", "a40", "a41", "a42", "a43", "a44", "a45", "a46", "a47", "a48", "a49", "a50", "a51", "a52", "a53", "a54", "a55", "a56", "a57", "a58", "a59", "a60", "a61", "a62", "a63", "a64", "a65", "a66", "a67", "a68", "a69", "a70", "a71", "a72", "a73", "a74", "a75", "a76", "a77", "a78", "a79", "a80", "a81", "a82", "a83", "a84", "a85", "a86", "a87", "a88", "a89", "a90", "a91", "a92", "a93", "a94", "a95", "a96", "a97", "a98", "a99", "a100", "a101", "a102", "a103", "a104", "a105", "a106", "a107", "a108", "a109", "a110", "a111", "a112", "a113", "a114", "a115", "a116", "a117", "a118", "a119", "a120", "a121", "a122", "a123", "a124", "a125", "a126", "a127", "a128", "a129", "a130", "a131", "a132", "a133", "a134", "a135", "a136", "a137", "a138", "a139", "a140", "a141", "a142", "a143", "a144", "a145", "a146", "a147", "a148", "a149", "a150", "a151", "a152", "a153", "a154", "a155", "a156", "a157", "a158", "a159", "a160", "a161", "a162", "a163", "a164", "a165", "a166", "a167", "a168", "a169", "a170", "a171", "a172", "a173", "a174", "a175", "a176", "a177", "a178", "a179", "a180", "a181", "a182", "a183", "a184", "a185", "a186", "a187", "a188", "a189", "a190", "a191", "a192", "a193", "a194", "a195", "a196", "a197", "a198", "a199", "a200", "a201", "a202", "a203", "a204", "a205", "a206", "a207", "a208", "a209", "a210", "a211", "a212", "a213", "a214", "a215", "a216", "a217", "a218", "a219", "a220", "a221", "a222", "a223", "a224", "a225", "a226", "a227", "a228", "a229", "a230", "a231", "a232", "a233", "a234", "a235", "a236", "a237", "a238", "a239", "a240", "a241", "a242", "a243", "a244", "a245", "a246", "a247", "a248", "a249", "a250", "a251", "a252", "a253", "a254", "a255", "s35", "s36", "s37", "s38", "s39", "s40", "s41", "s42", "s43", "s44", "s45", "s46", "s47", "s48", "s49", "s50", "s51", "s52", "s53", "s54", "s55", "s56", "s57", "s58", "s59", "s60", "s61", "s62", "s63", "s64", "s65", "s66", "s67", "s68", "s69", "s70", "s71", "s72", "s73", "s74", "s75", "s76", "s77", "s78", "s79", "s80", "s81", "s82", "s83", "s84", "s85", "s86", "s87", "s88", "s89", "s90", "s91", "s92", "s93", "s94", "s95", "s96", "s97", "s98", "s99");
  }
  asm volatile("LM_FIN:");
}

extern "C" void kernel_launch(void* const* d_in, const int* in_sizes, int n_in, void* d_out, int out_size,
                              void* d_ws, size_t ws_size, hipStream_t stream) {
  (void)hipFuncSetAttribute((const void*)mega, hipFuncAttributeMaxDynamicSharedMemorySize, 163840);
  mega<<<256, 256, 163840, stream>>>((const u16*)d_in[0], (const u16*)d_in[1], (const u16*)d_in[2], (const u16*)d_in[3], (const u16*)d_in[4],
                                     (const u16*)d_in[6], (const u16*)d_in[7], (const u16*)d_in[8], (const u16*)d_in[9], (u16*)d_out, (char*)d_ws);
}
```
